```python
import torch

def _softmax(x, axis=-1):
    return torch.softmax(x, dim=axis)
def _gelu(x):
    return torch.nn.functional.gelu(x, approximate="tanh")
def _silu(x):
    return torch.nn.functional.silu(x)
def _sigmoid(x):
    return torch.sigmoid(x)
def _layernorm(x, axis=-1, eps=1e-5):
    m = x.mean(dim=axis, keepdim=True)
    v = x.var(dim=axis, keepdim=True, unbiased=False)
    return (x - m) / torch.sqrt(v + eps)
def _scatter_add(tgt, idx, src, axis=0):
    return tgt.clone().index_add_(axis, idx, src)
def _segment_sum(src, seg, n):
    out = torch.zeros((n,) + src.shape[1:], dtype=src.dtype)
    return out.index_add_(0, seg, src)
def _pad(x, pw):
    flat = tuple(v for pair in reversed(pw) for v in pair)
    return torch.nn.functional.pad(x, flat)

kernel_name = 'recurrent_scan__sliding_block_cached__glu_gated_glu__prompt'

def setup_inputs():

    _w = (lambda a: (lambda t: t.to(torch.bfloat16) if t.is_floating_point() else t)(torch.as_tensor(a)))
    def _A_setup():
        B, Sq, D = 1, 2048, 1024
        Hq, Hkv, Sk, Dh = 32, 4, 2048, 128
        assert Hq % Hkv == 0, (Hq, Hkv)
        return {
            "x": _w((lambda *s: torch.randn(*s, dtype=torch.bfloat16) * 0.05)(B, Sq, D)),
            "k_cache": _w((lambda *s: torch.randn(*s, dtype=torch.bfloat16) * 0.8)(B, Hkv, Sk, Dh)),
            "v_cache": _w((lambda *s: torch.randn(*s, dtype=torch.bfloat16) * 0.2)(B, Hkv, Sk, Dh)),
            "wq": _w((lambda *s: torch.randn(*s, dtype=torch.bfloat16) * 0.8)(D, Hq * Dh) * (D ** -0.5)),
            "wo": _w((lambda *s: torch.randn(*s, dtype=torch.bfloat16) * 0.2)(Hq * Dh, D) * ((Hq * Dh) ** -0.5)),
        }
    _A_in = _A_setup()

    def _B_setup():
        B, S, D, F = 1, 2048, 1024, 3584
        return {
            "x": _w((lambda *s: torch.randn(*s, dtype=torch.bfloat16) * 0.05)(B, S, D)),
            "w_gate": _w((lambda *s: torch.randn(*s, dtype=torch.bfloat16) * 0.2)(D, F)),
            "w_up": _w((lambda *s: torch.randn(*s, dtype=torch.bfloat16) * 0.1)(D, F)),
            "w_down": _w((lambda *s: torch.randn(*s, dtype=torch.bfloat16) * 0.1)(F, D)),
        }
    _B_in = _B_setup()

    return {
        "A_x": _A_in["x"],
        "A_k_cache": _A_in["k_cache"],
        "A_v_cache": _A_in["v_cache"],
        "A_wq": _A_in["wq"],
        "A_wo": _A_in["wo"],
        "B_x": _B_in["x"],
        "B_w_gate": _B_in["w_gate"],
        "B_w_up": _B_in["w_up"],
        "B_w_down": _B_in["w_down"],
    }

def reference(A_x, A_k_cache, A_v_cache, A_wq, A_wo, B_x, B_w_gate, B_w_up, B_w_down):
    def _A_ref(x, k_cache, v_cache, wq, wo):
        B, Sq, D = x.shape
        Hq, Dh = 32, 128
        Hkv, Sk = k_cache.shape[1], k_cache.shape[2]
        W = min(4096, max(256, Sk // 4))
        rep = Hq // Hkv
        q = (lambda x, *d: x.permute(*d))((x @ wq).reshape(B, Sq, Hq, Dh), 0, 2, 1, 3)
        k = (lambda x, n, axis: torch.repeat_interleave(x, n, dim=axis))(k_cache[:, :, Sk - W:, :], rep, axis=1)
        v = (lambda x, n, axis: torch.repeat_interleave(x, n, dim=axis))(v_cache[:, :, Sk - W:, :], rep, axis=1)
        scores = torch.einsum("bhqd,bhkd->bhqk", q, k) * (Dh ** -0.5)
        p = _softmax(scores, axis=-1)
        o = torch.einsum("bhqk,bhkd->bhqd", p, v)
        return (lambda x, *d: x.permute(*d))(o, 0, 2, 1, 3).reshape(B, Sq, Hq * Dh) @ wo

    def _B_ref(x, w_gate, w_up, w_down):
        g = torch.einsum("bsd,df->bsf", x, w_gate)
        u = torch.einsum("bsd,df->bsf", x, w_up)
        h = _sigmoid(g) * u
        return torch.einsum("bsf,fd->bsd", h, w_down)

    x = A_x
    xn = _layernorm(x, axis=-1)
    s = _A_ref(xn, A_k_cache, A_v_cache, A_wq, A_wo)
    g = _silu(_B_ref(xn, B_w_gate, B_w_up, B_w_down))
    return x + s * g
```

```cpp
#include <hip/hip_runtime.h>
#include <hip/hip_bf16.h>
#include <stdint.h>

struct Params { const void* x; const void* kc; const void* vc; const void* wq; const void* wo; const void* wg; const void* wu; const void* wd; void* out; void* ws; };

extern "C" __global__ __launch_bounds__(256,1) void fused_main(Params prm) {
  asm volatile(
R"ASM(KSTART_%=:
s_mov_b32 s4, %2
s_mov_b32 s5, %1
s_load_dwordx16 s[48:63], %0, 0x0
s_load_dwordx4 s[64:67], %0, 0x40
s_getpc_b64 s[40:41]
CTa_%=:
s_sub_u32 s40, s40, CTa_%=-KSTART_%=
s_subb_u32 s41, s41, 0
s_lshr_b32 s45, s5, 3
s_lshl_b32 s45, s45, 2
s_add_u32 s45, s45, s4
s_lshl_b32 s45, s45, 13
s_cmp_lt_u32 s45, END_%=-KSTART_%=
s_cselect_b32 s44, 0x80, 0
s_min_u32 s45, s45, END_%=-KSTART_%=-8192
v_mbcnt_lo_u32_b32 v254, -1, 0
v_mbcnt_hi_u32_b32 v254, -1, v254
v_mul_u32_u24 v254, s44, v254
v_add_u32 v254, s45, v254
global_load_dword a0, v254, s[40:41]
v_mbcnt_lo_u32_b32 v74, -1, 0
v_mbcnt_hi_u32_b32 v74, -1, v74
v_lshrrev_b32 v75, 5, v74
s_lshl_b32 s36, s4, 1
v_add_u32 v72, s36, v75
v_lshlrev_b32 v72, 11, v72
v_and_b32 v76, 31, v74
v_lshl_add_u32 v72, v76, 4, v72
s_lshl_b32 s47, s5, 14
s_movk_i32 s38, 0x820
v_add_u32 v73, s36, v75
v_mul_u32_u24 v73, s38, v73
v_lshl_add_u32 v73, v76, 4, v73
v_add_u32 v73, 0x14000, v73
v_and_b32 v76, 7, v74
v_mul_u32_u24 v130, s38, v76
v_bfe_u32 v131, v74, 3, 2
v_lshl_add_u32 v130, v131, 3, v130
v_lshl_add_u32 v130, v75, 6, v130
s_lshl_b32 s38, s4, 9
s_add_u32 s38, s38, 0x14000
v_add_u32 v130, s38, v130
s_waitcnt lgkmcnt(0)
s_load_dword s33, s[66:67], 0x0
s_mov_b32 s20, s48
s_and_b32 s21, s49, 0xffff
s_mov_b32 s22, 0x400000
s_mov_b32 s23, 0x20000
buffer_load_dwordx4 v[24:27], v72, s[20:23], s47 offen
buffer_load_dwordx4 v[28:31], v72, s[20:23], s47 offen offset:512
buffer_load_dwordx4 v[32:35], v72, s[20:23], s47 offen offset:1024
buffer_load_dwordx4 v[36:39], v72, s[20:23], s47 offen offset:1536
v_mbcnt_lo_u32_b32 v20, -1, 0
v_mbcnt_hi_u32_b32 v20, -1, v20
v_lshlrev_b32 v0, 4, v20
v_lshrrev_b32 v21, 4, v20
v_lshlrev_b32 v21, 16, v21
v_and_b32 v22, 15, v20
v_lshlrev_b32 v22, 4, v22
v_add_u32 v1, v21, v22
v_add_u32 v2, 0x1be0, v1
v_add_u32 v3, 0x37c0, v1
v_add_u32 v4, 0x53a0, v1
s_movk_i32 s34, 0x124a
s_movk_i32 s35, 0x1c00
v_add_u32 v21, 0, v20
v_mul_u32_u24 v22, s34, v21
v_lshrrev_b32 v22, 16, v22
v_mul_u32_u24 v23, 14, v22
v_sub_u32 v21, v21, v23
v_lshlrev_b32 v21, 4, v21
v_mul_u32_u24 v22, s35, v22
v_add_u32 v5, v21, v22
v_add_u32 v21, 64, v20
v_mul_u32_u24 v22, s34, v21
v_lshrrev_b32 v22, 16, v22
v_mul_u32_u24 v23, 14, v22
v_sub_u32 v21, v21, v23
v_lshlrev_b32 v21, 4, v21
v_mul_u32_u24 v22, s35, v22
v_add_u32 v6, v21, v22
v_subrev_u32 v6, 0x400, v6
v_add_u32 v21, 0x80, v20
v_mul_u32_u24 v22, s34, v21
v_lshrrev_b32 v22, 16, v22
v_mul_u32_u24 v23, 14, v22
v_sub_u32 v21, v21, v23
v_lshlrev_b32 v21, 4, v21
v_mul_u32_u24 v22, s35, v22
v_add_u32 v7, v21, v22
v_subrev_u32 v7, 0x800, v7
v_add_u32 v21, 0xc0, v20
v_mul_u32_u24 v22, s34, v21
v_lshrrev_b32 v22, 16, v22
v_mul_u32_u24 v23, 14, v22
v_sub_u32 v21, v21, v23
v_lshlrev_b32 v21, 4, v21
v_mul_u32_u24 v22, s35, v22
v_add_u32 v8, v21, v22
v_subrev_u32 v8, 0xc00, v8
v_add_u32 v21, 0x100, v20
v_mul_u32_u24 v22, s34, v21
v_lshrrev_b32 v22, 16, v22
v_mul_u32_u24 v23, 14, v22
v_sub_u32 v21, v21, v23
v_lshlrev_b32 v21, 4, v21
v_mul_u32_u24 v22, s35, v22
v_add_u32 v9, v21, v22
v_add_u32 v21, 0x140, v20
v_mul_u32_u24 v22, s34, v21
v_lshrrev_b32 v22, 16, v22
v_mul_u32_u24 v23, 14, v22
v_sub_u32 v21, v21, v23
v_lshlrev_b32 v21, 4, v21
v_mul_u32_u24 v22, s35, v22
v_add_u32 v10, v21, v22
v_subrev_u32 v10, 0x400, v10
v_add_u32 v21, 0x180, v20
v_mul_u32_u24 v22, s34, v21
v_lshrrev_b32 v22, 16, v22
v_mul_u32_u24 v23, 14, v22
v_sub_u32 v21, v21, v23
v_lshlrev_b32 v21, 4, v21
v_mul_u32_u24 v22, s35, v22
v_add_u32 v11, v21, v22
v_subrev_u32 v11, 0x800, v11
s_and_b32 s36, s5, 7
s_lshr_b32 s37, s5, 3
s_and_b32 s6, s37, 7
s_lshr_b32 s37, s37, 3
s_lshl_b32 s36, s36, 2
s_add_u32 s7, s36, s37
s_lshr_b32 s68, s4, 1
s_and_b32 s69, s4, 1
s_lshl_b32 s36, s68, 14
v_add_u32 v12, s36, v0
v_add_u32 v13, 0x13200, v12
v_lshrrev_b32 v21, 4, v20
v_bfe_u32 v22, v20, 2, 2
v_and_b32 v23, 3, v20
v_lshlrev_b32 v23, 3, v23
v_and_b32 v15, 1, v21
v_lshl_add_u32 v15, v15, 2, v22
s_movk_i32 s34, 0x420
v_mul_u32_u24 v15, s34, v15
v_lshrrev_b32 v14, 1, v21
v_lshlrev_b32 v14, 8, v14
v_add3_u32 v14, v14, v15, v23
v_add_u32 v14, 0x8000, v14
v_add_u32 v15, 0x13200, v14
v_lshl_add_u32 v16, v21, 2, v22
s_movk_i32 s34, 0xe0
v_mul_u32_u24 v16, s34, v16
v_add_u32 v16, v16, v23
v_add_u32 v16, 0xc200, v16
s_and_b32 s36, s7, 1
s_mul_i32 s37, s36, 0x60
s_cmp_eq_u32 s36, 1
s_cselect_b32 s38, 32, 0x80
s_cselect_b32 s39, 0, 0xc0
s_cmp_eq_u32 s69, 0
s_cselect_b32 s37, s37, s38
v_add_u32 v18, s39, v16
v_add_u32 v19, 0x13200, v18
v_add_u32 v16, s37, v16
v_add_u32 v17, 0x13200, v16
s_mov_b32 s76, s66
s_and_b32 s77, s67, 0xffff
s_mov_b32 s78, 0x3c00000
s_mov_b32 s79, 0x20000
s_mov_b32 s8, s66
s_and_b32 s9, s67, 0xffff
s_mov_b32 s10, 0x500000
s_mov_b32 s11, 0x20000
s_mov_b32 s12, s54
s_and_b32 s13, s55, 0xffff
s_mov_b32 s14, 0x800000
s_mov_b32 s15, 0x20000
s_cmp_eq_u32 s69, 0
s_cselect_b32 s36, s58, s60
s_cselect_b32 s37, s59, s61
s_mov_b32 s16, s36
s_and_b32 s17, s37, 0xffff
s_mov_b32 s18, 0x700000
s_mov_b32 s19, 0x20000
s_lshl_b32 s24, s6, 19
s_lshl_b32 s36, s4, 13
s_add_u32 s24, s24, s36
s_add_u32 s24, s24, 0x100000
s_lshl_b32 s25, s68, 18
s_lshl_b32 s36, s69, 15
s_add_u32 s25, s25, s36
s_lshl_b32 s36, s7, 8
s_add_u32 s25, s25, s36
s_mul_i32 s26, s68, 0x38000
s_mul_i32 s36, s7, 0xe0
s_add_u32 s26, s26, s36
s_lshl_b32 s28, s4, 13
s_mul_i32 s29, s68, 0x2100
s_mul_i32 s36, s69, 0x1080
s_add_u32 s29, s29, s36
s_add_u32 s29, s29, 0x8000
s_mul_i32 s30, s69, 0x3800
s_mul_i32 s36, s68, 0x1c00
s_add_u32 s30, s30, s36
s_add_u32 s30, s30, 0xc200
s_waitcnt lgkmcnt(0)
s_add_u32 s33, s33, 1
s_waitcnt vmcnt(0)
ds_write_b128 v73, v[24:27]
ds_write_b128 v73, v[28:31] offset:512
ds_write_b128 v73, v[32:35] offset:1024
ds_write_b128 v73, v[36:39] offset:1536
v_mov_b32 v77, 0x3f803f80
v_mov_b32 v78, 0
v_mov_b32 v79, 0
v_mov_b32 v80, 0
v_mov_b32 v81, 0
v_dot2c_f32_bf16 v78, v24, v77
v_dot2c_f32_bf16 v79, v25, v77
v_dot2c_f32_bf16 v80, v26, v77
v_dot2c_f32_bf16 v81, v27, v77
v_dot2c_f32_bf16 v78, v28, v77
v_dot2c_f32_bf16 v79, v29, v77
v_dot2c_f32_bf16 v80, v30, v77
v_dot2c_f32_bf16 v81, v31, v77
v_dot2c_f32_bf16 v78, v32, v77
v_dot2c_f32_bf16 v79, v33, v77
v_dot2c_f32_bf16 v80, v34, v77
v_dot2c_f32_bf16 v81, v35, v77
v_dot2c_f32_bf16 v78, v36, v77
v_dot2c_f32_bf16 v79, v37, v77
v_dot2c_f32_bf16 v80, v38, v77
v_dot2c_f32_bf16 v81, v39, v77
s_nop 2
v_add_f32 v78, v78, v79
v_add_f32 v80, v80, v81
v_add_f32 v78, v78, v80
s_nop 1
v_add_f32_dpp v78, v78, v78 row_ror:8 row_mask:0xf bank_mask:0xf
s_nop 1
v_add_f32_dpp v78, v78, v78 row_ror:4 row_mask:0xf bank_mask:0xf
s_nop 1
v_add_f32_dpp v78, v78, v78 row_ror:2 row_mask:0xf bank_mask:0xf
s_nop 1
v_add_f32_dpp v78, v78, v78 row_ror:1 row_mask:0xf bank_mask:0xf
v_mov_b32 v79, v78
s_nop 1
v_permlane16_swap_b32 v78, v79
v_add_f32 v78, v78, v79
v_mul_f32 v82, 0x3a800000, v78
v_mov_b32 v78, 0
v_mov_b32 v79, 0
v_mov_b32 v80, 0
v_mov_b32 v81, 0
v_lshlrev_b32 v84, 16, v24
v_and_b32 v85, 0xffff0000, v24
v_lshlrev_b32 v86, 16, v25
v_and_b32 v87, 0xffff0000, v25
v_lshlrev_b32 v88, 16, v26
v_and_b32 v89, 0xffff0000, v26
v_lshlrev_b32 v90, 16, v27
v_and_b32 v91, 0xffff0000, v27
v_sub_f32 v84, v84, v82
v_sub_f32 v85, v85, v82
v_sub_f32 v86, v86, v82
v_sub_f32 v87, v87, v82
v_sub_f32 v88, v88, v82
v_sub_f32 v89, v89, v82
v_sub_f32 v90, v90, v82
v_sub_f32 v91, v91, v82
v_fmac_f32 v78, v84, v84
v_fmac_f32 v79, v85, v85
v_fmac_f32 v80, v86, v86
v_fmac_f32 v81, v87, v87
v_fmac_f32 v78, v88, v88
v_fmac_f32 v79, v89, v89
v_fmac_f32 v80, v90, v90
v_fmac_f32 v81, v91, v91
v_lshlrev_b32 v92, 16, v28
v_and_b32 v93, 0xffff0000, v28
v_lshlrev_b32 v94, 16, v29
v_and_b32 v95, 0xffff0000, v29
v_lshlrev_b32 v96, 16, v30
v_and_b32 v97, 0xffff0000, v30
v_lshlrev_b32 v98, 16, v31
v_and_b32 v99, 0xffff0000, v31
v_sub_f32 v92, v92, v82
v_sub_f32 v93, v93, v82
v_sub_f32 v94, v94, v82
v_sub_f32 v95, v95, v82
v_sub_f32 v96, v96, v82
v_sub_f32 v97, v97, v82
v_sub_f32 v98, v98, v82
v_sub_f32 v99, v99, v82
v_fmac_f32 v78, v92, v92
v_fmac_f32 v79, v93, v93
v_fmac_f32 v80, v94, v94
v_fmac_f32 v81, v95, v95
v_fmac_f32 v78, v96, v96
v_fmac_f32 v79, v97, v97
v_fmac_f32 v80, v98, v98
v_fmac_f32 v81, v99, v99
v_lshlrev_b32 v84, 16, v32
v_and_b32 v85, 0xffff0000, v32
v_lshlrev_b32 v86, 16, v33
v_and_b32 v87, 0xffff0000, v33
v_lshlrev_b32 v88, 16, v34
v_and_b32 v89, 0xffff0000, v34
v_lshlrev_b32 v90, 16, v35
v_and_b32 v91, 0xffff0000, v35
v_sub_f32 v84, v84, v82
v_sub_f32 v85, v85, v82
v_sub_f32 v86, v86, v82
v_sub_f32 v87, v87, v82
v_sub_f32 v88, v88, v82
v_sub_f32 v89, v89, v82
v_sub_f32 v90, v90, v82
v_sub_f32 v91, v91, v82
v_fmac_f32 v78, v84, v84
v_fmac_f32 v79, v85, v85
v_fmac_f32 v80, v86, v86
v_fmac_f32 v81, v87, v87
v_fmac_f32 v78, v88, v88
v_fmac_f32 v79, v89, v89
v_fmac_f32 v80, v90, v90
v_fmac_f32 v81, v91, v91
v_lshlrev_b32 v92, 16, v36
v_and_b32 v93, 0xffff0000, v36
v_lshlrev_b32 v94, 16, v37
v_and_b32 v95, 0xffff0000, v37
v_lshlrev_b32 v96, 16, v38
v_and_b32 v97, 0xffff0000, v38
v_lshlrev_b32 v98, 16, v39
v_and_b32 v99, 0xffff0000, v39
v_sub_f32 v92, v92, v82
v_sub_f32 v93, v93, v82
v_sub_f32 v94, v94, v82
v_sub_f32 v95, v95, v82
v_sub_f32 v96, v96, v82
v_sub_f32 v97, v97, v82
v_sub_f32 v98, v98, v82
v_sub_f32 v99, v99, v82
v_fmac_f32 v78, v92, v92
v_fmac_f32 v79, v93, v93
v_fmac_f32 v80, v94, v94
v_fmac_f32 v81, v95, v95
v_fmac_f32 v78, v96, v96
v_fmac_f32 v79, v97, v97
v_fmac_f32 v80, v98, v98
v_fmac_f32 v81, v99, v99
v_add_f32 v78, v78, v79
v_add_f32 v80, v80, v81
v_add_f32 v78, v78, v80
s_nop 1
v_add_f32_dpp v78, v78, v78 row_ror:8 row_mask:0xf bank_mask:0xf
s_nop 1
v_add_f32_dpp v78, v78, v78 row_ror:4 row_mask:0xf bank_mask:0xf
s_nop 1
v_add_f32_dpp v78, v78, v78 row_ror:2 row_mask:0xf bank_mask:0xf
s_nop 1
v_add_f32_dpp v78, v78, v78 row_ror:1 row_mask:0xf bank_mask:0xf
v_mov_b32 v79, v78
s_nop 1
v_permlane16_swap_b32 v78, v79
v_add_f32 v78, v78, v79
s_mov_b32 s40, 0x3a800000
v_mov_b32 v79, 0x3727c5ac
v_fma_f32 v78, v78, s40, v79
v_rsq_f32 v83, v78
s_lshl_b32 s40, s4, 4
v_lshlrev_b32 v100, 3, v75
v_add_u32 v100, s40, v100
v_add_u32 v100, 0x13880, v100
ds_write_b64 v100, v[82:83]
v_lshlrev_b32 v101, 3, v76
v_add_u32 v101, 0x13880, v101
v_lshlrev_b32 v102, 4, v76
v_bfe_u32 v103, v74, 3, 2
v_lshl_add_u32 v102, v103, 8, v102
v_lshl_add_u32 v102, v75, 10, v102
s_lshr_b32 s40, s5, 5
s_lshl_b32 s40, s40, 19
s_bfe_u32 s41, s5, 0x40001
s_lshl_b32 s41, s41, 11
s_add_u32 s40, s40, s41
s_and_b32 s41, s5, 1
s_lshl_b32 s41, s41, 7
s_add_u32 s40, s40, s41
s_lshl_b32 s41, s4, 17
s_add_u32 s40, s40, s41
s_add_u32 s40, s40, 0x100000
s_waitcnt lgkmcnt(0)
s_barrier
ds_read_b64 v[104:105], v101
ds_read_b64 v[56:57], v130
ds_read_b64 v[58:59], v130 offset:32
ds_read_b64 v[60:61], v130 offset:128
ds_read_b64 v[62:63], v130 offset:160
)ASM"
R"ASM(ds_read_b64 v[64:65], v130 offset:256
ds_read_b64 v[66:67], v130 offset:288
ds_read_b64 v[68:69], v130 offset:384
ds_read_b64 v[70:71], v130 offset:416
s_waitcnt lgkmcnt(0)
v_mul_f32 v106, v104, v105
v_sub_f32 v106, 0, v106
v_lshlrev_b32 v84, 16, v56
v_and_b32 v85, 0xffff0000, v56
v_lshlrev_b32 v86, 16, v57
v_and_b32 v87, 0xffff0000, v57
v_lshlrev_b32 v88, 16, v58
v_and_b32 v89, 0xffff0000, v58
v_lshlrev_b32 v90, 16, v59
v_and_b32 v91, 0xffff0000, v59
v_fma_f32 v84, v84, v105, v106
v_fma_f32 v85, v85, v105, v106
v_fma_f32 v86, v86, v105, v106
v_fma_f32 v87, v87, v105, v106
v_fma_f32 v88, v88, v105, v106
v_fma_f32 v89, v89, v105, v106
v_fma_f32 v90, v90, v105, v106
v_fma_f32 v91, v91, v105, v106
v_cvt_pk_bf16_f32 v108, v84, v85
v_cvt_pk_bf16_f32 v109, v86, v87
v_cvt_pk_bf16_f32 v110, v88, v89
v_cvt_pk_bf16_f32 v111, v90, v91
s_add_u32 s41, s40, 0
buffer_store_dwordx4 v[108:111], v102, s[76:79], s41 offen sc1
v_lshlrev_b32 v84, 16, v60
v_and_b32 v85, 0xffff0000, v60
v_lshlrev_b32 v86, 16, v61
v_and_b32 v87, 0xffff0000, v61
v_lshlrev_b32 v88, 16, v62
v_and_b32 v89, 0xffff0000, v62
v_lshlrev_b32 v90, 16, v63
v_and_b32 v91, 0xffff0000, v63
v_fma_f32 v84, v84, v105, v106
v_fma_f32 v85, v85, v105, v106
v_fma_f32 v86, v86, v105, v106
v_fma_f32 v87, v87, v105, v106
v_fma_f32 v88, v88, v105, v106
v_fma_f32 v89, v89, v105, v106
v_fma_f32 v90, v90, v105, v106
v_fma_f32 v91, v91, v105, v106
v_cvt_pk_bf16_f32 v112, v84, v85
v_cvt_pk_bf16_f32 v113, v86, v87
v_cvt_pk_bf16_f32 v114, v88, v89
v_cvt_pk_bf16_f32 v115, v90, v91
s_add_u32 s41, s40, 0x8000
buffer_store_dwordx4 v[112:115], v102, s[76:79], s41 offen sc1
v_lshlrev_b32 v84, 16, v64
v_and_b32 v85, 0xffff0000, v64
v_lshlrev_b32 v86, 16, v65
v_and_b32 v87, 0xffff0000, v65
v_lshlrev_b32 v88, 16, v66
v_and_b32 v89, 0xffff0000, v66
v_lshlrev_b32 v90, 16, v67
v_and_b32 v91, 0xffff0000, v67
v_fma_f32 v84, v84, v105, v106
v_fma_f32 v85, v85, v105, v106
v_fma_f32 v86, v86, v105, v106
v_fma_f32 v87, v87, v105, v106
v_fma_f32 v88, v88, v105, v106
v_fma_f32 v89, v89, v105, v106
v_fma_f32 v90, v90, v105, v106
v_fma_f32 v91, v91, v105, v106
v_cvt_pk_bf16_f32 v116, v84, v85
v_cvt_pk_bf16_f32 v117, v86, v87
v_cvt_pk_bf16_f32 v118, v88, v89
v_cvt_pk_bf16_f32 v119, v90, v91
s_add_u32 s41, s40, 0x10000
buffer_store_dwordx4 v[116:119], v102, s[76:79], s41 offen sc1
v_lshlrev_b32 v84, 16, v68
v_and_b32 v85, 0xffff0000, v68
v_lshlrev_b32 v86, 16, v69
v_and_b32 v87, 0xffff0000, v69
v_lshlrev_b32 v88, 16, v70
v_and_b32 v89, 0xffff0000, v70
v_lshlrev_b32 v90, 16, v71
v_and_b32 v91, 0xffff0000, v71
v_fma_f32 v84, v84, v105, v106
v_fma_f32 v85, v85, v105, v106
v_fma_f32 v86, v86, v105, v106
v_fma_f32 v87, v87, v105, v106
v_fma_f32 v88, v88, v105, v106
v_fma_f32 v89, v89, v105, v106
v_fma_f32 v90, v90, v105, v106
v_fma_f32 v91, v91, v105, v106
v_cvt_pk_bf16_f32 v120, v84, v85
v_cvt_pk_bf16_f32 v121, v86, v87
v_cvt_pk_bf16_f32 v122, v88, v89
v_cvt_pk_bf16_f32 v123, v90, v91
s_add_u32 s41, s40, 0x18000
buffer_store_dwordx4 v[120:123], v102, s[76:79], s41 offen sc1
s_add_u32 m0, s29, 0
s_nop 0
buffer_load_dwordx4 v1, s[12:15], s25 offen lds
buffer_load_dwordx4 v2, s[12:15], s25 offen offset:1056 lds
buffer_load_dwordx4 v3, s[12:15], s25 offen offset:2112 lds
buffer_load_dwordx4 v4, s[12:15], s25 offen offset:3168 lds
s_add_u32 m0, s30, 0
s_nop 0
buffer_load_dwordx4 v5, s[16:19], s26 offen lds
buffer_load_dwordx4 v6, s[16:19], s26 offen offset:1024 lds
buffer_load_dwordx4 v7, s[16:19], s26 offen offset:2048 lds
buffer_load_dwordx4 v8, s[16:19], s26 offen offset:3072 lds
s_add_u32 m0, s30, 0x1000
s_nop 0
buffer_load_dwordx4 v9, s[16:19], s26 offen lds
buffer_load_dwordx4 v10, s[16:19], s26 offen offset:1024 lds
buffer_load_dwordx4 v11, s[16:19], s26 offen offset:2048 lds
v_mov_b32 v248, -1
v_mov_b32 v249, -1
v_mov_b32 v250, -1
v_mov_b32 v251, -1
s_mul_i32 s96, s5, 0x18000
s_mul_i32 s35, s4, 0x6000
s_add_u32 s96, s96, s35
s_add_u32 s96, s96, 0x2303000
v_mbcnt_lo_u32_b32 v20, -1, 0
v_mbcnt_hi_u32_b32 v20, -1, v20
v_and_b32 v21, 7, v20
v_bfe_u32 v22, v20, 3, 1
s_lshl_b32 s36, s6, 3
s_lshl_b32 s37, s4, 1
s_add_u32 s36, s36, s37
s_add_u32 s36, s36, 0x80
v_add_u32 v22, s36, v22
v_mov_b32 v23, 0x1c00
v_mov_b32 v254, 0x2000
v_cmp_gt_u32 vcc, 2, v21
s_nop 1
v_cndmask_b32 v23, v23, v254, vcc
v_mul_lo_u32 v252, v23, v22
v_lshlrev_b32 v254, 6, v23
s_lshl_b32 s38, s7, 8
s_mul_i32 s39, s7, 0xe0
s_and_b32 s39, s39, 0xffffff80
v_lshlrev_b32 v253, 7, v21
v_add_u32 v255, s38, v253
v_mov_b32 v23, v255
v_subrev_u32 v253, 0x100, v253
v_add_u32 v253, s39, v253
v_cmp_lt_u32 vcc, 4, v21
v_subrev_u32 v255, 0x180, v253
s_nop 0
v_cndmask_b32 v253, v253, v255, vcc
v_min_u32 v253, 0x1bfc, v253
v_cmp_gt_u32 vcc, 2, v21
s_nop 1
v_cndmask_b32 v253, v253, v23, vcc
v_add_u32 v252, v252, v253
v_mov_b32 v22, s60
v_mov_b32 v23, s61
v_cmp_gt_u32 vcc, 5, v21
v_mov_b32 v255, s58
s_nop 0
v_cndmask_b32 v22, v22, v255, vcc
v_mov_b32 v255, s59
v_cndmask_b32 v23, v23, v255, vcc
v_cmp_gt_u32 vcc, 2, v21
v_mov_b32 v255, s54
s_nop 0
v_cndmask_b32 v22, v22, v255, vcc
v_mov_b32 v255, s55
v_cndmask_b32 v23, v23, v255, vcc
v_and_b32 v21, 31, v20
s_lshl_b32 s36, s5, 2
s_add_u32 s36, s36, s4
s_lshl_b32 s37, s36, 13
s_min_u32 s37, s37, 0x7fdd00
s_mul_i32 s38, s36, 0x1c00
v_subrev_u32 v24, 16, v21
v_cmp_lt_u32 vcc, 24, v21
v_mov_b32 v25, 0
s_nop 0
v_cndmask_b32 v24, v24, v25, vcc
v_lshlrev_b32 v24, 7, v24
v_add_u32 v25, s37, v24
v_subrev_u32 v26, 0x280, v24
v_add_u32 v26, s38, v26
v_mov_b32 v27, 0x280
v_mov_b32 v28, s56
v_mov_b32 v29, s57
v_subrev_u32 v30, 21, v21
v_cmp_gt_u32 vcc, 4, v30
v_mov_b32 v31, 0x200
s_nop 0
v_cndmask_b32 v25, v25, v26, vcc
v_cndmask_b32 v27, v27, v31, vcc
v_mov_b32 v31, s62
s_nop 0
v_cndmask_b32 v28, v28, v31, vcc
v_mov_b32 v31, s63
s_nop 0
v_cndmask_b32 v29, v29, v31, vcc
v_cmp_lt_u32 vcc, 15, v21
s_nop 1
v_cndmask_b32 v252, v252, v25, vcc
v_cndmask_b32 v254, v254, v27, vcc
v_cndmask_b32 v22, v22, v28, vcc
v_cndmask_b32 v23, v23, v29, vcc
v_add_co_u32 v252, vcc, v22, v252
s_nop 1
v_addc_co_u32 v253, vcc, 0, v23, vcc
s_waitcnt vmcnt(11)
s_barrier
s_cmp_eq_u32 s4, 0
s_cbranch_scc1 OOLwxf_%=
RETwxf_%=:
s_cmp_eq_u32 s4, 0
s_cbranch_scc1 OOLxp_%=
RETxp_%=:
s_barrier
s_or_b32 s34, s5, s4
s_cmp_eq_u32 s34, 0
s_cbranch_scc1 OOLall_%=
RETall_%=:
s_add_u32 m0, s28, 0
s_nop 0
buffer_load_dwordx4 v0, s[8:11], s24 offen sc1 lds
buffer_load_dwordx4 v0, s[8:11], s24 offen offset:1024 sc1 lds
buffer_load_dwordx4 v0, s[8:11], s24 offen offset:2048 sc1 lds
buffer_load_dwordx4 v0, s[8:11], s24 offen offset:3072 sc1 lds
s_add_u32 m0, s28, 0x1000
s_add_u32 s34, s24, 0x1000
buffer_load_dwordx4 v0, s[8:11], s34 offen sc1 lds
buffer_load_dwordx4 v0, s[8:11], s34 offen offset:1024 sc1 lds
buffer_load_dwordx4 v0, s[8:11], s34 offen offset:2048 sc1 lds
buffer_load_dwordx4 v0, s[8:11], s34 offen offset:3072 sc1 lds
s_add_u32 s24, s24, 0x8000
s_add_u32 s25, s25, 0x80000
s_add_u32 s26, s26, 0x70000
s_waitcnt vmcnt(0) lgkmcnt(0)
s_barrier
s_cmp_eq_u32 s69, 0
s_cbranch_scc0 TYPEB_%=
ds_read_b128 a[0:3], v12
ds_read_b128 a[4:7], v12 offset:1024
ds_read_b128 a[8:11], v12 offset:2048
ds_read_b128 a[12:15], v12 offset:3072
ds_read_b128 a[16:19], v12 offset:4096
ds_read_b128 a[20:23], v12 offset:5120
ds_read_b128 a[24:27], v12 offset:6144
ds_read_b128 a[28:31], v12 offset:7168
ds_read_b128 a[32:35], v12 offset:8192
ds_read_b128 a[36:39], v12 offset:9216
ds_read_b128 a[40:43], v12 offset:10240
ds_read_b128 a[44:47], v12 offset:11264
ds_read_b128 a[48:51], v12 offset:12288
ds_read_b128 a[52:55], v12 offset:13312
ds_read_b128 a[56:59], v12 offset:14336
ds_read_b128 a[60:63], v12 offset:15360
ds_read_b64_tr_b16 a[64:65], v16
ds_read_b64_tr_b16 a[66:67], v16 offset:3584
ds_read_b64_tr_b16 a[68:69], v16 offset:7168
ds_read_b64_tr_b16 a[70:71], v16 offset:10752
s_waitcnt lgkmcnt(0)
v_mfma_f32_16x16x32_bf16 v[24:27], a[64:67], a[0:3], 0
v_mfma_f32_16x16x32_bf16 v[24:27], a[68:71], a[4:7], v[24:27]
ds_read_b64_tr_b16 a[72:73], v16 offset:14336
s_add_u32 m0, s29, 0x13200
v_mfma_f32_16x16x32_bf16 v[28:31], a[64:67], a[8:11], 0
buffer_load_dwordx4 v1, s[12:15], s25 offen lds
v_mfma_f32_16x16x32_bf16 v[28:31], a[68:71], a[12:15], v[28:31]
v_mfma_f32_16x16x32_bf16 v[32:35], a[64:67], a[16:19], 0
ds_read_b64_tr_b16 a[74:75], v16 offset:17920
v_mfma_f32_16x16x32_bf16 v[32:35], a[68:71], a[20:23], v[32:35]
v_mfma_f32_16x16x32_bf16 v[36:39], a[64:67], a[24:27], 0
v_mfma_f32_16x16x32_bf16 v[36:39], a[68:71], a[28:31], v[36:39]
ds_read_b64_tr_b16 a[76:77], v16 offset:21504
v_mfma_f32_16x16x32_bf16 v[40:43], a[64:67], a[32:35], 0
buffer_load_dwordx4 v2, s[12:15], s25 offen offset:1056 lds
v_mfma_f32_16x16x32_bf16 v[40:43], a[68:71], a[36:39], v[40:43]
v_mfma_f32_16x16x32_bf16 v[44:47], a[64:67], a[40:43], 0
ds_read_b64_tr_b16 a[78:79], v16 offset:25088
v_mfma_f32_16x16x32_bf16 v[44:47], a[68:71], a[44:47], v[44:47]
v_mfma_f32_16x16x32_bf16 v[48:51], a[64:67], a[48:51], 0
v_mfma_f32_16x16x32_bf16 v[48:51], a[68:71], a[52:55], v[48:51]
v_mfma_f32_16x16x32_bf16 v[52:55], a[64:67], a[56:59], 0
buffer_load_dwordx4 v3, s[12:15], s25 offen offset:2112 lds
v_mfma_f32_16x16x32_bf16 v[52:55], a[68:71], a[60:63], v[52:55]
s_waitcnt lgkmcnt(1)
v_mfma_f32_16x16x32_bf16 v[56:59], a[72:75], a[0:3], 0
s_waitcnt lgkmcnt(0)
v_mfma_f32_16x16x32_bf16 v[56:59], a[76:79], a[4:7], v[56:59]
ds_read_b64_tr_b16 a[64:65], v16 offset:32
v_mfma_f32_16x16x32_bf16 v[60:63], a[72:75], a[8:11], 0
v_mfma_f32_16x16x32_bf16 v[60:63], a[76:79], a[12:15], v[60:63]
v_mfma_f32_16x16x32_bf16 v[64:67], a[72:75], a[16:19], 0
ds_read_b64_tr_b16 a[66:67], v16 offset:3616
buffer_load_dwordx4 v4, s[12:15], s25 offen offset:3168 lds
v_mfma_f32_16x16x32_bf16 v[64:67], a[76:79], a[20:23], v[64:67]
v_mfma_f32_16x16x32_bf16 v[68:71], a[72:75], a[24:27], 0
v_mfma_f32_16x16x32_bf16 v[68:71], a[76:79], a[28:31], v[68:71]
ds_read_b64_tr_b16 a[68:69], v16 offset:7200
v_mfma_f32_16x16x32_bf16 v[72:75], a[72:75], a[32:35], 0
v_mfma_f32_16x16x32_bf16 v[72:75], a[76:79], a[36:39], v[72:75]
s_add_u32 m0, s30, 0x13200
v_mfma_f32_16x16x32_bf16 v[76:79], a[72:75], a[40:43], 0
ds_read_b64_tr_b16 a[70:71], v16 offset:10784
buffer_load_dwordx4 v5, s[16:19], s26 offen lds
v_mfma_f32_16x16x32_bf16 v[76:79], a[76:79], a[44:47], v[76:79]
v_mfma_f32_16x16x32_bf16 v[80:83], a[72:75], a[48:51], 0
v_mfma_f32_16x16x32_bf16 v[80:83], a[76:79], a[52:55], v[80:83]
v_mfma_f32_16x16x32_bf16 v[84:87], a[72:75], a[56:59], 0
v_mfma_f32_16x16x32_bf16 v[84:87], a[76:79], a[60:63], v[84:87]
s_waitcnt lgkmcnt(1)
v_mfma_f32_16x16x32_bf16 v[88:91], a[64:67], a[0:3], 0
buffer_load_dwordx4 v6, s[16:19], s26 offen offset:1024 lds
s_waitcnt lgkmcnt(0)
v_mfma_f32_16x16x32_bf16 v[88:91], a[68:71], a[4:7], v[88:91]
ds_read_b64_tr_b16 a[72:73], v16 offset:14368
v_mfma_f32_16x16x32_bf16 v[92:95], a[64:67], a[8:11], 0
v_mfma_f32_16x16x32_bf16 v[92:95], a[68:71], a[12:15], v[92:95]
v_mfma_f32_16x16x32_bf16 v[96:99], a[64:67], a[16:19], 0
ds_read_b64_tr_b16 a[74:75], v16 offset:17952
v_mfma_f32_16x16x32_bf16 v[96:99], a[68:71], a[20:23], v[96:99]
v_mfma_f32_16x16x32_bf16 v[100:103], a[64:67], a[24:27], 0
buffer_load_dwordx4 v7, s[16:19], s26 offen offset:2048 lds
v_mfma_f32_16x16x32_bf16 v[100:103], a[68:71], a[28:31], v[100:103]
ds_read_b64_tr_b16 a[76:77], v16 offset:21536
v_mfma_f32_16x16x32_bf16 v[104:107], a[64:67], a[32:35], 0
v_mfma_f32_16x16x32_bf16 v[104:107], a[68:71], a[36:39], v[104:107]
v_mfma_f32_16x16x32_bf16 v[108:111], a[64:67], a[40:43], 0
ds_read_b64_tr_b16 a[78:79], v16 offset:25120
v_mfma_f32_16x16x32_bf16 v[108:111], a[68:71], a[44:47], v[108:111]
v_mfma_f32_16x16x32_bf16 v[112:115], a[64:67], a[48:51], 0
buffer_load_dwordx4 v8, s[16:19], s26 offen offset:3072 lds
v_mfma_f32_16x16x32_bf16 v[112:115], a[68:71], a[52:55], v[112:115]
v_mfma_f32_16x16x32_bf16 v[116:119], a[64:67], a[56:59], 0
v_mfma_f32_16x16x32_bf16 v[116:119], a[68:71], a[60:63], v[116:119]
s_waitcnt lgkmcnt(1)
v_mfma_f32_16x16x32_bf16 v[120:123], a[72:75], a[0:3], 0
s_waitcnt lgkmcnt(0)
v_mfma_f32_16x16x32_bf16 v[120:123], a[76:79], a[4:7], v[120:123]
ds_read_b64_tr_b16 a[64:65], v16 offset:64
s_add_u32 m0, s30, 0x14200
v_mfma_f32_16x16x32_bf16 v[124:127], a[72:75], a[8:11], 0
buffer_load_dwordx4 v9, s[16:19], s26 offen lds
v_mfma_f32_16x16x32_bf16 v[124:127], a[76:79], a[12:15], v[124:127]
v_mfma_f32_16x16x32_bf16 v[128:131], a[72:75], a[16:19], 0
ds_read_b64_tr_b16 a[66:67], v16 offset:3648
v_mfma_f32_16x16x32_bf16 v[128:131], a[76:79], a[20:23], v[128:131]
v_mfma_f32_16x16x32_bf16 v[132:135], a[72:75], a[24:27], 0
v_mfma_f32_16x16x32_bf16 v[132:135], a[76:79], a[28:31], v[132:135]
ds_read_b64_tr_b16 a[68:69], v16 offset:7232
v_mfma_f32_16x16x32_bf16 v[136:139], a[72:75], a[32:35], 0
buffer_load_dwordx4 v10, s[16:19], s26 offen offset:1024 lds
v_mfma_f32_16x16x32_bf16 v[136:139], a[76:79], a[36:39], v[136:139]
v_mfma_f32_16x16x32_bf16 v[140:143], a[72:75], a[40:43], 0
ds_read_b64_tr_b16 a[70:71], v16 offset:10816
v_mfma_f32_16x16x32_bf16 v[140:143], a[76:79], a[44:47], v[140:143]
v_mfma_f32_16x16x32_bf16 v[144:147], a[72:75], a[48:51], 0
v_mfma_f32_16x16x32_bf16 v[144:147], a[76:79], a[52:55], v[144:147]
v_mfma_f32_16x16x32_bf16 v[148:151], a[72:75], a[56:59], 0
buffer_load_dwordx4 v11, s[16:19], s26 offen offset:2048 lds
v_mfma_f32_16x16x32_bf16 v[148:151], a[76:79], a[60:63], v[148:151]
s_waitcnt lgkmcnt(1)
v_mfma_f32_16x16x32_bf16 v[152:155], a[64:67], a[0:3], 0
s_waitcnt lgkmcnt(0)
v_mfma_f32_16x16x32_bf16 v[152:155], a[68:71], a[4:7], v[152:155]
ds_read_b64_tr_b16 a[72:73], v16 offset:14400
v_mfma_f32_16x16x32_bf16 v[156:159], a[64:67], a[8:11], 0
v_mfma_f32_16x16x32_bf16 v[156:159], a[68:71], a[12:15], v[156:159]
s_add_u32 m0, s28, 0x13200
v_mfma_f32_16x16x32_bf16 v[160:163], a[64:67], a[16:19], 0
ds_read_b64_tr_b16 a[74:75], v16 offset:17984
buffer_load_dwordx4 v0, s[8:11], s24 offen sc1 lds
v_mfma_f32_16x16x32_bf16 v[160:163], a[68:71], a[20:23], v[160:163]
v_mfma_f32_16x16x32_bf16 v[164:167], a[64:67], a[24:27], 0
v_mfma_f32_16x16x32_bf16 v[164:167], a[68:71], a[28:31], v[164:167]
ds_read_b64_tr_b16 a[76:77], v16 offset:21568
v_mfma_f32_16x16x32_bf16 v[168:171], a[64:67], a[32:35], 0
v_mfma_f32_16x16x32_bf16 v[168:171], a[68:71], a[36:39], v[168:171]
v_mfma_f32_16x16x32_bf16 v[172:175], a[64:67], a[40:43], 0
ds_read_b64_tr_b16 a[78:79], v16 offset:25152
buffer_load_dwordx4 v0, s[8:11], s24 offen offset:1024 sc1 lds
v_mfma_f32_16x16x32_bf16 v[172:175], a[68:71], a[44:47], v[172:175]
v_mfma_f32_16x16x32_bf16 v[176:179], a[64:67], a[48:51], 0
v_mfma_f32_16x16x32_bf16 v[176:179], a[68:71], a[52:55], v[176:179]
v_mfma_f32_16x16x32_bf16 v[180:183], a[64:67], a[56:59], 0
v_mfma_f32_16x16x32_bf16 v[180:183], a[68:71], a[60:63], v[180:183]
s_waitcnt lgkmcnt(1)
v_mfma_f32_16x16x32_bf16 v[184:187], a[72:75], a[0:3], 0
buffer_load_dwordx4 v0, s[8:11], s24 offen offset:2048 sc1 lds
s_waitcnt lgkmcnt(0)
v_mfma_f32_16x16x32_bf16 v[184:187], a[76:79], a[4:7], v[184:187]
ds_read_b64_tr_b16 a[64:65], v16 offset:96
v_mfma_f32_16x16x32_bf16 v[188:191], a[72:75], a[8:11], 0
v_mfma_f32_16x16x32_bf16 v[188:191], a[76:79], a[12:15], v[188:191]
v_mfma_f32_16x16x32_bf16 v[192:195], a[72:75], a[16:19], 0
ds_read_b64_tr_b16 a[66:67], v16 offset:3680
v_mfma_f32_16x16x32_bf16 v[192:195], a[76:79], a[20:23], v[192:195]
v_mfma_f32_16x16x32_bf16 v[196:199], a[72:75], a[24:27], 0
buffer_load_dwordx4 v0, s[8:11], s24 offen offset:3072 sc1 lds
v_mfma_f32_16x16x32_bf16 v[196:199], a[76:79], a[28:31], v[196:199]
ds_read_b64_tr_b16 a[68:69], v16 offset:7264
v_mfma_f32_16x16x32_bf16 v[200:203], a[72:75], a[32:35], 0
)ASM"
R"ASM(v_mfma_f32_16x16x32_bf16 v[200:203], a[76:79], a[36:39], v[200:203]
v_mfma_f32_16x16x32_bf16 v[204:207], a[72:75], a[40:43], 0
ds_read_b64_tr_b16 a[70:71], v16 offset:10848
v_mfma_f32_16x16x32_bf16 v[204:207], a[76:79], a[44:47], v[204:207]
s_add_u32 m0, s28, 0x14200
s_add_u32 s34, s24, 0x1000
v_mfma_f32_16x16x32_bf16 v[208:211], a[72:75], a[48:51], 0
buffer_load_dwordx4 v0, s[8:11], s34 offen sc1 lds
v_mfma_f32_16x16x32_bf16 v[208:211], a[76:79], a[52:55], v[208:211]
v_mfma_f32_16x16x32_bf16 v[212:215], a[72:75], a[56:59], 0
v_mfma_f32_16x16x32_bf16 v[212:215], a[76:79], a[60:63], v[212:215]
s_waitcnt lgkmcnt(1)
v_mfma_f32_16x16x32_bf16 v[216:219], a[64:67], a[0:3], 0
s_waitcnt lgkmcnt(0)
v_mfma_f32_16x16x32_bf16 v[216:219], a[68:71], a[4:7], v[216:219]
ds_read_b64_tr_b16 a[72:73], v16 offset:14432
v_mfma_f32_16x16x32_bf16 v[220:223], a[64:67], a[8:11], 0
buffer_load_dwordx4 v0, s[8:11], s34 offen offset:1024 sc1 lds
v_mfma_f32_16x16x32_bf16 v[220:223], a[68:71], a[12:15], v[220:223]
v_mfma_f32_16x16x32_bf16 v[224:227], a[64:67], a[16:19], 0
ds_read_b64_tr_b16 a[74:75], v16 offset:18016
v_mfma_f32_16x16x32_bf16 v[224:227], a[68:71], a[20:23], v[224:227]
v_mfma_f32_16x16x32_bf16 v[228:231], a[64:67], a[24:27], 0
v_mfma_f32_16x16x32_bf16 v[228:231], a[68:71], a[28:31], v[228:231]
ds_read_b64_tr_b16 a[76:77], v16 offset:21600
v_mfma_f32_16x16x32_bf16 v[232:235], a[64:67], a[32:35], 0
buffer_load_dwordx4 v0, s[8:11], s34 offen offset:2048 sc1 lds
v_mfma_f32_16x16x32_bf16 v[232:235], a[68:71], a[36:39], v[232:235]
v_mfma_f32_16x16x32_bf16 v[236:239], a[64:67], a[40:43], 0
ds_read_b64_tr_b16 a[78:79], v16 offset:25184
v_mfma_f32_16x16x32_bf16 v[236:239], a[68:71], a[44:47], v[236:239]
v_mfma_f32_16x16x32_bf16 v[240:243], a[64:67], a[48:51], 0
v_mfma_f32_16x16x32_bf16 v[240:243], a[68:71], a[52:55], v[240:243]
v_mfma_f32_16x16x32_bf16 v[244:247], a[64:67], a[56:59], 0
buffer_load_dwordx4 v0, s[8:11], s34 offen offset:3072 sc1 lds
s_add_u32 s24, s24, 0x8000
s_add_u32 s25, s25, 0x80000
s_add_u32 s26, s26, 0x70000
v_mfma_f32_16x16x32_bf16 v[244:247], a[68:71], a[60:63], v[244:247]
s_waitcnt lgkmcnt(1)
v_mfma_f32_16x16x32_bf16 a[88:91], a[72:75], a[0:3], 0
s_waitcnt lgkmcnt(0)
v_mfma_f32_16x16x32_bf16 a[88:91], a[76:79], a[4:7], a[88:91]
ds_read_b64_tr_b16 a[64:65], v14
v_mfma_f32_16x16x32_bf16 a[92:95], a[72:75], a[8:11], 0
v_mfma_f32_16x16x32_bf16 a[92:95], a[76:79], a[12:15], a[92:95]
v_mfma_f32_16x16x32_bf16 a[96:99], a[72:75], a[16:19], 0
ds_read_b64_tr_b16 a[66:67], v14 offset:512
v_mfma_f32_16x16x32_bf16 a[96:99], a[76:79], a[20:23], a[96:99]
v_mfma_f32_16x16x32_bf16 a[100:103], a[72:75], a[24:27], 0
global_load_dword v255, v[252:253], off
v_mfma_f32_16x16x32_bf16 a[100:103], a[76:79], a[28:31], a[100:103]
ds_read_b64_tr_b16 a[68:69], v14 offset:8448
v_mfma_f32_16x16x32_bf16 a[104:107], a[72:75], a[32:35], 0
v_mfma_f32_16x16x32_bf16 a[104:107], a[76:79], a[36:39], a[104:107]
v_add_co_u32 v252, vcc, v254, v252
v_mfma_f32_16x16x32_bf16 a[108:111], a[72:75], a[40:43], 0
ds_read_b64_tr_b16 a[70:71], v14 offset:8960
v_mfma_f32_16x16x32_bf16 a[108:111], a[76:79], a[44:47], a[108:111]
v_mfma_f32_16x16x32_bf16 a[112:115], a[72:75], a[48:51], 0
v_addc_co_u32 v253, vcc, 0, v253, vcc
v_mfma_f32_16x16x32_bf16 a[112:115], a[76:79], a[52:55], a[112:115]
v_mfma_f32_16x16x32_bf16 a[116:119], a[72:75], a[56:59], 0
v_mfma_f32_16x16x32_bf16 a[116:119], a[76:79], a[60:63], a[116:119]
s_waitcnt lgkmcnt(1)
v_mfma_f32_16x16x32_bf16 a[120:123], a[64:67], a[0:3], 0
s_waitcnt lgkmcnt(0)
v_mfma_f32_16x16x32_bf16 a[120:123], a[68:71], a[4:7], a[120:123]
ds_read_b64_tr_b16 a[72:73], v14 offset:32
v_mfma_f32_16x16x32_bf16 a[124:127], a[64:67], a[8:11], 0
v_mfma_f32_16x16x32_bf16 a[124:127], a[68:71], a[12:15], a[124:127]
v_mfma_f32_16x16x32_bf16 a[128:131], a[64:67], a[16:19], 0
ds_read_b64_tr_b16 a[74:75], v14 offset:544
v_mfma_f32_16x16x32_bf16 a[128:131], a[68:71], a[20:23], a[128:131]
v_mfma_f32_16x16x32_bf16 a[132:135], a[64:67], a[24:27], 0
v_mfma_f32_16x16x32_bf16 a[132:135], a[68:71], a[28:31], a[132:135]
ds_read_b64_tr_b16 a[76:77], v14 offset:8480
v_mfma_f32_16x16x32_bf16 a[136:139], a[64:67], a[32:35], 0
v_mfma_f32_16x16x32_bf16 a[136:139], a[68:71], a[36:39], a[136:139]
v_mfma_f32_16x16x32_bf16 a[140:143], a[64:67], a[40:43], 0
ds_read_b64_tr_b16 a[78:79], v14 offset:8992
v_mfma_f32_16x16x32_bf16 a[140:143], a[68:71], a[44:47], a[140:143]
v_mfma_f32_16x16x32_bf16 a[144:147], a[64:67], a[48:51], 0
v_mfma_f32_16x16x32_bf16 a[144:147], a[68:71], a[52:55], a[144:147]
v_mfma_f32_16x16x32_bf16 a[148:151], a[64:67], a[56:59], 0
v_mfma_f32_16x16x32_bf16 a[148:151], a[68:71], a[60:63], a[148:151]
s_waitcnt lgkmcnt(1)
v_mfma_f32_16x16x32_bf16 a[152:155], a[72:75], a[0:3], 0
s_waitcnt lgkmcnt(0)
v_mfma_f32_16x16x32_bf16 a[152:155], a[76:79], a[4:7], a[152:155]
ds_read_b64_tr_b16 a[64:65], v14 offset:64
v_mfma_f32_16x16x32_bf16 a[156:159], a[72:75], a[8:11], 0
ds_read_b64_tr_b16 a[66:67], v14 offset:576
v_mfma_f32_16x16x32_bf16 a[156:159], a[76:79], a[12:15], a[156:159]
ds_read_b64_tr_b16 a[68:69], v14 offset:8512
v_mfma_f32_16x16x32_bf16 a[160:163], a[72:75], a[16:19], 0
ds_read_b64_tr_b16 a[70:71], v14 offset:9024
v_mfma_f32_16x16x32_bf16 a[160:163], a[76:79], a[20:23], a[160:163]
v_mfma_f32_16x16x32_bf16 a[164:167], a[72:75], a[24:27], 0
v_mfma_f32_16x16x32_bf16 a[164:167], a[76:79], a[28:31], a[164:167]
v_mfma_f32_16x16x32_bf16 a[168:171], a[72:75], a[32:35], 0
v_mfma_f32_16x16x32_bf16 a[168:171], a[76:79], a[36:39], a[168:171]
v_mfma_f32_16x16x32_bf16 a[172:175], a[72:75], a[40:43], 0
v_mfma_f32_16x16x32_bf16 a[172:175], a[76:79], a[44:47], a[172:175]
v_mfma_f32_16x16x32_bf16 a[176:179], a[72:75], a[48:51], 0
v_mfma_f32_16x16x32_bf16 a[176:179], a[76:79], a[52:55], a[176:179]
v_mfma_f32_16x16x32_bf16 a[180:183], a[72:75], a[56:59], 0
v_mfma_f32_16x16x32_bf16 a[180:183], a[76:79], a[60:63], a[180:183]
s_waitcnt vmcnt(1) lgkmcnt(0)
s_barrier
v_mfma_f32_16x16x32_bf16 a[184:187], a[64:67], a[0:3], 0
ds_read_b64_tr_b16 a[72:73], v17
ds_read_b128 a[0:3], v13
v_mfma_f32_16x16x32_bf16 a[184:187], a[68:71], a[4:7], a[184:187]
ds_read_b64_tr_b16 a[74:75], v17 offset:3584
ds_read_b128 a[4:7], v13 offset:1024
v_mfma_f32_16x16x32_bf16 a[188:191], a[64:67], a[8:11], 0
ds_read_b64_tr_b16 a[76:77], v17 offset:7168
ds_read_b128 a[8:11], v13 offset:2048
v_mfma_f32_16x16x32_bf16 a[188:191], a[68:71], a[12:15], a[188:191]
ds_read_b64_tr_b16 a[78:79], v17 offset:10752
ds_read_b128 a[12:15], v13 offset:3072
v_mfma_f32_16x16x32_bf16 a[192:195], a[64:67], a[16:19], 0
ds_read_b128 a[16:19], v13 offset:4096
v_mfma_f32_16x16x32_bf16 a[192:195], a[68:71], a[20:23], a[192:195]
ds_read_b128 a[20:23], v13 offset:5120
v_mfma_f32_16x16x32_bf16 a[196:199], a[64:67], a[24:27], 0
ds_read_b128 a[24:27], v13 offset:6144
v_mfma_f32_16x16x32_bf16 a[196:199], a[68:71], a[28:31], a[196:199]
ds_read_b128 a[28:31], v13 offset:7168
v_mfma_f32_16x16x32_bf16 a[200:203], a[64:67], a[32:35], 0
ds_read_b128 a[32:35], v13 offset:8192
v_mfma_f32_16x16x32_bf16 a[200:203], a[68:71], a[36:39], a[200:203]
ds_read_b128 a[36:39], v13 offset:9216
v_mfma_f32_16x16x32_bf16 a[204:207], a[64:67], a[40:43], 0
ds_read_b128 a[40:43], v13 offset:10240
v_mfma_f32_16x16x32_bf16 a[204:207], a[68:71], a[44:47], a[204:207]
ds_read_b128 a[44:47], v13 offset:11264
v_mfma_f32_16x16x32_bf16 a[208:211], a[64:67], a[48:51], 0
ds_read_b128 a[48:51], v13 offset:12288
v_mfma_f32_16x16x32_bf16 a[208:211], a[68:71], a[52:55], a[208:211]
ds_read_b128 a[52:55], v13 offset:13312
v_mfma_f32_16x16x32_bf16 a[212:215], a[64:67], a[56:59], 0
ds_read_b128 a[56:59], v13 offset:14336
v_mfma_f32_16x16x32_bf16 a[212:215], a[68:71], a[60:63], a[212:215]
ds_read_b128 a[60:63], v13 offset:15360
s_waitcnt lgkmcnt(6)
v_mfma_f32_16x16x32_bf16 v[24:27], a[72:75], a[0:3], v[24:27]
v_mfma_f32_16x16x32_bf16 v[24:27], a[76:79], a[4:7], v[24:27]
ds_read_b64_tr_b16 a[64:65], v17 offset:14336
s_add_u32 m0, s29, 0
v_mfma_f32_16x16x32_bf16 v[28:31], a[72:75], a[8:11], v[28:31]
buffer_load_dwordx4 v1, s[12:15], s25 offen lds
v_mfma_f32_16x16x32_bf16 v[28:31], a[76:79], a[12:15], v[28:31]
v_mfma_f32_16x16x32_bf16 v[32:35], a[72:75], a[16:19], v[32:35]
ds_read_b64_tr_b16 a[66:67], v17 offset:17920
v_mfma_f32_16x16x32_bf16 v[32:35], a[76:79], a[20:23], v[32:35]
v_mfma_f32_16x16x32_bf16 v[36:39], a[72:75], a[24:27], v[36:39]
v_mfma_f32_16x16x32_bf16 v[36:39], a[76:79], a[28:31], v[36:39]
ds_read_b64_tr_b16 a[68:69], v17 offset:21504
v_mfma_f32_16x16x32_bf16 v[40:43], a[72:75], a[32:35], v[40:43]
buffer_load_dwordx4 v2, s[12:15], s25 offen offset:1056 lds
v_mfma_f32_16x16x32_bf16 v[40:43], a[76:79], a[36:39], v[40:43]
s_waitcnt lgkmcnt(2)
v_mfma_f32_16x16x32_bf16 v[44:47], a[72:75], a[40:43], v[44:47]
ds_read_b64_tr_b16 a[70:71], v17 offset:25088
v_mfma_f32_16x16x32_bf16 v[44:47], a[76:79], a[44:47], v[44:47]
v_mfma_f32_16x16x32_bf16 v[48:51], a[72:75], a[48:51], v[48:51]
v_mfma_f32_16x16x32_bf16 v[48:51], a[76:79], a[52:55], v[48:51]
v_mfma_f32_16x16x32_bf16 v[52:55], a[72:75], a[56:59], v[52:55]
buffer_load_dwordx4 v3, s[12:15], s25 offen offset:2112 lds
v_mfma_f32_16x16x32_bf16 v[52:55], a[76:79], a[60:63], v[52:55]
s_waitcnt lgkmcnt(1)
v_mfma_f32_16x16x32_bf16 v[56:59], a[64:67], a[0:3], v[56:59]
s_waitcnt lgkmcnt(0)
v_mfma_f32_16x16x32_bf16 v[56:59], a[68:71], a[4:7], v[56:59]
ds_read_b64_tr_b16 a[72:73], v17 offset:32
v_mfma_f32_16x16x32_bf16 v[60:63], a[64:67], a[8:11], v[60:63]
v_mfma_f32_16x16x32_bf16 v[60:63], a[68:71], a[12:15], v[60:63]
v_mfma_f32_16x16x32_bf16 v[64:67], a[64:67], a[16:19], v[64:67]
ds_read_b64_tr_b16 a[74:75], v17 offset:3616
buffer_load_dwordx4 v4, s[12:15], s25 offen offset:3168 lds
v_mfma_f32_16x16x32_bf16 v[64:67], a[68:71], a[20:23], v[64:67]
v_mfma_f32_16x16x32_bf16 v[68:71], a[64:67], a[24:27], v[68:71]
v_mfma_f32_16x16x32_bf16 v[68:71], a[68:71], a[28:31], v[68:71]
ds_read_b64_tr_b16 a[76:77], v17 offset:7200
v_mfma_f32_16x16x32_bf16 v[72:75], a[64:67], a[32:35], v[72:75]
v_mfma_f32_16x16x32_bf16 v[72:75], a[68:71], a[36:39], v[72:75]
s_add_u32 m0, s30, 0
v_mfma_f32_16x16x32_bf16 v[76:79], a[64:67], a[40:43], v[76:79]
ds_read_b64_tr_b16 a[78:79], v17 offset:10784
buffer_load_dwordx4 v5, s[16:19], s26 offen lds
v_mfma_f32_16x16x32_bf16 v[76:79], a[68:71], a[44:47], v[76:79]
v_mfma_f32_16x16x32_bf16 v[80:83], a[64:67], a[48:51], v[80:83]
v_mfma_f32_16x16x32_bf16 v[80:83], a[68:71], a[52:55], v[80:83]
v_mfma_f32_16x16x32_bf16 v[84:87], a[64:67], a[56:59], v[84:87]
v_mfma_f32_16x16x32_bf16 v[84:87], a[68:71], a[60:63], v[84:87]
s_waitcnt lgkmcnt(1)
v_mfma_f32_16x16x32_bf16 v[88:91], a[72:75], a[0:3], v[88:91]
buffer_load_dwordx4 v6, s[16:19], s26 offen offset:1024 lds
s_waitcnt lgkmcnt(0)
v_mfma_f32_16x16x32_bf16 v[88:91], a[76:79], a[4:7], v[88:91]
ds_read_b64_tr_b16 a[64:65], v17 offset:14368
v_mfma_f32_16x16x32_bf16 v[92:95], a[72:75], a[8:11], v[92:95]
v_mfma_f32_16x16x32_bf16 v[92:95], a[76:79], a[12:15], v[92:95]
v_mfma_f32_16x16x32_bf16 v[96:99], a[72:75], a[16:19], v[96:99]
ds_read_b64_tr_b16 a[66:67], v17 offset:17952
v_mfma_f32_16x16x32_bf16 v[96:99], a[76:79], a[20:23], v[96:99]
v_mfma_f32_16x16x32_bf16 v[100:103], a[72:75], a[24:27], v[100:103]
buffer_load_dwordx4 v7, s[16:19], s26 offen offset:2048 lds
v_mfma_f32_16x16x32_bf16 v[100:103], a[76:79], a[28:31], v[100:103]
ds_read_b64_tr_b16 a[68:69], v17 offset:21536
v_mfma_f32_16x16x32_bf16 v[104:107], a[72:75], a[32:35], v[104:107]
v_mfma_f32_16x16x32_bf16 v[104:107], a[76:79], a[36:39], v[104:107]
v_mfma_f32_16x16x32_bf16 v[108:111], a[72:75], a[40:43], v[108:111]
ds_read_b64_tr_b16 a[70:71], v17 offset:25120
v_mfma_f32_16x16x32_bf16 v[108:111], a[76:79], a[44:47], v[108:111]
v_mfma_f32_16x16x32_bf16 v[112:115], a[72:75], a[48:51], v[112:115]
buffer_load_dwordx4 v8, s[16:19], s26 offen offset:3072 lds
v_mfma_f32_16x16x32_bf16 v[112:115], a[76:79], a[52:55], v[112:115]
v_mfma_f32_16x16x32_bf16 v[116:119], a[72:75], a[56:59], v[116:119]
v_mfma_f32_16x16x32_bf16 v[116:119], a[76:79], a[60:63], v[116:119]
s_waitcnt lgkmcnt(1)
v_mfma_f32_16x16x32_bf16 v[120:123], a[64:67], a[0:3], v[120:123]
s_waitcnt lgkmcnt(0)
v_mfma_f32_16x16x32_bf16 v[120:123], a[68:71], a[4:7], v[120:123]
ds_read_b64_tr_b16 a[72:73], v17 offset:64
s_add_u32 m0, s30, 0x1000
v_mfma_f32_16x16x32_bf16 v[124:127], a[64:67], a[8:11], v[124:127]
buffer_load_dwordx4 v9, s[16:19], s26 offen lds
v_mfma_f32_16x16x32_bf16 v[124:127], a[68:71], a[12:15], v[124:127]
v_mfma_f32_16x16x32_bf16 v[128:131], a[64:67], a[16:19], v[128:131]
ds_read_b64_tr_b16 a[74:75], v17 offset:3648
v_mfma_f32_16x16x32_bf16 v[128:131], a[68:71], a[20:23], v[128:131]
v_mfma_f32_16x16x32_bf16 v[132:135], a[64:67], a[24:27], v[132:135]
v_mfma_f32_16x16x32_bf16 v[132:135], a[68:71], a[28:31], v[132:135]
ds_read_b64_tr_b16 a[76:77], v17 offset:7232
v_mfma_f32_16x16x32_bf16 v[136:139], a[64:67], a[32:35], v[136:139]
buffer_load_dwordx4 v10, s[16:19], s26 offen offset:1024 lds
v_mfma_f32_16x16x32_bf16 v[136:139], a[68:71], a[36:39], v[136:139]
v_mfma_f32_16x16x32_bf16 v[140:143], a[64:67], a[40:43], v[140:143]
ds_read_b64_tr_b16 a[78:79], v17 offset:10816
v_mfma_f32_16x16x32_bf16 v[140:143], a[68:71], a[44:47], v[140:143]
v_mfma_f32_16x16x32_bf16 v[144:147], a[64:67], a[48:51], v[144:147]
v_mfma_f32_16x16x32_bf16 v[144:147], a[68:71], a[52:55], v[144:147]
v_mfma_f32_16x16x32_bf16 v[148:151], a[64:67], a[56:59], v[148:151]
buffer_load_dwordx4 v11, s[16:19], s26 offen offset:2048 lds
v_mfma_f32_16x16x32_bf16 v[148:151], a[68:71], a[60:63], v[148:151]
s_waitcnt lgkmcnt(1)
v_mfma_f32_16x16x32_bf16 v[152:155], a[72:75], a[0:3], v[152:155]
s_waitcnt lgkmcnt(0)
v_mfma_f32_16x16x32_bf16 v[152:155], a[76:79], a[4:7], v[152:155]
ds_read_b64_tr_b16 a[64:65], v17 offset:14400
v_mfma_f32_16x16x32_bf16 v[156:159], a[72:75], a[8:11], v[156:159]
v_mfma_f32_16x16x32_bf16 v[156:159], a[76:79], a[12:15], v[156:159]
s_add_u32 m0, s28, 0
v_mfma_f32_16x16x32_bf16 v[160:163], a[72:75], a[16:19], v[160:163]
ds_read_b64_tr_b16 a[66:67], v17 offset:17984
buffer_load_dwordx4 v0, s[8:11], s24 offen sc1 lds
v_mfma_f32_16x16x32_bf16 v[160:163], a[76:79], a[20:23], v[160:163]
v_mfma_f32_16x16x32_bf16 v[164:167], a[72:75], a[24:27], v[164:167]
v_mfma_f32_16x16x32_bf16 v[164:167], a[76:79], a[28:31], v[164:167]
ds_read_b64_tr_b16 a[68:69], v17 offset:21568
v_mfma_f32_16x16x32_bf16 v[168:171], a[72:75], a[32:35], v[168:171]
v_mfma_f32_16x16x32_bf16 v[168:171], a[76:79], a[36:39], v[168:171]
v_mfma_f32_16x16x32_bf16 v[172:175], a[72:75], a[40:43], v[172:175]
ds_read_b64_tr_b16 a[70:71], v17 offset:25152
buffer_load_dwordx4 v0, s[8:11], s24 offen offset:1024 sc1 lds
v_mfma_f32_16x16x32_bf16 v[172:175], a[76:79], a[44:47], v[172:175]
v_mfma_f32_16x16x32_bf16 v[176:179], a[72:75], a[48:51], v[176:179]
v_mfma_f32_16x16x32_bf16 v[176:179], a[76:79], a[52:55], v[176:179]
v_mfma_f32_16x16x32_bf16 v[180:183], a[72:75], a[56:59], v[180:183]
v_mfma_f32_16x16x32_bf16 v[180:183], a[76:79], a[60:63], v[180:183]
s_waitcnt lgkmcnt(1)
v_mfma_f32_16x16x32_bf16 v[184:187], a[64:67], a[0:3], v[184:187]
buffer_load_dwordx4 v0, s[8:11], s24 offen offset:2048 sc1 lds
s_waitcnt lgkmcnt(0)
v_mfma_f32_16x16x32_bf16 v[184:187], a[68:71], a[4:7], v[184:187]
ds_read_b64_tr_b16 a[72:73], v17 offset:96
v_mfma_f32_16x16x32_bf16 v[188:191], a[64:67], a[8:11], v[188:191]
v_mfma_f32_16x16x32_bf16 v[188:191], a[68:71], a[12:15], v[188:191]
v_mfma_f32_16x16x32_bf16 v[192:195], a[64:67], a[16:19], v[192:195]
ds_read_b64_tr_b16 a[74:75], v17 offset:3680
v_mfma_f32_16x16x32_bf16 v[192:195], a[68:71], a[20:23], v[192:195]
v_mfma_f32_16x16x32_bf16 v[196:199], a[64:67], a[24:27], v[196:199]
buffer_load_dwordx4 v0, s[8:11], s24 offen offset:3072 sc1 lds
v_mfma_f32_16x16x32_bf16 v[196:199], a[68:71], a[28:31], v[196:199]
ds_read_b64_tr_b16 a[76:77], v17 offset:7264
v_mfma_f32_16x16x32_bf16 v[200:203], a[64:67], a[32:35], v[200:203]
v_mfma_f32_16x16x32_bf16 v[200:203], a[68:71], a[36:39], v[200:203]
v_mfma_f32_16x16x32_bf16 v[204:207], a[64:67], a[40:43], v[204:207]
ds_read_b64_tr_b16 a[78:79], v17 offset:10848
v_mfma_f32_16x16x32_bf16 v[204:207], a[68:71], a[44:47], v[204:207]
s_add_u32 m0, s28, 0x1000
s_add_u32 s34, s24, 0x1000
v_mfma_f32_16x16x32_bf16 v[208:211], a[64:67], a[48:51], v[208:211]
buffer_load_dwordx4 v0, s[8:11], s34 offen sc1 lds
v_mfma_f32_16x16x32_bf16 v[208:211], a[68:71], a[52:55], v[208:211]
v_mfma_f32_16x16x32_bf16 v[212:215], a[64:67], a[56:59], v[212:215]
v_mfma_f32_16x16x32_bf16 v[212:215], a[68:71], a[60:63], v[212:215]
s_waitcnt lgkmcnt(1)
v_mfma_f32_16x16x32_bf16 v[216:219], a[72:75], a[0:3], v[216:219]
s_waitcnt lgkmcnt(0)
v_mfma_f32_16x16x32_bf16 v[216:219], a[76:79], a[4:7], v[216:219]
ds_read_b64_tr_b16 a[64:65], v17 offset:14432
v_mfma_f32_16x16x32_bf16 v[220:223], a[72:75], a[8:11], v[220:223]
buffer_load_dwordx4 v0, s[8:11], s34 offen offset:1024 sc1 lds
v_mfma_f32_16x16x32_bf16 v[220:223], a[76:79], a[12:15], v[220:223]
v_mfma_f32_16x16x32_bf16 v[224:227], a[72:75], a[16:19], v[224:227]
ds_read_b64_tr_b16 a[66:67], v17 offset:18016
v_mfma_f32_16x16x32_bf16 v[224:227], a[76:79], a[20:23], v[224:227]
v_mfma_f32_16x16x32_bf16 v[228:231], a[72:75], a[24:27], v[228:231]
v_mfma_f32_16x16x32_bf16 v[228:231], a[76:79], a[28:31], v[228:231]
ds_read_b64_tr_b16 a[68:69], v17 offset:21600
v_mfma_f32_16x16x32_bf16 v[232:235], a[72:75], a[32:35], v[232:235]
buffer_load_dwordx4 v0, s[8:11], s34 offen offset:2048 sc1 lds
v_mfma_f32_16x16x32_bf16 v[232:235], a[76:79], a[36:39], v[232:235]
v_mfma_f32_16x16x32_bf16 v[236:239], a[72:75], a[40:43], v[236:239]
ds_read_b64_tr_b16 a[70:71], v17 offset:25184
v_mfma_f32_16x16x32_bf16 v[236:239], a[76:79], a[44:47], v[236:239]
v_mfma_f32_16x16x32_bf16 v[240:243], a[72:75], a[48:51], v[240:243]
v_mfma_f32_16x16x32_bf16 v[240:243], a[76:79], a[52:55], v[240:243]
v_mfma_f32_16x16x32_bf16 v[244:247], a[72:75], a[56:59], v[244:247]
buffer_load_dwordx4 v0, s[8:11], s34 offen offset:3072 sc1 lds
s_add_u32 s24, s24, 0x8000
s_add_u32 s25, s25, 0x80000
s_add_u32 s26, s26, 0x70000
v_mfma_f32_16x16x32_bf16 v[244:247], a[76:79], a[60:63], v[244:247]
s_waitcnt lgkmcnt(1)
v_mfma_f32_16x16x32_bf16 a[88:91], a[64:67], a[0:3], a[88:91]
s_waitcnt lgkmcnt(0)
v_mfma_f32_16x16x32_bf16 a[88:91], a[68:71], a[4:7], a[88:91]
ds_read_b64_tr_b16 a[72:73], v15
v_mfma_f32_16x16x32_bf16 a[92:95], a[64:67], a[8:11], a[92:95]
v_mfma_f32_16x16x32_bf16 a[92:95], a[68:71], a[12:15], a[92:95]
v_mfma_f32_16x16x32_bf16 a[96:99], a[64:67], a[16:19], a[96:99]
ds_read_b64_tr_b16 a[74:75], v15 offset:512
v_mfma_f32_16x16x32_bf16 a[96:99], a[68:71], a[20:23], a[96:99]
v_mfma_f32_16x16x32_bf16 a[100:103], a[64:67], a[24:27], a[100:103]
global_load_dword v255, v[252:253], off
v_mfma_f32_16x16x32_bf16 a[100:103], a[68:71], a[28:31], a[100:103]
ds_read_b64_tr_b16 a[76:77], v15 offset:8448
v_mfma_f32_16x16x32_bf16 a[104:107], a[64:67], a[32:35], a[104:107]
v_mfma_f32_16x16x32_bf16 a[104:107], a[68:71], a[36:39], a[104:107]
v_add_co_u32 v252, vcc, v254, v252
v_mfma_f32_16x16x32_bf16 a[108:111], a[64:67], a[40:43], a[108:111]
ds_read_b64_tr_b16 a[78:79], v15 offset:8960
v_mfma_f32_16x16x32_bf16 a[108:111], a[68:71], a[44:47], a[108:111]
v_mfma_f32_16x16x32_bf16 a[112:115], a[64:67], a[48:51], a[112:115]
v_addc_co_u32 v253, vcc, 0, v253, vcc
v_mfma_f32_16x16x32_bf16 a[112:115], a[68:71], a[52:55], a[112:115]
v_mfma_f32_16x16x32_bf16 a[116:119], a[64:67], a[56:59], a[116:119]
v_mfma_f32_16x16x32_bf16 a[116:119], a[68:71], a[60:63], a[116:119]
s_waitcnt lgkmcnt(1)
v_mfma_f32_16x16x32_bf16 a[120:123], a[72:75], a[0:3], a[120:123]
s_waitcnt lgkmcnt(0)
v_mfma_f32_16x16x32_bf16 a[120:123], a[76:79], a[4:7], a[120:123]
ds_read_b64_tr_b16 a[64:65], v15 offset:32
v_mfma_f32_16x16x32_bf16 a[124:127], a[72:75], a[8:11], a[124:127]
v_mfma_f32_16x16x32_bf16 a[124:127], a[76:79], a[12:15], a[124:127]
v_mfma_f32_16x16x32_bf16 a[128:131], a[72:75], a[16:19], a[128:131]
ds_read_b64_tr_b16 a[66:67], v15 offset:544
v_mfma_f32_16x16x32_bf16 a[128:131], a[76:79], a[20:23], a[128:131]
v_mfma_f32_16x16x32_bf16 a[132:135], a[72:75], a[24:27], a[132:135]
v_mfma_f32_16x16x32_bf16 a[132:135], a[76:79], a[28:31], a[132:135]
ds_read_b64_tr_b16 a[68:69], v15 offset:8480
v_mfma_f32_16x16x32_bf16 a[136:139], a[72:75], a[32:35], a[136:139]
v_mfma_f32_16x16x32_bf16 a[136:139], a[76:79], a[36:39], a[136:139]
v_mfma_f32_16x16x32_bf16 a[140:143], a[72:75], a[40:43], a[140:143]
ds_read_b64_tr_b16 a[70:71], v15 offset:8992
v_mfma_f32_16x16x32_bf16 a[140:143], a[76:79], a[44:47], a[140:143]
v_mfma_f32_16x16x32_bf16 a[144:147], a[72:75], a[48:51], a[144:147]
v_mfma_f32_16x16x32_bf16 a[144:147], a[76:79], a[52:55], a[144:147]
v_mfma_f32_16x16x32_bf16 a[148:151], a[72:75], a[56:59], a[148:151]
v_mfma_f32_16x16x32_bf16 a[148:151], a[76:79], a[60:63], a[148:151]
s_waitcnt lgkmcnt(1)
v_mfma_f32_16x16x32_bf16 a[152:155], a[64:67], a[0:3], a[152:155]
s_waitcnt lgkmcnt(0)
v_mfma_f32_16x16x32_bf16 a[152:155], a[68:71], a[4:7], a[152:155]
ds_read_b64_tr_b16 a[72:73], v15 offset:64
v_mfma_f32_16x16x32_bf16 a[156:159], a[64:67], a[8:11], a[156:159]
ds_read_b64_tr_b16 a[74:75], v15 offset:576
v_mfma_f32_16x16x32_bf16 a[156:159], a[68:71], a[12:15], a[156:159]
ds_read_b64_tr_b16 a[76:77], v15 offset:8512
v_mfma_f32_16x16x32_bf16 a[160:163], a[64:67], a[16:19], a[160:163]
ds_read_b64_tr_b16 a[78:79], v15 offset:9024
v_mfma_f32_16x16x32_bf16 a[160:163], a[68:71], a[20:23], a[160:163]
v_mfma_f32_16x16x32_bf16 a[164:167], a[64:67], a[24:27], a[164:167]
v_mfma_f32_16x16x32_bf16 a[164:167], a[68:71], a[28:31], a[164:167]
v_mfma_f32_16x16x32_bf16 a[168:171], a[64:67], a[32:35], a[168:171]
v_mfma_f32_16x16x32_bf16 a[168:171], a[68:71], a[36:39], a[168:171]
v_mfma_f32_16x16x32_bf16 a[172:175], a[64:67], a[40:43], a[172:175]
v_mfma_f32_16x16x32_bf16 a[172:175], a[68:71], a[44:47], a[172:175]
v_mfma_f32_16x16x32_bf16 a[176:179], a[64:67], a[48:51], a[176:179]
v_mfma_f32_16x16x32_bf16 a[176:179], a[68:71], a[52:55], a[176:179]
v_mfma_f32_16x16x32_bf16 a[180:183], a[64:67], a[56:59], a[180:183]
v_mfma_f32_16x16x32_bf16 a[180:183], a[68:71], a[60:63], a[180:183]
s_waitcnt vmcnt(1) lgkmcnt(0)
s_barrier
v_mfma_f32_16x16x32_bf16 a[184:187], a[72:75], a[0:3], a[184:187]
)ASM"
R"ASM(ds_read_b64_tr_b16 a[64:65], v16
ds_read_b128 a[0:3], v12
v_mfma_f32_16x16x32_bf16 a[184:187], a[76:79], a[4:7], a[184:187]
ds_read_b64_tr_b16 a[66:67], v16 offset:3584
ds_read_b128 a[4:7], v12 offset:1024
v_mfma_f32_16x16x32_bf16 a[188:191], a[72:75], a[8:11], a[188:191]
ds_read_b64_tr_b16 a[68:69], v16 offset:7168
ds_read_b128 a[8:11], v12 offset:2048
v_mfma_f32_16x16x32_bf16 a[188:191], a[76:79], a[12:15], a[188:191]
ds_read_b64_tr_b16 a[70:71], v16 offset:10752
ds_read_b128 a[12:15], v12 offset:3072
v_mfma_f32_16x16x32_bf16 a[192:195], a[72:75], a[16:19], a[192:195]
ds_read_b128 a[16:19], v12 offset:4096
v_mfma_f32_16x16x32_bf16 a[192:195], a[76:79], a[20:23], a[192:195]
ds_read_b128 a[20:23], v12 offset:5120
v_mfma_f32_16x16x32_bf16 a[196:199], a[72:75], a[24:27], a[196:199]
ds_read_b128 a[24:27], v12 offset:6144
v_mfma_f32_16x16x32_bf16 a[196:199], a[76:79], a[28:31], a[196:199]
ds_read_b128 a[28:31], v12 offset:7168
v_mfma_f32_16x16x32_bf16 a[200:203], a[72:75], a[32:35], a[200:203]
ds_read_b128 a[32:35], v12 offset:8192
v_mfma_f32_16x16x32_bf16 a[200:203], a[76:79], a[36:39], a[200:203]
ds_read_b128 a[36:39], v12 offset:9216
v_mfma_f32_16x16x32_bf16 a[204:207], a[72:75], a[40:43], a[204:207]
ds_read_b128 a[40:43], v12 offset:10240
v_mfma_f32_16x16x32_bf16 a[204:207], a[76:79], a[44:47], a[204:207]
ds_read_b128 a[44:47], v12 offset:11264
v_mfma_f32_16x16x32_bf16 a[208:211], a[72:75], a[48:51], a[208:211]
ds_read_b128 a[48:51], v12 offset:12288
v_mfma_f32_16x16x32_bf16 a[208:211], a[76:79], a[52:55], a[208:211]
ds_read_b128 a[52:55], v12 offset:13312
v_mfma_f32_16x16x32_bf16 a[212:215], a[72:75], a[56:59], a[212:215]
ds_read_b128 a[56:59], v12 offset:14336
v_mfma_f32_16x16x32_bf16 a[212:215], a[76:79], a[60:63], a[212:215]
ds_read_b128 a[60:63], v12 offset:15360
s_mov_b32 s70, 6
G1A_%=:
s_waitcnt lgkmcnt(0)
v_mfma_f32_16x16x32_bf16 v[24:27], a[64:67], a[0:3], v[24:27]
v_mfma_f32_16x16x32_bf16 v[24:27], a[68:71], a[4:7], v[24:27]
ds_read_b64_tr_b16 a[72:73], v16 offset:14336
s_add_u32 m0, s29, 0x13200
v_mfma_f32_16x16x32_bf16 v[28:31], a[64:67], a[8:11], v[28:31]
buffer_load_dwordx4 v1, s[12:15], s25 offen lds
v_mfma_f32_16x16x32_bf16 v[28:31], a[68:71], a[12:15], v[28:31]
v_mfma_f32_16x16x32_bf16 v[32:35], a[64:67], a[16:19], v[32:35]
ds_read_b64_tr_b16 a[74:75], v16 offset:17920
v_mfma_f32_16x16x32_bf16 v[32:35], a[68:71], a[20:23], v[32:35]
v_mfma_f32_16x16x32_bf16 v[36:39], a[64:67], a[24:27], v[36:39]
v_mfma_f32_16x16x32_bf16 v[36:39], a[68:71], a[28:31], v[36:39]
ds_read_b64_tr_b16 a[76:77], v16 offset:21504
v_mfma_f32_16x16x32_bf16 v[40:43], a[64:67], a[32:35], v[40:43]
buffer_load_dwordx4 v2, s[12:15], s25 offen offset:1056 lds
v_mfma_f32_16x16x32_bf16 v[40:43], a[68:71], a[36:39], v[40:43]
v_mfma_f32_16x16x32_bf16 v[44:47], a[64:67], a[40:43], v[44:47]
ds_read_b64_tr_b16 a[78:79], v16 offset:25088
v_mfma_f32_16x16x32_bf16 v[44:47], a[68:71], a[44:47], v[44:47]
v_mfma_f32_16x16x32_bf16 v[48:51], a[64:67], a[48:51], v[48:51]
v_mfma_f32_16x16x32_bf16 v[48:51], a[68:71], a[52:55], v[48:51]
v_mfma_f32_16x16x32_bf16 v[52:55], a[64:67], a[56:59], v[52:55]
buffer_load_dwordx4 v3, s[12:15], s25 offen offset:2112 lds
v_mfma_f32_16x16x32_bf16 v[52:55], a[68:71], a[60:63], v[52:55]
s_waitcnt lgkmcnt(1)
v_mfma_f32_16x16x32_bf16 v[56:59], a[72:75], a[0:3], v[56:59]
s_waitcnt lgkmcnt(0)
v_mfma_f32_16x16x32_bf16 v[56:59], a[76:79], a[4:7], v[56:59]
ds_read_b64_tr_b16 a[64:65], v16 offset:32
v_mfma_f32_16x16x32_bf16 v[60:63], a[72:75], a[8:11], v[60:63]
v_mfma_f32_16x16x32_bf16 v[60:63], a[76:79], a[12:15], v[60:63]
v_mfma_f32_16x16x32_bf16 v[64:67], a[72:75], a[16:19], v[64:67]
ds_read_b64_tr_b16 a[66:67], v16 offset:3616
buffer_load_dwordx4 v4, s[12:15], s25 offen offset:3168 lds
v_mfma_f32_16x16x32_bf16 v[64:67], a[76:79], a[20:23], v[64:67]
v_mfma_f32_16x16x32_bf16 v[68:71], a[72:75], a[24:27], v[68:71]
v_mfma_f32_16x16x32_bf16 v[68:71], a[76:79], a[28:31], v[68:71]
ds_read_b64_tr_b16 a[68:69], v16 offset:7200
v_mfma_f32_16x16x32_bf16 v[72:75], a[72:75], a[32:35], v[72:75]
v_mfma_f32_16x16x32_bf16 v[72:75], a[76:79], a[36:39], v[72:75]
s_add_u32 m0, s30, 0x13200
v_mfma_f32_16x16x32_bf16 v[76:79], a[72:75], a[40:43], v[76:79]
ds_read_b64_tr_b16 a[70:71], v16 offset:10784
buffer_load_dwordx4 v5, s[16:19], s26 offen lds
v_mfma_f32_16x16x32_bf16 v[76:79], a[76:79], a[44:47], v[76:79]
v_mfma_f32_16x16x32_bf16 v[80:83], a[72:75], a[48:51], v[80:83]
v_mfma_f32_16x16x32_bf16 v[80:83], a[76:79], a[52:55], v[80:83]
v_mfma_f32_16x16x32_bf16 v[84:87], a[72:75], a[56:59], v[84:87]
v_mfma_f32_16x16x32_bf16 v[84:87], a[76:79], a[60:63], v[84:87]
s_waitcnt lgkmcnt(1)
v_mfma_f32_16x16x32_bf16 v[88:91], a[64:67], a[0:3], v[88:91]
buffer_load_dwordx4 v6, s[16:19], s26 offen offset:1024 lds
s_waitcnt lgkmcnt(0)
v_mfma_f32_16x16x32_bf16 v[88:91], a[68:71], a[4:7], v[88:91]
ds_read_b64_tr_b16 a[72:73], v16 offset:14368
v_mfma_f32_16x16x32_bf16 v[92:95], a[64:67], a[8:11], v[92:95]
v_mfma_f32_16x16x32_bf16 v[92:95], a[68:71], a[12:15], v[92:95]
v_mfma_f32_16x16x32_bf16 v[96:99], a[64:67], a[16:19], v[96:99]
ds_read_b64_tr_b16 a[74:75], v16 offset:17952
v_mfma_f32_16x16x32_bf16 v[96:99], a[68:71], a[20:23], v[96:99]
v_mfma_f32_16x16x32_bf16 v[100:103], a[64:67], a[24:27], v[100:103]
buffer_load_dwordx4 v7, s[16:19], s26 offen offset:2048 lds
v_mfma_f32_16x16x32_bf16 v[100:103], a[68:71], a[28:31], v[100:103]
ds_read_b64_tr_b16 a[76:77], v16 offset:21536
v_mfma_f32_16x16x32_bf16 v[104:107], a[64:67], a[32:35], v[104:107]
v_mfma_f32_16x16x32_bf16 v[104:107], a[68:71], a[36:39], v[104:107]
v_mfma_f32_16x16x32_bf16 v[108:111], a[64:67], a[40:43], v[108:111]
ds_read_b64_tr_b16 a[78:79], v16 offset:25120
v_mfma_f32_16x16x32_bf16 v[108:111], a[68:71], a[44:47], v[108:111]
v_mfma_f32_16x16x32_bf16 v[112:115], a[64:67], a[48:51], v[112:115]
buffer_load_dwordx4 v8, s[16:19], s26 offen offset:3072 lds
v_mfma_f32_16x16x32_bf16 v[112:115], a[68:71], a[52:55], v[112:115]
v_mfma_f32_16x16x32_bf16 v[116:119], a[64:67], a[56:59], v[116:119]
v_mfma_f32_16x16x32_bf16 v[116:119], a[68:71], a[60:63], v[116:119]
s_waitcnt lgkmcnt(1)
v_mfma_f32_16x16x32_bf16 v[120:123], a[72:75], a[0:3], v[120:123]
s_waitcnt lgkmcnt(0)
v_mfma_f32_16x16x32_bf16 v[120:123], a[76:79], a[4:7], v[120:123]
ds_read_b64_tr_b16 a[64:65], v16 offset:64
s_add_u32 m0, s30, 0x14200
v_mfma_f32_16x16x32_bf16 v[124:127], a[72:75], a[8:11], v[124:127]
buffer_load_dwordx4 v9, s[16:19], s26 offen lds
v_mfma_f32_16x16x32_bf16 v[124:127], a[76:79], a[12:15], v[124:127]
v_mfma_f32_16x16x32_bf16 v[128:131], a[72:75], a[16:19], v[128:131]
ds_read_b64_tr_b16 a[66:67], v16 offset:3648
v_mfma_f32_16x16x32_bf16 v[128:131], a[76:79], a[20:23], v[128:131]
v_mfma_f32_16x16x32_bf16 v[132:135], a[72:75], a[24:27], v[132:135]
v_mfma_f32_16x16x32_bf16 v[132:135], a[76:79], a[28:31], v[132:135]
ds_read_b64_tr_b16 a[68:69], v16 offset:7232
v_mfma_f32_16x16x32_bf16 v[136:139], a[72:75], a[32:35], v[136:139]
buffer_load_dwordx4 v10, s[16:19], s26 offen offset:1024 lds
v_mfma_f32_16x16x32_bf16 v[136:139], a[76:79], a[36:39], v[136:139]
v_mfma_f32_16x16x32_bf16 v[140:143], a[72:75], a[40:43], v[140:143]
ds_read_b64_tr_b16 a[70:71], v16 offset:10816
v_mfma_f32_16x16x32_bf16 v[140:143], a[76:79], a[44:47], v[140:143]
v_mfma_f32_16x16x32_bf16 v[144:147], a[72:75], a[48:51], v[144:147]
v_mfma_f32_16x16x32_bf16 v[144:147], a[76:79], a[52:55], v[144:147]
v_mfma_f32_16x16x32_bf16 v[148:151], a[72:75], a[56:59], v[148:151]
buffer_load_dwordx4 v11, s[16:19], s26 offen offset:2048 lds
v_mfma_f32_16x16x32_bf16 v[148:151], a[76:79], a[60:63], v[148:151]
s_waitcnt lgkmcnt(1)
v_mfma_f32_16x16x32_bf16 v[152:155], a[64:67], a[0:3], v[152:155]
s_waitcnt lgkmcnt(0)
v_mfma_f32_16x16x32_bf16 v[152:155], a[68:71], a[4:7], v[152:155]
ds_read_b64_tr_b16 a[72:73], v16 offset:14400
v_mfma_f32_16x16x32_bf16 v[156:159], a[64:67], a[8:11], v[156:159]
v_mfma_f32_16x16x32_bf16 v[156:159], a[68:71], a[12:15], v[156:159]
s_add_u32 m0, s28, 0x13200
v_mfma_f32_16x16x32_bf16 v[160:163], a[64:67], a[16:19], v[160:163]
ds_read_b64_tr_b16 a[74:75], v16 offset:17984
buffer_load_dwordx4 v0, s[8:11], s24 offen sc1 lds
v_mfma_f32_16x16x32_bf16 v[160:163], a[68:71], a[20:23], v[160:163]
v_mfma_f32_16x16x32_bf16 v[164:167], a[64:67], a[24:27], v[164:167]
v_mfma_f32_16x16x32_bf16 v[164:167], a[68:71], a[28:31], v[164:167]
ds_read_b64_tr_b16 a[76:77], v16 offset:21568
v_mfma_f32_16x16x32_bf16 v[168:171], a[64:67], a[32:35], v[168:171]
v_mfma_f32_16x16x32_bf16 v[168:171], a[68:71], a[36:39], v[168:171]
v_mfma_f32_16x16x32_bf16 v[172:175], a[64:67], a[40:43], v[172:175]
ds_read_b64_tr_b16 a[78:79], v16 offset:25152
buffer_load_dwordx4 v0, s[8:11], s24 offen offset:1024 sc1 lds
v_mfma_f32_16x16x32_bf16 v[172:175], a[68:71], a[44:47], v[172:175]
v_mfma_f32_16x16x32_bf16 v[176:179], a[64:67], a[48:51], v[176:179]
v_mfma_f32_16x16x32_bf16 v[176:179], a[68:71], a[52:55], v[176:179]
v_mfma_f32_16x16x32_bf16 v[180:183], a[64:67], a[56:59], v[180:183]
v_mfma_f32_16x16x32_bf16 v[180:183], a[68:71], a[60:63], v[180:183]
s_waitcnt lgkmcnt(1)
v_mfma_f32_16x16x32_bf16 v[184:187], a[72:75], a[0:3], v[184:187]
buffer_load_dwordx4 v0, s[8:11], s24 offen offset:2048 sc1 lds
s_waitcnt lgkmcnt(0)
v_mfma_f32_16x16x32_bf16 v[184:187], a[76:79], a[4:7], v[184:187]
ds_read_b64_tr_b16 a[64:65], v16 offset:96
v_mfma_f32_16x16x32_bf16 v[188:191], a[72:75], a[8:11], v[188:191]
v_mfma_f32_16x16x32_bf16 v[188:191], a[76:79], a[12:15], v[188:191]
v_mfma_f32_16x16x32_bf16 v[192:195], a[72:75], a[16:19], v[192:195]
ds_read_b64_tr_b16 a[66:67], v16 offset:3680
v_mfma_f32_16x16x32_bf16 v[192:195], a[76:79], a[20:23], v[192:195]
v_mfma_f32_16x16x32_bf16 v[196:199], a[72:75], a[24:27], v[196:199]
buffer_load_dwordx4 v0, s[8:11], s24 offen offset:3072 sc1 lds
v_mfma_f32_16x16x32_bf16 v[196:199], a[76:79], a[28:31], v[196:199]
ds_read_b64_tr_b16 a[68:69], v16 offset:7264
v_mfma_f32_16x16x32_bf16 v[200:203], a[72:75], a[32:35], v[200:203]
v_mfma_f32_16x16x32_bf16 v[200:203], a[76:79], a[36:39], v[200:203]
v_mfma_f32_16x16x32_bf16 v[204:207], a[72:75], a[40:43], v[204:207]
ds_read_b64_tr_b16 a[70:71], v16 offset:10848
v_mfma_f32_16x16x32_bf16 v[204:207], a[76:79], a[44:47], v[204:207]
s_add_u32 m0, s28, 0x14200
s_add_u32 s34, s24, 0x1000
v_mfma_f32_16x16x32_bf16 v[208:211], a[72:75], a[48:51], v[208:211]
buffer_load_dwordx4 v0, s[8:11], s34 offen sc1 lds
v_mfma_f32_16x16x32_bf16 v[208:211], a[76:79], a[52:55], v[208:211]
v_mfma_f32_16x16x32_bf16 v[212:215], a[72:75], a[56:59], v[212:215]
v_mfma_f32_16x16x32_bf16 v[212:215], a[76:79], a[60:63], v[212:215]
s_waitcnt lgkmcnt(1)
v_mfma_f32_16x16x32_bf16 v[216:219], a[64:67], a[0:3], v[216:219]
s_waitcnt lgkmcnt(0)
v_mfma_f32_16x16x32_bf16 v[216:219], a[68:71], a[4:7], v[216:219]
ds_read_b64_tr_b16 a[72:73], v16 offset:14432
v_mfma_f32_16x16x32_bf16 v[220:223], a[64:67], a[8:11], v[220:223]
buffer_load_dwordx4 v0, s[8:11], s34 offen offset:1024 sc1 lds
v_mfma_f32_16x16x32_bf16 v[220:223], a[68:71], a[12:15], v[220:223]
v_mfma_f32_16x16x32_bf16 v[224:227], a[64:67], a[16:19], v[224:227]
ds_read_b64_tr_b16 a[74:75], v16 offset:18016
v_mfma_f32_16x16x32_bf16 v[224:227], a[68:71], a[20:23], v[224:227]
v_mfma_f32_16x16x32_bf16 v[228:231], a[64:67], a[24:27], v[228:231]
v_mfma_f32_16x16x32_bf16 v[228:231], a[68:71], a[28:31], v[228:231]
ds_read_b64_tr_b16 a[76:77], v16 offset:21600
v_mfma_f32_16x16x32_bf16 v[232:235], a[64:67], a[32:35], v[232:235]
buffer_load_dwordx4 v0, s[8:11], s34 offen offset:2048 sc1 lds
v_mfma_f32_16x16x32_bf16 v[232:235], a[68:71], a[36:39], v[232:235]
v_mfma_f32_16x16x32_bf16 v[236:239], a[64:67], a[40:43], v[236:239]
ds_read_b64_tr_b16 a[78:79], v16 offset:25184
v_mfma_f32_16x16x32_bf16 v[236:239], a[68:71], a[44:47], v[236:239]
v_mfma_f32_16x16x32_bf16 v[240:243], a[64:67], a[48:51], v[240:243]
v_mfma_f32_16x16x32_bf16 v[240:243], a[68:71], a[52:55], v[240:243]
v_mfma_f32_16x16x32_bf16 v[244:247], a[64:67], a[56:59], v[244:247]
buffer_load_dwordx4 v0, s[8:11], s34 offen offset:3072 sc1 lds
s_add_u32 s24, s24, 0x8000
s_add_u32 s25, s25, 0x80000
s_add_u32 s26, s26, 0x70000
v_mfma_f32_16x16x32_bf16 v[244:247], a[68:71], a[60:63], v[244:247]
s_waitcnt lgkmcnt(1)
v_mfma_f32_16x16x32_bf16 a[88:91], a[72:75], a[0:3], a[88:91]
s_waitcnt lgkmcnt(0)
v_mfma_f32_16x16x32_bf16 a[88:91], a[76:79], a[4:7], a[88:91]
ds_read_b64_tr_b16 a[64:65], v14
v_mfma_f32_16x16x32_bf16 a[92:95], a[72:75], a[8:11], a[92:95]
v_mfma_f32_16x16x32_bf16 a[92:95], a[76:79], a[12:15], a[92:95]
v_mfma_f32_16x16x32_bf16 a[96:99], a[72:75], a[16:19], a[96:99]
ds_read_b64_tr_b16 a[66:67], v14 offset:512
v_mfma_f32_16x16x32_bf16 a[96:99], a[76:79], a[20:23], a[96:99]
v_mfma_f32_16x16x32_bf16 a[100:103], a[72:75], a[24:27], a[100:103]
global_load_dword v255, v[252:253], off
v_mfma_f32_16x16x32_bf16 a[100:103], a[76:79], a[28:31], a[100:103]
ds_read_b64_tr_b16 a[68:69], v14 offset:8448
v_mfma_f32_16x16x32_bf16 a[104:107], a[72:75], a[32:35], a[104:107]
v_mfma_f32_16x16x32_bf16 a[104:107], a[76:79], a[36:39], a[104:107]
v_add_co_u32 v252, vcc, v254, v252
v_mfma_f32_16x16x32_bf16 a[108:111], a[72:75], a[40:43], a[108:111]
ds_read_b64_tr_b16 a[70:71], v14 offset:8960
v_mfma_f32_16x16x32_bf16 a[108:111], a[76:79], a[44:47], a[108:111]
v_mfma_f32_16x16x32_bf16 a[112:115], a[72:75], a[48:51], a[112:115]
v_addc_co_u32 v253, vcc, 0, v253, vcc
v_mfma_f32_16x16x32_bf16 a[112:115], a[76:79], a[52:55], a[112:115]
v_mfma_f32_16x16x32_bf16 a[116:119], a[72:75], a[56:59], a[116:119]
v_mfma_f32_16x16x32_bf16 a[116:119], a[76:79], a[60:63], a[116:119]
buffer_store_dwordx4 v[248:251], v0, s[76:79], s96 offen sc1
s_waitcnt lgkmcnt(1)
v_mfma_f32_16x16x32_bf16 a[120:123], a[64:67], a[0:3], a[120:123]
s_waitcnt lgkmcnt(0)
v_mfma_f32_16x16x32_bf16 a[120:123], a[68:71], a[4:7], a[120:123]
ds_read_b64_tr_b16 a[72:73], v14 offset:32
v_mfma_f32_16x16x32_bf16 a[124:127], a[64:67], a[8:11], a[124:127]
s_add_u32 s96, s96, 0x400
v_mfma_f32_16x16x32_bf16 a[124:127], a[68:71], a[12:15], a[124:127]
v_mfma_f32_16x16x32_bf16 a[128:131], a[64:67], a[16:19], a[128:131]
ds_read_b64_tr_b16 a[74:75], v14 offset:544
v_mfma_f32_16x16x32_bf16 a[128:131], a[68:71], a[20:23], a[128:131]
v_mfma_f32_16x16x32_bf16 a[132:135], a[64:67], a[24:27], a[132:135]
v_mfma_f32_16x16x32_bf16 a[132:135], a[68:71], a[28:31], a[132:135]
ds_read_b64_tr_b16 a[76:77], v14 offset:8480
v_mfma_f32_16x16x32_bf16 a[136:139], a[64:67], a[32:35], a[136:139]
v_mfma_f32_16x16x32_bf16 a[136:139], a[68:71], a[36:39], a[136:139]
v_mfma_f32_16x16x32_bf16 a[140:143], a[64:67], a[40:43], a[140:143]
ds_read_b64_tr_b16 a[78:79], v14 offset:8992
v_mfma_f32_16x16x32_bf16 a[140:143], a[68:71], a[44:47], a[140:143]
v_mfma_f32_16x16x32_bf16 a[144:147], a[64:67], a[48:51], a[144:147]
v_mfma_f32_16x16x32_bf16 a[144:147], a[68:71], a[52:55], a[144:147]
v_mfma_f32_16x16x32_bf16 a[148:151], a[64:67], a[56:59], a[148:151]
v_mfma_f32_16x16x32_bf16 a[148:151], a[68:71], a[60:63], a[148:151]
s_waitcnt lgkmcnt(1)
v_mfma_f32_16x16x32_bf16 a[152:155], a[72:75], a[0:3], a[152:155]
s_waitcnt lgkmcnt(0)
v_mfma_f32_16x16x32_bf16 a[152:155], a[76:79], a[4:7], a[152:155]
ds_read_b64_tr_b16 a[64:65], v14 offset:64
v_mfma_f32_16x16x32_bf16 a[156:159], a[72:75], a[8:11], a[156:159]
ds_read_b64_tr_b16 a[66:67], v14 offset:576
v_mfma_f32_16x16x32_bf16 a[156:159], a[76:79], a[12:15], a[156:159]
ds_read_b64_tr_b16 a[68:69], v14 offset:8512
v_mfma_f32_16x16x32_bf16 a[160:163], a[72:75], a[16:19], a[160:163]
ds_read_b64_tr_b16 a[70:71], v14 offset:9024
v_mfma_f32_16x16x32_bf16 a[160:163], a[76:79], a[20:23], a[160:163]
v_mfma_f32_16x16x32_bf16 a[164:167], a[72:75], a[24:27], a[164:167]
v_mfma_f32_16x16x32_bf16 a[164:167], a[76:79], a[28:31], a[164:167]
v_mfma_f32_16x16x32_bf16 a[168:171], a[72:75], a[32:35], a[168:171]
v_mfma_f32_16x16x32_bf16 a[168:171], a[76:79], a[36:39], a[168:171]
v_mfma_f32_16x16x32_bf16 a[172:175], a[72:75], a[40:43], a[172:175]
v_mfma_f32_16x16x32_bf16 a[172:175], a[76:79], a[44:47], a[172:175]
v_mfma_f32_16x16x32_bf16 a[176:179], a[72:75], a[48:51], a[176:179]
v_mfma_f32_16x16x32_bf16 a[176:179], a[76:79], a[52:55], a[176:179]
v_mfma_f32_16x16x32_bf16 a[180:183], a[72:75], a[56:59], a[180:183]
v_mfma_f32_16x16x32_bf16 a[180:183], a[76:79], a[60:63], a[180:183]
s_waitcnt vmcnt(2) lgkmcnt(0)
s_barrier
v_mfma_f32_16x16x32_bf16 a[184:187], a[64:67], a[0:3], a[184:187]
ds_read_b64_tr_b16 a[72:73], v17
ds_read_b128 a[0:3], v13
v_mfma_f32_16x16x32_bf16 a[184:187], a[68:71], a[4:7], a[184:187]
ds_read_b64_tr_b16 a[74:75], v17 offset:3584
ds_read_b128 a[4:7], v13 offset:1024
v_mfma_f32_16x16x32_bf16 a[188:191], a[64:67], a[8:11], a[188:191]
ds_read_b64_tr_b16 a[76:77], v17 offset:7168
ds_read_b128 a[8:11], v13 offset:2048
v_mfma_f32_16x16x32_bf16 a[188:191], a[68:71], a[12:15], a[188:191]
ds_read_b64_tr_b16 a[78:79], v17 offset:10752
ds_read_b128 a[12:15], v13 offset:3072
v_mfma_f32_16x16x32_bf16 a[192:195], a[64:67], a[16:19], a[192:195]
ds_read_b128 a[16:19], v13 offset:4096
v_mfma_f32_16x16x32_bf16 a[192:195], a[68:71], a[20:23], a[192:195]
ds_read_b128 a[20:23], v13 offset:5120
v_mfma_f32_16x16x32_bf16 a[196:199], a[64:67], a[24:27], a[196:199]
ds_read_b128 a[24:27], v13 offset:6144
v_mfma_f32_16x16x32_bf16 a[196:199], a[68:71], a[28:31], a[196:199]
ds_read_b128 a[28:31], v13 offset:7168
v_mfma_f32_16x16x32_bf16 a[200:203], a[64:67], a[32:35], a[200:203]
ds_read_b128 a[32:35], v13 offset:8192
v_mfma_f32_16x16x32_bf16 a[200:203], a[68:71], a[36:39], a[200:203]
ds_read_b128 a[36:39], v13 offset:9216
v_mfma_f32_16x16x32_bf16 a[204:207], a[64:67], a[40:43], a[204:207]
ds_read_b128 a[40:43], v13 offset:10240
v_mfma_f32_16x16x32_bf16 a[204:207], a[68:71], a[44:47], a[204:207]
ds_read_b128 a[44:47], v13 offset:11264
v_mfma_f32_16x16x32_bf16 a[208:211], a[64:67], a[48:51], a[208:211]
ds_read_b128 a[48:51], v13 offset:12288
v_mfma_f32_16x16x32_bf16 a[208:211], a[68:71], a[52:55], a[208:211]
ds_read_b128 a[52:55], v13 offset:13312
v_mfma_f32_16x16x32_bf16 a[212:215], a[64:67], a[56:59], a[212:215]
ds_read_b128 a[56:59], v13 offset:14336
v_mfma_f32_16x16x32_bf16 a[212:215], a[68:71], a[60:63], a[212:215]
ds_read_b128 a[60:63], v13 offset:15360
s_waitcnt lgkmcnt(6)
v_mfma_f32_16x16x32_bf16 v[24:27], a[72:75], a[0:3], v[24:27]
v_mfma_f32_16x16x32_bf16 v[24:27], a[76:79], a[4:7], v[24:27]
ds_read_b64_tr_b16 a[64:65], v17 offset:14336
s_add_u32 m0, s29, 0
v_mfma_f32_16x16x32_bf16 v[28:31], a[72:75], a[8:11], v[28:31]
buffer_load_dwordx4 v1, s[12:15], s25 offen lds
v_mfma_f32_16x16x32_bf16 v[28:31], a[76:79], a[12:15], v[28:31]
v_mfma_f32_16x16x32_bf16 v[32:35], a[72:75], a[16:19], v[32:35]
ds_read_b64_tr_b16 a[66:67], v17 offset:17920
v_mfma_f32_16x16x32_bf16 v[32:35], a[76:79], a[20:23], v[32:35]
v_mfma_f32_16x16x32_bf16 v[36:39], a[72:75], a[24:27], v[36:39]
v_mfma_f32_16x16x32_bf16 v[36:39], a[76:79], a[28:31], v[36:39]
ds_read_b64_tr_b16 a[68:69], v17 offset:21504
v_mfma_f32_16x16x32_bf16 v[40:43], a[72:75], a[32:35], v[40:43]
buffer_load_dwordx4 v2, s[12:15], s25 offen offset:1056 lds
v_mfma_f32_16x16x32_bf16 v[40:43], a[76:79], a[36:39], v[40:43]
s_waitcnt lgkmcnt(2)
v_mfma_f32_16x16x32_bf16 v[44:47], a[72:75], a[40:43], v[44:47]
ds_read_b64_tr_b16 a[70:71], v17 offset:25088
v_mfma_f32_16x16x32_bf16 v[44:47], a[76:79], a[44:47], v[44:47]
v_mfma_f32_16x16x32_bf16 v[48:51], a[72:75], a[48:51], v[48:51]
v_mfma_f32_16x16x32_bf16 v[48:51], a[76:79], a[52:55], v[48:51]
v_mfma_f32_16x16x32_bf16 v[52:55], a[72:75], a[56:59], v[52:55]
buffer_load_dwordx4 v3, s[12:15], s25 offen offset:2112 lds
v_mfma_f32_16x16x32_bf16 v[52:55], a[76:79], a[60:63], v[52:55]
s_waitcnt lgkmcnt(1)
v_mfma_f32_16x16x32_bf16 v[56:59], a[64:67], a[0:3], v[56:59]
s_waitcnt lgkmcnt(0)
v_mfma_f32_16x16x32_bf16 v[56:59], a[68:71], a[4:7], v[56:59]
ds_read_b64_tr_b16 a[72:73], v17 offset:32
v_mfma_f32_16x16x32_bf16 v[60:63], a[64:67], a[8:11], v[60:63]
v_mfma_f32_16x16x32_bf16 v[60:63], a[68:71], a[12:15], v[60:63]
v_mfma_f32_16x16x32_bf16 v[64:67], a[64:67], a[16:19], v[64:67]
ds_read_b64_tr_b16 a[74:75], v17 offset:3616
buffer_load_dwordx4 v4, s[12:15], s25 offen offset:3168 lds
v_mfma_f32_16x16x32_bf16 v[64:67], a[68:71], a[20:23], v[64:67]
v_mfma_f32_16x16x32_bf16 v[68:71], a[64:67], a[24:27], v[68:71]
v_mfma_f32_16x16x32_bf16 v[68:71], a[68:71], a[28:31], v[68:71]
ds_read_b64_tr_b16 a[76:77], v17 offset:7200
v_mfma_f32_16x16x32_bf16 v[72:75], a[64:67], a[32:35], v[72:75]
v_mfma_f32_16x16x32_bf16 v[72:75], a[68:71], a[36:39], v[72:75]
s_add_u32 m0, s30, 0
v_mfma_f32_16x16x32_bf16 v[76:79], a[64:67], a[40:43], v[76:79]
ds_read_b64_tr_b16 a[78:79], v17 offset:10784
buffer_load_dwordx4 v5, s[16:19], s26 offen lds
v_mfma_f32_16x16x32_bf16 v[76:79], a[68:71], a[44:47], v[76:79]
v_mfma_f32_16x16x32_bf16 v[80:83], a[64:67], a[48:51], v[80:83]
v_mfma_f32_16x16x32_bf16 v[80:83], a[68:71], a[52:55], v[80:83]
v_mfma_f32_16x16x32_bf16 v[84:87], a[64:67], a[56:59], v[84:87]
v_mfma_f32_16x16x32_bf16 v[84:87], a[68:71], a[60:63], v[84:87]
s_waitcnt lgkmcnt(1)
v_mfma_f32_16x16x32_bf16 v[88:91], a[72:75], a[0:3], v[88:91]
buffer_load_dwordx4 v6, s[16:19], s26 offen offset:1024 lds
s_waitcnt lgkmcnt(0)
v_mfma_f32_16x16x32_bf16 v[88:91], a[76:79], a[4:7], v[88:91]
ds_read_b64_tr_b16 a[64:65], v17 offset:14368
v_mfma_f32_16x16x32_bf16 v[92:95], a[72:75], a[8:11], v[92:95]
v_mfma_f32_16x16x32_bf16 v[92:95], a[76:79], a[12:15], v[92:95]
v_mfma_f32_16x16x32_bf16 v[96:99], a[72:75], a[16:19], v[96:99]
ds_read_b64_tr_b16 a[66:67], v17 offset:17952
v_mfma_f32_16x16x32_bf16 v[96:99], a[76:79], a[20:23], v[96:99]
v_mfma_f32_16x16x32_bf16 v[100:103], a[72:75], a[24:27], v[100:103]
buffer_load_dwordx4 v7, s[16:19], s26 offen offset:2048 lds
v_mfma_f32_16x16x32_bf16 v[100:103], a[76:79], a[28:31], v[100:103]
ds_read_b64_tr_b16 a[68:69], v17 offset:21536
v_mfma_f32_16x16x32_bf16 v[104:107], a[72:75], a[32:35], v[104:107]
v_mfma_f32_16x16x32_bf16 v[104:107], a[76:79], a[36:39], v[104:107]
v_mfma_f32_16x16x32_bf16 v[108:111], a[72:75], a[40:43], v[108:111]
ds_read_b64_tr_b16 a[70:71], v17 offset:25120
v_mfma_f32_16x16x32_bf16 v[108:111], a[76:79], a[44:47], v[108:111]
v_mfma_f32_16x16x32_bf16 v[112:115], a[72:75], a[48:51], v[112:115]
buffer_load_dwordx4 v8, s[16:19], s26 offen offset:3072 lds
)ASM"
R"ASM(v_mfma_f32_16x16x32_bf16 v[112:115], a[76:79], a[52:55], v[112:115]
v_mfma_f32_16x16x32_bf16 v[116:119], a[72:75], a[56:59], v[116:119]
v_mfma_f32_16x16x32_bf16 v[116:119], a[76:79], a[60:63], v[116:119]
s_waitcnt lgkmcnt(1)
v_mfma_f32_16x16x32_bf16 v[120:123], a[64:67], a[0:3], v[120:123]
s_waitcnt lgkmcnt(0)
v_mfma_f32_16x16x32_bf16 v[120:123], a[68:71], a[4:7], v[120:123]
ds_read_b64_tr_b16 a[72:73], v17 offset:64
s_add_u32 m0, s30, 0x1000
v_mfma_f32_16x16x32_bf16 v[124:127], a[64:67], a[8:11], v[124:127]
buffer_load_dwordx4 v9, s[16:19], s26 offen lds
v_mfma_f32_16x16x32_bf16 v[124:127], a[68:71], a[12:15], v[124:127]
v_mfma_f32_16x16x32_bf16 v[128:131], a[64:67], a[16:19], v[128:131]
ds_read_b64_tr_b16 a[74:75], v17 offset:3648
v_mfma_f32_16x16x32_bf16 v[128:131], a[68:71], a[20:23], v[128:131]
v_mfma_f32_16x16x32_bf16 v[132:135], a[64:67], a[24:27], v[132:135]
v_mfma_f32_16x16x32_bf16 v[132:135], a[68:71], a[28:31], v[132:135]
ds_read_b64_tr_b16 a[76:77], v17 offset:7232
v_mfma_f32_16x16x32_bf16 v[136:139], a[64:67], a[32:35], v[136:139]
buffer_load_dwordx4 v10, s[16:19], s26 offen offset:1024 lds
v_mfma_f32_16x16x32_bf16 v[136:139], a[68:71], a[36:39], v[136:139]
v_mfma_f32_16x16x32_bf16 v[140:143], a[64:67], a[40:43], v[140:143]
ds_read_b64_tr_b16 a[78:79], v17 offset:10816
v_mfma_f32_16x16x32_bf16 v[140:143], a[68:71], a[44:47], v[140:143]
v_mfma_f32_16x16x32_bf16 v[144:147], a[64:67], a[48:51], v[144:147]
v_mfma_f32_16x16x32_bf16 v[144:147], a[68:71], a[52:55], v[144:147]
v_mfma_f32_16x16x32_bf16 v[148:151], a[64:67], a[56:59], v[148:151]
buffer_load_dwordx4 v11, s[16:19], s26 offen offset:2048 lds
v_mfma_f32_16x16x32_bf16 v[148:151], a[68:71], a[60:63], v[148:151]
s_waitcnt lgkmcnt(1)
v_mfma_f32_16x16x32_bf16 v[152:155], a[72:75], a[0:3], v[152:155]
s_waitcnt lgkmcnt(0)
v_mfma_f32_16x16x32_bf16 v[152:155], a[76:79], a[4:7], v[152:155]
ds_read_b64_tr_b16 a[64:65], v17 offset:14400
v_mfma_f32_16x16x32_bf16 v[156:159], a[72:75], a[8:11], v[156:159]
v_mfma_f32_16x16x32_bf16 v[156:159], a[76:79], a[12:15], v[156:159]
s_add_u32 m0, s28, 0
v_mfma_f32_16x16x32_bf16 v[160:163], a[72:75], a[16:19], v[160:163]
ds_read_b64_tr_b16 a[66:67], v17 offset:17984
buffer_load_dwordx4 v0, s[8:11], s24 offen sc1 lds
v_mfma_f32_16x16x32_bf16 v[160:163], a[76:79], a[20:23], v[160:163]
v_mfma_f32_16x16x32_bf16 v[164:167], a[72:75], a[24:27], v[164:167]
v_mfma_f32_16x16x32_bf16 v[164:167], a[76:79], a[28:31], v[164:167]
ds_read_b64_tr_b16 a[68:69], v17 offset:21568
v_mfma_f32_16x16x32_bf16 v[168:171], a[72:75], a[32:35], v[168:171]
v_mfma_f32_16x16x32_bf16 v[168:171], a[76:79], a[36:39], v[168:171]
v_mfma_f32_16x16x32_bf16 v[172:175], a[72:75], a[40:43], v[172:175]
ds_read_b64_tr_b16 a[70:71], v17 offset:25152
buffer_load_dwordx4 v0, s[8:11], s24 offen offset:1024 sc1 lds
v_mfma_f32_16x16x32_bf16 v[172:175], a[76:79], a[44:47], v[172:175]
v_mfma_f32_16x16x32_bf16 v[176:179], a[72:75], a[48:51], v[176:179]
v_mfma_f32_16x16x32_bf16 v[176:179], a[76:79], a[52:55], v[176:179]
v_mfma_f32_16x16x32_bf16 v[180:183], a[72:75], a[56:59], v[180:183]
v_mfma_f32_16x16x32_bf16 v[180:183], a[76:79], a[60:63], v[180:183]
s_waitcnt lgkmcnt(1)
v_mfma_f32_16x16x32_bf16 v[184:187], a[64:67], a[0:3], v[184:187]
buffer_load_dwordx4 v0, s[8:11], s24 offen offset:2048 sc1 lds
s_waitcnt lgkmcnt(0)
v_mfma_f32_16x16x32_bf16 v[184:187], a[68:71], a[4:7], v[184:187]
ds_read_b64_tr_b16 a[72:73], v17 offset:96
v_mfma_f32_16x16x32_bf16 v[188:191], a[64:67], a[8:11], v[188:191]
v_mfma_f32_16x16x32_bf16 v[188:191], a[68:71], a[12:15], v[188:191]
v_mfma_f32_16x16x32_bf16 v[192:195], a[64:67], a[16:19], v[192:195]
ds_read_b64_tr_b16 a[74:75], v17 offset:3680
v_mfma_f32_16x16x32_bf16 v[192:195], a[68:71], a[20:23], v[192:195]
v_mfma_f32_16x16x32_bf16 v[196:199], a[64:67], a[24:27], v[196:199]
buffer_load_dwordx4 v0, s[8:11], s24 offen offset:3072 sc1 lds
v_mfma_f32_16x16x32_bf16 v[196:199], a[68:71], a[28:31], v[196:199]
ds_read_b64_tr_b16 a[76:77], v17 offset:7264
v_mfma_f32_16x16x32_bf16 v[200:203], a[64:67], a[32:35], v[200:203]
v_mfma_f32_16x16x32_bf16 v[200:203], a[68:71], a[36:39], v[200:203]
v_mfma_f32_16x16x32_bf16 v[204:207], a[64:67], a[40:43], v[204:207]
ds_read_b64_tr_b16 a[78:79], v17 offset:10848
v_mfma_f32_16x16x32_bf16 v[204:207], a[68:71], a[44:47], v[204:207]
s_add_u32 m0, s28, 0x1000
s_add_u32 s34, s24, 0x1000
v_mfma_f32_16x16x32_bf16 v[208:211], a[64:67], a[48:51], v[208:211]
buffer_load_dwordx4 v0, s[8:11], s34 offen sc1 lds
v_mfma_f32_16x16x32_bf16 v[208:211], a[68:71], a[52:55], v[208:211]
v_mfma_f32_16x16x32_bf16 v[212:215], a[64:67], a[56:59], v[212:215]
v_mfma_f32_16x16x32_bf16 v[212:215], a[68:71], a[60:63], v[212:215]
s_waitcnt lgkmcnt(1)
v_mfma_f32_16x16x32_bf16 v[216:219], a[72:75], a[0:3], v[216:219]
s_waitcnt lgkmcnt(0)
v_mfma_f32_16x16x32_bf16 v[216:219], a[76:79], a[4:7], v[216:219]
ds_read_b64_tr_b16 a[64:65], v17 offset:14432
v_mfma_f32_16x16x32_bf16 v[220:223], a[72:75], a[8:11], v[220:223]
buffer_load_dwordx4 v0, s[8:11], s34 offen offset:1024 sc1 lds
v_mfma_f32_16x16x32_bf16 v[220:223], a[76:79], a[12:15], v[220:223]
v_mfma_f32_16x16x32_bf16 v[224:227], a[72:75], a[16:19], v[224:227]
ds_read_b64_tr_b16 a[66:67], v17 offset:18016
v_mfma_f32_16x16x32_bf16 v[224:227], a[76:79], a[20:23], v[224:227]
v_mfma_f32_16x16x32_bf16 v[228:231], a[72:75], a[24:27], v[228:231]
v_mfma_f32_16x16x32_bf16 v[228:231], a[76:79], a[28:31], v[228:231]
ds_read_b64_tr_b16 a[68:69], v17 offset:21600
v_mfma_f32_16x16x32_bf16 v[232:235], a[72:75], a[32:35], v[232:235]
buffer_load_dwordx4 v0, s[8:11], s34 offen offset:2048 sc1 lds
v_mfma_f32_16x16x32_bf16 v[232:235], a[76:79], a[36:39], v[232:235]
v_mfma_f32_16x16x32_bf16 v[236:239], a[72:75], a[40:43], v[236:239]
ds_read_b64_tr_b16 a[70:71], v17 offset:25184
v_mfma_f32_16x16x32_bf16 v[236:239], a[76:79], a[44:47], v[236:239]
v_mfma_f32_16x16x32_bf16 v[240:243], a[72:75], a[48:51], v[240:243]
v_mfma_f32_16x16x32_bf16 v[240:243], a[76:79], a[52:55], v[240:243]
v_mfma_f32_16x16x32_bf16 v[244:247], a[72:75], a[56:59], v[244:247]
buffer_load_dwordx4 v0, s[8:11], s34 offen offset:3072 sc1 lds
s_add_u32 s24, s24, 0x8000
s_add_u32 s25, s25, 0x80000
s_add_u32 s26, s26, 0x70000
v_mfma_f32_16x16x32_bf16 v[244:247], a[76:79], a[60:63], v[244:247]
s_waitcnt lgkmcnt(1)
v_mfma_f32_16x16x32_bf16 a[88:91], a[64:67], a[0:3], a[88:91]
s_waitcnt lgkmcnt(0)
v_mfma_f32_16x16x32_bf16 a[88:91], a[68:71], a[4:7], a[88:91]
ds_read_b64_tr_b16 a[72:73], v15
v_mfma_f32_16x16x32_bf16 a[92:95], a[64:67], a[8:11], a[92:95]
v_mfma_f32_16x16x32_bf16 a[92:95], a[68:71], a[12:15], a[92:95]
v_mfma_f32_16x16x32_bf16 a[96:99], a[64:67], a[16:19], a[96:99]
ds_read_b64_tr_b16 a[74:75], v15 offset:512
v_mfma_f32_16x16x32_bf16 a[96:99], a[68:71], a[20:23], a[96:99]
v_mfma_f32_16x16x32_bf16 a[100:103], a[64:67], a[24:27], a[100:103]
global_load_dword v255, v[252:253], off
v_mfma_f32_16x16x32_bf16 a[100:103], a[68:71], a[28:31], a[100:103]
ds_read_b64_tr_b16 a[76:77], v15 offset:8448
v_mfma_f32_16x16x32_bf16 a[104:107], a[64:67], a[32:35], a[104:107]
v_mfma_f32_16x16x32_bf16 a[104:107], a[68:71], a[36:39], a[104:107]
v_add_co_u32 v252, vcc, v254, v252
v_mfma_f32_16x16x32_bf16 a[108:111], a[64:67], a[40:43], a[108:111]
ds_read_b64_tr_b16 a[78:79], v15 offset:8960
v_mfma_f32_16x16x32_bf16 a[108:111], a[68:71], a[44:47], a[108:111]
v_mfma_f32_16x16x32_bf16 a[112:115], a[64:67], a[48:51], a[112:115]
v_addc_co_u32 v253, vcc, 0, v253, vcc
v_mfma_f32_16x16x32_bf16 a[112:115], a[68:71], a[52:55], a[112:115]
v_mfma_f32_16x16x32_bf16 a[116:119], a[64:67], a[56:59], a[116:119]
v_mfma_f32_16x16x32_bf16 a[116:119], a[68:71], a[60:63], a[116:119]
buffer_store_dwordx4 v[248:251], v0, s[76:79], s96 offen sc1
s_waitcnt lgkmcnt(1)
v_mfma_f32_16x16x32_bf16 a[120:123], a[72:75], a[0:3], a[120:123]
s_waitcnt lgkmcnt(0)
v_mfma_f32_16x16x32_bf16 a[120:123], a[76:79], a[4:7], a[120:123]
ds_read_b64_tr_b16 a[64:65], v15 offset:32
v_mfma_f32_16x16x32_bf16 a[124:127], a[72:75], a[8:11], a[124:127]
s_add_u32 s96, s96, 0x400
v_mfma_f32_16x16x32_bf16 a[124:127], a[76:79], a[12:15], a[124:127]
v_mfma_f32_16x16x32_bf16 a[128:131], a[72:75], a[16:19], a[128:131]
ds_read_b64_tr_b16 a[66:67], v15 offset:544
v_mfma_f32_16x16x32_bf16 a[128:131], a[76:79], a[20:23], a[128:131]
v_mfma_f32_16x16x32_bf16 a[132:135], a[72:75], a[24:27], a[132:135]
v_mfma_f32_16x16x32_bf16 a[132:135], a[76:79], a[28:31], a[132:135]
ds_read_b64_tr_b16 a[68:69], v15 offset:8480
v_mfma_f32_16x16x32_bf16 a[136:139], a[72:75], a[32:35], a[136:139]
v_mfma_f32_16x16x32_bf16 a[136:139], a[76:79], a[36:39], a[136:139]
v_mfma_f32_16x16x32_bf16 a[140:143], a[72:75], a[40:43], a[140:143]
ds_read_b64_tr_b16 a[70:71], v15 offset:8992
v_mfma_f32_16x16x32_bf16 a[140:143], a[76:79], a[44:47], a[140:143]
v_mfma_f32_16x16x32_bf16 a[144:147], a[72:75], a[48:51], a[144:147]
v_mfma_f32_16x16x32_bf16 a[144:147], a[76:79], a[52:55], a[144:147]
v_mfma_f32_16x16x32_bf16 a[148:151], a[72:75], a[56:59], a[148:151]
v_mfma_f32_16x16x32_bf16 a[148:151], a[76:79], a[60:63], a[148:151]
s_waitcnt lgkmcnt(1)
v_mfma_f32_16x16x32_bf16 a[152:155], a[64:67], a[0:3], a[152:155]
s_waitcnt lgkmcnt(0)
v_mfma_f32_16x16x32_bf16 a[152:155], a[68:71], a[4:7], a[152:155]
ds_read_b64_tr_b16 a[72:73], v15 offset:64
v_mfma_f32_16x16x32_bf16 a[156:159], a[64:67], a[8:11], a[156:159]
ds_read_b64_tr_b16 a[74:75], v15 offset:576
v_mfma_f32_16x16x32_bf16 a[156:159], a[68:71], a[12:15], a[156:159]
ds_read_b64_tr_b16 a[76:77], v15 offset:8512
v_mfma_f32_16x16x32_bf16 a[160:163], a[64:67], a[16:19], a[160:163]
ds_read_b64_tr_b16 a[78:79], v15 offset:9024
v_mfma_f32_16x16x32_bf16 a[160:163], a[68:71], a[20:23], a[160:163]
v_mfma_f32_16x16x32_bf16 a[164:167], a[64:67], a[24:27], a[164:167]
v_mfma_f32_16x16x32_bf16 a[164:167], a[68:71], a[28:31], a[164:167]
v_mfma_f32_16x16x32_bf16 a[168:171], a[64:67], a[32:35], a[168:171]
v_mfma_f32_16x16x32_bf16 a[168:171], a[68:71], a[36:39], a[168:171]
v_mfma_f32_16x16x32_bf16 a[172:175], a[64:67], a[40:43], a[172:175]
v_mfma_f32_16x16x32_bf16 a[172:175], a[68:71], a[44:47], a[172:175]
v_mfma_f32_16x16x32_bf16 a[176:179], a[64:67], a[48:51], a[176:179]
v_mfma_f32_16x16x32_bf16 a[176:179], a[68:71], a[52:55], a[176:179]
v_mfma_f32_16x16x32_bf16 a[180:183], a[64:67], a[56:59], a[180:183]
v_mfma_f32_16x16x32_bf16 a[180:183], a[68:71], a[60:63], a[180:183]
s_waitcnt vmcnt(2) lgkmcnt(0)
s_barrier
v_mfma_f32_16x16x32_bf16 a[184:187], a[72:75], a[0:3], a[184:187]
ds_read_b64_tr_b16 a[64:65], v16
ds_read_b128 a[0:3], v12
v_mfma_f32_16x16x32_bf16 a[184:187], a[76:79], a[4:7], a[184:187]
ds_read_b64_tr_b16 a[66:67], v16 offset:3584
ds_read_b128 a[4:7], v12 offset:1024
v_mfma_f32_16x16x32_bf16 a[188:191], a[72:75], a[8:11], a[188:191]
ds_read_b64_tr_b16 a[68:69], v16 offset:7168
ds_read_b128 a[8:11], v12 offset:2048
v_mfma_f32_16x16x32_bf16 a[188:191], a[76:79], a[12:15], a[188:191]
ds_read_b64_tr_b16 a[70:71], v16 offset:10752
ds_read_b128 a[12:15], v12 offset:3072
v_mfma_f32_16x16x32_bf16 a[192:195], a[72:75], a[16:19], a[192:195]
ds_read_b128 a[16:19], v12 offset:4096
v_mfma_f32_16x16x32_bf16 a[192:195], a[76:79], a[20:23], a[192:195]
ds_read_b128 a[20:23], v12 offset:5120
v_mfma_f32_16x16x32_bf16 a[196:199], a[72:75], a[24:27], a[196:199]
ds_read_b128 a[24:27], v12 offset:6144
v_mfma_f32_16x16x32_bf16 a[196:199], a[76:79], a[28:31], a[196:199]
ds_read_b128 a[28:31], v12 offset:7168
v_mfma_f32_16x16x32_bf16 a[200:203], a[72:75], a[32:35], a[200:203]
ds_read_b128 a[32:35], v12 offset:8192
v_mfma_f32_16x16x32_bf16 a[200:203], a[76:79], a[36:39], a[200:203]
ds_read_b128 a[36:39], v12 offset:9216
v_mfma_f32_16x16x32_bf16 a[204:207], a[72:75], a[40:43], a[204:207]
ds_read_b128 a[40:43], v12 offset:10240
v_mfma_f32_16x16x32_bf16 a[204:207], a[76:79], a[44:47], a[204:207]
ds_read_b128 a[44:47], v12 offset:11264
v_mfma_f32_16x16x32_bf16 a[208:211], a[72:75], a[48:51], a[208:211]
ds_read_b128 a[48:51], v12 offset:12288
v_mfma_f32_16x16x32_bf16 a[208:211], a[76:79], a[52:55], a[208:211]
ds_read_b128 a[52:55], v12 offset:13312
v_mfma_f32_16x16x32_bf16 a[212:215], a[72:75], a[56:59], a[212:215]
ds_read_b128 a[56:59], v12 offset:14336
v_mfma_f32_16x16x32_bf16 a[212:215], a[76:79], a[60:63], a[212:215]
ds_read_b128 a[60:63], v12 offset:15360
s_sub_u32 s70, s70, 1
s_cmp_lg_u32 s70, 0
s_cbranch_scc1 G1A_%=
s_waitcnt lgkmcnt(0)
v_mfma_f32_16x16x32_bf16 v[24:27], a[64:67], a[0:3], v[24:27]
v_mfma_f32_16x16x32_bf16 v[24:27], a[68:71], a[4:7], v[24:27]
ds_read_b64_tr_b16 a[72:73], v16 offset:14336
s_add_u32 m0, s29, 0x13200
v_mfma_f32_16x16x32_bf16 v[28:31], a[64:67], a[8:11], v[28:31]
buffer_load_dwordx4 v1, s[12:15], s25 offen lds
v_mfma_f32_16x16x32_bf16 v[28:31], a[68:71], a[12:15], v[28:31]
v_mfma_f32_16x16x32_bf16 v[32:35], a[64:67], a[16:19], v[32:35]
ds_read_b64_tr_b16 a[74:75], v16 offset:17920
v_mfma_f32_16x16x32_bf16 v[32:35], a[68:71], a[20:23], v[32:35]
v_mfma_f32_16x16x32_bf16 v[36:39], a[64:67], a[24:27], v[36:39]
v_mfma_f32_16x16x32_bf16 v[36:39], a[68:71], a[28:31], v[36:39]
ds_read_b64_tr_b16 a[76:77], v16 offset:21504
v_mfma_f32_16x16x32_bf16 v[40:43], a[64:67], a[32:35], v[40:43]
buffer_load_dwordx4 v2, s[12:15], s25 offen offset:1056 lds
v_mfma_f32_16x16x32_bf16 v[40:43], a[68:71], a[36:39], v[40:43]
v_mfma_f32_16x16x32_bf16 v[44:47], a[64:67], a[40:43], v[44:47]
ds_read_b64_tr_b16 a[78:79], v16 offset:25088
v_mfma_f32_16x16x32_bf16 v[44:47], a[68:71], a[44:47], v[44:47]
v_mfma_f32_16x16x32_bf16 v[48:51], a[64:67], a[48:51], v[48:51]
v_mfma_f32_16x16x32_bf16 v[48:51], a[68:71], a[52:55], v[48:51]
v_mfma_f32_16x16x32_bf16 v[52:55], a[64:67], a[56:59], v[52:55]
buffer_load_dwordx4 v3, s[12:15], s25 offen offset:2112 lds
v_mfma_f32_16x16x32_bf16 v[52:55], a[68:71], a[60:63], v[52:55]
s_waitcnt lgkmcnt(1)
v_mfma_f32_16x16x32_bf16 v[56:59], a[72:75], a[0:3], v[56:59]
s_waitcnt lgkmcnt(0)
v_mfma_f32_16x16x32_bf16 v[56:59], a[76:79], a[4:7], v[56:59]
ds_read_b64_tr_b16 a[64:65], v16 offset:32
v_mfma_f32_16x16x32_bf16 v[60:63], a[72:75], a[8:11], v[60:63]
v_mfma_f32_16x16x32_bf16 v[60:63], a[76:79], a[12:15], v[60:63]
v_mfma_f32_16x16x32_bf16 v[64:67], a[72:75], a[16:19], v[64:67]
ds_read_b64_tr_b16 a[66:67], v16 offset:3616
buffer_load_dwordx4 v4, s[12:15], s25 offen offset:3168 lds
v_mfma_f32_16x16x32_bf16 v[64:67], a[76:79], a[20:23], v[64:67]
v_mfma_f32_16x16x32_bf16 v[68:71], a[72:75], a[24:27], v[68:71]
v_mfma_f32_16x16x32_bf16 v[68:71], a[76:79], a[28:31], v[68:71]
ds_read_b64_tr_b16 a[68:69], v16 offset:7200
v_mfma_f32_16x16x32_bf16 v[72:75], a[72:75], a[32:35], v[72:75]
v_mfma_f32_16x16x32_bf16 v[72:75], a[76:79], a[36:39], v[72:75]
s_add_u32 m0, s30, 0x13200
v_mfma_f32_16x16x32_bf16 v[76:79], a[72:75], a[40:43], v[76:79]
ds_read_b64_tr_b16 a[70:71], v16 offset:10784
buffer_load_dwordx4 v5, s[16:19], s26 offen lds
v_mfma_f32_16x16x32_bf16 v[76:79], a[76:79], a[44:47], v[76:79]
v_mfma_f32_16x16x32_bf16 v[80:83], a[72:75], a[48:51], v[80:83]
v_mfma_f32_16x16x32_bf16 v[80:83], a[76:79], a[52:55], v[80:83]
v_mfma_f32_16x16x32_bf16 v[84:87], a[72:75], a[56:59], v[84:87]
v_mfma_f32_16x16x32_bf16 v[84:87], a[76:79], a[60:63], v[84:87]
s_waitcnt lgkmcnt(1)
v_mfma_f32_16x16x32_bf16 v[88:91], a[64:67], a[0:3], v[88:91]
buffer_load_dwordx4 v6, s[16:19], s26 offen offset:1024 lds
s_waitcnt lgkmcnt(0)
v_mfma_f32_16x16x32_bf16 v[88:91], a[68:71], a[4:7], v[88:91]
ds_read_b64_tr_b16 a[72:73], v16 offset:14368
v_mfma_f32_16x16x32_bf16 v[92:95], a[64:67], a[8:11], v[92:95]
v_mfma_f32_16x16x32_bf16 v[92:95], a[68:71], a[12:15], v[92:95]
v_mfma_f32_16x16x32_bf16 v[96:99], a[64:67], a[16:19], v[96:99]
ds_read_b64_tr_b16 a[74:75], v16 offset:17952
v_mfma_f32_16x16x32_bf16 v[96:99], a[68:71], a[20:23], v[96:99]
v_mfma_f32_16x16x32_bf16 v[100:103], a[64:67], a[24:27], v[100:103]
buffer_load_dwordx4 v7, s[16:19], s26 offen offset:2048 lds
v_mfma_f32_16x16x32_bf16 v[100:103], a[68:71], a[28:31], v[100:103]
ds_read_b64_tr_b16 a[76:77], v16 offset:21536
v_mfma_f32_16x16x32_bf16 v[104:107], a[64:67], a[32:35], v[104:107]
v_mfma_f32_16x16x32_bf16 v[104:107], a[68:71], a[36:39], v[104:107]
v_mfma_f32_16x16x32_bf16 v[108:111], a[64:67], a[40:43], v[108:111]
ds_read_b64_tr_b16 a[78:79], v16 offset:25120
v_mfma_f32_16x16x32_bf16 v[108:111], a[68:71], a[44:47], v[108:111]
v_mfma_f32_16x16x32_bf16 v[112:115], a[64:67], a[48:51], v[112:115]
buffer_load_dwordx4 v8, s[16:19], s26 offen offset:3072 lds
v_mfma_f32_16x16x32_bf16 v[112:115], a[68:71], a[52:55], v[112:115]
v_mfma_f32_16x16x32_bf16 v[116:119], a[64:67], a[56:59], v[116:119]
v_mfma_f32_16x16x32_bf16 v[116:119], a[68:71], a[60:63], v[116:119]
s_waitcnt lgkmcnt(1)
v_mfma_f32_16x16x32_bf16 v[120:123], a[72:75], a[0:3], v[120:123]
s_waitcnt lgkmcnt(0)
v_mfma_f32_16x16x32_bf16 v[120:123], a[76:79], a[4:7], v[120:123]
ds_read_b64_tr_b16 a[64:65], v16 offset:64
s_add_u32 m0, s30, 0x14200
v_mfma_f32_16x16x32_bf16 v[124:127], a[72:75], a[8:11], v[124:127]
buffer_load_dwordx4 v9, s[16:19], s26 offen lds
v_mfma_f32_16x16x32_bf16 v[124:127], a[76:79], a[12:15], v[124:127]
v_mfma_f32_16x16x32_bf16 v[128:131], a[72:75], a[16:19], v[128:131]
ds_read_b64_tr_b16 a[66:67], v16 offset:3648
v_mfma_f32_16x16x32_bf16 v[128:131], a[76:79], a[20:23], v[128:131]
v_mfma_f32_16x16x32_bf16 v[132:135], a[72:75], a[24:27], v[132:135]
v_mfma_f32_16x16x32_bf16 v[132:135], a[76:79], a[28:31], v[132:135]
ds_read_b64_tr_b16 a[68:69], v16 offset:7232
v_mfma_f32_16x16x32_bf16 v[136:139], a[72:75], a[32:35], v[136:139]
buffer_load_dwordx4 v10, s[16:19], s26 offen offset:1024 lds
v_mfma_f32_16x16x32_bf16 v[136:139], a[76:79], a[36:39], v[136:139]
v_mfma_f32_16x16x32_bf16 v[140:143], a[72:75], a[40:43], v[140:143]
ds_read_b64_tr_b16 a[70:71], v16 offset:10816
v_mfma_f32_16x16x32_bf16 v[140:143], a[76:79], a[44:47], v[140:143]
v_mfma_f32_16x16x32_bf16 v[144:147], a[72:75], a[48:51], v[144:147]
v_mfma_f32_16x16x32_bf16 v[144:147], a[76:79], a[52:55], v[144:147]
v_mfma_f32_16x16x32_bf16 v[148:151], a[72:75], a[56:59], v[148:151]
buffer_load_dwordx4 v11, s[16:19], s26 offen offset:2048 lds
v_mfma_f32_16x16x32_bf16 v[148:151], a[76:79], a[60:63], v[148:151]
s_waitcnt lgkmcnt(1)
v_mfma_f32_16x16x32_bf16 v[152:155], a[64:67], a[0:3], v[152:155]
s_waitcnt lgkmcnt(0)
v_mfma_f32_16x16x32_bf16 v[152:155], a[68:71], a[4:7], v[152:155]
ds_read_b64_tr_b16 a[72:73], v16 offset:14400
v_mfma_f32_16x16x32_bf16 v[156:159], a[64:67], a[8:11], v[156:159]
v_mfma_f32_16x16x32_bf16 v[156:159], a[68:71], a[12:15], v[156:159]
s_add_u32 m0, s28, 0x13200
v_mfma_f32_16x16x32_bf16 v[160:163], a[64:67], a[16:19], v[160:163]
ds_read_b64_tr_b16 a[74:75], v16 offset:17984
buffer_load_dwordx4 v0, s[8:11], s24 offen sc1 lds
v_mfma_f32_16x16x32_bf16 v[160:163], a[68:71], a[20:23], v[160:163]
v_mfma_f32_16x16x32_bf16 v[164:167], a[64:67], a[24:27], v[164:167]
v_mfma_f32_16x16x32_bf16 v[164:167], a[68:71], a[28:31], v[164:167]
ds_read_b64_tr_b16 a[76:77], v16 offset:21568
v_mfma_f32_16x16x32_bf16 v[168:171], a[64:67], a[32:35], v[168:171]
v_mfma_f32_16x16x32_bf16 v[168:171], a[68:71], a[36:39], v[168:171]
v_mfma_f32_16x16x32_bf16 v[172:175], a[64:67], a[40:43], v[172:175]
ds_read_b64_tr_b16 a[78:79], v16 offset:25152
buffer_load_dwordx4 v0, s[8:11], s24 offen offset:1024 sc1 lds
v_mfma_f32_16x16x32_bf16 v[172:175], a[68:71], a[44:47], v[172:175]
v_mfma_f32_16x16x32_bf16 v[176:179], a[64:67], a[48:51], v[176:179]
v_mfma_f32_16x16x32_bf16 v[176:179], a[68:71], a[52:55], v[176:179]
v_mfma_f32_16x16x32_bf16 v[180:183], a[64:67], a[56:59], v[180:183]
v_mfma_f32_16x16x32_bf16 v[180:183], a[68:71], a[60:63], v[180:183]
s_waitcnt lgkmcnt(1)
v_mfma_f32_16x16x32_bf16 v[184:187], a[72:75], a[0:3], v[184:187]
buffer_load_dwordx4 v0, s[8:11], s24 offen offset:2048 sc1 lds
s_waitcnt lgkmcnt(0)
v_mfma_f32_16x16x32_bf16 v[184:187], a[76:79], a[4:7], v[184:187]
ds_read_b64_tr_b16 a[64:65], v16 offset:96
v_mfma_f32_16x16x32_bf16 v[188:191], a[72:75], a[8:11], v[188:191]
v_mfma_f32_16x16x32_bf16 v[188:191], a[76:79], a[12:15], v[188:191]
v_mfma_f32_16x16x32_bf16 v[192:195], a[72:75], a[16:19], v[192:195]
ds_read_b64_tr_b16 a[66:67], v16 offset:3680
v_mfma_f32_16x16x32_bf16 v[192:195], a[76:79], a[20:23], v[192:195]
v_mfma_f32_16x16x32_bf16 v[196:199], a[72:75], a[24:27], v[196:199]
buffer_load_dwordx4 v0, s[8:11], s24 offen offset:3072 sc1 lds
v_mfma_f32_16x16x32_bf16 v[196:199], a[76:79], a[28:31], v[196:199]
ds_read_b64_tr_b16 a[68:69], v16 offset:7264
v_mfma_f32_16x16x32_bf16 v[200:203], a[72:75], a[32:35], v[200:203]
v_mfma_f32_16x16x32_bf16 v[200:203], a[76:79], a[36:39], v[200:203]
v_mfma_f32_16x16x32_bf16 v[204:207], a[72:75], a[40:43], v[204:207]
ds_read_b64_tr_b16 a[70:71], v16 offset:10848
v_mfma_f32_16x16x32_bf16 v[204:207], a[76:79], a[44:47], v[204:207]
s_add_u32 m0, s28, 0x14200
s_add_u32 s34, s24, 0x1000
v_mfma_f32_16x16x32_bf16 v[208:211], a[72:75], a[48:51], v[208:211]
buffer_load_dwordx4 v0, s[8:11], s34 offen sc1 lds
v_mfma_f32_16x16x32_bf16 v[208:211], a[76:79], a[52:55], v[208:211]
v_mfma_f32_16x16x32_bf16 v[212:215], a[72:75], a[56:59], v[212:215]
v_mfma_f32_16x16x32_bf16 v[212:215], a[76:79], a[60:63], v[212:215]
s_waitcnt lgkmcnt(1)
v_mfma_f32_16x16x32_bf16 v[216:219], a[64:67], a[0:3], v[216:219]
s_waitcnt lgkmcnt(0)
v_mfma_f32_16x16x32_bf16 v[216:219], a[68:71], a[4:7], v[216:219]
ds_read_b64_tr_b16 a[72:73], v16 offset:14432
v_mfma_f32_16x16x32_bf16 v[220:223], a[64:67], a[8:11], v[220:223]
buffer_load_dwordx4 v0, s[8:11], s34 offen offset:1024 sc1 lds
v_mfma_f32_16x16x32_bf16 v[220:223], a[68:71], a[12:15], v[220:223]
v_mfma_f32_16x16x32_bf16 v[224:227], a[64:67], a[16:19], v[224:227]
ds_read_b64_tr_b16 a[74:75], v16 offset:18016
v_mfma_f32_16x16x32_bf16 v[224:227], a[68:71], a[20:23], v[224:227]
v_mfma_f32_16x16x32_bf16 v[228:231], a[64:67], a[24:27], v[228:231]
v_mfma_f32_16x16x32_bf16 v[228:231], a[68:71], a[28:31], v[228:231]
ds_read_b64_tr_b16 a[76:77], v16 offset:21600
v_mfma_f32_16x16x32_bf16 v[232:235], a[64:67], a[32:35], v[232:235]
buffer_load_dwordx4 v0, s[8:11], s34 offen offset:2048 sc1 lds
v_mfma_f32_16x16x32_bf16 v[232:235], a[68:71], a[36:39], v[232:235]
v_mfma_f32_16x16x32_bf16 v[236:239], a[64:67], a[40:43], v[236:239]
ds_read_b64_tr_b16 a[78:79], v16 offset:25184
v_mfma_f32_16x16x32_bf16 v[236:239], a[68:71], a[44:47], v[236:239]
v_mfma_f32_16x16x32_bf16 v[240:243], a[64:67], a[48:51], v[240:243]
v_mfma_f32_16x16x32_bf16 v[240:243], a[68:71], a[52:55], v[240:243]
v_mfma_f32_16x16x32_bf16 v[244:247], a[64:67], a[56:59], v[244:247]
buffer_load_dwordx4 v0, s[8:11], s34 offen offset:3072 sc1 lds
s_add_u32 s24, s24, 0x8000
s_add_u32 s25, s25, 0x80000
)ASM"
R"ASM(s_add_u32 s26, s26, 0x70000
v_mfma_f32_16x16x32_bf16 v[244:247], a[68:71], a[60:63], v[244:247]
s_waitcnt lgkmcnt(1)
v_mfma_f32_16x16x32_bf16 a[88:91], a[72:75], a[0:3], a[88:91]
s_waitcnt lgkmcnt(0)
v_mfma_f32_16x16x32_bf16 a[88:91], a[76:79], a[4:7], a[88:91]
ds_read_b64_tr_b16 a[64:65], v14
v_mfma_f32_16x16x32_bf16 a[92:95], a[72:75], a[8:11], a[92:95]
v_mfma_f32_16x16x32_bf16 a[92:95], a[76:79], a[12:15], a[92:95]
v_mfma_f32_16x16x32_bf16 a[96:99], a[72:75], a[16:19], a[96:99]
ds_read_b64_tr_b16 a[66:67], v14 offset:512
v_mfma_f32_16x16x32_bf16 a[96:99], a[76:79], a[20:23], a[96:99]
s_getpc_b64 s[40:41]
CTbA_%=:
s_sub_u32 s40, s40, CTbA_%=-KSTART_%=
s_subb_u32 s41, s41, 0
v_mfma_f32_16x16x32_bf16 a[100:103], a[72:75], a[24:27], a[100:103]
s_lshr_b32 s45, s5, 3
v_mfma_f32_16x16x32_bf16 a[100:103], a[76:79], a[28:31], a[100:103]
ds_read_b64_tr_b16 a[68:69], v14 offset:8448
s_lshl_b32 s45, s45, 2
v_mfma_f32_16x16x32_bf16 a[104:107], a[72:75], a[32:35], a[104:107]
s_add_u32 s45, s45, s4
v_mfma_f32_16x16x32_bf16 a[104:107], a[76:79], a[36:39], a[104:107]
s_lshl_b32 s45, s45, 13
v_mfma_f32_16x16x32_bf16 a[108:111], a[72:75], a[40:43], a[108:111]
ds_read_b64_tr_b16 a[70:71], v14 offset:8960
s_cmp_lt_u32 s45, END_%=-KSTART_%=
s_cselect_b32 s44, 0x80, 0
v_mfma_f32_16x16x32_bf16 a[108:111], a[76:79], a[44:47], a[108:111]
s_min_u32 s45, s45, END_%=-KSTART_%=-8192
v_mfma_f32_16x16x32_bf16 a[112:115], a[72:75], a[48:51], a[112:115]
v_mbcnt_lo_u32_b32 v254, -1, 0
v_mfma_f32_16x16x32_bf16 a[112:115], a[76:79], a[52:55], a[112:115]
v_mbcnt_hi_u32_b32 v254, -1, v254
v_mfma_f32_16x16x32_bf16 a[116:119], a[72:75], a[56:59], a[116:119]
v_mul_u32_u24 v254, s44, v254
v_mfma_f32_16x16x32_bf16 a[116:119], a[76:79], a[60:63], a[116:119]
v_add_u32 v254, s45, v254
s_waitcnt lgkmcnt(1)
v_mfma_f32_16x16x32_bf16 a[120:123], a[64:67], a[0:3], a[120:123]
global_load_dword v255, v254, s[40:41]
s_waitcnt lgkmcnt(0)
v_mfma_f32_16x16x32_bf16 a[120:123], a[68:71], a[4:7], a[120:123]
ds_read_b64_tr_b16 a[72:73], v14 offset:32
v_mfma_f32_16x16x32_bf16 a[124:127], a[64:67], a[8:11], a[124:127]
v_mfma_f32_16x16x32_bf16 a[124:127], a[68:71], a[12:15], a[124:127]
v_mfma_f32_16x16x32_bf16 a[128:131], a[64:67], a[16:19], a[128:131]
ds_read_b64_tr_b16 a[74:75], v14 offset:544
v_mfma_f32_16x16x32_bf16 a[128:131], a[68:71], a[20:23], a[128:131]
v_mfma_f32_16x16x32_bf16 a[132:135], a[64:67], a[24:27], a[132:135]
v_mfma_f32_16x16x32_bf16 a[132:135], a[68:71], a[28:31], a[132:135]
ds_read_b64_tr_b16 a[76:77], v14 offset:8480
v_mfma_f32_16x16x32_bf16 a[136:139], a[64:67], a[32:35], a[136:139]
v_mfma_f32_16x16x32_bf16 a[136:139], a[68:71], a[36:39], a[136:139]
v_mfma_f32_16x16x32_bf16 a[140:143], a[64:67], a[40:43], a[140:143]
ds_read_b64_tr_b16 a[78:79], v14 offset:8992
v_mfma_f32_16x16x32_bf16 a[140:143], a[68:71], a[44:47], a[140:143]
v_mfma_f32_16x16x32_bf16 a[144:147], a[64:67], a[48:51], a[144:147]
v_mfma_f32_16x16x32_bf16 a[144:147], a[68:71], a[52:55], a[144:147]
v_mfma_f32_16x16x32_bf16 a[148:151], a[64:67], a[56:59], a[148:151]
v_mfma_f32_16x16x32_bf16 a[148:151], a[68:71], a[60:63], a[148:151]
s_waitcnt lgkmcnt(1)
v_mfma_f32_16x16x32_bf16 a[152:155], a[72:75], a[0:3], a[152:155]
s_waitcnt lgkmcnt(0)
v_mfma_f32_16x16x32_bf16 a[152:155], a[76:79], a[4:7], a[152:155]
ds_read_b64_tr_b16 a[64:65], v14 offset:64
v_mfma_f32_16x16x32_bf16 a[156:159], a[72:75], a[8:11], a[156:159]
ds_read_b64_tr_b16 a[66:67], v14 offset:576
v_mfma_f32_16x16x32_bf16 a[156:159], a[76:79], a[12:15], a[156:159]
ds_read_b64_tr_b16 a[68:69], v14 offset:8512
v_mfma_f32_16x16x32_bf16 a[160:163], a[72:75], a[16:19], a[160:163]
ds_read_b64_tr_b16 a[70:71], v14 offset:9024
v_mfma_f32_16x16x32_bf16 a[160:163], a[76:79], a[20:23], a[160:163]
v_mfma_f32_16x16x32_bf16 a[164:167], a[72:75], a[24:27], a[164:167]
v_mfma_f32_16x16x32_bf16 a[164:167], a[76:79], a[28:31], a[164:167]
v_mfma_f32_16x16x32_bf16 a[168:171], a[72:75], a[32:35], a[168:171]
v_mfma_f32_16x16x32_bf16 a[168:171], a[76:79], a[36:39], a[168:171]
v_mfma_f32_16x16x32_bf16 a[172:175], a[72:75], a[40:43], a[172:175]
v_mfma_f32_16x16x32_bf16 a[172:175], a[76:79], a[44:47], a[172:175]
v_mfma_f32_16x16x32_bf16 a[176:179], a[72:75], a[48:51], a[176:179]
v_mfma_f32_16x16x32_bf16 a[176:179], a[76:79], a[52:55], a[176:179]
v_mfma_f32_16x16x32_bf16 a[180:183], a[72:75], a[56:59], a[180:183]
v_mfma_f32_16x16x32_bf16 a[180:183], a[76:79], a[60:63], a[180:183]
s_waitcnt vmcnt(1) lgkmcnt(0)
s_barrier
v_mfma_f32_16x16x32_bf16 a[184:187], a[64:67], a[0:3], a[184:187]
ds_read_b64_tr_b16 a[72:73], v17
ds_read_b128 a[0:3], v13
v_mfma_f32_16x16x32_bf16 a[184:187], a[68:71], a[4:7], a[184:187]
ds_read_b64_tr_b16 a[74:75], v17 offset:3584
ds_read_b128 a[4:7], v13 offset:1024
v_mfma_f32_16x16x32_bf16 a[188:191], a[64:67], a[8:11], a[188:191]
ds_read_b64_tr_b16 a[76:77], v17 offset:7168
ds_read_b128 a[8:11], v13 offset:2048
v_mfma_f32_16x16x32_bf16 a[188:191], a[68:71], a[12:15], a[188:191]
ds_read_b64_tr_b16 a[78:79], v17 offset:10752
ds_read_b128 a[12:15], v13 offset:3072
v_mfma_f32_16x16x32_bf16 a[192:195], a[64:67], a[16:19], a[192:195]
ds_read_b128 a[16:19], v13 offset:4096
v_mfma_f32_16x16x32_bf16 a[192:195], a[68:71], a[20:23], a[192:195]
ds_read_b128 a[20:23], v13 offset:5120
v_mfma_f32_16x16x32_bf16 a[196:199], a[64:67], a[24:27], a[196:199]
ds_read_b128 a[24:27], v13 offset:6144
v_mfma_f32_16x16x32_bf16 a[196:199], a[68:71], a[28:31], a[196:199]
ds_read_b128 a[28:31], v13 offset:7168
v_mfma_f32_16x16x32_bf16 a[200:203], a[64:67], a[32:35], a[200:203]
ds_read_b128 a[32:35], v13 offset:8192
v_mfma_f32_16x16x32_bf16 a[200:203], a[68:71], a[36:39], a[200:203]
ds_read_b128 a[36:39], v13 offset:9216
v_mfma_f32_16x16x32_bf16 a[204:207], a[64:67], a[40:43], a[204:207]
ds_read_b128 a[40:43], v13 offset:10240
v_mfma_f32_16x16x32_bf16 a[204:207], a[68:71], a[44:47], a[204:207]
ds_read_b128 a[44:47], v13 offset:11264
v_mfma_f32_16x16x32_bf16 a[208:211], a[64:67], a[48:51], a[208:211]
ds_read_b128 a[48:51], v13 offset:12288
v_mfma_f32_16x16x32_bf16 a[208:211], a[68:71], a[52:55], a[208:211]
ds_read_b128 a[52:55], v13 offset:13312
v_mfma_f32_16x16x32_bf16 a[212:215], a[64:67], a[56:59], a[212:215]
ds_read_b128 a[56:59], v13 offset:14336
v_mfma_f32_16x16x32_bf16 a[212:215], a[68:71], a[60:63], a[212:215]
ds_read_b128 a[60:63], v13 offset:15360
s_waitcnt lgkmcnt(6)
v_mfma_f32_16x16x32_bf16 v[24:27], a[72:75], a[0:3], v[24:27]
v_mbcnt_lo_u32_b32 v20, -1, 0
v_mbcnt_hi_u32_b32 v20, -1, v20
v_mfma_f32_16x16x32_bf16 v[24:27], a[76:79], a[4:7], v[24:27]
ds_read_b64_tr_b16 a[64:65], v17 offset:14336
v_lshrrev_b32 v21, 4, v20
v_and_b32 v22, 15, v20
v_mfma_f32_16x16x32_bf16 v[28:31], a[72:75], a[8:11], v[28:31]
v_add_u32 v23, 0, v21
v_xor_b32 v23, v22, v23
v_mfma_f32_16x16x32_bf16 v[28:31], a[76:79], a[12:15], v[28:31]
v_lshlrev_b32 v23, 4, v23
v_lshl_add_u32 v1, v21, 8, v23
v_mfma_f32_16x16x32_bf16 v[32:35], a[72:75], a[16:19], v[32:35]
ds_read_b64_tr_b16 a[66:67], v17 offset:17920
v_add_u32 v23, 4, v21
v_xor_b32 v23, v22, v23
v_mfma_f32_16x16x32_bf16 v[32:35], a[76:79], a[20:23], v[32:35]
v_lshlrev_b32 v23, 4, v23
v_lshl_add_u32 v2, v21, 8, v23
v_mfma_f32_16x16x32_bf16 v[36:39], a[72:75], a[24:27], v[36:39]
v_add_u32 v23, 8, v21
v_xor_b32 v23, v22, v23
v_mfma_f32_16x16x32_bf16 v[36:39], a[76:79], a[28:31], v[36:39]
ds_read_b64_tr_b16 a[68:69], v17 offset:21504
v_lshlrev_b32 v23, 4, v23
v_lshl_add_u32 v3, v21, 8, v23
v_mfma_f32_16x16x32_bf16 v[40:43], a[72:75], a[32:35], v[40:43]
v_add_u32 v23, 12, v21
v_xor_b32 v23, v22, v23
v_mfma_f32_16x16x32_bf16 v[40:43], a[76:79], a[36:39], v[40:43]
v_lshlrev_b32 v23, 4, v23
v_lshl_add_u32 v4, v21, 8, v23
s_waitcnt lgkmcnt(2)
v_mfma_f32_16x16x32_bf16 v[44:47], a[72:75], a[40:43], v[44:47]
ds_read_b64_tr_b16 a[70:71], v17 offset:25088
v_lshlrev_b32 v23, 4, v22
v_lshl_add_u32 v5, v21, 11, v23
v_mfma_f32_16x16x32_bf16 v[44:47], a[76:79], a[44:47], v[44:47]
v_add_u32 v5, 0x960, v5
v_subrev_u32 v6, 0x320, v5
v_mfma_f32_16x16x32_bf16 v[48:51], a[72:75], a[48:51], v[48:51]
v_subrev_u32 v7, 0x640, v5
v_subrev_u32 v8, 0x960, v5
v_mfma_f32_16x16x32_bf16 v[48:51], a[76:79], a[52:55], v[48:51]
s_mov_b32 s8, s50
s_and_b32 s9, s51, 0xffff
v_mfma_f32_16x16x32_bf16 v[52:55], a[72:75], a[56:59], v[52:55]
s_mov_b32 s10, 0x200000
s_mov_b32 s11, 0x20000
v_mfma_f32_16x16x32_bf16 v[52:55], a[76:79], a[60:63], v[52:55]
s_mov_b32 s12, s52
s_and_b32 s13, s53, 0xffff
s_waitcnt lgkmcnt(1)
v_mfma_f32_16x16x32_bf16 v[56:59], a[64:67], a[0:3], v[56:59]
s_mov_b32 s14, 0x200000
s_mov_b32 s15, 0x20000
s_waitcnt lgkmcnt(0)
v_mfma_f32_16x16x32_bf16 v[56:59], a[68:71], a[4:7], v[56:59]
ds_read_b64_tr_b16 a[72:73], v17 offset:32
s_lshr_b32 s35, s7, 3
s_lshl_b32 s35, s35, 19
v_mfma_f32_16x16x32_bf16 v[60:63], a[64:67], a[8:11], v[60:63]
s_add_u32 s35, s35, 0x60000
s_lshl_b32 s36, s4, 12
v_mfma_f32_16x16x32_bf16 v[60:63], a[68:71], a[12:15], v[60:63]
s_add_u32 s24, s35, s36
s_lshl_b32 s36, s68, 13
v_mfma_f32_16x16x32_bf16 v[64:67], a[64:67], a[16:19], v[64:67]
ds_read_b64_tr_b16 a[74:75], v17 offset:3616
s_add_u32 s25, s35, s36
s_lshl_b32 s36, s69, 10
v_mfma_f32_16x16x32_bf16 v[64:67], a[68:71], a[20:23], v[64:67]
s_add_u32 s25, s25, s36
s_sub_u32 s25, s25, 0x960
v_mfma_f32_16x16x32_bf16 v[68:71], a[64:67], a[24:27], v[68:71]
s_lshl_b32 s28, s4, 12
s_add_u32 s28, s28, 0
v_mfma_f32_16x16x32_bf16 v[68:71], a[68:71], a[28:31], v[68:71]
ds_read_b64_tr_b16 a[76:77], v17 offset:7200
s_mul_i32 s29, s68, 0x2100
s_mul_i32 s36, s69, 0x1080
v_mfma_f32_16x16x32_bf16 v[72:75], a[64:67], a[32:35], v[72:75]
s_add_u32 s29, s29, s36
s_add_u32 s29, s29, 0x8000
v_mfma_f32_16x16x32_bf16 v[72:75], a[68:71], a[36:39], v[72:75]
v_mfma_f32_16x16x32_bf16 v[76:79], a[64:67], a[40:43], v[76:79]
ds_read_b64_tr_b16 a[78:79], v17 offset:10784
s_add_u32 m0, s28, 0
v_mfma_f32_16x16x32_bf16 v[76:79], a[68:71], a[44:47], v[76:79]
buffer_load_dwordx4 v1, s[8:11], s24 offen lds
v_mfma_f32_16x16x32_bf16 v[80:83], a[64:67], a[48:51], v[80:83]
v_mfma_f32_16x16x32_bf16 v[80:83], a[68:71], a[52:55], v[80:83]
v_mfma_f32_16x16x32_bf16 v[84:87], a[64:67], a[56:59], v[84:87]
v_mfma_f32_16x16x32_bf16 v[84:87], a[68:71], a[60:63], v[84:87]
s_waitcnt lgkmcnt(1)
v_mfma_f32_16x16x32_bf16 v[88:91], a[72:75], a[0:3], v[88:91]
s_waitcnt lgkmcnt(0)
v_mfma_f32_16x16x32_bf16 v[88:91], a[76:79], a[4:7], v[88:91]
ds_read_b64_tr_b16 a[64:65], v17 offset:14368
buffer_load_dwordx4 v2, s[8:11], s24 offen offset:1024 lds
v_mfma_f32_16x16x32_bf16 v[92:95], a[72:75], a[8:11], v[92:95]
v_mfma_f32_16x16x32_bf16 v[92:95], a[76:79], a[12:15], v[92:95]
v_mfma_f32_16x16x32_bf16 v[96:99], a[72:75], a[16:19], v[96:99]
ds_read_b64_tr_b16 a[66:67], v17 offset:17952
v_mfma_f32_16x16x32_bf16 v[96:99], a[76:79], a[20:23], v[96:99]
v_mfma_f32_16x16x32_bf16 v[100:103], a[72:75], a[24:27], v[100:103]
v_mfma_f32_16x16x32_bf16 v[100:103], a[76:79], a[28:31], v[100:103]
ds_read_b64_tr_b16 a[68:69], v17 offset:21536
buffer_load_dwordx4 v3, s[8:11], s24 offen offset:2048 lds
v_mfma_f32_16x16x32_bf16 v[104:107], a[72:75], a[32:35], v[104:107]
v_mfma_f32_16x16x32_bf16 v[104:107], a[76:79], a[36:39], v[104:107]
v_mfma_f32_16x16x32_bf16 v[108:111], a[72:75], a[40:43], v[108:111]
ds_read_b64_tr_b16 a[70:71], v17 offset:25120
v_mfma_f32_16x16x32_bf16 v[108:111], a[76:79], a[44:47], v[108:111]
v_mfma_f32_16x16x32_bf16 v[112:115], a[72:75], a[48:51], v[112:115]
v_mfma_f32_16x16x32_bf16 v[112:115], a[76:79], a[52:55], v[112:115]
buffer_load_dwordx4 v4, s[8:11], s24 offen offset:3072 lds
s_add_u32 s24, s24, 0x4000
v_mfma_f32_16x16x32_bf16 v[116:119], a[72:75], a[56:59], v[116:119]
v_mfma_f32_16x16x32_bf16 v[116:119], a[76:79], a[60:63], v[116:119]
s_waitcnt lgkmcnt(1)
v_mfma_f32_16x16x32_bf16 v[120:123], a[64:67], a[0:3], v[120:123]
s_waitcnt lgkmcnt(0)
v_mfma_f32_16x16x32_bf16 v[120:123], a[68:71], a[4:7], v[120:123]
ds_read_b64_tr_b16 a[72:73], v17 offset:64
v_mfma_f32_16x16x32_bf16 v[124:127], a[64:67], a[8:11], v[124:127]
s_add_u32 m0, s29, 0
v_mfma_f32_16x16x32_bf16 v[124:127], a[68:71], a[12:15], v[124:127]
buffer_load_dwordx4 v5, s[12:15], s25 offen lds
v_mfma_f32_16x16x32_bf16 v[128:131], a[64:67], a[16:19], v[128:131]
ds_read_b64_tr_b16 a[74:75], v17 offset:3648
v_mfma_f32_16x16x32_bf16 v[128:131], a[68:71], a[20:23], v[128:131]
v_mfma_f32_16x16x32_bf16 v[132:135], a[64:67], a[24:27], v[132:135]
v_mfma_f32_16x16x32_bf16 v[132:135], a[68:71], a[28:31], v[132:135]
ds_read_b64_tr_b16 a[76:77], v17 offset:7232
v_mfma_f32_16x16x32_bf16 v[136:139], a[64:67], a[32:35], v[136:139]
v_mfma_f32_16x16x32_bf16 v[136:139], a[68:71], a[36:39], v[136:139]
buffer_load_dwordx4 v6, s[12:15], s25 offen offset:1056 lds
v_mfma_f32_16x16x32_bf16 v[140:143], a[64:67], a[40:43], v[140:143]
ds_read_b64_tr_b16 a[78:79], v17 offset:10816
v_mfma_f32_16x16x32_bf16 v[140:143], a[68:71], a[44:47], v[140:143]
v_mfma_f32_16x16x32_bf16 v[144:147], a[64:67], a[48:51], v[144:147]
v_mfma_f32_16x16x32_bf16 v[144:147], a[68:71], a[52:55], v[144:147]
v_mfma_f32_16x16x32_bf16 v[148:151], a[64:67], a[56:59], v[148:151]
v_mfma_f32_16x16x32_bf16 v[148:151], a[68:71], a[60:63], v[148:151]
buffer_load_dwordx4 v7, s[12:15], s25 offen offset:2112 lds
s_waitcnt lgkmcnt(1)
v_mfma_f32_16x16x32_bf16 v[152:155], a[72:75], a[0:3], v[152:155]
s_waitcnt lgkmcnt(0)
v_mfma_f32_16x16x32_bf16 v[152:155], a[76:79], a[4:7], v[152:155]
ds_read_b64_tr_b16 a[64:65], v17 offset:14400
v_mfma_f32_16x16x32_bf16 v[156:159], a[72:75], a[8:11], v[156:159]
v_mfma_f32_16x16x32_bf16 v[156:159], a[76:79], a[12:15], v[156:159]
v_mfma_f32_16x16x32_bf16 v[160:163], a[72:75], a[16:19], v[160:163]
ds_read_b64_tr_b16 a[66:67], v17 offset:17984
v_mfma_f32_16x16x32_bf16 v[160:163], a[76:79], a[20:23], v[160:163]
buffer_load_dwordx4 v8, s[12:15], s25 offen offset:3168 lds
s_add_u32 s25, s25, 0x4000
v_mfma_f32_16x16x32_bf16 v[164:167], a[72:75], a[24:27], v[164:167]
v_mfma_f32_16x16x32_bf16 v[164:167], a[76:79], a[28:31], v[164:167]
ds_read_b64_tr_b16 a[68:69], v17 offset:21568
v_mfma_f32_16x16x32_bf16 v[168:171], a[72:75], a[32:35], v[168:171]
v_mfma_f32_16x16x32_bf16 v[168:171], a[76:79], a[36:39], v[168:171]
v_mfma_f32_16x16x32_bf16 v[172:175], a[72:75], a[40:43], v[172:175]
ds_read_b64_tr_b16 a[70:71], v17 offset:25152
s_add_u32 m0, s28, 0x4000
v_mfma_f32_16x16x32_bf16 v[172:175], a[76:79], a[44:47], v[172:175]
buffer_load_dwordx4 v1, s[8:11], s24 offen lds
v_mfma_f32_16x16x32_bf16 v[176:179], a[72:75], a[48:51], v[176:179]
v_mfma_f32_16x16x32_bf16 v[176:179], a[76:79], a[52:55], v[176:179]
v_mfma_f32_16x16x32_bf16 v[180:183], a[72:75], a[56:59], v[180:183]
v_mfma_f32_16x16x32_bf16 v[180:183], a[76:79], a[60:63], v[180:183]
s_waitcnt lgkmcnt(1)
v_mfma_f32_16x16x32_bf16 v[184:187], a[64:67], a[0:3], v[184:187]
s_waitcnt lgkmcnt(0)
v_mfma_f32_16x16x32_bf16 v[184:187], a[68:71], a[4:7], v[184:187]
ds_read_b64_tr_b16 a[72:73], v17 offset:96
buffer_load_dwordx4 v2, s[8:11], s24 offen offset:1024 lds
v_mfma_f32_16x16x32_bf16 v[188:191], a[64:67], a[8:11], v[188:191]
v_mfma_f32_16x16x32_bf16 v[188:191], a[68:71], a[12:15], v[188:191]
v_mfma_f32_16x16x32_bf16 v[192:195], a[64:67], a[16:19], v[192:195]
ds_read_b64_tr_b16 a[74:75], v17 offset:3680
v_mfma_f32_16x16x32_bf16 v[192:195], a[68:71], a[20:23], v[192:195]
v_mfma_f32_16x16x32_bf16 v[196:199], a[64:67], a[24:27], v[196:199]
v_mfma_f32_16x16x32_bf16 v[196:199], a[68:71], a[28:31], v[196:199]
ds_read_b64_tr_b16 a[76:77], v17 offset:7264
buffer_load_dwordx4 v3, s[8:11], s24 offen offset:2048 lds
v_mfma_f32_16x16x32_bf16 v[200:203], a[64:67], a[32:35], v[200:203]
v_mfma_f32_16x16x32_bf16 v[200:203], a[68:71], a[36:39], v[200:203]
v_mfma_f32_16x16x32_bf16 v[204:207], a[64:67], a[40:43], v[204:207]
ds_read_b64_tr_b16 a[78:79], v17 offset:10848
v_mfma_f32_16x16x32_bf16 v[204:207], a[68:71], a[44:47], v[204:207]
v_mfma_f32_16x16x32_bf16 v[208:211], a[64:67], a[48:51], v[208:211]
v_mfma_f32_16x16x32_bf16 v[208:211], a[68:71], a[52:55], v[208:211]
buffer_load_dwordx4 v4, s[8:11], s24 offen offset:3072 lds
s_add_u32 s24, s24, 0x4000
v_mfma_f32_16x16x32_bf16 v[212:215], a[64:67], a[56:59], v[212:215]
v_mfma_f32_16x16x32_bf16 v[212:215], a[68:71], a[60:63], v[212:215]
s_waitcnt lgkmcnt(1)
v_mfma_f32_16x16x32_bf16 v[216:219], a[72:75], a[0:3], v[216:219]
s_waitcnt lgkmcnt(0)
v_mfma_f32_16x16x32_bf16 v[216:219], a[76:79], a[4:7], v[216:219]
ds_read_b64_tr_b16 a[64:65], v17 offset:14432
v_mfma_f32_16x16x32_bf16 v[220:223], a[72:75], a[8:11], v[220:223]
v_mfma_f32_16x16x32_bf16 v[220:223], a[76:79], a[12:15], v[220:223]
v_mfma_f32_16x16x32_bf16 v[224:227], a[72:75], a[16:19], v[224:227]
ds_read_b64_tr_b16 a[66:67], v17 offset:18016
v_mfma_f32_16x16x32_bf16 v[224:227], a[76:79], a[20:23], v[224:227]
v_mfma_f32_16x16x32_bf16 v[228:231], a[72:75], a[24:27], v[228:231]
v_mfma_f32_16x16x32_bf16 v[228:231], a[76:79], a[28:31], v[228:231]
ds_read_b64_tr_b16 a[68:69], v17 offset:21600
v_mfma_f32_16x16x32_bf16 v[232:235], a[72:75], a[32:35], v[232:235]
v_mfma_f32_16x16x32_bf16 v[232:235], a[76:79], a[36:39], v[232:235]
v_mfma_f32_16x16x32_bf16 v[236:239], a[72:75], a[40:43], v[236:239]
ds_read_b64_tr_b16 a[70:71], v17 offset:25184
v_mfma_f32_16x16x32_bf16 v[236:239], a[76:79], a[44:47], v[236:239]
v_mfma_f32_16x16x32_bf16 v[240:243], a[72:75], a[48:51], v[240:243]
v_mfma_f32_16x16x32_bf16 v[240:243], a[76:79], a[52:55], v[240:243]
v_mfma_f32_16x16x32_bf16 v[244:247], a[72:75], a[56:59], v[244:247]
s_bfe_u32 s74, s5, 0x20001
v_mfma_f32_16x16x32_bf16 v[244:247], a[76:79], a[60:63], v[244:247]
s_and_b32 s41, s5, 1
s_waitcnt lgkmcnt(1)
v_mfma_f32_16x16x32_bf16 a[88:91], a[64:67], a[0:3], a[88:91]
s_lshl_b32 s41, s41, 2
s_waitcnt lgkmcnt(0)
v_mfma_f32_16x16x32_bf16 a[88:91], a[68:71], a[4:7], a[88:91]
ds_read_b64_tr_b16 a[72:73], v15
s_bfe_u32 s42, s5, 0x20003
v_mfma_f32_16x16x32_bf16 a[92:95], a[64:67], a[8:11], a[92:95]
s_add_u32 s72, s41, s42
v_mfma_f32_16x16x32_bf16 a[92:95], a[68:71], a[12:15], a[92:95]
s_lshr_b32 s73, s5, 5
v_mfma_f32_16x16x32_bf16 a[96:99], a[64:67], a[16:19], a[96:99]
ds_read_b64_tr_b16 a[74:75], v15 offset:512
s_mov_b32 s16, s62
v_mfma_f32_16x16x32_bf16 a[96:99], a[68:71], a[20:23], a[96:99]
s_and_b32 s17, s63, 0xffff
v_mfma_f32_16x16x32_bf16 a[100:103], a[64:67], a[24:27], a[100:103]
s_mov_b32 s18, 0x700000
v_mfma_f32_16x16x32_bf16 a[100:103], a[68:71], a[28:31], a[100:103]
ds_read_b64_tr_b16 a[76:77], v15 offset:8448
s_mov_b32 s19, 0x20000
v_mfma_f32_16x16x32_bf16 a[104:107], a[64:67], a[32:35], a[104:107]
s_mov_b32 s20, s66
v_mfma_f32_16x16x32_bf16 a[104:107], a[68:71], a[36:39], a[104:107]
s_and_b32 s21, s67, 0xffff
v_mfma_f32_16x16x32_bf16 a[108:111], a[64:67], a[40:43], a[108:111]
ds_read_b64_tr_b16 a[78:79], v15 offset:8960
s_mov_b32 s22, 0x2300000
v_mfma_f32_16x16x32_bf16 a[108:111], a[68:71], a[44:47], a[108:111]
s_mov_b32 s23, 0x20000
v_mfma_f32_16x16x32_bf16 a[112:115], a[64:67], a[48:51], a[112:115]
s_lshl_b32 s40, s72, 1
v_mfma_f32_16x16x32_bf16 a[112:115], a[68:71], a[52:55], a[112:115]
s_add_u32 s40, s40, s68
v_mfma_f32_16x16x32_bf16 a[116:119], a[64:67], a[56:59], a[116:119]
s_mul_i32 s26, s40, 56
v_mfma_f32_16x16x32_bf16 a[116:119], a[68:71], a[60:63], a[116:119]
s_mul_i32 s39, s74, 14
s_waitcnt lgkmcnt(1)
v_mfma_f32_16x16x32_bf16 a[120:123], a[72:75], a[0:3], a[120:123]
s_add_u32 s26, s26, s39
s_waitcnt lgkmcnt(0)
v_mfma_f32_16x16x32_bf16 a[120:123], a[76:79], a[4:7], a[120:123]
ds_read_b64_tr_b16 a[64:65], v15 offset:32
s_lshl_b32 s26, s26, 14
v_mfma_f32_16x16x32_bf16 a[124:127], a[72:75], a[8:11], a[124:127]
s_lshl_b32 s39, s69, 13
v_mfma_f32_16x16x32_bf16 a[124:127], a[76:79], a[12:15], a[124:127]
s_add_u32 s26, s26, s39
v_mfma_f32_16x16x32_bf16 a[128:131], a[72:75], a[16:19], a[128:131]
ds_read_b64_tr_b16 a[66:67], v15 offset:544
s_add_u32 s26, s26, 0x500000
v_mfma_f32_16x16x32_bf16 a[128:131], a[76:79], a[20:23], a[128:131]
s_mul_i32 s27, s74, 0x1c0000
)ASM"
R"ASM(v_mfma_f32_16x16x32_bf16 a[132:135], a[72:75], a[24:27], a[132:135]
s_lshl_b32 s39, s68, 16
v_mfma_f32_16x16x32_bf16 a[132:135], a[76:79], a[28:31], a[132:135]
ds_read_b64_tr_b16 a[68:69], v15 offset:8480
s_add_u32 s27, s27, s39
v_mfma_f32_16x16x32_bf16 a[136:139], a[72:75], a[32:35], a[136:139]
s_lshl_b32 s39, s69, 13
v_mfma_f32_16x16x32_bf16 a[136:139], a[76:79], a[36:39], a[136:139]
s_add_u32 s27, s27, s39
v_mfma_f32_16x16x32_bf16 a[140:143], a[72:75], a[40:43], a[140:143]
ds_read_b64_tr_b16 a[70:71], v15 offset:8992
s_lshl_b32 s39, s73, 8
v_mfma_f32_16x16x32_bf16 a[140:143], a[76:79], a[44:47], a[140:143]
s_add_u32 s27, s27, s39
v_mfma_f32_16x16x32_bf16 a[144:147], a[72:75], a[48:51], a[144:147]
s_mul_i32 s31, s40, 64
v_mfma_f32_16x16x32_bf16 a[144:147], a[76:79], a[52:55], a[144:147]
s_mul_i32 s39, s74, 16
v_mfma_f32_16x16x32_bf16 a[148:151], a[72:75], a[56:59], a[148:151]
s_add_u32 s31, s31, s39
v_mfma_f32_16x16x32_bf16 a[148:151], a[76:79], a[60:63], a[148:151]
s_lshl_b32 s31, s31, 14
s_waitcnt lgkmcnt(1)
v_mfma_f32_16x16x32_bf16 a[152:155], a[64:67], a[0:3], a[152:155]
s_lshl_b32 s39, s69, 13
s_waitcnt lgkmcnt(0)
v_mfma_f32_16x16x32_bf16 a[152:155], a[68:71], a[4:7], a[152:155]
ds_read_b64_tr_b16 a[72:73], v15 offset:64
s_add_u32 s31, s31, s39
v_mfma_f32_16x16x32_bf16 a[156:159], a[64:67], a[8:11], a[156:159]
ds_read_b64_tr_b16 a[74:75], v15 offset:576
s_add_u32 s31, s31, 0x1300000
v_mfma_f32_16x16x32_bf16 a[156:159], a[68:71], a[12:15], a[156:159]
ds_read_b64_tr_b16 a[76:77], v15 offset:8512
s_mul_i32 s75, s74, 0x200000
v_mfma_f32_16x16x32_bf16 a[160:163], a[64:67], a[16:19], a[160:163]
ds_read_b64_tr_b16 a[78:79], v15 offset:9024
s_lshl_b32 s39, s68, 16
v_mfma_f32_16x16x32_bf16 a[160:163], a[68:71], a[20:23], a[160:163]
s_add_u32 s75, s75, s39
v_mfma_f32_16x16x32_bf16 a[164:167], a[64:67], a[24:27], a[164:167]
s_lshl_b32 s39, s69, 13
v_mfma_f32_16x16x32_bf16 a[164:167], a[68:71], a[28:31], a[164:167]
s_add_u32 s75, s75, s39
v_mfma_f32_16x16x32_bf16 a[168:171], a[64:67], a[32:35], a[168:171]
s_lshl_b32 s39, s73, 8
v_mfma_f32_16x16x32_bf16 a[168:171], a[68:71], a[36:39], a[168:171]
s_add_u32 s75, s75, s39
v_mfma_f32_16x16x32_bf16 a[172:175], a[64:67], a[40:43], a[172:175]
s_mul_i32 s30, s68, 0x2100
v_mfma_f32_16x16x32_bf16 a[172:175], a[68:71], a[44:47], a[172:175]
s_mul_i32 s39, s69, 0x1080
v_mfma_f32_16x16x32_bf16 a[176:179], a[64:67], a[48:51], a[176:179]
s_add_u32 s30, s30, s39
v_mfma_f32_16x16x32_bf16 a[176:179], a[68:71], a[52:55], a[176:179]
v_mfma_f32_16x16x32_bf16 a[180:183], a[64:67], a[56:59], a[180:183]
v_mfma_f32_16x16x32_bf16 a[180:183], a[68:71], a[60:63], a[180:183]
s_waitcnt lgkmcnt(0)
s_barrier
v_mfma_f32_16x16x32_bf16 a[184:187], a[72:75], a[0:3], a[184:187]
v_mfma_f32_16x16x32_bf16 a[184:187], a[76:79], a[4:7], a[184:187]
v_mfma_f32_16x16x32_bf16 a[188:191], a[72:75], a[8:11], a[188:191]
v_mfma_f32_16x16x32_bf16 a[188:191], a[76:79], a[12:15], a[188:191]
v_mfma_f32_16x16x32_bf16 a[192:195], a[72:75], a[16:19], a[192:195]
v_mfma_f32_16x16x32_bf16 a[192:195], a[76:79], a[20:23], a[192:195]
v_mfma_f32_16x16x32_bf16 a[196:199], a[72:75], a[24:27], a[196:199]
v_mfma_f32_16x16x32_bf16 a[196:199], a[76:79], a[28:31], a[196:199]
v_mfma_f32_16x16x32_bf16 a[200:203], a[72:75], a[32:35], a[200:203]
v_mfma_f32_16x16x32_bf16 a[200:203], a[76:79], a[36:39], a[200:203]
v_mfma_f32_16x16x32_bf16 a[204:207], a[72:75], a[40:43], a[204:207]
v_mfma_f32_16x16x32_bf16 a[204:207], a[76:79], a[44:47], a[204:207]
v_mfma_f32_16x16x32_bf16 a[208:211], a[72:75], a[48:51], a[208:211]
v_mfma_f32_16x16x32_bf16 a[208:211], a[76:79], a[52:55], a[208:211]
v_mfma_f32_16x16x32_bf16 a[212:215], a[72:75], a[56:59], a[212:215]
v_mfma_f32_16x16x32_bf16 a[212:215], a[76:79], a[60:63], a[212:215]
s_nop 7
v_mbcnt_lo_u32_b32 v9, -1, 0
v_mbcnt_hi_u32_b32 v9, -1, v9
v_lshrrev_b32 v12, 5, v9
v_lshlrev_b32 v12, 8, v12
v_and_b32 v13, 15, v9
v_lshl_add_u32 v12, v13, 4, v12
v_bfe_u32 v13, v9, 4, 1
v_lshl_add_u32 v12, v13, 3, v12
s_lshl_b32 s34, s68, 15
s_add_u32 s34, s34, 0x13200
v_add_u32 v10, s34, v12
s_mov_b32 s36, 0x3e0293ee
s_mov_b32 s37, 0x3e0293ee
v_accvgpr_read_b32 v12, a120
v_accvgpr_read_b32 v13, a121
v_accvgpr_read_b32 v14, a122
v_accvgpr_read_b32 v15, a123
v_pk_mul_f32 v[12:13], v[12:13], s[36:37]
v_pk_mul_f32 v[14:15], v[14:15], s[36:37]
v_cvt_pk_bf16_f32 v16, v12, v13
v_cvt_pk_bf16_f32 v17, v14, v15
ds_write_b64 v10, v[16:17]
v_accvgpr_read_b32 v18, a124
v_accvgpr_read_b32 v19, a125
v_accvgpr_read_b32 v20, a126
v_accvgpr_read_b32 v21, a127
v_pk_mul_f32 v[18:19], v[18:19], s[36:37]
v_pk_mul_f32 v[20:21], v[20:21], s[36:37]
v_cvt_pk_bf16_f32 v22, v18, v19
v_cvt_pk_bf16_f32 v23, v20, v21
ds_write_b64 v10, v[22:23] offset:4096
v_accvgpr_read_b32 v12, a128
v_accvgpr_read_b32 v13, a129
v_accvgpr_read_b32 v14, a130
v_accvgpr_read_b32 v15, a131
v_pk_mul_f32 v[12:13], v[12:13], s[36:37]
v_pk_mul_f32 v[14:15], v[14:15], s[36:37]
v_cvt_pk_bf16_f32 v16, v12, v13
v_cvt_pk_bf16_f32 v17, v14, v15
ds_write_b64 v10, v[16:17] offset:8192
v_accvgpr_read_b32 v18, a132
v_accvgpr_read_b32 v19, a133
v_accvgpr_read_b32 v20, a134
v_accvgpr_read_b32 v21, a135
v_pk_mul_f32 v[18:19], v[18:19], s[36:37]
v_pk_mul_f32 v[20:21], v[20:21], s[36:37]
v_cvt_pk_bf16_f32 v22, v18, v19
v_cvt_pk_bf16_f32 v23, v20, v21
ds_write_b64 v10, v[22:23] offset:12288
v_accvgpr_read_b32 v12, a136
v_accvgpr_read_b32 v13, a137
v_accvgpr_read_b32 v14, a138
v_accvgpr_read_b32 v15, a139
v_pk_mul_f32 v[12:13], v[12:13], s[36:37]
v_pk_mul_f32 v[14:15], v[14:15], s[36:37]
v_cvt_pk_bf16_f32 v16, v12, v13
v_cvt_pk_bf16_f32 v17, v14, v15
ds_write_b64 v10, v[16:17] offset:16384
v_accvgpr_read_b32 v18, a140
v_accvgpr_read_b32 v19, a141
v_accvgpr_read_b32 v20, a142
v_accvgpr_read_b32 v21, a143
v_pk_mul_f32 v[18:19], v[18:19], s[36:37]
v_pk_mul_f32 v[20:21], v[20:21], s[36:37]
v_cvt_pk_bf16_f32 v22, v18, v19
v_cvt_pk_bf16_f32 v23, v20, v21
ds_write_b64 v10, v[22:23] offset:20480
v_accvgpr_read_b32 v12, a144
v_accvgpr_read_b32 v13, a145
v_accvgpr_read_b32 v14, a146
v_accvgpr_read_b32 v15, a147
v_pk_mul_f32 v[12:13], v[12:13], s[36:37]
v_pk_mul_f32 v[14:15], v[14:15], s[36:37]
v_cvt_pk_bf16_f32 v16, v12, v13
v_cvt_pk_bf16_f32 v17, v14, v15
ds_write_b64 v10, v[16:17] offset:24576
v_accvgpr_read_b32 v18, a148
v_accvgpr_read_b32 v19, a149
v_accvgpr_read_b32 v20, a150
v_accvgpr_read_b32 v21, a151
v_pk_mul_f32 v[18:19], v[18:19], s[36:37]
v_pk_mul_f32 v[20:21], v[20:21], s[36:37]
v_cvt_pk_bf16_f32 v22, v18, v19
v_cvt_pk_bf16_f32 v23, v20, v21
ds_write_b64 v10, v[22:23] offset:28672
v_accvgpr_read_b32 v12, a152
v_accvgpr_read_b32 v13, a153
v_accvgpr_read_b32 v14, a154
v_accvgpr_read_b32 v15, a155
v_pk_mul_f32 v[12:13], v[12:13], s[36:37]
v_pk_mul_f32 v[14:15], v[14:15], s[36:37]
v_cvt_pk_bf16_f32 v16, v12, v13
v_cvt_pk_bf16_f32 v17, v14, v15
ds_write_b64 v10, v[16:17] offset:512
v_accvgpr_read_b32 v18, a156
v_accvgpr_read_b32 v19, a157
v_accvgpr_read_b32 v20, a158
v_accvgpr_read_b32 v21, a159
v_pk_mul_f32 v[18:19], v[18:19], s[36:37]
v_pk_mul_f32 v[20:21], v[20:21], s[36:37]
v_cvt_pk_bf16_f32 v22, v18, v19
v_cvt_pk_bf16_f32 v23, v20, v21
ds_write_b64 v10, v[22:23] offset:4608
v_accvgpr_read_b32 v12, a160
v_accvgpr_read_b32 v13, a161
v_accvgpr_read_b32 v14, a162
v_accvgpr_read_b32 v15, a163
v_pk_mul_f32 v[12:13], v[12:13], s[36:37]
v_pk_mul_f32 v[14:15], v[14:15], s[36:37]
v_cvt_pk_bf16_f32 v16, v12, v13
v_cvt_pk_bf16_f32 v17, v14, v15
ds_write_b64 v10, v[16:17] offset:8704
v_accvgpr_read_b32 v18, a164
v_accvgpr_read_b32 v19, a165
v_accvgpr_read_b32 v20, a166
v_accvgpr_read_b32 v21, a167
v_pk_mul_f32 v[18:19], v[18:19], s[36:37]
v_pk_mul_f32 v[20:21], v[20:21], s[36:37]
v_cvt_pk_bf16_f32 v22, v18, v19
v_cvt_pk_bf16_f32 v23, v20, v21
ds_write_b64 v10, v[22:23] offset:12800
v_accvgpr_read_b32 v12, a168
v_accvgpr_read_b32 v13, a169
v_accvgpr_read_b32 v14, a170
v_accvgpr_read_b32 v15, a171
v_pk_mul_f32 v[12:13], v[12:13], s[36:37]
v_pk_mul_f32 v[14:15], v[14:15], s[36:37]
v_cvt_pk_bf16_f32 v16, v12, v13
v_cvt_pk_bf16_f32 v17, v14, v15
ds_write_b64 v10, v[16:17] offset:16896
v_accvgpr_read_b32 v18, a172
v_accvgpr_read_b32 v19, a173
v_accvgpr_read_b32 v20, a174
v_accvgpr_read_b32 v21, a175
v_pk_mul_f32 v[18:19], v[18:19], s[36:37]
v_pk_mul_f32 v[20:21], v[20:21], s[36:37]
v_cvt_pk_bf16_f32 v22, v18, v19
v_cvt_pk_bf16_f32 v23, v20, v21
ds_write_b64 v10, v[22:23] offset:20992
v_accvgpr_read_b32 v12, a176
v_accvgpr_read_b32 v13, a177
v_accvgpr_read_b32 v14, a178
v_accvgpr_read_b32 v15, a179
v_pk_mul_f32 v[12:13], v[12:13], s[36:37]
v_pk_mul_f32 v[14:15], v[14:15], s[36:37]
v_cvt_pk_bf16_f32 v16, v12, v13
v_cvt_pk_bf16_f32 v17, v14, v15
ds_write_b64 v10, v[16:17] offset:25088
v_accvgpr_read_b32 v18, a180
v_accvgpr_read_b32 v19, a181
v_accvgpr_read_b32 v20, a182
v_accvgpr_read_b32 v21, a183
v_pk_mul_f32 v[18:19], v[18:19], s[36:37]
v_pk_mul_f32 v[20:21], v[20:21], s[36:37]
v_cvt_pk_bf16_f32 v22, v18, v19
v_cvt_pk_bf16_f32 v23, v20, v21
ds_write_b64 v10, v[22:23] offset:29184
v_accvgpr_read_b32 v12, a184
v_accvgpr_read_b32 v13, a185
v_accvgpr_read_b32 v14, a186
v_accvgpr_read_b32 v15, a187
v_pk_mul_f32 v[12:13], v[12:13], s[36:37]
v_pk_mul_f32 v[14:15], v[14:15], s[36:37]
v_cvt_pk_bf16_f32 v16, v12, v13
v_cvt_pk_bf16_f32 v17, v14, v15
ds_write_b64 v10, v[16:17] offset:1024
v_accvgpr_read_b32 v18, a188
v_accvgpr_read_b32 v19, a189
v_accvgpr_read_b32 v20, a190
v_accvgpr_read_b32 v21, a191
v_pk_mul_f32 v[18:19], v[18:19], s[36:37]
v_pk_mul_f32 v[20:21], v[20:21], s[36:37]
v_cvt_pk_bf16_f32 v22, v18, v19
v_cvt_pk_bf16_f32 v23, v20, v21
ds_write_b64 v10, v[22:23] offset:5120
v_accvgpr_read_b32 v12, a192
v_accvgpr_read_b32 v13, a193
v_accvgpr_read_b32 v14, a194
v_accvgpr_read_b32 v15, a195
v_pk_mul_f32 v[12:13], v[12:13], s[36:37]
v_pk_mul_f32 v[14:15], v[14:15], s[36:37]
v_cvt_pk_bf16_f32 v16, v12, v13
v_cvt_pk_bf16_f32 v17, v14, v15
ds_write_b64 v10, v[16:17] offset:9216
v_accvgpr_read_b32 v18, a196
v_accvgpr_read_b32 v19, a197
v_accvgpr_read_b32 v20, a198
v_accvgpr_read_b32 v21, a199
v_pk_mul_f32 v[18:19], v[18:19], s[36:37]
v_pk_mul_f32 v[20:21], v[20:21], s[36:37]
v_cvt_pk_bf16_f32 v22, v18, v19
v_cvt_pk_bf16_f32 v23, v20, v21
ds_write_b64 v10, v[22:23] offset:13312
v_accvgpr_read_b32 v12, a200
v_accvgpr_read_b32 v13, a201
v_accvgpr_read_b32 v14, a202
v_accvgpr_read_b32 v15, a203
v_pk_mul_f32 v[12:13], v[12:13], s[36:37]
v_pk_mul_f32 v[14:15], v[14:15], s[36:37]
v_cvt_pk_bf16_f32 v16, v12, v13
v_cvt_pk_bf16_f32 v17, v14, v15
ds_write_b64 v10, v[16:17] offset:17408
v_accvgpr_read_b32 v18, a204
v_accvgpr_read_b32 v19, a205
v_accvgpr_read_b32 v20, a206
v_accvgpr_read_b32 v21, a207
v_pk_mul_f32 v[18:19], v[18:19], s[36:37]
v_pk_mul_f32 v[20:21], v[20:21], s[36:37]
v_cvt_pk_bf16_f32 v22, v18, v19
v_cvt_pk_bf16_f32 v23, v20, v21
ds_write_b64 v10, v[22:23] offset:21504
v_accvgpr_read_b32 v12, a208
v_accvgpr_read_b32 v13, a209
v_accvgpr_read_b32 v14, a210
v_accvgpr_read_b32 v15, a211
v_pk_mul_f32 v[12:13], v[12:13], s[36:37]
v_pk_mul_f32 v[14:15], v[14:15], s[36:37]
v_cvt_pk_bf16_f32 v16, v12, v13
v_cvt_pk_bf16_f32 v17, v14, v15
ds_write_b64 v10, v[16:17] offset:25600
v_accvgpr_read_b32 v18, a212
v_accvgpr_read_b32 v19, a213
v_accvgpr_read_b32 v20, a214
v_accvgpr_read_b32 v21, a215
v_pk_mul_f32 v[18:19], v[18:19], s[36:37]
v_pk_mul_f32 v[20:21], v[20:21], s[36:37]
v_cvt_pk_bf16_f32 v22, v18, v19
v_cvt_pk_bf16_f32 v23, v20, v21
ds_write_b64 v10, v[22:23] offset:29696
v_lshlrev_b32 v0, 4, v9
s_lshl_b32 s38, s6, 1
s_add_u32 s38, s38, s68
s_mul_i32 s38, s38, 0xe0000
s_add_u32 s38, s38, 0x500000
s_mul_i32 s39, s7, 7
s_and_b32 s40, s7, 1
s_mul_i32 s41, s40, 3
s_add_u32 s41, s41, s39
s_lshr_b32 s42, s41, 2
s_lshl_b32 s42, s42, 14
s_add_u32 s43, s38, s42
s_bfe_u32 s42, s41, 0x10001
s_lshl_b32 s42, s42, 10
s_add_u32 s43, s43, s42
s_add_u32 s41, s41, 2
s_lshr_b32 s42, s41, 2
s_lshl_b32 s42, s42, 14
s_add_u32 s44, s38, s42
s_bfe_u32 s42, s41, 0x10001
s_lshl_b32 s42, s42, 10
s_add_u32 s44, s44, s42
s_mov_b32 s36, 0xbfb8aa3b
s_mov_b32 s37, 0xbfb8aa3b
s_mov_b32 s46, 1.0
s_mov_b32 s47, 1.0
v_pk_mul_f32 v[180:181], v[180:181], s[36:37]
v_pk_mul_f32 v[182:183], v[182:183], s[36:37]
v_exp_f32 v180, v180
v_exp_f32 v181, v181
v_exp_f32 v182, v182
v_exp_f32 v183, v183
v_pk_add_f32 v[180:181], v[180:181], s[46:47]
v_pk_add_f32 v[182:183], v[182:183], s[46:47]
v_rcp_f32 v180, v180
v_rcp_f32 v181, v181
v_rcp_f32 v182, v182
v_rcp_f32 v183, v183
v_pk_mul_f32 v[180:181], v[180:181], v[212:213]
v_pk_mul_f32 v[182:183], v[182:183], v[214:215]
v_cvt_pk_bf16_f32 v252, v180, v181
v_cvt_pk_bf16_f32 v253, v182, v183
v_accvgpr_read_b32 v20, a116
v_accvgpr_read_b32 v21, a117
v_accvgpr_read_b32 v22, a118
v_accvgpr_read_b32 v23, a119
v_pk_mul_f32 v[244:245], v[244:245], s[36:37]
v_pk_mul_f32 v[246:247], v[246:247], s[36:37]
v_exp_f32 v244, v244
v_exp_f32 v245, v245
v_exp_f32 v246, v246
v_exp_f32 v247, v247
v_pk_add_f32 v[244:245], v[244:245], s[46:47]
v_pk_add_f32 v[246:247], v[246:247], s[46:47]
v_rcp_f32 v244, v244
v_rcp_f32 v245, v245
v_rcp_f32 v246, v246
v_rcp_f32 v247, v247
v_pk_mul_f32 v[244:245], v[244:245], v[20:21]
v_pk_mul_f32 v[246:247], v[246:247], v[22:23]
v_cvt_pk_bf16_f32 v254, v244, v245
v_cvt_pk_bf16_f32 v255, v246, v247
s_add_u32 s42, s44, 0x3800
buffer_store_dwordx4 v[252:255], v0, s[76:79], s42 offen sc1
v_pk_mul_f32 v[176:177], v[176:177], s[36:37]
v_pk_mul_f32 v[178:179], v[178:179], s[36:37]
v_exp_f32 v176, v176
v_exp_f32 v177, v177
v_exp_f32 v178, v178
v_exp_f32 v179, v179
v_pk_add_f32 v[176:177], v[176:177], s[46:47]
v_pk_add_f32 v[178:179], v[178:179], s[46:47]
v_rcp_f32 v176, v176
v_rcp_f32 v177, v177
v_rcp_f32 v178, v178
v_rcp_f32 v179, v179
v_pk_mul_f32 v[176:177], v[176:177], v[208:209]
v_pk_mul_f32 v[178:179], v[178:179], v[210:211]
v_cvt_pk_bf16_f32 v248, v176, v177
v_cvt_pk_bf16_f32 v249, v178, v179
v_accvgpr_read_b32 v20, a112
v_accvgpr_read_b32 v21, a113
v_accvgpr_read_b32 v22, a114
v_accvgpr_read_b32 v23, a115
v_pk_mul_f32 v[240:241], v[240:241], s[36:37]
v_pk_mul_f32 v[242:243], v[242:243], s[36:37]
v_exp_f32 v240, v240
v_exp_f32 v241, v241
v_exp_f32 v242, v242
v_exp_f32 v243, v243
v_pk_add_f32 v[240:241], v[240:241], s[46:47]
v_pk_add_f32 v[242:243], v[242:243], s[46:47]
v_rcp_f32 v240, v240
v_rcp_f32 v241, v241
v_rcp_f32 v242, v242
)ASM"
R"ASM(v_rcp_f32 v243, v243
v_pk_mul_f32 v[240:241], v[240:241], v[20:21]
v_pk_mul_f32 v[242:243], v[242:243], v[22:23]
v_cvt_pk_bf16_f32 v250, v240, v241
v_cvt_pk_bf16_f32 v251, v242, v243
s_add_u32 s42, s44, 0x3000
buffer_store_dwordx4 v[248:251], v0, s[76:79], s42 offen sc1
v_pk_mul_f32 v[172:173], v[172:173], s[36:37]
v_pk_mul_f32 v[174:175], v[174:175], s[36:37]
v_exp_f32 v172, v172
v_exp_f32 v173, v173
v_exp_f32 v174, v174
v_exp_f32 v175, v175
v_pk_add_f32 v[172:173], v[172:173], s[46:47]
v_pk_add_f32 v[174:175], v[174:175], s[46:47]
v_rcp_f32 v172, v172
v_rcp_f32 v173, v173
v_rcp_f32 v174, v174
v_rcp_f32 v175, v175
v_pk_mul_f32 v[172:173], v[172:173], v[204:205]
v_pk_mul_f32 v[174:175], v[174:175], v[206:207]
v_cvt_pk_bf16_f32 v244, v172, v173
v_cvt_pk_bf16_f32 v245, v174, v175
v_accvgpr_read_b32 v20, a108
v_accvgpr_read_b32 v21, a109
v_accvgpr_read_b32 v22, a110
v_accvgpr_read_b32 v23, a111
v_pk_mul_f32 v[236:237], v[236:237], s[36:37]
v_pk_mul_f32 v[238:239], v[238:239], s[36:37]
v_exp_f32 v236, v236
v_exp_f32 v237, v237
v_exp_f32 v238, v238
v_exp_f32 v239, v239
v_pk_add_f32 v[236:237], v[236:237], s[46:47]
v_pk_add_f32 v[238:239], v[238:239], s[46:47]
v_rcp_f32 v236, v236
v_rcp_f32 v237, v237
v_rcp_f32 v238, v238
v_rcp_f32 v239, v239
v_pk_mul_f32 v[236:237], v[236:237], v[20:21]
v_pk_mul_f32 v[238:239], v[238:239], v[22:23]
v_cvt_pk_bf16_f32 v246, v236, v237
v_cvt_pk_bf16_f32 v247, v238, v239
s_add_u32 s42, s44, 0x2800
buffer_store_dwordx4 v[244:247], v0, s[76:79], s42 offen sc1
v_pk_mul_f32 v[168:169], v[168:169], s[36:37]
v_pk_mul_f32 v[170:171], v[170:171], s[36:37]
v_exp_f32 v168, v168
v_exp_f32 v169, v169
v_exp_f32 v170, v170
v_exp_f32 v171, v171
v_pk_add_f32 v[168:169], v[168:169], s[46:47]
v_pk_add_f32 v[170:171], v[170:171], s[46:47]
v_rcp_f32 v168, v168
v_rcp_f32 v169, v169
v_rcp_f32 v170, v170
v_rcp_f32 v171, v171
v_pk_mul_f32 v[168:169], v[168:169], v[200:201]
v_pk_mul_f32 v[170:171], v[170:171], v[202:203]
v_cvt_pk_bf16_f32 v240, v168, v169
v_cvt_pk_bf16_f32 v241, v170, v171
v_accvgpr_read_b32 v20, a104
v_accvgpr_read_b32 v21, a105
v_accvgpr_read_b32 v22, a106
v_accvgpr_read_b32 v23, a107
v_pk_mul_f32 v[232:233], v[232:233], s[36:37]
v_pk_mul_f32 v[234:235], v[234:235], s[36:37]
v_exp_f32 v232, v232
v_exp_f32 v233, v233
v_exp_f32 v234, v234
v_exp_f32 v235, v235
v_pk_add_f32 v[232:233], v[232:233], s[46:47]
v_pk_add_f32 v[234:235], v[234:235], s[46:47]
v_rcp_f32 v232, v232
v_rcp_f32 v233, v233
v_rcp_f32 v234, v234
v_rcp_f32 v235, v235
v_pk_mul_f32 v[232:233], v[232:233], v[20:21]
v_pk_mul_f32 v[234:235], v[234:235], v[22:23]
v_cvt_pk_bf16_f32 v242, v232, v233
v_cvt_pk_bf16_f32 v243, v234, v235
s_add_u32 s42, s44, 0x2000
buffer_store_dwordx4 v[240:243], v0, s[76:79], s42 offen sc1
v_pk_mul_f32 v[164:165], v[164:165], s[36:37]
v_pk_mul_f32 v[166:167], v[166:167], s[36:37]
v_exp_f32 v164, v164
v_exp_f32 v165, v165
v_exp_f32 v166, v166
v_exp_f32 v167, v167
v_pk_add_f32 v[164:165], v[164:165], s[46:47]
v_pk_add_f32 v[166:167], v[166:167], s[46:47]
v_rcp_f32 v164, v164
v_rcp_f32 v165, v165
v_rcp_f32 v166, v166
v_rcp_f32 v167, v167
v_pk_mul_f32 v[164:165], v[164:165], v[196:197]
v_pk_mul_f32 v[166:167], v[166:167], v[198:199]
v_cvt_pk_bf16_f32 v236, v164, v165
v_cvt_pk_bf16_f32 v237, v166, v167
v_accvgpr_read_b32 v20, a100
v_accvgpr_read_b32 v21, a101
v_accvgpr_read_b32 v22, a102
v_accvgpr_read_b32 v23, a103
v_pk_mul_f32 v[228:229], v[228:229], s[36:37]
v_pk_mul_f32 v[230:231], v[230:231], s[36:37]
v_exp_f32 v228, v228
v_exp_f32 v229, v229
v_exp_f32 v230, v230
v_exp_f32 v231, v231
v_pk_add_f32 v[228:229], v[228:229], s[46:47]
v_pk_add_f32 v[230:231], v[230:231], s[46:47]
v_rcp_f32 v228, v228
v_rcp_f32 v229, v229
v_rcp_f32 v230, v230
v_rcp_f32 v231, v231
v_pk_mul_f32 v[228:229], v[228:229], v[20:21]
v_pk_mul_f32 v[230:231], v[230:231], v[22:23]
v_cvt_pk_bf16_f32 v238, v228, v229
v_cvt_pk_bf16_f32 v239, v230, v231
s_add_u32 s42, s44, 0x1800
buffer_store_dwordx4 v[236:239], v0, s[76:79], s42 offen sc1
v_pk_mul_f32 v[160:161], v[160:161], s[36:37]
v_pk_mul_f32 v[162:163], v[162:163], s[36:37]
v_exp_f32 v160, v160
v_exp_f32 v161, v161
v_exp_f32 v162, v162
v_exp_f32 v163, v163
v_pk_add_f32 v[160:161], v[160:161], s[46:47]
v_pk_add_f32 v[162:163], v[162:163], s[46:47]
v_rcp_f32 v160, v160
v_rcp_f32 v161, v161
v_rcp_f32 v162, v162
v_rcp_f32 v163, v163
v_pk_mul_f32 v[160:161], v[160:161], v[192:193]
v_pk_mul_f32 v[162:163], v[162:163], v[194:195]
v_cvt_pk_bf16_f32 v232, v160, v161
v_cvt_pk_bf16_f32 v233, v162, v163
v_accvgpr_read_b32 v20, a96
v_accvgpr_read_b32 v21, a97
v_accvgpr_read_b32 v22, a98
v_accvgpr_read_b32 v23, a99
v_pk_mul_f32 v[224:225], v[224:225], s[36:37]
v_pk_mul_f32 v[226:227], v[226:227], s[36:37]
v_exp_f32 v224, v224
v_exp_f32 v225, v225
v_exp_f32 v226, v226
v_exp_f32 v227, v227
v_pk_add_f32 v[224:225], v[224:225], s[46:47]
v_pk_add_f32 v[226:227], v[226:227], s[46:47]
v_rcp_f32 v224, v224
v_rcp_f32 v225, v225
v_rcp_f32 v226, v226
v_rcp_f32 v227, v227
v_pk_mul_f32 v[224:225], v[224:225], v[20:21]
v_pk_mul_f32 v[226:227], v[226:227], v[22:23]
v_cvt_pk_bf16_f32 v234, v224, v225
v_cvt_pk_bf16_f32 v235, v226, v227
s_add_u32 s42, s44, 0x1000
buffer_store_dwordx4 v[232:235], v0, s[76:79], s42 offen sc1
v_pk_mul_f32 v[156:157], v[156:157], s[36:37]
v_pk_mul_f32 v[158:159], v[158:159], s[36:37]
v_exp_f32 v156, v156
v_exp_f32 v157, v157
v_exp_f32 v158, v158
v_exp_f32 v159, v159
v_pk_add_f32 v[156:157], v[156:157], s[46:47]
v_pk_add_f32 v[158:159], v[158:159], s[46:47]
v_rcp_f32 v156, v156
v_rcp_f32 v157, v157
v_rcp_f32 v158, v158
v_rcp_f32 v159, v159
v_pk_mul_f32 v[156:157], v[156:157], v[188:189]
v_pk_mul_f32 v[158:159], v[158:159], v[190:191]
v_cvt_pk_bf16_f32 v228, v156, v157
v_cvt_pk_bf16_f32 v229, v158, v159
v_accvgpr_read_b32 v20, a92
v_accvgpr_read_b32 v21, a93
v_accvgpr_read_b32 v22, a94
v_accvgpr_read_b32 v23, a95
v_pk_mul_f32 v[220:221], v[220:221], s[36:37]
v_pk_mul_f32 v[222:223], v[222:223], s[36:37]
v_exp_f32 v220, v220
v_exp_f32 v221, v221
v_exp_f32 v222, v222
v_exp_f32 v223, v223
v_pk_add_f32 v[220:221], v[220:221], s[46:47]
v_pk_add_f32 v[222:223], v[222:223], s[46:47]
v_rcp_f32 v220, v220
v_rcp_f32 v221, v221
v_rcp_f32 v222, v222
v_rcp_f32 v223, v223
v_pk_mul_f32 v[220:221], v[220:221], v[20:21]
v_pk_mul_f32 v[222:223], v[222:223], v[22:23]
v_cvt_pk_bf16_f32 v230, v220, v221
v_cvt_pk_bf16_f32 v231, v222, v223
s_add_u32 s42, s44, 0x800
buffer_store_dwordx4 v[228:231], v0, s[76:79], s42 offen sc1
v_pk_mul_f32 v[152:153], v[152:153], s[36:37]
v_pk_mul_f32 v[154:155], v[154:155], s[36:37]
v_exp_f32 v152, v152
v_exp_f32 v153, v153
v_exp_f32 v154, v154
v_exp_f32 v155, v155
v_pk_add_f32 v[152:153], v[152:153], s[46:47]
v_pk_add_f32 v[154:155], v[154:155], s[46:47]
v_rcp_f32 v152, v152
v_rcp_f32 v153, v153
v_rcp_f32 v154, v154
v_rcp_f32 v155, v155
v_pk_mul_f32 v[152:153], v[152:153], v[184:185]
v_pk_mul_f32 v[154:155], v[154:155], v[186:187]
v_cvt_pk_bf16_f32 v224, v152, v153
v_cvt_pk_bf16_f32 v225, v154, v155
v_accvgpr_read_b32 v20, a88
v_accvgpr_read_b32 v21, a89
v_accvgpr_read_b32 v22, a90
v_accvgpr_read_b32 v23, a91
v_pk_mul_f32 v[216:217], v[216:217], s[36:37]
v_pk_mul_f32 v[218:219], v[218:219], s[36:37]
v_exp_f32 v216, v216
v_exp_f32 v217, v217
v_exp_f32 v218, v218
v_exp_f32 v219, v219
v_pk_add_f32 v[216:217], v[216:217], s[46:47]
v_pk_add_f32 v[218:219], v[218:219], s[46:47]
v_rcp_f32 v216, v216
v_rcp_f32 v217, v217
v_rcp_f32 v218, v218
v_rcp_f32 v219, v219
v_pk_mul_f32 v[216:217], v[216:217], v[20:21]
v_pk_mul_f32 v[218:219], v[218:219], v[22:23]
v_cvt_pk_bf16_f32 v226, v216, v217
v_cvt_pk_bf16_f32 v227, v218, v219
s_add_u32 s42, s44, 0
buffer_store_dwordx4 v[224:227], v0, s[76:79], s42 offen sc1
v_pk_mul_f32 v[24:25], v[24:25], s[36:37]
v_pk_mul_f32 v[26:27], v[26:27], s[36:37]
v_exp_f32 v24, v24
v_exp_f32 v25, v25
v_exp_f32 v26, v26
v_exp_f32 v27, v27
v_pk_add_f32 v[24:25], v[24:25], s[46:47]
v_pk_add_f32 v[26:27], v[26:27], s[46:47]
v_rcp_f32 v24, v24
v_rcp_f32 v25, v25
v_rcp_f32 v26, v26
v_rcp_f32 v27, v27
v_pk_mul_f32 v[24:25], v[24:25], v[56:57]
v_pk_mul_f32 v[26:27], v[26:27], v[58:59]
v_cvt_pk_bf16_f32 v192, v24, v25
v_cvt_pk_bf16_f32 v193, v26, v27
v_pk_mul_f32 v[88:89], v[88:89], s[36:37]
v_pk_mul_f32 v[90:91], v[90:91], s[36:37]
v_exp_f32 v88, v88
v_exp_f32 v89, v89
v_exp_f32 v90, v90
v_exp_f32 v91, v91
v_pk_add_f32 v[88:89], v[88:89], s[46:47]
v_pk_add_f32 v[90:91], v[90:91], s[46:47]
v_rcp_f32 v88, v88
v_rcp_f32 v89, v89
v_rcp_f32 v90, v90
v_rcp_f32 v91, v91
v_pk_mul_f32 v[88:89], v[88:89], v[120:121]
v_pk_mul_f32 v[90:91], v[90:91], v[122:123]
v_cvt_pk_bf16_f32 v194, v88, v89
v_cvt_pk_bf16_f32 v195, v90, v91
s_add_u32 s42, s43, 0
buffer_store_dwordx4 v[192:195], v0, s[76:79], s42 offen sc1
v_pk_mul_f32 v[28:29], v[28:29], s[36:37]
v_pk_mul_f32 v[30:31], v[30:31], s[36:37]
v_exp_f32 v28, v28
v_exp_f32 v29, v29
v_exp_f32 v30, v30
v_exp_f32 v31, v31
v_pk_add_f32 v[28:29], v[28:29], s[46:47]
v_pk_add_f32 v[30:31], v[30:31], s[46:47]
v_rcp_f32 v28, v28
v_rcp_f32 v29, v29
v_rcp_f32 v30, v30
v_rcp_f32 v31, v31
v_pk_mul_f32 v[28:29], v[28:29], v[60:61]
v_pk_mul_f32 v[30:31], v[30:31], v[62:63]
v_cvt_pk_bf16_f32 v196, v28, v29
v_cvt_pk_bf16_f32 v197, v30, v31
v_pk_mul_f32 v[92:93], v[92:93], s[36:37]
v_pk_mul_f32 v[94:95], v[94:95], s[36:37]
v_exp_f32 v92, v92
v_exp_f32 v93, v93
v_exp_f32 v94, v94
v_exp_f32 v95, v95
v_pk_add_f32 v[92:93], v[92:93], s[46:47]
v_pk_add_f32 v[94:95], v[94:95], s[46:47]
v_rcp_f32 v92, v92
v_rcp_f32 v93, v93
v_rcp_f32 v94, v94
v_rcp_f32 v95, v95
v_pk_mul_f32 v[92:93], v[92:93], v[124:125]
v_pk_mul_f32 v[94:95], v[94:95], v[126:127]
v_cvt_pk_bf16_f32 v198, v92, v93
v_cvt_pk_bf16_f32 v199, v94, v95
s_add_u32 s42, s43, 0x800
buffer_store_dwordx4 v[196:199], v0, s[76:79], s42 offen sc1
v_pk_mul_f32 v[32:33], v[32:33], s[36:37]
v_pk_mul_f32 v[34:35], v[34:35], s[36:37]
v_exp_f32 v32, v32
v_exp_f32 v33, v33
v_exp_f32 v34, v34
v_exp_f32 v35, v35
v_pk_add_f32 v[32:33], v[32:33], s[46:47]
v_pk_add_f32 v[34:35], v[34:35], s[46:47]
v_rcp_f32 v32, v32
v_rcp_f32 v33, v33
v_rcp_f32 v34, v34
v_rcp_f32 v35, v35
v_pk_mul_f32 v[32:33], v[32:33], v[64:65]
v_pk_mul_f32 v[34:35], v[34:35], v[66:67]
v_cvt_pk_bf16_f32 v200, v32, v33
v_cvt_pk_bf16_f32 v201, v34, v35
v_pk_mul_f32 v[96:97], v[96:97], s[36:37]
v_pk_mul_f32 v[98:99], v[98:99], s[36:37]
v_exp_f32 v96, v96
v_exp_f32 v97, v97
v_exp_f32 v98, v98
v_exp_f32 v99, v99
v_pk_add_f32 v[96:97], v[96:97], s[46:47]
v_pk_add_f32 v[98:99], v[98:99], s[46:47]
v_rcp_f32 v96, v96
v_rcp_f32 v97, v97
v_rcp_f32 v98, v98
v_rcp_f32 v99, v99
v_pk_mul_f32 v[96:97], v[96:97], v[128:129]
v_pk_mul_f32 v[98:99], v[98:99], v[130:131]
v_cvt_pk_bf16_f32 v202, v96, v97
v_cvt_pk_bf16_f32 v203, v98, v99
s_add_u32 s42, s43, 0x1000
buffer_store_dwordx4 v[200:203], v0, s[76:79], s42 offen sc1
v_pk_mul_f32 v[36:37], v[36:37], s[36:37]
v_pk_mul_f32 v[38:39], v[38:39], s[36:37]
v_exp_f32 v36, v36
v_exp_f32 v37, v37
v_exp_f32 v38, v38
v_exp_f32 v39, v39
v_pk_add_f32 v[36:37], v[36:37], s[46:47]
v_pk_add_f32 v[38:39], v[38:39], s[46:47]
v_rcp_f32 v36, v36
v_rcp_f32 v37, v37
v_rcp_f32 v38, v38
v_rcp_f32 v39, v39
v_pk_mul_f32 v[36:37], v[36:37], v[68:69]
v_pk_mul_f32 v[38:39], v[38:39], v[70:71]
v_cvt_pk_bf16_f32 v204, v36, v37
v_cvt_pk_bf16_f32 v205, v38, v39
v_pk_mul_f32 v[100:101], v[100:101], s[36:37]
v_pk_mul_f32 v[102:103], v[102:103], s[36:37]
v_exp_f32 v100, v100
v_exp_f32 v101, v101
v_exp_f32 v102, v102
v_exp_f32 v103, v103
v_pk_add_f32 v[100:101], v[100:101], s[46:47]
v_pk_add_f32 v[102:103], v[102:103], s[46:47]
v_rcp_f32 v100, v100
v_rcp_f32 v101, v101
v_rcp_f32 v102, v102
v_rcp_f32 v103, v103
v_pk_mul_f32 v[100:101], v[100:101], v[132:133]
v_pk_mul_f32 v[102:103], v[102:103], v[134:135]
v_cvt_pk_bf16_f32 v206, v100, v101
v_cvt_pk_bf16_f32 v207, v102, v103
s_add_u32 s42, s43, 0x1800
buffer_store_dwordx4 v[204:207], v0, s[76:79], s42 offen sc1
v_pk_mul_f32 v[40:41], v[40:41], s[36:37]
v_pk_mul_f32 v[42:43], v[42:43], s[36:37]
v_exp_f32 v40, v40
v_exp_f32 v41, v41
v_exp_f32 v42, v42
v_exp_f32 v43, v43
v_pk_add_f32 v[40:41], v[40:41], s[46:47]
v_pk_add_f32 v[42:43], v[42:43], s[46:47]
v_rcp_f32 v40, v40
v_rcp_f32 v41, v41
v_rcp_f32 v42, v42
v_rcp_f32 v43, v43
v_pk_mul_f32 v[40:41], v[40:41], v[72:73]
v_pk_mul_f32 v[42:43], v[42:43], v[74:75]
v_cvt_pk_bf16_f32 v208, v40, v41
v_cvt_pk_bf16_f32 v209, v42, v43
v_pk_mul_f32 v[104:105], v[104:105], s[36:37]
v_pk_mul_f32 v[106:107], v[106:107], s[36:37]
v_exp_f32 v104, v104
v_exp_f32 v105, v105
v_exp_f32 v106, v106
v_exp_f32 v107, v107
v_pk_add_f32 v[104:105], v[104:105], s[46:47]
v_pk_add_f32 v[106:107], v[106:107], s[46:47]
v_rcp_f32 v104, v104
v_rcp_f32 v105, v105
v_rcp_f32 v106, v106
v_rcp_f32 v107, v107
v_pk_mul_f32 v[104:105], v[104:105], v[136:137]
)ASM"
R"ASM(v_pk_mul_f32 v[106:107], v[106:107], v[138:139]
v_cvt_pk_bf16_f32 v210, v104, v105
v_cvt_pk_bf16_f32 v211, v106, v107
s_add_u32 s42, s43, 0x2000
buffer_store_dwordx4 v[208:211], v0, s[76:79], s42 offen sc1
v_pk_mul_f32 v[44:45], v[44:45], s[36:37]
v_pk_mul_f32 v[46:47], v[46:47], s[36:37]
v_exp_f32 v44, v44
v_exp_f32 v45, v45
v_exp_f32 v46, v46
v_exp_f32 v47, v47
v_pk_add_f32 v[44:45], v[44:45], s[46:47]
v_pk_add_f32 v[46:47], v[46:47], s[46:47]
v_rcp_f32 v44, v44
v_rcp_f32 v45, v45
v_rcp_f32 v46, v46
v_rcp_f32 v47, v47
v_pk_mul_f32 v[44:45], v[44:45], v[76:77]
v_pk_mul_f32 v[46:47], v[46:47], v[78:79]
v_cvt_pk_bf16_f32 v212, v44, v45
v_cvt_pk_bf16_f32 v213, v46, v47
v_pk_mul_f32 v[108:109], v[108:109], s[36:37]
v_pk_mul_f32 v[110:111], v[110:111], s[36:37]
v_exp_f32 v108, v108
v_exp_f32 v109, v109
v_exp_f32 v110, v110
v_exp_f32 v111, v111
v_pk_add_f32 v[108:109], v[108:109], s[46:47]
v_pk_add_f32 v[110:111], v[110:111], s[46:47]
v_rcp_f32 v108, v108
v_rcp_f32 v109, v109
v_rcp_f32 v110, v110
v_rcp_f32 v111, v111
v_pk_mul_f32 v[108:109], v[108:109], v[140:141]
v_pk_mul_f32 v[110:111], v[110:111], v[142:143]
v_cvt_pk_bf16_f32 v214, v108, v109
v_cvt_pk_bf16_f32 v215, v110, v111
s_add_u32 s42, s43, 0x2800
buffer_store_dwordx4 v[212:215], v0, s[76:79], s42 offen sc1
v_pk_mul_f32 v[48:49], v[48:49], s[36:37]
v_pk_mul_f32 v[50:51], v[50:51], s[36:37]
v_exp_f32 v48, v48
v_exp_f32 v49, v49
v_exp_f32 v50, v50
v_exp_f32 v51, v51
v_pk_add_f32 v[48:49], v[48:49], s[46:47]
v_pk_add_f32 v[50:51], v[50:51], s[46:47]
v_rcp_f32 v48, v48
v_rcp_f32 v49, v49
v_rcp_f32 v50, v50
v_rcp_f32 v51, v51
v_pk_mul_f32 v[48:49], v[48:49], v[80:81]
v_pk_mul_f32 v[50:51], v[50:51], v[82:83]
v_cvt_pk_bf16_f32 v216, v48, v49
v_cvt_pk_bf16_f32 v217, v50, v51
v_pk_mul_f32 v[112:113], v[112:113], s[36:37]
v_pk_mul_f32 v[114:115], v[114:115], s[36:37]
v_exp_f32 v112, v112
v_exp_f32 v113, v113
v_exp_f32 v114, v114
v_exp_f32 v115, v115
v_pk_add_f32 v[112:113], v[112:113], s[46:47]
v_pk_add_f32 v[114:115], v[114:115], s[46:47]
v_rcp_f32 v112, v112
v_rcp_f32 v113, v113
v_rcp_f32 v114, v114
v_rcp_f32 v115, v115
v_pk_mul_f32 v[112:113], v[112:113], v[144:145]
v_pk_mul_f32 v[114:115], v[114:115], v[146:147]
v_cvt_pk_bf16_f32 v218, v112, v113
v_cvt_pk_bf16_f32 v219, v114, v115
s_add_u32 s42, s43, 0x3000
buffer_store_dwordx4 v[216:219], v0, s[76:79], s42 offen sc1
v_pk_mul_f32 v[52:53], v[52:53], s[36:37]
v_pk_mul_f32 v[54:55], v[54:55], s[36:37]
v_exp_f32 v52, v52
v_exp_f32 v53, v53
v_exp_f32 v54, v54
v_exp_f32 v55, v55
v_pk_add_f32 v[52:53], v[52:53], s[46:47]
v_pk_add_f32 v[54:55], v[54:55], s[46:47]
v_rcp_f32 v52, v52
v_rcp_f32 v53, v53
v_rcp_f32 v54, v54
v_rcp_f32 v55, v55
v_pk_mul_f32 v[52:53], v[52:53], v[84:85]
v_pk_mul_f32 v[54:55], v[54:55], v[86:87]
v_cvt_pk_bf16_f32 v220, v52, v53
v_cvt_pk_bf16_f32 v221, v54, v55
v_pk_mul_f32 v[116:117], v[116:117], s[36:37]
v_pk_mul_f32 v[118:119], v[118:119], s[36:37]
v_exp_f32 v116, v116
v_exp_f32 v117, v117
v_exp_f32 v118, v118
v_exp_f32 v119, v119
v_pk_add_f32 v[116:117], v[116:117], s[46:47]
v_pk_add_f32 v[118:119], v[118:119], s[46:47]
v_rcp_f32 v116, v116
v_rcp_f32 v117, v117
v_rcp_f32 v118, v118
v_rcp_f32 v119, v119
v_pk_mul_f32 v[116:117], v[116:117], v[148:149]
v_pk_mul_f32 v[118:119], v[118:119], v[150:151]
v_cvt_pk_bf16_f32 v222, v116, v117
v_cvt_pk_bf16_f32 v223, v118, v119
s_add_u32 s42, s43, 0x3800
buffer_store_dwordx4 v[220:223], v0, s[76:79], s42 offen sc1
s_mov_b32 s97, s43
s_mov_b32 s96, s44
s_waitcnt vmcnt(16) lgkmcnt(0)
s_branch JOIN_%=
TYPEB_%=:
ds_read_b128 a[0:3], v12
ds_read_b128 a[4:7], v12 offset:1024
ds_read_b128 a[8:11], v12 offset:2048
ds_read_b128 a[12:15], v12 offset:3072
ds_read_b128 a[16:19], v12 offset:4096
ds_read_b128 a[20:23], v12 offset:5120
ds_read_b128 a[24:27], v12 offset:6144
ds_read_b128 a[28:31], v12 offset:7168
ds_read_b128 a[32:35], v12 offset:8192
ds_read_b128 a[36:39], v12 offset:9216
ds_read_b128 a[40:43], v12 offset:10240
ds_read_b128 a[44:47], v12 offset:11264
ds_read_b128 a[48:51], v12 offset:12288
ds_read_b128 a[52:55], v12 offset:13312
ds_read_b128 a[56:59], v12 offset:14336
ds_read_b128 a[60:63], v12 offset:15360
ds_read_b64_tr_b16 a[64:65], v16
ds_read_b64_tr_b16 a[66:67], v16 offset:3584
ds_read_b64_tr_b16 a[68:69], v16 offset:7168
ds_read_b64_tr_b16 a[70:71], v16 offset:10752
s_waitcnt lgkmcnt(0)
v_mfma_f32_16x16x32_bf16 v[24:27], a[64:67], a[0:3], 0
v_mfma_f32_16x16x32_bf16 v[24:27], a[68:71], a[4:7], v[24:27]
ds_read_b64_tr_b16 a[72:73], v16 offset:14336
s_add_u32 m0, s29, 0x13200
v_mfma_f32_16x16x32_bf16 v[28:31], a[64:67], a[8:11], 0
buffer_load_dwordx4 v1, s[12:15], s25 offen lds
v_mfma_f32_16x16x32_bf16 v[28:31], a[68:71], a[12:15], v[28:31]
v_mfma_f32_16x16x32_bf16 v[32:35], a[64:67], a[16:19], 0
ds_read_b64_tr_b16 a[74:75], v16 offset:17920
v_mfma_f32_16x16x32_bf16 v[32:35], a[68:71], a[20:23], v[32:35]
v_mfma_f32_16x16x32_bf16 v[36:39], a[64:67], a[24:27], 0
v_mfma_f32_16x16x32_bf16 v[36:39], a[68:71], a[28:31], v[36:39]
ds_read_b64_tr_b16 a[76:77], v16 offset:21504
v_mfma_f32_16x16x32_bf16 v[40:43], a[64:67], a[32:35], 0
buffer_load_dwordx4 v2, s[12:15], s25 offen offset:1056 lds
v_mfma_f32_16x16x32_bf16 v[40:43], a[68:71], a[36:39], v[40:43]
v_mfma_f32_16x16x32_bf16 v[44:47], a[64:67], a[40:43], 0
ds_read_b64_tr_b16 a[78:79], v16 offset:25088
v_mfma_f32_16x16x32_bf16 v[44:47], a[68:71], a[44:47], v[44:47]
v_mfma_f32_16x16x32_bf16 v[48:51], a[64:67], a[48:51], 0
v_mfma_f32_16x16x32_bf16 v[48:51], a[68:71], a[52:55], v[48:51]
v_mfma_f32_16x16x32_bf16 v[52:55], a[64:67], a[56:59], 0
buffer_load_dwordx4 v3, s[12:15], s25 offen offset:2112 lds
v_mfma_f32_16x16x32_bf16 v[52:55], a[68:71], a[60:63], v[52:55]
s_waitcnt lgkmcnt(1)
v_mfma_f32_16x16x32_bf16 v[56:59], a[72:75], a[0:3], 0
s_waitcnt lgkmcnt(0)
v_mfma_f32_16x16x32_bf16 v[56:59], a[76:79], a[4:7], v[56:59]
ds_read_b64_tr_b16 a[64:65], v16 offset:32
v_mfma_f32_16x16x32_bf16 v[60:63], a[72:75], a[8:11], 0
v_mfma_f32_16x16x32_bf16 v[60:63], a[76:79], a[12:15], v[60:63]
v_mfma_f32_16x16x32_bf16 v[64:67], a[72:75], a[16:19], 0
ds_read_b64_tr_b16 a[66:67], v16 offset:3616
buffer_load_dwordx4 v4, s[12:15], s25 offen offset:3168 lds
v_mfma_f32_16x16x32_bf16 v[64:67], a[76:79], a[20:23], v[64:67]
v_mfma_f32_16x16x32_bf16 v[68:71], a[72:75], a[24:27], 0
v_mfma_f32_16x16x32_bf16 v[68:71], a[76:79], a[28:31], v[68:71]
ds_read_b64_tr_b16 a[68:69], v16 offset:7200
v_mfma_f32_16x16x32_bf16 v[72:75], a[72:75], a[32:35], 0
v_mfma_f32_16x16x32_bf16 v[72:75], a[76:79], a[36:39], v[72:75]
s_add_u32 m0, s30, 0x13200
v_mfma_f32_16x16x32_bf16 v[76:79], a[72:75], a[40:43], 0
ds_read_b64_tr_b16 a[70:71], v16 offset:10784
buffer_load_dwordx4 v5, s[16:19], s26 offen lds
v_mfma_f32_16x16x32_bf16 v[76:79], a[76:79], a[44:47], v[76:79]
v_mfma_f32_16x16x32_bf16 v[80:83], a[72:75], a[48:51], 0
v_mfma_f32_16x16x32_bf16 v[80:83], a[76:79], a[52:55], v[80:83]
v_mfma_f32_16x16x32_bf16 v[84:87], a[72:75], a[56:59], 0
v_mfma_f32_16x16x32_bf16 v[84:87], a[76:79], a[60:63], v[84:87]
s_waitcnt lgkmcnt(1)
v_mfma_f32_16x16x32_bf16 v[88:91], a[64:67], a[0:3], 0
buffer_load_dwordx4 v6, s[16:19], s26 offen offset:1024 lds
s_waitcnt lgkmcnt(0)
v_mfma_f32_16x16x32_bf16 v[88:91], a[68:71], a[4:7], v[88:91]
ds_read_b64_tr_b16 a[72:73], v16 offset:14368
v_mfma_f32_16x16x32_bf16 v[92:95], a[64:67], a[8:11], 0
v_mfma_f32_16x16x32_bf16 v[92:95], a[68:71], a[12:15], v[92:95]
v_mfma_f32_16x16x32_bf16 v[96:99], a[64:67], a[16:19], 0
ds_read_b64_tr_b16 a[74:75], v16 offset:17952
v_mfma_f32_16x16x32_bf16 v[96:99], a[68:71], a[20:23], v[96:99]
v_mfma_f32_16x16x32_bf16 v[100:103], a[64:67], a[24:27], 0
buffer_load_dwordx4 v7, s[16:19], s26 offen offset:2048 lds
v_mfma_f32_16x16x32_bf16 v[100:103], a[68:71], a[28:31], v[100:103]
ds_read_b64_tr_b16 a[76:77], v16 offset:21536
v_mfma_f32_16x16x32_bf16 v[104:107], a[64:67], a[32:35], 0
v_mfma_f32_16x16x32_bf16 v[104:107], a[68:71], a[36:39], v[104:107]
v_mfma_f32_16x16x32_bf16 v[108:111], a[64:67], a[40:43], 0
ds_read_b64_tr_b16 a[78:79], v16 offset:25120
v_mfma_f32_16x16x32_bf16 v[108:111], a[68:71], a[44:47], v[108:111]
v_mfma_f32_16x16x32_bf16 v[112:115], a[64:67], a[48:51], 0
buffer_load_dwordx4 v8, s[16:19], s26 offen offset:3072 lds
v_mfma_f32_16x16x32_bf16 v[112:115], a[68:71], a[52:55], v[112:115]
v_mfma_f32_16x16x32_bf16 v[116:119], a[64:67], a[56:59], 0
v_mfma_f32_16x16x32_bf16 v[116:119], a[68:71], a[60:63], v[116:119]
s_waitcnt lgkmcnt(1)
v_mfma_f32_16x16x32_bf16 v[120:123], a[72:75], a[0:3], 0
s_waitcnt lgkmcnt(0)
v_mfma_f32_16x16x32_bf16 v[120:123], a[76:79], a[4:7], v[120:123]
ds_read_b64_tr_b16 a[64:65], v18
s_add_u32 m0, s30, 0x14200
v_mfma_f32_16x16x32_bf16 v[124:127], a[72:75], a[8:11], 0
buffer_load_dwordx4 v9, s[16:19], s26 offen lds
v_mfma_f32_16x16x32_bf16 v[124:127], a[76:79], a[12:15], v[124:127]
v_mfma_f32_16x16x32_bf16 v[128:131], a[72:75], a[16:19], 0
ds_read_b64_tr_b16 a[66:67], v18 offset:3584
v_mfma_f32_16x16x32_bf16 v[128:131], a[76:79], a[20:23], v[128:131]
v_mfma_f32_16x16x32_bf16 v[132:135], a[72:75], a[24:27], 0
v_mfma_f32_16x16x32_bf16 v[132:135], a[76:79], a[28:31], v[132:135]
ds_read_b64_tr_b16 a[68:69], v18 offset:7168
v_mfma_f32_16x16x32_bf16 v[136:139], a[72:75], a[32:35], 0
buffer_load_dwordx4 v10, s[16:19], s26 offen offset:1024 lds
v_mfma_f32_16x16x32_bf16 v[136:139], a[76:79], a[36:39], v[136:139]
v_mfma_f32_16x16x32_bf16 v[140:143], a[72:75], a[40:43], 0
ds_read_b64_tr_b16 a[70:71], v18 offset:10752
v_mfma_f32_16x16x32_bf16 v[140:143], a[76:79], a[44:47], v[140:143]
v_mfma_f32_16x16x32_bf16 v[144:147], a[72:75], a[48:51], 0
v_mfma_f32_16x16x32_bf16 v[144:147], a[76:79], a[52:55], v[144:147]
v_mfma_f32_16x16x32_bf16 v[148:151], a[72:75], a[56:59], 0
buffer_load_dwordx4 v11, s[16:19], s26 offen offset:2048 lds
v_mfma_f32_16x16x32_bf16 v[148:151], a[76:79], a[60:63], v[148:151]
s_waitcnt lgkmcnt(1)
v_mfma_f32_16x16x32_bf16 v[152:155], a[64:67], a[0:3], 0
s_waitcnt lgkmcnt(0)
v_mfma_f32_16x16x32_bf16 v[152:155], a[68:71], a[4:7], v[152:155]
ds_read_b64_tr_b16 a[72:73], v18 offset:14336
v_mfma_f32_16x16x32_bf16 v[156:159], a[64:67], a[8:11], 0
v_mfma_f32_16x16x32_bf16 v[156:159], a[68:71], a[12:15], v[156:159]
s_add_u32 m0, s28, 0x13200
v_mfma_f32_16x16x32_bf16 v[160:163], a[64:67], a[16:19], 0
ds_read_b64_tr_b16 a[74:75], v18 offset:17920
buffer_load_dwordx4 v0, s[8:11], s24 offen sc1 lds
v_mfma_f32_16x16x32_bf16 v[160:163], a[68:71], a[20:23], v[160:163]
v_mfma_f32_16x16x32_bf16 v[164:167], a[64:67], a[24:27], 0
v_mfma_f32_16x16x32_bf16 v[164:167], a[68:71], a[28:31], v[164:167]
ds_read_b64_tr_b16 a[76:77], v18 offset:21504
v_mfma_f32_16x16x32_bf16 v[168:171], a[64:67], a[32:35], 0
v_mfma_f32_16x16x32_bf16 v[168:171], a[68:71], a[36:39], v[168:171]
v_mfma_f32_16x16x32_bf16 v[172:175], a[64:67], a[40:43], 0
ds_read_b64_tr_b16 a[78:79], v18 offset:25088
buffer_load_dwordx4 v0, s[8:11], s24 offen offset:1024 sc1 lds
v_mfma_f32_16x16x32_bf16 v[172:175], a[68:71], a[44:47], v[172:175]
v_mfma_f32_16x16x32_bf16 v[176:179], a[64:67], a[48:51], 0
v_mfma_f32_16x16x32_bf16 v[176:179], a[68:71], a[52:55], v[176:179]
v_mfma_f32_16x16x32_bf16 v[180:183], a[64:67], a[56:59], 0
v_mfma_f32_16x16x32_bf16 v[180:183], a[68:71], a[60:63], v[180:183]
s_waitcnt lgkmcnt(1)
v_mfma_f32_16x16x32_bf16 v[184:187], a[72:75], a[0:3], 0
buffer_load_dwordx4 v0, s[8:11], s24 offen offset:2048 sc1 lds
s_waitcnt lgkmcnt(0)
v_mfma_f32_16x16x32_bf16 v[184:187], a[76:79], a[4:7], v[184:187]
ds_read_b64_tr_b16 a[64:65], v14 offset:96
v_mfma_f32_16x16x32_bf16 v[188:191], a[72:75], a[8:11], 0
v_mfma_f32_16x16x32_bf16 v[188:191], a[76:79], a[12:15], v[188:191]
v_mfma_f32_16x16x32_bf16 v[192:195], a[72:75], a[16:19], 0
ds_read_b64_tr_b16 a[66:67], v14 offset:608
v_mfma_f32_16x16x32_bf16 v[192:195], a[76:79], a[20:23], v[192:195]
v_mfma_f32_16x16x32_bf16 v[196:199], a[72:75], a[24:27], 0
buffer_load_dwordx4 v0, s[8:11], s24 offen offset:3072 sc1 lds
v_mfma_f32_16x16x32_bf16 v[196:199], a[76:79], a[28:31], v[196:199]
ds_read_b64_tr_b16 a[68:69], v14 offset:8544
v_mfma_f32_16x16x32_bf16 v[200:203], a[72:75], a[32:35], 0
v_mfma_f32_16x16x32_bf16 v[200:203], a[76:79], a[36:39], v[200:203]
v_mfma_f32_16x16x32_bf16 v[204:207], a[72:75], a[40:43], 0
ds_read_b64_tr_b16 a[70:71], v14 offset:9056
v_mfma_f32_16x16x32_bf16 v[204:207], a[76:79], a[44:47], v[204:207]
s_add_u32 m0, s28, 0x14200
s_add_u32 s34, s24, 0x1000
v_mfma_f32_16x16x32_bf16 v[208:211], a[72:75], a[48:51], 0
buffer_load_dwordx4 v0, s[8:11], s34 offen sc1 lds
v_mfma_f32_16x16x32_bf16 v[208:211], a[76:79], a[52:55], v[208:211]
v_mfma_f32_16x16x32_bf16 v[212:215], a[72:75], a[56:59], 0
v_mfma_f32_16x16x32_bf16 v[212:215], a[76:79], a[60:63], v[212:215]
s_waitcnt lgkmcnt(1)
v_mfma_f32_16x16x32_bf16 a[88:91], a[64:67], a[0:3], 0
s_waitcnt lgkmcnt(0)
v_mfma_f32_16x16x32_bf16 a[88:91], a[68:71], a[4:7], a[88:91]
ds_read_b64_tr_b16 a[72:73], v14 offset:128
v_mfma_f32_16x16x32_bf16 a[92:95], a[64:67], a[8:11], 0
buffer_load_dwordx4 v0, s[8:11], s34 offen offset:1024 sc1 lds
v_mfma_f32_16x16x32_bf16 a[92:95], a[68:71], a[12:15], a[92:95]
v_mfma_f32_16x16x32_bf16 a[96:99], a[64:67], a[16:19], 0
ds_read_b64_tr_b16 a[74:75], v14 offset:640
v_mfma_f32_16x16x32_bf16 a[96:99], a[68:71], a[20:23], a[96:99]
v_mfma_f32_16x16x32_bf16 a[100:103], a[64:67], a[24:27], 0
v_mfma_f32_16x16x32_bf16 a[100:103], a[68:71], a[28:31], a[100:103]
ds_read_b64_tr_b16 a[76:77], v14 offset:8576
v_mfma_f32_16x16x32_bf16 a[104:107], a[64:67], a[32:35], 0
buffer_load_dwordx4 v0, s[8:11], s34 offen offset:2048 sc1 lds
v_mfma_f32_16x16x32_bf16 a[104:107], a[68:71], a[36:39], a[104:107]
v_mfma_f32_16x16x32_bf16 a[108:111], a[64:67], a[40:43], 0
ds_read_b64_tr_b16 a[78:79], v14 offset:9088
v_mfma_f32_16x16x32_bf16 a[108:111], a[68:71], a[44:47], a[108:111]
v_mfma_f32_16x16x32_bf16 a[112:115], a[64:67], a[48:51], 0
v_mfma_f32_16x16x32_bf16 a[112:115], a[68:71], a[52:55], a[112:115]
v_mfma_f32_16x16x32_bf16 a[116:119], a[64:67], a[56:59], 0
buffer_load_dwordx4 v0, s[8:11], s34 offen offset:3072 sc1 lds
s_add_u32 s24, s24, 0x8000
s_add_u32 s25, s25, 0x80000
s_add_u32 s26, s26, 0x70000
v_mfma_f32_16x16x32_bf16 a[116:119], a[68:71], a[60:63], a[116:119]
s_waitcnt lgkmcnt(1)
v_mfma_f32_16x16x32_bf16 a[120:123], a[72:75], a[0:3], 0
s_waitcnt lgkmcnt(0)
v_mfma_f32_16x16x32_bf16 a[120:123], a[76:79], a[4:7], a[120:123]
ds_read_b64_tr_b16 a[64:65], v14 offset:160
v_mfma_f32_16x16x32_bf16 a[124:127], a[72:75], a[8:11], 0
v_mfma_f32_16x16x32_bf16 a[124:127], a[76:79], a[12:15], a[124:127]
v_mfma_f32_16x16x32_bf16 a[128:131], a[72:75], a[16:19], 0
ds_read_b64_tr_b16 a[66:67], v14 offset:672
v_mfma_f32_16x16x32_bf16 a[128:131], a[76:79], a[20:23], a[128:131]
v_mfma_f32_16x16x32_bf16 a[132:135], a[72:75], a[24:27], 0
global_load_dword v255, v[252:253], off
v_mfma_f32_16x16x32_bf16 a[132:135], a[76:79], a[28:31], a[132:135]
ds_read_b64_tr_b16 a[68:69], v14 offset:8608
v_mfma_f32_16x16x32_bf16 a[136:139], a[72:75], a[32:35], 0
v_mfma_f32_16x16x32_bf16 a[136:139], a[76:79], a[36:39], a[136:139]
v_add_co_u32 v252, vcc, v254, v252
v_mfma_f32_16x16x32_bf16 a[140:143], a[72:75], a[40:43], 0
ds_read_b64_tr_b16 a[70:71], v14 offset:9120
v_mfma_f32_16x16x32_bf16 a[140:143], a[76:79], a[44:47], a[140:143]
v_mfma_f32_16x16x32_bf16 a[144:147], a[72:75], a[48:51], 0
v_addc_co_u32 v253, vcc, 0, v253, vcc
v_mfma_f32_16x16x32_bf16 a[144:147], a[76:79], a[52:55], a[144:147]
v_mfma_f32_16x16x32_bf16 a[148:151], a[72:75], a[56:59], 0
v_mfma_f32_16x16x32_bf16 a[148:151], a[76:79], a[60:63], a[148:151]
s_waitcnt lgkmcnt(1)
v_mfma_f32_16x16x32_bf16 a[152:155], a[64:67], a[0:3], 0
s_waitcnt lgkmcnt(0)
v_mfma_f32_16x16x32_bf16 a[152:155], a[68:71], a[4:7], a[152:155]
ds_read_b64_tr_b16 a[72:73], v14 offset:192
v_mfma_f32_16x16x32_bf16 a[156:159], a[64:67], a[8:11], 0
v_mfma_f32_16x16x32_bf16 a[156:159], a[68:71], a[12:15], a[156:159]
v_mfma_f32_16x16x32_bf16 a[160:163], a[64:67], a[16:19], 0
ds_read_b64_tr_b16 a[74:75], v14 offset:704
v_mfma_f32_16x16x32_bf16 a[160:163], a[68:71], a[20:23], a[160:163]
v_mfma_f32_16x16x32_bf16 a[164:167], a[64:67], a[24:27], 0
v_mfma_f32_16x16x32_bf16 a[164:167], a[68:71], a[28:31], a[164:167]
ds_read_b64_tr_b16 a[76:77], v14 offset:8640
v_mfma_f32_16x16x32_bf16 a[168:171], a[64:67], a[32:35], 0
v_mfma_f32_16x16x32_bf16 a[168:171], a[68:71], a[36:39], a[168:171]
v_mfma_f32_16x16x32_bf16 a[172:175], a[64:67], a[40:43], 0
ds_read_b64_tr_b16 a[78:79], v14 offset:9152
v_mfma_f32_16x16x32_bf16 a[172:175], a[68:71], a[44:47], a[172:175]
v_mfma_f32_16x16x32_bf16 a[176:179], a[64:67], a[48:51], 0
v_mfma_f32_16x16x32_bf16 a[176:179], a[68:71], a[52:55], a[176:179]
v_mfma_f32_16x16x32_bf16 a[180:183], a[64:67], a[56:59], 0
v_mfma_f32_16x16x32_bf16 a[180:183], a[68:71], a[60:63], a[180:183]
s_waitcnt lgkmcnt(1)
v_mfma_f32_16x16x32_bf16 a[184:187], a[72:75], a[0:3], 0
s_waitcnt lgkmcnt(0)
v_mfma_f32_16x16x32_bf16 a[184:187], a[76:79], a[4:7], a[184:187]
ds_read_b64_tr_b16 a[64:65], v14 offset:224
v_mfma_f32_16x16x32_bf16 a[188:191], a[72:75], a[8:11], 0
ds_read_b64_tr_b16 a[66:67], v14 offset:736
v_mfma_f32_16x16x32_bf16 a[188:191], a[76:79], a[12:15], a[188:191]
ds_read_b64_tr_b16 a[68:69], v14 offset:8672
v_mfma_f32_16x16x32_bf16 a[192:195], a[72:75], a[16:19], 0
ds_read_b64_tr_b16 a[70:71], v14 offset:9184
v_mfma_f32_16x16x32_bf16 a[192:195], a[76:79], a[20:23], a[192:195]
v_mfma_f32_16x16x32_bf16 a[196:199], a[72:75], a[24:27], 0
v_mfma_f32_16x16x32_bf16 a[196:199], a[76:79], a[28:31], a[196:199]
v_mfma_f32_16x16x32_bf16 a[200:203], a[72:75], a[32:35], 0
v_mfma_f32_16x16x32_bf16 a[200:203], a[76:79], a[36:39], a[200:203]
v_mfma_f32_16x16x32_bf16 a[204:207], a[72:75], a[40:43], 0
v_mfma_f32_16x16x32_bf16 a[204:207], a[76:79], a[44:47], a[204:207]
v_mfma_f32_16x16x32_bf16 a[208:211], a[72:75], a[48:51], 0
v_mfma_f32_16x16x32_bf16 a[208:211], a[76:79], a[52:55], a[208:211]
v_mfma_f32_16x16x32_bf16 a[212:215], a[72:75], a[56:59], 0
v_mfma_f32_16x16x32_bf16 a[212:215], a[76:79], a[60:63], a[212:215]
s_waitcnt vmcnt(1) lgkmcnt(0)
s_barrier
v_mfma_f32_16x16x32_bf16 a[216:219], a[64:67], a[0:3], 0
ds_read_b64_tr_b16 a[72:73], v17
ds_read_b128 a[0:3], v13
v_mfma_f32_16x16x32_bf16 a[216:219], a[68:71], a[4:7], a[216:219]
ds_read_b64_tr_b16 a[74:75], v17 offset:3584
ds_read_b128 a[4:7], v13 offset:1024
v_mfma_f32_16x16x32_bf16 a[220:223], a[64:67], a[8:11], 0
ds_read_b64_tr_b16 a[76:77], v17 offset:7168
ds_read_b128 a[8:11], v13 offset:2048
v_mfma_f32_16x16x32_bf16 a[220:223], a[68:71], a[12:15], a[220:223]
ds_read_b64_tr_b16 a[78:79], v17 offset:10752
ds_read_b128 a[12:15], v13 offset:3072
v_mfma_f32_16x16x32_bf16 a[224:227], a[64:67], a[16:19], 0
ds_read_b128 a[16:19], v13 offset:4096
v_mfma_f32_16x16x32_bf16 a[224:227], a[68:71], a[20:23], a[224:227]
ds_read_b128 a[20:23], v13 offset:5120
)ASM"
R"ASM(v_mfma_f32_16x16x32_bf16 a[228:231], a[64:67], a[24:27], 0
ds_read_b128 a[24:27], v13 offset:6144
v_mfma_f32_16x16x32_bf16 a[228:231], a[68:71], a[28:31], a[228:231]
ds_read_b128 a[28:31], v13 offset:7168
v_mfma_f32_16x16x32_bf16 a[232:235], a[64:67], a[32:35], 0
ds_read_b128 a[32:35], v13 offset:8192
v_mfma_f32_16x16x32_bf16 a[232:235], a[68:71], a[36:39], a[232:235]
ds_read_b128 a[36:39], v13 offset:9216
v_mfma_f32_16x16x32_bf16 a[236:239], a[64:67], a[40:43], 0
ds_read_b128 a[40:43], v13 offset:10240
v_mfma_f32_16x16x32_bf16 a[236:239], a[68:71], a[44:47], a[236:239]
ds_read_b128 a[44:47], v13 offset:11264
v_mfma_f32_16x16x32_bf16 a[240:243], a[64:67], a[48:51], 0
ds_read_b128 a[48:51], v13 offset:12288
v_mfma_f32_16x16x32_bf16 a[240:243], a[68:71], a[52:55], a[240:243]
ds_read_b128 a[52:55], v13 offset:13312
v_mfma_f32_16x16x32_bf16 a[244:247], a[64:67], a[56:59], 0
ds_read_b128 a[56:59], v13 offset:14336
v_mfma_f32_16x16x32_bf16 a[244:247], a[68:71], a[60:63], a[244:247]
ds_read_b128 a[60:63], v13 offset:15360
s_waitcnt lgkmcnt(6)
v_mfma_f32_16x16x32_bf16 v[24:27], a[72:75], a[0:3], v[24:27]
v_mfma_f32_16x16x32_bf16 v[24:27], a[76:79], a[4:7], v[24:27]
ds_read_b64_tr_b16 a[64:65], v17 offset:14336
s_add_u32 m0, s29, 0
v_mfma_f32_16x16x32_bf16 v[28:31], a[72:75], a[8:11], v[28:31]
buffer_load_dwordx4 v1, s[12:15], s25 offen lds
v_mfma_f32_16x16x32_bf16 v[28:31], a[76:79], a[12:15], v[28:31]
v_mfma_f32_16x16x32_bf16 v[32:35], a[72:75], a[16:19], v[32:35]
ds_read_b64_tr_b16 a[66:67], v17 offset:17920
v_mfma_f32_16x16x32_bf16 v[32:35], a[76:79], a[20:23], v[32:35]
v_mfma_f32_16x16x32_bf16 v[36:39], a[72:75], a[24:27], v[36:39]
v_mfma_f32_16x16x32_bf16 v[36:39], a[76:79], a[28:31], v[36:39]
ds_read_b64_tr_b16 a[68:69], v17 offset:21504
v_mfma_f32_16x16x32_bf16 v[40:43], a[72:75], a[32:35], v[40:43]
buffer_load_dwordx4 v2, s[12:15], s25 offen offset:1056 lds
v_mfma_f32_16x16x32_bf16 v[40:43], a[76:79], a[36:39], v[40:43]
s_waitcnt lgkmcnt(2)
v_mfma_f32_16x16x32_bf16 v[44:47], a[72:75], a[40:43], v[44:47]
ds_read_b64_tr_b16 a[70:71], v17 offset:25088
v_mfma_f32_16x16x32_bf16 v[44:47], a[76:79], a[44:47], v[44:47]
v_mfma_f32_16x16x32_bf16 v[48:51], a[72:75], a[48:51], v[48:51]
v_mfma_f32_16x16x32_bf16 v[48:51], a[76:79], a[52:55], v[48:51]
v_mfma_f32_16x16x32_bf16 v[52:55], a[72:75], a[56:59], v[52:55]
buffer_load_dwordx4 v3, s[12:15], s25 offen offset:2112 lds
v_mfma_f32_16x16x32_bf16 v[52:55], a[76:79], a[60:63], v[52:55]
s_waitcnt lgkmcnt(1)
v_mfma_f32_16x16x32_bf16 v[56:59], a[64:67], a[0:3], v[56:59]
s_waitcnt lgkmcnt(0)
v_mfma_f32_16x16x32_bf16 v[56:59], a[68:71], a[4:7], v[56:59]
ds_read_b64_tr_b16 a[72:73], v17 offset:32
v_mfma_f32_16x16x32_bf16 v[60:63], a[64:67], a[8:11], v[60:63]
v_mfma_f32_16x16x32_bf16 v[60:63], a[68:71], a[12:15], v[60:63]
v_mfma_f32_16x16x32_bf16 v[64:67], a[64:67], a[16:19], v[64:67]
ds_read_b64_tr_b16 a[74:75], v17 offset:3616
buffer_load_dwordx4 v4, s[12:15], s25 offen offset:3168 lds
v_mfma_f32_16x16x32_bf16 v[64:67], a[68:71], a[20:23], v[64:67]
v_mfma_f32_16x16x32_bf16 v[68:71], a[64:67], a[24:27], v[68:71]
v_mfma_f32_16x16x32_bf16 v[68:71], a[68:71], a[28:31], v[68:71]
ds_read_b64_tr_b16 a[76:77], v17 offset:7200
v_mfma_f32_16x16x32_bf16 v[72:75], a[64:67], a[32:35], v[72:75]
v_mfma_f32_16x16x32_bf16 v[72:75], a[68:71], a[36:39], v[72:75]
s_add_u32 m0, s30, 0
v_mfma_f32_16x16x32_bf16 v[76:79], a[64:67], a[40:43], v[76:79]
ds_read_b64_tr_b16 a[78:79], v17 offset:10784
buffer_load_dwordx4 v5, s[16:19], s26 offen lds
v_mfma_f32_16x16x32_bf16 v[76:79], a[68:71], a[44:47], v[76:79]
v_mfma_f32_16x16x32_bf16 v[80:83], a[64:67], a[48:51], v[80:83]
v_mfma_f32_16x16x32_bf16 v[80:83], a[68:71], a[52:55], v[80:83]
v_mfma_f32_16x16x32_bf16 v[84:87], a[64:67], a[56:59], v[84:87]
v_mfma_f32_16x16x32_bf16 v[84:87], a[68:71], a[60:63], v[84:87]
s_waitcnt lgkmcnt(1)
v_mfma_f32_16x16x32_bf16 v[88:91], a[72:75], a[0:3], v[88:91]
buffer_load_dwordx4 v6, s[16:19], s26 offen offset:1024 lds
s_waitcnt lgkmcnt(0)
v_mfma_f32_16x16x32_bf16 v[88:91], a[76:79], a[4:7], v[88:91]
ds_read_b64_tr_b16 a[64:65], v17 offset:14368
v_mfma_f32_16x16x32_bf16 v[92:95], a[72:75], a[8:11], v[92:95]
v_mfma_f32_16x16x32_bf16 v[92:95], a[76:79], a[12:15], v[92:95]
v_mfma_f32_16x16x32_bf16 v[96:99], a[72:75], a[16:19], v[96:99]
ds_read_b64_tr_b16 a[66:67], v17 offset:17952
v_mfma_f32_16x16x32_bf16 v[96:99], a[76:79], a[20:23], v[96:99]
v_mfma_f32_16x16x32_bf16 v[100:103], a[72:75], a[24:27], v[100:103]
buffer_load_dwordx4 v7, s[16:19], s26 offen offset:2048 lds
v_mfma_f32_16x16x32_bf16 v[100:103], a[76:79], a[28:31], v[100:103]
ds_read_b64_tr_b16 a[68:69], v17 offset:21536
v_mfma_f32_16x16x32_bf16 v[104:107], a[72:75], a[32:35], v[104:107]
v_mfma_f32_16x16x32_bf16 v[104:107], a[76:79], a[36:39], v[104:107]
v_mfma_f32_16x16x32_bf16 v[108:111], a[72:75], a[40:43], v[108:111]
ds_read_b64_tr_b16 a[70:71], v17 offset:25120
v_mfma_f32_16x16x32_bf16 v[108:111], a[76:79], a[44:47], v[108:111]
v_mfma_f32_16x16x32_bf16 v[112:115], a[72:75], a[48:51], v[112:115]
buffer_load_dwordx4 v8, s[16:19], s26 offen offset:3072 lds
v_mfma_f32_16x16x32_bf16 v[112:115], a[76:79], a[52:55], v[112:115]
v_mfma_f32_16x16x32_bf16 v[116:119], a[72:75], a[56:59], v[116:119]
v_mfma_f32_16x16x32_bf16 v[116:119], a[76:79], a[60:63], v[116:119]
s_waitcnt lgkmcnt(1)
v_mfma_f32_16x16x32_bf16 v[120:123], a[64:67], a[0:3], v[120:123]
s_waitcnt lgkmcnt(0)
v_mfma_f32_16x16x32_bf16 v[120:123], a[68:71], a[4:7], v[120:123]
ds_read_b64_tr_b16 a[72:73], v19
s_add_u32 m0, s30, 0x1000
v_mfma_f32_16x16x32_bf16 v[124:127], a[64:67], a[8:11], v[124:127]
buffer_load_dwordx4 v9, s[16:19], s26 offen lds
v_mfma_f32_16x16x32_bf16 v[124:127], a[68:71], a[12:15], v[124:127]
v_mfma_f32_16x16x32_bf16 v[128:131], a[64:67], a[16:19], v[128:131]
ds_read_b64_tr_b16 a[74:75], v19 offset:3584
v_mfma_f32_16x16x32_bf16 v[128:131], a[68:71], a[20:23], v[128:131]
v_mfma_f32_16x16x32_bf16 v[132:135], a[64:67], a[24:27], v[132:135]
v_mfma_f32_16x16x32_bf16 v[132:135], a[68:71], a[28:31], v[132:135]
ds_read_b64_tr_b16 a[76:77], v19 offset:7168
v_mfma_f32_16x16x32_bf16 v[136:139], a[64:67], a[32:35], v[136:139]
buffer_load_dwordx4 v10, s[16:19], s26 offen offset:1024 lds
v_mfma_f32_16x16x32_bf16 v[136:139], a[68:71], a[36:39], v[136:139]
v_mfma_f32_16x16x32_bf16 v[140:143], a[64:67], a[40:43], v[140:143]
ds_read_b64_tr_b16 a[78:79], v19 offset:10752
v_mfma_f32_16x16x32_bf16 v[140:143], a[68:71], a[44:47], v[140:143]
v_mfma_f32_16x16x32_bf16 v[144:147], a[64:67], a[48:51], v[144:147]
v_mfma_f32_16x16x32_bf16 v[144:147], a[68:71], a[52:55], v[144:147]
v_mfma_f32_16x16x32_bf16 v[148:151], a[64:67], a[56:59], v[148:151]
buffer_load_dwordx4 v11, s[16:19], s26 offen offset:2048 lds
v_mfma_f32_16x16x32_bf16 v[148:151], a[68:71], a[60:63], v[148:151]
s_waitcnt lgkmcnt(1)
v_mfma_f32_16x16x32_bf16 v[152:155], a[72:75], a[0:3], v[152:155]
s_waitcnt lgkmcnt(0)
v_mfma_f32_16x16x32_bf16 v[152:155], a[76:79], a[4:7], v[152:155]
ds_read_b64_tr_b16 a[64:65], v19 offset:14336
v_mfma_f32_16x16x32_bf16 v[156:159], a[72:75], a[8:11], v[156:159]
v_mfma_f32_16x16x32_bf16 v[156:159], a[76:79], a[12:15], v[156:159]
s_add_u32 m0, s28, 0
v_mfma_f32_16x16x32_bf16 v[160:163], a[72:75], a[16:19], v[160:163]
ds_read_b64_tr_b16 a[66:67], v19 offset:17920
buffer_load_dwordx4 v0, s[8:11], s24 offen sc1 lds
v_mfma_f32_16x16x32_bf16 v[160:163], a[76:79], a[20:23], v[160:163]
v_mfma_f32_16x16x32_bf16 v[164:167], a[72:75], a[24:27], v[164:167]
v_mfma_f32_16x16x32_bf16 v[164:167], a[76:79], a[28:31], v[164:167]
ds_read_b64_tr_b16 a[68:69], v19 offset:21504
v_mfma_f32_16x16x32_bf16 v[168:171], a[72:75], a[32:35], v[168:171]
v_mfma_f32_16x16x32_bf16 v[168:171], a[76:79], a[36:39], v[168:171]
v_mfma_f32_16x16x32_bf16 v[172:175], a[72:75], a[40:43], v[172:175]
ds_read_b64_tr_b16 a[70:71], v19 offset:25088
buffer_load_dwordx4 v0, s[8:11], s24 offen offset:1024 sc1 lds
v_mfma_f32_16x16x32_bf16 v[172:175], a[76:79], a[44:47], v[172:175]
v_mfma_f32_16x16x32_bf16 v[176:179], a[72:75], a[48:51], v[176:179]
v_mfma_f32_16x16x32_bf16 v[176:179], a[76:79], a[52:55], v[176:179]
v_mfma_f32_16x16x32_bf16 v[180:183], a[72:75], a[56:59], v[180:183]
v_mfma_f32_16x16x32_bf16 v[180:183], a[76:79], a[60:63], v[180:183]
s_waitcnt lgkmcnt(1)
v_mfma_f32_16x16x32_bf16 v[184:187], a[64:67], a[0:3], v[184:187]
buffer_load_dwordx4 v0, s[8:11], s24 offen offset:2048 sc1 lds
s_waitcnt lgkmcnt(0)
v_mfma_f32_16x16x32_bf16 v[184:187], a[68:71], a[4:7], v[184:187]
ds_read_b64_tr_b16 a[72:73], v15 offset:96
v_mfma_f32_16x16x32_bf16 v[188:191], a[64:67], a[8:11], v[188:191]
v_mfma_f32_16x16x32_bf16 v[188:191], a[68:71], a[12:15], v[188:191]
v_mfma_f32_16x16x32_bf16 v[192:195], a[64:67], a[16:19], v[192:195]
ds_read_b64_tr_b16 a[74:75], v15 offset:608
v_mfma_f32_16x16x32_bf16 v[192:195], a[68:71], a[20:23], v[192:195]
v_mfma_f32_16x16x32_bf16 v[196:199], a[64:67], a[24:27], v[196:199]
buffer_load_dwordx4 v0, s[8:11], s24 offen offset:3072 sc1 lds
v_mfma_f32_16x16x32_bf16 v[196:199], a[68:71], a[28:31], v[196:199]
ds_read_b64_tr_b16 a[76:77], v15 offset:8544
v_mfma_f32_16x16x32_bf16 v[200:203], a[64:67], a[32:35], v[200:203]
v_mfma_f32_16x16x32_bf16 v[200:203], a[68:71], a[36:39], v[200:203]
v_mfma_f32_16x16x32_bf16 v[204:207], a[64:67], a[40:43], v[204:207]
ds_read_b64_tr_b16 a[78:79], v15 offset:9056
v_mfma_f32_16x16x32_bf16 v[204:207], a[68:71], a[44:47], v[204:207]
s_add_u32 m0, s28, 0x1000
s_add_u32 s34, s24, 0x1000
v_mfma_f32_16x16x32_bf16 v[208:211], a[64:67], a[48:51], v[208:211]
buffer_load_dwordx4 v0, s[8:11], s34 offen sc1 lds
v_mfma_f32_16x16x32_bf16 v[208:211], a[68:71], a[52:55], v[208:211]
v_mfma_f32_16x16x32_bf16 v[212:215], a[64:67], a[56:59], v[212:215]
v_mfma_f32_16x16x32_bf16 v[212:215], a[68:71], a[60:63], v[212:215]
s_waitcnt lgkmcnt(1)
v_mfma_f32_16x16x32_bf16 a[88:91], a[72:75], a[0:3], a[88:91]
s_waitcnt lgkmcnt(0)
v_mfma_f32_16x16x32_bf16 a[88:91], a[76:79], a[4:7], a[88:91]
ds_read_b64_tr_b16 a[64:65], v15 offset:128
v_mfma_f32_16x16x32_bf16 a[92:95], a[72:75], a[8:11], a[92:95]
buffer_load_dwordx4 v0, s[8:11], s34 offen offset:1024 sc1 lds
v_mfma_f32_16x16x32_bf16 a[92:95], a[76:79], a[12:15], a[92:95]
v_mfma_f32_16x16x32_bf16 a[96:99], a[72:75], a[16:19], a[96:99]
ds_read_b64_tr_b16 a[66:67], v15 offset:640
v_mfma_f32_16x16x32_bf16 a[96:99], a[76:79], a[20:23], a[96:99]
v_mfma_f32_16x16x32_bf16 a[100:103], a[72:75], a[24:27], a[100:103]
v_mfma_f32_16x16x32_bf16 a[100:103], a[76:79], a[28:31], a[100:103]
ds_read_b64_tr_b16 a[68:69], v15 offset:8576
v_mfma_f32_16x16x32_bf16 a[104:107], a[72:75], a[32:35], a[104:107]
buffer_load_dwordx4 v0, s[8:11], s34 offen offset:2048 sc1 lds
v_mfma_f32_16x16x32_bf16 a[104:107], a[76:79], a[36:39], a[104:107]
v_mfma_f32_16x16x32_bf16 a[108:111], a[72:75], a[40:43], a[108:111]
ds_read_b64_tr_b16 a[70:71], v15 offset:9088
v_mfma_f32_16x16x32_bf16 a[108:111], a[76:79], a[44:47], a[108:111]
v_mfma_f32_16x16x32_bf16 a[112:115], a[72:75], a[48:51], a[112:115]
v_mfma_f32_16x16x32_bf16 a[112:115], a[76:79], a[52:55], a[112:115]
v_mfma_f32_16x16x32_bf16 a[116:119], a[72:75], a[56:59], a[116:119]
buffer_load_dwordx4 v0, s[8:11], s34 offen offset:3072 sc1 lds
s_add_u32 s24, s24, 0x8000
s_add_u32 s25, s25, 0x80000
s_add_u32 s26, s26, 0x70000
v_mfma_f32_16x16x32_bf16 a[116:119], a[76:79], a[60:63], a[116:119]
s_waitcnt lgkmcnt(1)
v_mfma_f32_16x16x32_bf16 a[120:123], a[64:67], a[0:3], a[120:123]
s_waitcnt lgkmcnt(0)
v_mfma_f32_16x16x32_bf16 a[120:123], a[68:71], a[4:7], a[120:123]
ds_read_b64_tr_b16 a[72:73], v15 offset:160
v_mfma_f32_16x16x32_bf16 a[124:127], a[64:67], a[8:11], a[124:127]
v_mfma_f32_16x16x32_bf16 a[124:127], a[68:71], a[12:15], a[124:127]
v_mfma_f32_16x16x32_bf16 a[128:131], a[64:67], a[16:19], a[128:131]
ds_read_b64_tr_b16 a[74:75], v15 offset:672
v_mfma_f32_16x16x32_bf16 a[128:131], a[68:71], a[20:23], a[128:131]
v_mfma_f32_16x16x32_bf16 a[132:135], a[64:67], a[24:27], a[132:135]
global_load_dword v255, v[252:253], off
v_mfma_f32_16x16x32_bf16 a[132:135], a[68:71], a[28:31], a[132:135]
ds_read_b64_tr_b16 a[76:77], v15 offset:8608
v_mfma_f32_16x16x32_bf16 a[136:139], a[64:67], a[32:35], a[136:139]
v_mfma_f32_16x16x32_bf16 a[136:139], a[68:71], a[36:39], a[136:139]
v_add_co_u32 v252, vcc, v254, v252
v_mfma_f32_16x16x32_bf16 a[140:143], a[64:67], a[40:43], a[140:143]
ds_read_b64_tr_b16 a[78:79], v15 offset:9120
v_mfma_f32_16x16x32_bf16 a[140:143], a[68:71], a[44:47], a[140:143]
v_mfma_f32_16x16x32_bf16 a[144:147], a[64:67], a[48:51], a[144:147]
v_addc_co_u32 v253, vcc, 0, v253, vcc
v_mfma_f32_16x16x32_bf16 a[144:147], a[68:71], a[52:55], a[144:147]
v_mfma_f32_16x16x32_bf16 a[148:151], a[64:67], a[56:59], a[148:151]
v_mfma_f32_16x16x32_bf16 a[148:151], a[68:71], a[60:63], a[148:151]
s_waitcnt lgkmcnt(1)
v_mfma_f32_16x16x32_bf16 a[152:155], a[72:75], a[0:3], a[152:155]
s_waitcnt lgkmcnt(0)
v_mfma_f32_16x16x32_bf16 a[152:155], a[76:79], a[4:7], a[152:155]
ds_read_b64_tr_b16 a[64:65], v15 offset:192
v_mfma_f32_16x16x32_bf16 a[156:159], a[72:75], a[8:11], a[156:159]
v_mfma_f32_16x16x32_bf16 a[156:159], a[76:79], a[12:15], a[156:159]
v_mfma_f32_16x16x32_bf16 a[160:163], a[72:75], a[16:19], a[160:163]
ds_read_b64_tr_b16 a[66:67], v15 offset:704
v_mfma_f32_16x16x32_bf16 a[160:163], a[76:79], a[20:23], a[160:163]
v_mfma_f32_16x16x32_bf16 a[164:167], a[72:75], a[24:27], a[164:167]
v_mfma_f32_16x16x32_bf16 a[164:167], a[76:79], a[28:31], a[164:167]
ds_read_b64_tr_b16 a[68:69], v15 offset:8640
v_mfma_f32_16x16x32_bf16 a[168:171], a[72:75], a[32:35], a[168:171]
v_mfma_f32_16x16x32_bf16 a[168:171], a[76:79], a[36:39], a[168:171]
v_mfma_f32_16x16x32_bf16 a[172:175], a[72:75], a[40:43], a[172:175]
ds_read_b64_tr_b16 a[70:71], v15 offset:9152
v_mfma_f32_16x16x32_bf16 a[172:175], a[76:79], a[44:47], a[172:175]
v_mfma_f32_16x16x32_bf16 a[176:179], a[72:75], a[48:51], a[176:179]
v_mfma_f32_16x16x32_bf16 a[176:179], a[76:79], a[52:55], a[176:179]
v_mfma_f32_16x16x32_bf16 a[180:183], a[72:75], a[56:59], a[180:183]
v_mfma_f32_16x16x32_bf16 a[180:183], a[76:79], a[60:63], a[180:183]
s_waitcnt lgkmcnt(1)
v_mfma_f32_16x16x32_bf16 a[184:187], a[64:67], a[0:3], a[184:187]
s_waitcnt lgkmcnt(0)
v_mfma_f32_16x16x32_bf16 a[184:187], a[68:71], a[4:7], a[184:187]
ds_read_b64_tr_b16 a[72:73], v15 offset:224
v_mfma_f32_16x16x32_bf16 a[188:191], a[64:67], a[8:11], a[188:191]
ds_read_b64_tr_b16 a[74:75], v15 offset:736
v_mfma_f32_16x16x32_bf16 a[188:191], a[68:71], a[12:15], a[188:191]
ds_read_b64_tr_b16 a[76:77], v15 offset:8672
v_mfma_f32_16x16x32_bf16 a[192:195], a[64:67], a[16:19], a[192:195]
ds_read_b64_tr_b16 a[78:79], v15 offset:9184
v_mfma_f32_16x16x32_bf16 a[192:195], a[68:71], a[20:23], a[192:195]
v_mfma_f32_16x16x32_bf16 a[196:199], a[64:67], a[24:27], a[196:199]
v_mfma_f32_16x16x32_bf16 a[196:199], a[68:71], a[28:31], a[196:199]
v_mfma_f32_16x16x32_bf16 a[200:203], a[64:67], a[32:35], a[200:203]
v_mfma_f32_16x16x32_bf16 a[200:203], a[68:71], a[36:39], a[200:203]
v_mfma_f32_16x16x32_bf16 a[204:207], a[64:67], a[40:43], a[204:207]
v_mfma_f32_16x16x32_bf16 a[204:207], a[68:71], a[44:47], a[204:207]
v_mfma_f32_16x16x32_bf16 a[208:211], a[64:67], a[48:51], a[208:211]
v_mfma_f32_16x16x32_bf16 a[208:211], a[68:71], a[52:55], a[208:211]
v_mfma_f32_16x16x32_bf16 a[212:215], a[64:67], a[56:59], a[212:215]
v_mfma_f32_16x16x32_bf16 a[212:215], a[68:71], a[60:63], a[212:215]
s_waitcnt vmcnt(1) lgkmcnt(0)
s_barrier
v_mfma_f32_16x16x32_bf16 a[216:219], a[72:75], a[0:3], a[216:219]
ds_read_b64_tr_b16 a[64:65], v16
ds_read_b128 a[0:3], v12
v_mfma_f32_16x16x32_bf16 a[216:219], a[76:79], a[4:7], a[216:219]
ds_read_b64_tr_b16 a[66:67], v16 offset:3584
ds_read_b128 a[4:7], v12 offset:1024
v_mfma_f32_16x16x32_bf16 a[220:223], a[72:75], a[8:11], a[220:223]
ds_read_b64_tr_b16 a[68:69], v16 offset:7168
ds_read_b128 a[8:11], v12 offset:2048
v_mfma_f32_16x16x32_bf16 a[220:223], a[76:79], a[12:15], a[220:223]
ds_read_b64_tr_b16 a[70:71], v16 offset:10752
ds_read_b128 a[12:15], v12 offset:3072
v_mfma_f32_16x16x32_bf16 a[224:227], a[72:75], a[16:19], a[224:227]
ds_read_b128 a[16:19], v12 offset:4096
v_mfma_f32_16x16x32_bf16 a[224:227], a[76:79], a[20:23], a[224:227]
ds_read_b128 a[20:23], v12 offset:5120
v_mfma_f32_16x16x32_bf16 a[228:231], a[72:75], a[24:27], a[228:231]
ds_read_b128 a[24:27], v12 offset:6144
v_mfma_f32_16x16x32_bf16 a[228:231], a[76:79], a[28:31], a[228:231]
ds_read_b128 a[28:31], v12 offset:7168
v_mfma_f32_16x16x32_bf16 a[232:235], a[72:75], a[32:35], a[232:235]
ds_read_b128 a[32:35], v12 offset:8192
v_mfma_f32_16x16x32_bf16 a[232:235], a[76:79], a[36:39], a[232:235]
ds_read_b128 a[36:39], v12 offset:9216
v_mfma_f32_16x16x32_bf16 a[236:239], a[72:75], a[40:43], a[236:239]
ds_read_b128 a[40:43], v12 offset:10240
v_mfma_f32_16x16x32_bf16 a[236:239], a[76:79], a[44:47], a[236:239]
ds_read_b128 a[44:47], v12 offset:11264
v_mfma_f32_16x16x32_bf16 a[240:243], a[72:75], a[48:51], a[240:243]
ds_read_b128 a[48:51], v12 offset:12288
v_mfma_f32_16x16x32_bf16 a[240:243], a[76:79], a[52:55], a[240:243]
ds_read_b128 a[52:55], v12 offset:13312
v_mfma_f32_16x16x32_bf16 a[244:247], a[72:75], a[56:59], a[244:247]
ds_read_b128 a[56:59], v12 offset:14336
v_mfma_f32_16x16x32_bf16 a[244:247], a[76:79], a[60:63], a[244:247]
ds_read_b128 a[60:63], v12 offset:15360
s_mov_b32 s70, 6
G1B_%=:
s_waitcnt lgkmcnt(0)
v_mfma_f32_16x16x32_bf16 v[24:27], a[64:67], a[0:3], v[24:27]
v_mfma_f32_16x16x32_bf16 v[24:27], a[68:71], a[4:7], v[24:27]
ds_read_b64_tr_b16 a[72:73], v16 offset:14336
s_add_u32 m0, s29, 0x13200
v_mfma_f32_16x16x32_bf16 v[28:31], a[64:67], a[8:11], v[28:31]
buffer_load_dwordx4 v1, s[12:15], s25 offen lds
v_mfma_f32_16x16x32_bf16 v[28:31], a[68:71], a[12:15], v[28:31]
v_mfma_f32_16x16x32_bf16 v[32:35], a[64:67], a[16:19], v[32:35]
ds_read_b64_tr_b16 a[74:75], v16 offset:17920
v_mfma_f32_16x16x32_bf16 v[32:35], a[68:71], a[20:23], v[32:35]
v_mfma_f32_16x16x32_bf16 v[36:39], a[64:67], a[24:27], v[36:39]
v_mfma_f32_16x16x32_bf16 v[36:39], a[68:71], a[28:31], v[36:39]
ds_read_b64_tr_b16 a[76:77], v16 offset:21504
v_mfma_f32_16x16x32_bf16 v[40:43], a[64:67], a[32:35], v[40:43]
buffer_load_dwordx4 v2, s[12:15], s25 offen offset:1056 lds
v_mfma_f32_16x16x32_bf16 v[40:43], a[68:71], a[36:39], v[40:43]
v_mfma_f32_16x16x32_bf16 v[44:47], a[64:67], a[40:43], v[44:47]
ds_read_b64_tr_b16 a[78:79], v16 offset:25088
v_mfma_f32_16x16x32_bf16 v[44:47], a[68:71], a[44:47], v[44:47]
v_mfma_f32_16x16x32_bf16 v[48:51], a[64:67], a[48:51], v[48:51]
v_mfma_f32_16x16x32_bf16 v[48:51], a[68:71], a[52:55], v[48:51]
v_mfma_f32_16x16x32_bf16 v[52:55], a[64:67], a[56:59], v[52:55]
buffer_load_dwordx4 v3, s[12:15], s25 offen offset:2112 lds
v_mfma_f32_16x16x32_bf16 v[52:55], a[68:71], a[60:63], v[52:55]
s_waitcnt lgkmcnt(1)
v_mfma_f32_16x16x32_bf16 v[56:59], a[72:75], a[0:3], v[56:59]
s_waitcnt lgkmcnt(0)
v_mfma_f32_16x16x32_bf16 v[56:59], a[76:79], a[4:7], v[56:59]
ds_read_b64_tr_b16 a[64:65], v16 offset:32
v_mfma_f32_16x16x32_bf16 v[60:63], a[72:75], a[8:11], v[60:63]
v_mfma_f32_16x16x32_bf16 v[60:63], a[76:79], a[12:15], v[60:63]
v_mfma_f32_16x16x32_bf16 v[64:67], a[72:75], a[16:19], v[64:67]
ds_read_b64_tr_b16 a[66:67], v16 offset:3616
buffer_load_dwordx4 v4, s[12:15], s25 offen offset:3168 lds
v_mfma_f32_16x16x32_bf16 v[64:67], a[76:79], a[20:23], v[64:67]
v_mfma_f32_16x16x32_bf16 v[68:71], a[72:75], a[24:27], v[68:71]
v_mfma_f32_16x16x32_bf16 v[68:71], a[76:79], a[28:31], v[68:71]
ds_read_b64_tr_b16 a[68:69], v16 offset:7200
v_mfma_f32_16x16x32_bf16 v[72:75], a[72:75], a[32:35], v[72:75]
v_mfma_f32_16x16x32_bf16 v[72:75], a[76:79], a[36:39], v[72:75]
s_add_u32 m0, s30, 0x13200
v_mfma_f32_16x16x32_bf16 v[76:79], a[72:75], a[40:43], v[76:79]
ds_read_b64_tr_b16 a[70:71], v16 offset:10784
buffer_load_dwordx4 v5, s[16:19], s26 offen lds
v_mfma_f32_16x16x32_bf16 v[76:79], a[76:79], a[44:47], v[76:79]
v_mfma_f32_16x16x32_bf16 v[80:83], a[72:75], a[48:51], v[80:83]
v_mfma_f32_16x16x32_bf16 v[80:83], a[76:79], a[52:55], v[80:83]
v_mfma_f32_16x16x32_bf16 v[84:87], a[72:75], a[56:59], v[84:87]
v_mfma_f32_16x16x32_bf16 v[84:87], a[76:79], a[60:63], v[84:87]
s_waitcnt lgkmcnt(1)
v_mfma_f32_16x16x32_bf16 v[88:91], a[64:67], a[0:3], v[88:91]
buffer_load_dwordx4 v6, s[16:19], s26 offen offset:1024 lds
s_waitcnt lgkmcnt(0)
v_mfma_f32_16x16x32_bf16 v[88:91], a[68:71], a[4:7], v[88:91]
ds_read_b64_tr_b16 a[72:73], v16 offset:14368
v_mfma_f32_16x16x32_bf16 v[92:95], a[64:67], a[8:11], v[92:95]
v_mfma_f32_16x16x32_bf16 v[92:95], a[68:71], a[12:15], v[92:95]
v_mfma_f32_16x16x32_bf16 v[96:99], a[64:67], a[16:19], v[96:99]
ds_read_b64_tr_b16 a[74:75], v16 offset:17952
v_mfma_f32_16x16x32_bf16 v[96:99], a[68:71], a[20:23], v[96:99]
v_mfma_f32_16x16x32_bf16 v[100:103], a[64:67], a[24:27], v[100:103]
buffer_load_dwordx4 v7, s[16:19], s26 offen offset:2048 lds
v_mfma_f32_16x16x32_bf16 v[100:103], a[68:71], a[28:31], v[100:103]
ds_read_b64_tr_b16 a[76:77], v16 offset:21536
v_mfma_f32_16x16x32_bf16 v[104:107], a[64:67], a[32:35], v[104:107]
v_mfma_f32_16x16x32_bf16 v[104:107], a[68:71], a[36:39], v[104:107]
v_mfma_f32_16x16x32_bf16 v[108:111], a[64:67], a[40:43], v[108:111]
ds_read_b64_tr_b16 a[78:79], v16 offset:25120
v_mfma_f32_16x16x32_bf16 v[108:111], a[68:71], a[44:47], v[108:111]
v_mfma_f32_16x16x32_bf16 v[112:115], a[64:67], a[48:51], v[112:115]
buffer_load_dwordx4 v8, s[16:19], s26 offen offset:3072 lds
v_mfma_f32_16x16x32_bf16 v[112:115], a[68:71], a[52:55], v[112:115]
v_mfma_f32_16x16x32_bf16 v[116:119], a[64:67], a[56:59], v[116:119]
v_mfma_f32_16x16x32_bf16 v[116:119], a[68:71], a[60:63], v[116:119]
s_waitcnt lgkmcnt(1)
v_mfma_f32_16x16x32_bf16 v[120:123], a[72:75], a[0:3], v[120:123]
s_waitcnt lgkmcnt(0)
v_mfma_f32_16x16x32_bf16 v[120:123], a[76:79], a[4:7], v[120:123]
ds_read_b64_tr_b16 a[64:65], v18
s_add_u32 m0, s30, 0x14200
v_mfma_f32_16x16x32_bf16 v[124:127], a[72:75], a[8:11], v[124:127]
buffer_load_dwordx4 v9, s[16:19], s26 offen lds
v_mfma_f32_16x16x32_bf16 v[124:127], a[76:79], a[12:15], v[124:127]
v_mfma_f32_16x16x32_bf16 v[128:131], a[72:75], a[16:19], v[128:131]
ds_read_b64_tr_b16 a[66:67], v18 offset:3584
v_mfma_f32_16x16x32_bf16 v[128:131], a[76:79], a[20:23], v[128:131]
v_mfma_f32_16x16x32_bf16 v[132:135], a[72:75], a[24:27], v[132:135]
v_mfma_f32_16x16x32_bf16 v[132:135], a[76:79], a[28:31], v[132:135]
)ASM"
R"ASM(ds_read_b64_tr_b16 a[68:69], v18 offset:7168
v_mfma_f32_16x16x32_bf16 v[136:139], a[72:75], a[32:35], v[136:139]
buffer_load_dwordx4 v10, s[16:19], s26 offen offset:1024 lds
v_mfma_f32_16x16x32_bf16 v[136:139], a[76:79], a[36:39], v[136:139]
v_mfma_f32_16x16x32_bf16 v[140:143], a[72:75], a[40:43], v[140:143]
ds_read_b64_tr_b16 a[70:71], v18 offset:10752
v_mfma_f32_16x16x32_bf16 v[140:143], a[76:79], a[44:47], v[140:143]
v_mfma_f32_16x16x32_bf16 v[144:147], a[72:75], a[48:51], v[144:147]
v_mfma_f32_16x16x32_bf16 v[144:147], a[76:79], a[52:55], v[144:147]
v_mfma_f32_16x16x32_bf16 v[148:151], a[72:75], a[56:59], v[148:151]
buffer_load_dwordx4 v11, s[16:19], s26 offen offset:2048 lds
v_mfma_f32_16x16x32_bf16 v[148:151], a[76:79], a[60:63], v[148:151]
s_waitcnt lgkmcnt(1)
v_mfma_f32_16x16x32_bf16 v[152:155], a[64:67], a[0:3], v[152:155]
s_waitcnt lgkmcnt(0)
v_mfma_f32_16x16x32_bf16 v[152:155], a[68:71], a[4:7], v[152:155]
ds_read_b64_tr_b16 a[72:73], v18 offset:14336
v_mfma_f32_16x16x32_bf16 v[156:159], a[64:67], a[8:11], v[156:159]
v_mfma_f32_16x16x32_bf16 v[156:159], a[68:71], a[12:15], v[156:159]
s_add_u32 m0, s28, 0x13200
v_mfma_f32_16x16x32_bf16 v[160:163], a[64:67], a[16:19], v[160:163]
ds_read_b64_tr_b16 a[74:75], v18 offset:17920
buffer_load_dwordx4 v0, s[8:11], s24 offen sc1 lds
v_mfma_f32_16x16x32_bf16 v[160:163], a[68:71], a[20:23], v[160:163]
v_mfma_f32_16x16x32_bf16 v[164:167], a[64:67], a[24:27], v[164:167]
v_mfma_f32_16x16x32_bf16 v[164:167], a[68:71], a[28:31], v[164:167]
ds_read_b64_tr_b16 a[76:77], v18 offset:21504
v_mfma_f32_16x16x32_bf16 v[168:171], a[64:67], a[32:35], v[168:171]
v_mfma_f32_16x16x32_bf16 v[168:171], a[68:71], a[36:39], v[168:171]
v_mfma_f32_16x16x32_bf16 v[172:175], a[64:67], a[40:43], v[172:175]
ds_read_b64_tr_b16 a[78:79], v18 offset:25088
buffer_load_dwordx4 v0, s[8:11], s24 offen offset:1024 sc1 lds
v_mfma_f32_16x16x32_bf16 v[172:175], a[68:71], a[44:47], v[172:175]
v_mfma_f32_16x16x32_bf16 v[176:179], a[64:67], a[48:51], v[176:179]
v_mfma_f32_16x16x32_bf16 v[176:179], a[68:71], a[52:55], v[176:179]
v_mfma_f32_16x16x32_bf16 v[180:183], a[64:67], a[56:59], v[180:183]
v_mfma_f32_16x16x32_bf16 v[180:183], a[68:71], a[60:63], v[180:183]
s_waitcnt lgkmcnt(1)
v_mfma_f32_16x16x32_bf16 v[184:187], a[72:75], a[0:3], v[184:187]
buffer_load_dwordx4 v0, s[8:11], s24 offen offset:2048 sc1 lds
s_waitcnt lgkmcnt(0)
v_mfma_f32_16x16x32_bf16 v[184:187], a[76:79], a[4:7], v[184:187]
ds_read_b64_tr_b16 a[64:65], v14 offset:96
v_mfma_f32_16x16x32_bf16 v[188:191], a[72:75], a[8:11], v[188:191]
v_mfma_f32_16x16x32_bf16 v[188:191], a[76:79], a[12:15], v[188:191]
v_mfma_f32_16x16x32_bf16 v[192:195], a[72:75], a[16:19], v[192:195]
ds_read_b64_tr_b16 a[66:67], v14 offset:608
v_mfma_f32_16x16x32_bf16 v[192:195], a[76:79], a[20:23], v[192:195]
v_mfma_f32_16x16x32_bf16 v[196:199], a[72:75], a[24:27], v[196:199]
buffer_load_dwordx4 v0, s[8:11], s24 offen offset:3072 sc1 lds
v_mfma_f32_16x16x32_bf16 v[196:199], a[76:79], a[28:31], v[196:199]
ds_read_b64_tr_b16 a[68:69], v14 offset:8544
v_mfma_f32_16x16x32_bf16 v[200:203], a[72:75], a[32:35], v[200:203]
v_mfma_f32_16x16x32_bf16 v[200:203], a[76:79], a[36:39], v[200:203]
v_mfma_f32_16x16x32_bf16 v[204:207], a[72:75], a[40:43], v[204:207]
ds_read_b64_tr_b16 a[70:71], v14 offset:9056
v_mfma_f32_16x16x32_bf16 v[204:207], a[76:79], a[44:47], v[204:207]
s_add_u32 m0, s28, 0x14200
s_add_u32 s34, s24, 0x1000
v_mfma_f32_16x16x32_bf16 v[208:211], a[72:75], a[48:51], v[208:211]
buffer_load_dwordx4 v0, s[8:11], s34 offen sc1 lds
v_mfma_f32_16x16x32_bf16 v[208:211], a[76:79], a[52:55], v[208:211]
v_mfma_f32_16x16x32_bf16 v[212:215], a[72:75], a[56:59], v[212:215]
v_mfma_f32_16x16x32_bf16 v[212:215], a[76:79], a[60:63], v[212:215]
s_waitcnt lgkmcnt(1)
v_mfma_f32_16x16x32_bf16 a[88:91], a[64:67], a[0:3], a[88:91]
s_waitcnt lgkmcnt(0)
v_mfma_f32_16x16x32_bf16 a[88:91], a[68:71], a[4:7], a[88:91]
ds_read_b64_tr_b16 a[72:73], v14 offset:128
v_mfma_f32_16x16x32_bf16 a[92:95], a[64:67], a[8:11], a[92:95]
buffer_load_dwordx4 v0, s[8:11], s34 offen offset:1024 sc1 lds
v_mfma_f32_16x16x32_bf16 a[92:95], a[68:71], a[12:15], a[92:95]
v_mfma_f32_16x16x32_bf16 a[96:99], a[64:67], a[16:19], a[96:99]
ds_read_b64_tr_b16 a[74:75], v14 offset:640
v_mfma_f32_16x16x32_bf16 a[96:99], a[68:71], a[20:23], a[96:99]
v_mfma_f32_16x16x32_bf16 a[100:103], a[64:67], a[24:27], a[100:103]
v_mfma_f32_16x16x32_bf16 a[100:103], a[68:71], a[28:31], a[100:103]
ds_read_b64_tr_b16 a[76:77], v14 offset:8576
v_mfma_f32_16x16x32_bf16 a[104:107], a[64:67], a[32:35], a[104:107]
buffer_load_dwordx4 v0, s[8:11], s34 offen offset:2048 sc1 lds
v_mfma_f32_16x16x32_bf16 a[104:107], a[68:71], a[36:39], a[104:107]
v_mfma_f32_16x16x32_bf16 a[108:111], a[64:67], a[40:43], a[108:111]
ds_read_b64_tr_b16 a[78:79], v14 offset:9088
v_mfma_f32_16x16x32_bf16 a[108:111], a[68:71], a[44:47], a[108:111]
v_mfma_f32_16x16x32_bf16 a[112:115], a[64:67], a[48:51], a[112:115]
v_mfma_f32_16x16x32_bf16 a[112:115], a[68:71], a[52:55], a[112:115]
v_mfma_f32_16x16x32_bf16 a[116:119], a[64:67], a[56:59], a[116:119]
buffer_load_dwordx4 v0, s[8:11], s34 offen offset:3072 sc1 lds
s_add_u32 s24, s24, 0x8000
s_add_u32 s25, s25, 0x80000
s_add_u32 s26, s26, 0x70000
v_mfma_f32_16x16x32_bf16 a[116:119], a[68:71], a[60:63], a[116:119]
s_waitcnt lgkmcnt(1)
v_mfma_f32_16x16x32_bf16 a[120:123], a[72:75], a[0:3], a[120:123]
s_waitcnt lgkmcnt(0)
v_mfma_f32_16x16x32_bf16 a[120:123], a[76:79], a[4:7], a[120:123]
ds_read_b64_tr_b16 a[64:65], v14 offset:160
v_mfma_f32_16x16x32_bf16 a[124:127], a[72:75], a[8:11], a[124:127]
v_mfma_f32_16x16x32_bf16 a[124:127], a[76:79], a[12:15], a[124:127]
v_mfma_f32_16x16x32_bf16 a[128:131], a[72:75], a[16:19], a[128:131]
ds_read_b64_tr_b16 a[66:67], v14 offset:672
v_mfma_f32_16x16x32_bf16 a[128:131], a[76:79], a[20:23], a[128:131]
v_mfma_f32_16x16x32_bf16 a[132:135], a[72:75], a[24:27], a[132:135]
global_load_dword v255, v[252:253], off
v_mfma_f32_16x16x32_bf16 a[132:135], a[76:79], a[28:31], a[132:135]
ds_read_b64_tr_b16 a[68:69], v14 offset:8608
v_mfma_f32_16x16x32_bf16 a[136:139], a[72:75], a[32:35], a[136:139]
v_mfma_f32_16x16x32_bf16 a[136:139], a[76:79], a[36:39], a[136:139]
v_add_co_u32 v252, vcc, v254, v252
v_mfma_f32_16x16x32_bf16 a[140:143], a[72:75], a[40:43], a[140:143]
ds_read_b64_tr_b16 a[70:71], v14 offset:9120
v_mfma_f32_16x16x32_bf16 a[140:143], a[76:79], a[44:47], a[140:143]
v_mfma_f32_16x16x32_bf16 a[144:147], a[72:75], a[48:51], a[144:147]
v_addc_co_u32 v253, vcc, 0, v253, vcc
v_mfma_f32_16x16x32_bf16 a[144:147], a[76:79], a[52:55], a[144:147]
v_mfma_f32_16x16x32_bf16 a[148:151], a[72:75], a[56:59], a[148:151]
v_mfma_f32_16x16x32_bf16 a[148:151], a[76:79], a[60:63], a[148:151]
buffer_store_dwordx4 v[248:251], v0, s[76:79], s96 offen sc1
s_waitcnt lgkmcnt(1)
v_mfma_f32_16x16x32_bf16 a[152:155], a[64:67], a[0:3], a[152:155]
s_waitcnt lgkmcnt(0)
v_mfma_f32_16x16x32_bf16 a[152:155], a[68:71], a[4:7], a[152:155]
ds_read_b64_tr_b16 a[72:73], v14 offset:192
v_mfma_f32_16x16x32_bf16 a[156:159], a[64:67], a[8:11], a[156:159]
s_add_u32 s96, s96, 0x400
v_mfma_f32_16x16x32_bf16 a[156:159], a[68:71], a[12:15], a[156:159]
v_mfma_f32_16x16x32_bf16 a[160:163], a[64:67], a[16:19], a[160:163]
ds_read_b64_tr_b16 a[74:75], v14 offset:704
v_mfma_f32_16x16x32_bf16 a[160:163], a[68:71], a[20:23], a[160:163]
v_mfma_f32_16x16x32_bf16 a[164:167], a[64:67], a[24:27], a[164:167]
v_mfma_f32_16x16x32_bf16 a[164:167], a[68:71], a[28:31], a[164:167]
ds_read_b64_tr_b16 a[76:77], v14 offset:8640
v_mfma_f32_16x16x32_bf16 a[168:171], a[64:67], a[32:35], a[168:171]
v_mfma_f32_16x16x32_bf16 a[168:171], a[68:71], a[36:39], a[168:171]
v_mfma_f32_16x16x32_bf16 a[172:175], a[64:67], a[40:43], a[172:175]
ds_read_b64_tr_b16 a[78:79], v14 offset:9152
v_mfma_f32_16x16x32_bf16 a[172:175], a[68:71], a[44:47], a[172:175]
v_mfma_f32_16x16x32_bf16 a[176:179], a[64:67], a[48:51], a[176:179]
v_mfma_f32_16x16x32_bf16 a[176:179], a[68:71], a[52:55], a[176:179]
v_mfma_f32_16x16x32_bf16 a[180:183], a[64:67], a[56:59], a[180:183]
v_mfma_f32_16x16x32_bf16 a[180:183], a[68:71], a[60:63], a[180:183]
s_waitcnt lgkmcnt(1)
v_mfma_f32_16x16x32_bf16 a[184:187], a[72:75], a[0:3], a[184:187]
s_waitcnt lgkmcnt(0)
v_mfma_f32_16x16x32_bf16 a[184:187], a[76:79], a[4:7], a[184:187]
ds_read_b64_tr_b16 a[64:65], v14 offset:224
v_mfma_f32_16x16x32_bf16 a[188:191], a[72:75], a[8:11], a[188:191]
ds_read_b64_tr_b16 a[66:67], v14 offset:736
v_mfma_f32_16x16x32_bf16 a[188:191], a[76:79], a[12:15], a[188:191]
ds_read_b64_tr_b16 a[68:69], v14 offset:8672
v_mfma_f32_16x16x32_bf16 a[192:195], a[72:75], a[16:19], a[192:195]
ds_read_b64_tr_b16 a[70:71], v14 offset:9184
v_mfma_f32_16x16x32_bf16 a[192:195], a[76:79], a[20:23], a[192:195]
v_mfma_f32_16x16x32_bf16 a[196:199], a[72:75], a[24:27], a[196:199]
v_mfma_f32_16x16x32_bf16 a[196:199], a[76:79], a[28:31], a[196:199]
v_mfma_f32_16x16x32_bf16 a[200:203], a[72:75], a[32:35], a[200:203]
v_mfma_f32_16x16x32_bf16 a[200:203], a[76:79], a[36:39], a[200:203]
v_mfma_f32_16x16x32_bf16 a[204:207], a[72:75], a[40:43], a[204:207]
v_mfma_f32_16x16x32_bf16 a[204:207], a[76:79], a[44:47], a[204:207]
v_mfma_f32_16x16x32_bf16 a[208:211], a[72:75], a[48:51], a[208:211]
v_mfma_f32_16x16x32_bf16 a[208:211], a[76:79], a[52:55], a[208:211]
v_mfma_f32_16x16x32_bf16 a[212:215], a[72:75], a[56:59], a[212:215]
v_mfma_f32_16x16x32_bf16 a[212:215], a[76:79], a[60:63], a[212:215]
s_waitcnt vmcnt(2) lgkmcnt(0)
s_barrier
v_mfma_f32_16x16x32_bf16 a[216:219], a[64:67], a[0:3], a[216:219]
ds_read_b64_tr_b16 a[72:73], v17
ds_read_b128 a[0:3], v13
v_mfma_f32_16x16x32_bf16 a[216:219], a[68:71], a[4:7], a[216:219]
ds_read_b64_tr_b16 a[74:75], v17 offset:3584
ds_read_b128 a[4:7], v13 offset:1024
v_mfma_f32_16x16x32_bf16 a[220:223], a[64:67], a[8:11], a[220:223]
ds_read_b64_tr_b16 a[76:77], v17 offset:7168
ds_read_b128 a[8:11], v13 offset:2048
v_mfma_f32_16x16x32_bf16 a[220:223], a[68:71], a[12:15], a[220:223]
ds_read_b64_tr_b16 a[78:79], v17 offset:10752
ds_read_b128 a[12:15], v13 offset:3072
v_mfma_f32_16x16x32_bf16 a[224:227], a[64:67], a[16:19], a[224:227]
ds_read_b128 a[16:19], v13 offset:4096
v_mfma_f32_16x16x32_bf16 a[224:227], a[68:71], a[20:23], a[224:227]
ds_read_b128 a[20:23], v13 offset:5120
v_mfma_f32_16x16x32_bf16 a[228:231], a[64:67], a[24:27], a[228:231]
ds_read_b128 a[24:27], v13 offset:6144
v_mfma_f32_16x16x32_bf16 a[228:231], a[68:71], a[28:31], a[228:231]
ds_read_b128 a[28:31], v13 offset:7168
v_mfma_f32_16x16x32_bf16 a[232:235], a[64:67], a[32:35], a[232:235]
ds_read_b128 a[32:35], v13 offset:8192
v_mfma_f32_16x16x32_bf16 a[232:235], a[68:71], a[36:39], a[232:235]
ds_read_b128 a[36:39], v13 offset:9216
v_mfma_f32_16x16x32_bf16 a[236:239], a[64:67], a[40:43], a[236:239]
ds_read_b128 a[40:43], v13 offset:10240
v_mfma_f32_16x16x32_bf16 a[236:239], a[68:71], a[44:47], a[236:239]
ds_read_b128 a[44:47], v13 offset:11264
v_mfma_f32_16x16x32_bf16 a[240:243], a[64:67], a[48:51], a[240:243]
ds_read_b128 a[48:51], v13 offset:12288
v_mfma_f32_16x16x32_bf16 a[240:243], a[68:71], a[52:55], a[240:243]
ds_read_b128 a[52:55], v13 offset:13312
v_mfma_f32_16x16x32_bf16 a[244:247], a[64:67], a[56:59], a[244:247]
ds_read_b128 a[56:59], v13 offset:14336
v_mfma_f32_16x16x32_bf16 a[244:247], a[68:71], a[60:63], a[244:247]
ds_read_b128 a[60:63], v13 offset:15360
s_waitcnt lgkmcnt(6)
v_mfma_f32_16x16x32_bf16 v[24:27], a[72:75], a[0:3], v[24:27]
v_mfma_f32_16x16x32_bf16 v[24:27], a[76:79], a[4:7], v[24:27]
ds_read_b64_tr_b16 a[64:65], v17 offset:14336
s_add_u32 m0, s29, 0
v_mfma_f32_16x16x32_bf16 v[28:31], a[72:75], a[8:11], v[28:31]
buffer_load_dwordx4 v1, s[12:15], s25 offen lds
v_mfma_f32_16x16x32_bf16 v[28:31], a[76:79], a[12:15], v[28:31]
v_mfma_f32_16x16x32_bf16 v[32:35], a[72:75], a[16:19], v[32:35]
ds_read_b64_tr_b16 a[66:67], v17 offset:17920
v_mfma_f32_16x16x32_bf16 v[32:35], a[76:79], a[20:23], v[32:35]
v_mfma_f32_16x16x32_bf16 v[36:39], a[72:75], a[24:27], v[36:39]
v_mfma_f32_16x16x32_bf16 v[36:39], a[76:79], a[28:31], v[36:39]
ds_read_b64_tr_b16 a[68:69], v17 offset:21504
v_mfma_f32_16x16x32_bf16 v[40:43], a[72:75], a[32:35], v[40:43]
buffer_load_dwordx4 v2, s[12:15], s25 offen offset:1056 lds
v_mfma_f32_16x16x32_bf16 v[40:43], a[76:79], a[36:39], v[40:43]
s_waitcnt lgkmcnt(2)
v_mfma_f32_16x16x32_bf16 v[44:47], a[72:75], a[40:43], v[44:47]
ds_read_b64_tr_b16 a[70:71], v17 offset:25088
v_mfma_f32_16x16x32_bf16 v[44:47], a[76:79], a[44:47], v[44:47]
v_mfma_f32_16x16x32_bf16 v[48:51], a[72:75], a[48:51], v[48:51]
v_mfma_f32_16x16x32_bf16 v[48:51], a[76:79], a[52:55], v[48:51]
v_mfma_f32_16x16x32_bf16 v[52:55], a[72:75], a[56:59], v[52:55]
buffer_load_dwordx4 v3, s[12:15], s25 offen offset:2112 lds
v_mfma_f32_16x16x32_bf16 v[52:55], a[76:79], a[60:63], v[52:55]
s_waitcnt lgkmcnt(1)
v_mfma_f32_16x16x32_bf16 v[56:59], a[64:67], a[0:3], v[56:59]
s_waitcnt lgkmcnt(0)
v_mfma_f32_16x16x32_bf16 v[56:59], a[68:71], a[4:7], v[56:59]
ds_read_b64_tr_b16 a[72:73], v17 offset:32
v_mfma_f32_16x16x32_bf16 v[60:63], a[64:67], a[8:11], v[60:63]
v_mfma_f32_16x16x32_bf16 v[60:63], a[68:71], a[12:15], v[60:63]
v_mfma_f32_16x16x32_bf16 v[64:67], a[64:67], a[16:19], v[64:67]
ds_read_b64_tr_b16 a[74:75], v17 offset:3616
buffer_load_dwordx4 v4, s[12:15], s25 offen offset:3168 lds
v_mfma_f32_16x16x32_bf16 v[64:67], a[68:71], a[20:23], v[64:67]
v_mfma_f32_16x16x32_bf16 v[68:71], a[64:67], a[24:27], v[68:71]
v_mfma_f32_16x16x32_bf16 v[68:71], a[68:71], a[28:31], v[68:71]
ds_read_b64_tr_b16 a[76:77], v17 offset:7200
v_mfma_f32_16x16x32_bf16 v[72:75], a[64:67], a[32:35], v[72:75]
v_mfma_f32_16x16x32_bf16 v[72:75], a[68:71], a[36:39], v[72:75]
s_add_u32 m0, s30, 0
v_mfma_f32_16x16x32_bf16 v[76:79], a[64:67], a[40:43], v[76:79]
ds_read_b64_tr_b16 a[78:79], v17 offset:10784
buffer_load_dwordx4 v5, s[16:19], s26 offen lds
v_mfma_f32_16x16x32_bf16 v[76:79], a[68:71], a[44:47], v[76:79]
v_mfma_f32_16x16x32_bf16 v[80:83], a[64:67], a[48:51], v[80:83]
v_mfma_f32_16x16x32_bf16 v[80:83], a[68:71], a[52:55], v[80:83]
v_mfma_f32_16x16x32_bf16 v[84:87], a[64:67], a[56:59], v[84:87]
v_mfma_f32_16x16x32_bf16 v[84:87], a[68:71], a[60:63], v[84:87]
s_waitcnt lgkmcnt(1)
v_mfma_f32_16x16x32_bf16 v[88:91], a[72:75], a[0:3], v[88:91]
buffer_load_dwordx4 v6, s[16:19], s26 offen offset:1024 lds
s_waitcnt lgkmcnt(0)
v_mfma_f32_16x16x32_bf16 v[88:91], a[76:79], a[4:7], v[88:91]
ds_read_b64_tr_b16 a[64:65], v17 offset:14368
v_mfma_f32_16x16x32_bf16 v[92:95], a[72:75], a[8:11], v[92:95]
v_mfma_f32_16x16x32_bf16 v[92:95], a[76:79], a[12:15], v[92:95]
v_mfma_f32_16x16x32_bf16 v[96:99], a[72:75], a[16:19], v[96:99]
ds_read_b64_tr_b16 a[66:67], v17 offset:17952
v_mfma_f32_16x16x32_bf16 v[96:99], a[76:79], a[20:23], v[96:99]
v_mfma_f32_16x16x32_bf16 v[100:103], a[72:75], a[24:27], v[100:103]
buffer_load_dwordx4 v7, s[16:19], s26 offen offset:2048 lds
v_mfma_f32_16x16x32_bf16 v[100:103], a[76:79], a[28:31], v[100:103]
ds_read_b64_tr_b16 a[68:69], v17 offset:21536
v_mfma_f32_16x16x32_bf16 v[104:107], a[72:75], a[32:35], v[104:107]
v_mfma_f32_16x16x32_bf16 v[104:107], a[76:79], a[36:39], v[104:107]
v_mfma_f32_16x16x32_bf16 v[108:111], a[72:75], a[40:43], v[108:111]
ds_read_b64_tr_b16 a[70:71], v17 offset:25120
v_mfma_f32_16x16x32_bf16 v[108:111], a[76:79], a[44:47], v[108:111]
v_mfma_f32_16x16x32_bf16 v[112:115], a[72:75], a[48:51], v[112:115]
buffer_load_dwordx4 v8, s[16:19], s26 offen offset:3072 lds
v_mfma_f32_16x16x32_bf16 v[112:115], a[76:79], a[52:55], v[112:115]
v_mfma_f32_16x16x32_bf16 v[116:119], a[72:75], a[56:59], v[116:119]
v_mfma_f32_16x16x32_bf16 v[116:119], a[76:79], a[60:63], v[116:119]
s_waitcnt lgkmcnt(1)
v_mfma_f32_16x16x32_bf16 v[120:123], a[64:67], a[0:3], v[120:123]
s_waitcnt lgkmcnt(0)
v_mfma_f32_16x16x32_bf16 v[120:123], a[68:71], a[4:7], v[120:123]
ds_read_b64_tr_b16 a[72:73], v19
s_add_u32 m0, s30, 0x1000
v_mfma_f32_16x16x32_bf16 v[124:127], a[64:67], a[8:11], v[124:127]
buffer_load_dwordx4 v9, s[16:19], s26 offen lds
v_mfma_f32_16x16x32_bf16 v[124:127], a[68:71], a[12:15], v[124:127]
v_mfma_f32_16x16x32_bf16 v[128:131], a[64:67], a[16:19], v[128:131]
ds_read_b64_tr_b16 a[74:75], v19 offset:3584
v_mfma_f32_16x16x32_bf16 v[128:131], a[68:71], a[20:23], v[128:131]
v_mfma_f32_16x16x32_bf16 v[132:135], a[64:67], a[24:27], v[132:135]
v_mfma_f32_16x16x32_bf16 v[132:135], a[68:71], a[28:31], v[132:135]
ds_read_b64_tr_b16 a[76:77], v19 offset:7168
v_mfma_f32_16x16x32_bf16 v[136:139], a[64:67], a[32:35], v[136:139]
buffer_load_dwordx4 v10, s[16:19], s26 offen offset:1024 lds
v_mfma_f32_16x16x32_bf16 v[136:139], a[68:71], a[36:39], v[136:139]
v_mfma_f32_16x16x32_bf16 v[140:143], a[64:67], a[40:43], v[140:143]
ds_read_b64_tr_b16 a[78:79], v19 offset:10752
v_mfma_f32_16x16x32_bf16 v[140:143], a[68:71], a[44:47], v[140:143]
v_mfma_f32_16x16x32_bf16 v[144:147], a[64:67], a[48:51], v[144:147]
v_mfma_f32_16x16x32_bf16 v[144:147], a[68:71], a[52:55], v[144:147]
v_mfma_f32_16x16x32_bf16 v[148:151], a[64:67], a[56:59], v[148:151]
buffer_load_dwordx4 v11, s[16:19], s26 offen offset:2048 lds
v_mfma_f32_16x16x32_bf16 v[148:151], a[68:71], a[60:63], v[148:151]
s_waitcnt lgkmcnt(1)
v_mfma_f32_16x16x32_bf16 v[152:155], a[72:75], a[0:3], v[152:155]
s_waitcnt lgkmcnt(0)
v_mfma_f32_16x16x32_bf16 v[152:155], a[76:79], a[4:7], v[152:155]
ds_read_b64_tr_b16 a[64:65], v19 offset:14336
v_mfma_f32_16x16x32_bf16 v[156:159], a[72:75], a[8:11], v[156:159]
v_mfma_f32_16x16x32_bf16 v[156:159], a[76:79], a[12:15], v[156:159]
s_add_u32 m0, s28, 0
v_mfma_f32_16x16x32_bf16 v[160:163], a[72:75], a[16:19], v[160:163]
ds_read_b64_tr_b16 a[66:67], v19 offset:17920
buffer_load_dwordx4 v0, s[8:11], s24 offen sc1 lds
v_mfma_f32_16x16x32_bf16 v[160:163], a[76:79], a[20:23], v[160:163]
v_mfma_f32_16x16x32_bf16 v[164:167], a[72:75], a[24:27], v[164:167]
v_mfma_f32_16x16x32_bf16 v[164:167], a[76:79], a[28:31], v[164:167]
ds_read_b64_tr_b16 a[68:69], v19 offset:21504
v_mfma_f32_16x16x32_bf16 v[168:171], a[72:75], a[32:35], v[168:171]
v_mfma_f32_16x16x32_bf16 v[168:171], a[76:79], a[36:39], v[168:171]
v_mfma_f32_16x16x32_bf16 v[172:175], a[72:75], a[40:43], v[172:175]
ds_read_b64_tr_b16 a[70:71], v19 offset:25088
buffer_load_dwordx4 v0, s[8:11], s24 offen offset:1024 sc1 lds
v_mfma_f32_16x16x32_bf16 v[172:175], a[76:79], a[44:47], v[172:175]
v_mfma_f32_16x16x32_bf16 v[176:179], a[72:75], a[48:51], v[176:179]
v_mfma_f32_16x16x32_bf16 v[176:179], a[76:79], a[52:55], v[176:179]
v_mfma_f32_16x16x32_bf16 v[180:183], a[72:75], a[56:59], v[180:183]
v_mfma_f32_16x16x32_bf16 v[180:183], a[76:79], a[60:63], v[180:183]
s_waitcnt lgkmcnt(1)
v_mfma_f32_16x16x32_bf16 v[184:187], a[64:67], a[0:3], v[184:187]
buffer_load_dwordx4 v0, s[8:11], s24 offen offset:2048 sc1 lds
s_waitcnt lgkmcnt(0)
v_mfma_f32_16x16x32_bf16 v[184:187], a[68:71], a[4:7], v[184:187]
ds_read_b64_tr_b16 a[72:73], v15 offset:96
v_mfma_f32_16x16x32_bf16 v[188:191], a[64:67], a[8:11], v[188:191]
v_mfma_f32_16x16x32_bf16 v[188:191], a[68:71], a[12:15], v[188:191]
v_mfma_f32_16x16x32_bf16 v[192:195], a[64:67], a[16:19], v[192:195]
ds_read_b64_tr_b16 a[74:75], v15 offset:608
v_mfma_f32_16x16x32_bf16 v[192:195], a[68:71], a[20:23], v[192:195]
v_mfma_f32_16x16x32_bf16 v[196:199], a[64:67], a[24:27], v[196:199]
buffer_load_dwordx4 v0, s[8:11], s24 offen offset:3072 sc1 lds
v_mfma_f32_16x16x32_bf16 v[196:199], a[68:71], a[28:31], v[196:199]
ds_read_b64_tr_b16 a[76:77], v15 offset:8544
v_mfma_f32_16x16x32_bf16 v[200:203], a[64:67], a[32:35], v[200:203]
v_mfma_f32_16x16x32_bf16 v[200:203], a[68:71], a[36:39], v[200:203]
v_mfma_f32_16x16x32_bf16 v[204:207], a[64:67], a[40:43], v[204:207]
ds_read_b64_tr_b16 a[78:79], v15 offset:9056
v_mfma_f32_16x16x32_bf16 v[204:207], a[68:71], a[44:47], v[204:207]
s_add_u32 m0, s28, 0x1000
s_add_u32 s34, s24, 0x1000
v_mfma_f32_16x16x32_bf16 v[208:211], a[64:67], a[48:51], v[208:211]
buffer_load_dwordx4 v0, s[8:11], s34 offen sc1 lds
v_mfma_f32_16x16x32_bf16 v[208:211], a[68:71], a[52:55], v[208:211]
v_mfma_f32_16x16x32_bf16 v[212:215], a[64:67], a[56:59], v[212:215]
v_mfma_f32_16x16x32_bf16 v[212:215], a[68:71], a[60:63], v[212:215]
s_waitcnt lgkmcnt(1)
v_mfma_f32_16x16x32_bf16 a[88:91], a[72:75], a[0:3], a[88:91]
s_waitcnt lgkmcnt(0)
v_mfma_f32_16x16x32_bf16 a[88:91], a[76:79], a[4:7], a[88:91]
ds_read_b64_tr_b16 a[64:65], v15 offset:128
v_mfma_f32_16x16x32_bf16 a[92:95], a[72:75], a[8:11], a[92:95]
buffer_load_dwordx4 v0, s[8:11], s34 offen offset:1024 sc1 lds
v_mfma_f32_16x16x32_bf16 a[92:95], a[76:79], a[12:15], a[92:95]
v_mfma_f32_16x16x32_bf16 a[96:99], a[72:75], a[16:19], a[96:99]
ds_read_b64_tr_b16 a[66:67], v15 offset:640
v_mfma_f32_16x16x32_bf16 a[96:99], a[76:79], a[20:23], a[96:99]
v_mfma_f32_16x16x32_bf16 a[100:103], a[72:75], a[24:27], a[100:103]
v_mfma_f32_16x16x32_bf16 a[100:103], a[76:79], a[28:31], a[100:103]
ds_read_b64_tr_b16 a[68:69], v15 offset:8576
v_mfma_f32_16x16x32_bf16 a[104:107], a[72:75], a[32:35], a[104:107]
buffer_load_dwordx4 v0, s[8:11], s34 offen offset:2048 sc1 lds
v_mfma_f32_16x16x32_bf16 a[104:107], a[76:79], a[36:39], a[104:107]
v_mfma_f32_16x16x32_bf16 a[108:111], a[72:75], a[40:43], a[108:111]
ds_read_b64_tr_b16 a[70:71], v15 offset:9088
v_mfma_f32_16x16x32_bf16 a[108:111], a[76:79], a[44:47], a[108:111]
v_mfma_f32_16x16x32_bf16 a[112:115], a[72:75], a[48:51], a[112:115]
v_mfma_f32_16x16x32_bf16 a[112:115], a[76:79], a[52:55], a[112:115]
v_mfma_f32_16x16x32_bf16 a[116:119], a[72:75], a[56:59], a[116:119]
buffer_load_dwordx4 v0, s[8:11], s34 offen offset:3072 sc1 lds
s_add_u32 s24, s24, 0x8000
s_add_u32 s25, s25, 0x80000
s_add_u32 s26, s26, 0x70000
v_mfma_f32_16x16x32_bf16 a[116:119], a[76:79], a[60:63], a[116:119]
s_waitcnt lgkmcnt(1)
v_mfma_f32_16x16x32_bf16 a[120:123], a[64:67], a[0:3], a[120:123]
s_waitcnt lgkmcnt(0)
v_mfma_f32_16x16x32_bf16 a[120:123], a[68:71], a[4:7], a[120:123]
ds_read_b64_tr_b16 a[72:73], v15 offset:160
v_mfma_f32_16x16x32_bf16 a[124:127], a[64:67], a[8:11], a[124:127]
v_mfma_f32_16x16x32_bf16 a[124:127], a[68:71], a[12:15], a[124:127]
v_mfma_f32_16x16x32_bf16 a[128:131], a[64:67], a[16:19], a[128:131]
ds_read_b64_tr_b16 a[74:75], v15 offset:672
v_mfma_f32_16x16x32_bf16 a[128:131], a[68:71], a[20:23], a[128:131]
v_mfma_f32_16x16x32_bf16 a[132:135], a[64:67], a[24:27], a[132:135]
global_load_dword v255, v[252:253], off
v_mfma_f32_16x16x32_bf16 a[132:135], a[68:71], a[28:31], a[132:135]
ds_read_b64_tr_b16 a[76:77], v15 offset:8608
v_mfma_f32_16x16x32_bf16 a[136:139], a[64:67], a[32:35], a[136:139]
v_mfma_f32_16x16x32_bf16 a[136:139], a[68:71], a[36:39], a[136:139]
v_add_co_u32 v252, vcc, v254, v252
)ASM"
R"ASM(v_mfma_f32_16x16x32_bf16 a[140:143], a[64:67], a[40:43], a[140:143]
ds_read_b64_tr_b16 a[78:79], v15 offset:9120
v_mfma_f32_16x16x32_bf16 a[140:143], a[68:71], a[44:47], a[140:143]
v_mfma_f32_16x16x32_bf16 a[144:147], a[64:67], a[48:51], a[144:147]
v_addc_co_u32 v253, vcc, 0, v253, vcc
v_mfma_f32_16x16x32_bf16 a[144:147], a[68:71], a[52:55], a[144:147]
v_mfma_f32_16x16x32_bf16 a[148:151], a[64:67], a[56:59], a[148:151]
v_mfma_f32_16x16x32_bf16 a[148:151], a[68:71], a[60:63], a[148:151]
buffer_store_dwordx4 v[248:251], v0, s[76:79], s96 offen sc1
s_waitcnt lgkmcnt(1)
v_mfma_f32_16x16x32_bf16 a[152:155], a[72:75], a[0:3], a[152:155]
s_waitcnt lgkmcnt(0)
v_mfma_f32_16x16x32_bf16 a[152:155], a[76:79], a[4:7], a[152:155]
ds_read_b64_tr_b16 a[64:65], v15 offset:192
v_mfma_f32_16x16x32_bf16 a[156:159], a[72:75], a[8:11], a[156:159]
s_add_u32 s96, s96, 0x400
v_mfma_f32_16x16x32_bf16 a[156:159], a[76:79], a[12:15], a[156:159]
v_mfma_f32_16x16x32_bf16 a[160:163], a[72:75], a[16:19], a[160:163]
ds_read_b64_tr_b16 a[66:67], v15 offset:704
v_mfma_f32_16x16x32_bf16 a[160:163], a[76:79], a[20:23], a[160:163]
v_mfma_f32_16x16x32_bf16 a[164:167], a[72:75], a[24:27], a[164:167]
v_mfma_f32_16x16x32_bf16 a[164:167], a[76:79], a[28:31], a[164:167]
ds_read_b64_tr_b16 a[68:69], v15 offset:8640
v_mfma_f32_16x16x32_bf16 a[168:171], a[72:75], a[32:35], a[168:171]
v_mfma_f32_16x16x32_bf16 a[168:171], a[76:79], a[36:39], a[168:171]
v_mfma_f32_16x16x32_bf16 a[172:175], a[72:75], a[40:43], a[172:175]
ds_read_b64_tr_b16 a[70:71], v15 offset:9152
v_mfma_f32_16x16x32_bf16 a[172:175], a[76:79], a[44:47], a[172:175]
v_mfma_f32_16x16x32_bf16 a[176:179], a[72:75], a[48:51], a[176:179]
v_mfma_f32_16x16x32_bf16 a[176:179], a[76:79], a[52:55], a[176:179]
v_mfma_f32_16x16x32_bf16 a[180:183], a[72:75], a[56:59], a[180:183]
v_mfma_f32_16x16x32_bf16 a[180:183], a[76:79], a[60:63], a[180:183]
s_waitcnt lgkmcnt(1)
v_mfma_f32_16x16x32_bf16 a[184:187], a[64:67], a[0:3], a[184:187]
s_waitcnt lgkmcnt(0)
v_mfma_f32_16x16x32_bf16 a[184:187], a[68:71], a[4:7], a[184:187]
ds_read_b64_tr_b16 a[72:73], v15 offset:224
v_mfma_f32_16x16x32_bf16 a[188:191], a[64:67], a[8:11], a[188:191]
ds_read_b64_tr_b16 a[74:75], v15 offset:736
v_mfma_f32_16x16x32_bf16 a[188:191], a[68:71], a[12:15], a[188:191]
ds_read_b64_tr_b16 a[76:77], v15 offset:8672
v_mfma_f32_16x16x32_bf16 a[192:195], a[64:67], a[16:19], a[192:195]
ds_read_b64_tr_b16 a[78:79], v15 offset:9184
v_mfma_f32_16x16x32_bf16 a[192:195], a[68:71], a[20:23], a[192:195]
v_mfma_f32_16x16x32_bf16 a[196:199], a[64:67], a[24:27], a[196:199]
v_mfma_f32_16x16x32_bf16 a[196:199], a[68:71], a[28:31], a[196:199]
v_mfma_f32_16x16x32_bf16 a[200:203], a[64:67], a[32:35], a[200:203]
v_mfma_f32_16x16x32_bf16 a[200:203], a[68:71], a[36:39], a[200:203]
v_mfma_f32_16x16x32_bf16 a[204:207], a[64:67], a[40:43], a[204:207]
v_mfma_f32_16x16x32_bf16 a[204:207], a[68:71], a[44:47], a[204:207]
v_mfma_f32_16x16x32_bf16 a[208:211], a[64:67], a[48:51], a[208:211]
v_mfma_f32_16x16x32_bf16 a[208:211], a[68:71], a[52:55], a[208:211]
v_mfma_f32_16x16x32_bf16 a[212:215], a[64:67], a[56:59], a[212:215]
v_mfma_f32_16x16x32_bf16 a[212:215], a[68:71], a[60:63], a[212:215]
s_waitcnt vmcnt(2) lgkmcnt(0)
s_barrier
v_mfma_f32_16x16x32_bf16 a[216:219], a[72:75], a[0:3], a[216:219]
ds_read_b64_tr_b16 a[64:65], v16
ds_read_b128 a[0:3], v12
v_mfma_f32_16x16x32_bf16 a[216:219], a[76:79], a[4:7], a[216:219]
ds_read_b64_tr_b16 a[66:67], v16 offset:3584
ds_read_b128 a[4:7], v12 offset:1024
v_mfma_f32_16x16x32_bf16 a[220:223], a[72:75], a[8:11], a[220:223]
ds_read_b64_tr_b16 a[68:69], v16 offset:7168
ds_read_b128 a[8:11], v12 offset:2048
v_mfma_f32_16x16x32_bf16 a[220:223], a[76:79], a[12:15], a[220:223]
ds_read_b64_tr_b16 a[70:71], v16 offset:10752
ds_read_b128 a[12:15], v12 offset:3072
v_mfma_f32_16x16x32_bf16 a[224:227], a[72:75], a[16:19], a[224:227]
ds_read_b128 a[16:19], v12 offset:4096
v_mfma_f32_16x16x32_bf16 a[224:227], a[76:79], a[20:23], a[224:227]
ds_read_b128 a[20:23], v12 offset:5120
v_mfma_f32_16x16x32_bf16 a[228:231], a[72:75], a[24:27], a[228:231]
ds_read_b128 a[24:27], v12 offset:6144
v_mfma_f32_16x16x32_bf16 a[228:231], a[76:79], a[28:31], a[228:231]
ds_read_b128 a[28:31], v12 offset:7168
v_mfma_f32_16x16x32_bf16 a[232:235], a[72:75], a[32:35], a[232:235]
ds_read_b128 a[32:35], v12 offset:8192
v_mfma_f32_16x16x32_bf16 a[232:235], a[76:79], a[36:39], a[232:235]
ds_read_b128 a[36:39], v12 offset:9216
v_mfma_f32_16x16x32_bf16 a[236:239], a[72:75], a[40:43], a[236:239]
ds_read_b128 a[40:43], v12 offset:10240
v_mfma_f32_16x16x32_bf16 a[236:239], a[76:79], a[44:47], a[236:239]
ds_read_b128 a[44:47], v12 offset:11264
v_mfma_f32_16x16x32_bf16 a[240:243], a[72:75], a[48:51], a[240:243]
ds_read_b128 a[48:51], v12 offset:12288
v_mfma_f32_16x16x32_bf16 a[240:243], a[76:79], a[52:55], a[240:243]
ds_read_b128 a[52:55], v12 offset:13312
v_mfma_f32_16x16x32_bf16 a[244:247], a[72:75], a[56:59], a[244:247]
ds_read_b128 a[56:59], v12 offset:14336
v_mfma_f32_16x16x32_bf16 a[244:247], a[76:79], a[60:63], a[244:247]
ds_read_b128 a[60:63], v12 offset:15360
s_sub_u32 s70, s70, 1
s_cmp_lg_u32 s70, 0
s_cbranch_scc1 G1B_%=
s_waitcnt lgkmcnt(0)
v_mfma_f32_16x16x32_bf16 v[24:27], a[64:67], a[0:3], v[24:27]
v_mfma_f32_16x16x32_bf16 v[24:27], a[68:71], a[4:7], v[24:27]
ds_read_b64_tr_b16 a[72:73], v16 offset:14336
s_add_u32 m0, s29, 0x13200
v_mfma_f32_16x16x32_bf16 v[28:31], a[64:67], a[8:11], v[28:31]
buffer_load_dwordx4 v1, s[12:15], s25 offen lds
v_mfma_f32_16x16x32_bf16 v[28:31], a[68:71], a[12:15], v[28:31]
v_mfma_f32_16x16x32_bf16 v[32:35], a[64:67], a[16:19], v[32:35]
ds_read_b64_tr_b16 a[74:75], v16 offset:17920
v_mfma_f32_16x16x32_bf16 v[32:35], a[68:71], a[20:23], v[32:35]
v_mfma_f32_16x16x32_bf16 v[36:39], a[64:67], a[24:27], v[36:39]
v_mfma_f32_16x16x32_bf16 v[36:39], a[68:71], a[28:31], v[36:39]
ds_read_b64_tr_b16 a[76:77], v16 offset:21504
v_mfma_f32_16x16x32_bf16 v[40:43], a[64:67], a[32:35], v[40:43]
buffer_load_dwordx4 v2, s[12:15], s25 offen offset:1056 lds
v_mfma_f32_16x16x32_bf16 v[40:43], a[68:71], a[36:39], v[40:43]
v_mfma_f32_16x16x32_bf16 v[44:47], a[64:67], a[40:43], v[44:47]
ds_read_b64_tr_b16 a[78:79], v16 offset:25088
v_mfma_f32_16x16x32_bf16 v[44:47], a[68:71], a[44:47], v[44:47]
v_mfma_f32_16x16x32_bf16 v[48:51], a[64:67], a[48:51], v[48:51]
v_mfma_f32_16x16x32_bf16 v[48:51], a[68:71], a[52:55], v[48:51]
v_mfma_f32_16x16x32_bf16 v[52:55], a[64:67], a[56:59], v[52:55]
buffer_load_dwordx4 v3, s[12:15], s25 offen offset:2112 lds
v_mfma_f32_16x16x32_bf16 v[52:55], a[68:71], a[60:63], v[52:55]
s_waitcnt lgkmcnt(1)
v_mfma_f32_16x16x32_bf16 v[56:59], a[72:75], a[0:3], v[56:59]
s_waitcnt lgkmcnt(0)
v_mfma_f32_16x16x32_bf16 v[56:59], a[76:79], a[4:7], v[56:59]
ds_read_b64_tr_b16 a[64:65], v16 offset:32
v_mfma_f32_16x16x32_bf16 v[60:63], a[72:75], a[8:11], v[60:63]
v_mfma_f32_16x16x32_bf16 v[60:63], a[76:79], a[12:15], v[60:63]
v_mfma_f32_16x16x32_bf16 v[64:67], a[72:75], a[16:19], v[64:67]
ds_read_b64_tr_b16 a[66:67], v16 offset:3616
buffer_load_dwordx4 v4, s[12:15], s25 offen offset:3168 lds
v_mfma_f32_16x16x32_bf16 v[64:67], a[76:79], a[20:23], v[64:67]
v_mfma_f32_16x16x32_bf16 v[68:71], a[72:75], a[24:27], v[68:71]
v_mfma_f32_16x16x32_bf16 v[68:71], a[76:79], a[28:31], v[68:71]
ds_read_b64_tr_b16 a[68:69], v16 offset:7200
v_mfma_f32_16x16x32_bf16 v[72:75], a[72:75], a[32:35], v[72:75]
v_mfma_f32_16x16x32_bf16 v[72:75], a[76:79], a[36:39], v[72:75]
s_add_u32 m0, s30, 0x13200
v_mfma_f32_16x16x32_bf16 v[76:79], a[72:75], a[40:43], v[76:79]
ds_read_b64_tr_b16 a[70:71], v16 offset:10784
buffer_load_dwordx4 v5, s[16:19], s26 offen lds
v_mfma_f32_16x16x32_bf16 v[76:79], a[76:79], a[44:47], v[76:79]
v_mfma_f32_16x16x32_bf16 v[80:83], a[72:75], a[48:51], v[80:83]
v_mfma_f32_16x16x32_bf16 v[80:83], a[76:79], a[52:55], v[80:83]
v_mfma_f32_16x16x32_bf16 v[84:87], a[72:75], a[56:59], v[84:87]
v_mfma_f32_16x16x32_bf16 v[84:87], a[76:79], a[60:63], v[84:87]
s_waitcnt lgkmcnt(1)
v_mfma_f32_16x16x32_bf16 v[88:91], a[64:67], a[0:3], v[88:91]
buffer_load_dwordx4 v6, s[16:19], s26 offen offset:1024 lds
s_waitcnt lgkmcnt(0)
v_mfma_f32_16x16x32_bf16 v[88:91], a[68:71], a[4:7], v[88:91]
ds_read_b64_tr_b16 a[72:73], v16 offset:14368
v_mfma_f32_16x16x32_bf16 v[92:95], a[64:67], a[8:11], v[92:95]
v_mfma_f32_16x16x32_bf16 v[92:95], a[68:71], a[12:15], v[92:95]
v_mfma_f32_16x16x32_bf16 v[96:99], a[64:67], a[16:19], v[96:99]
ds_read_b64_tr_b16 a[74:75], v16 offset:17952
v_mfma_f32_16x16x32_bf16 v[96:99], a[68:71], a[20:23], v[96:99]
v_mfma_f32_16x16x32_bf16 v[100:103], a[64:67], a[24:27], v[100:103]
buffer_load_dwordx4 v7, s[16:19], s26 offen offset:2048 lds
v_mfma_f32_16x16x32_bf16 v[100:103], a[68:71], a[28:31], v[100:103]
ds_read_b64_tr_b16 a[76:77], v16 offset:21536
v_mfma_f32_16x16x32_bf16 v[104:107], a[64:67], a[32:35], v[104:107]
v_mfma_f32_16x16x32_bf16 v[104:107], a[68:71], a[36:39], v[104:107]
v_mfma_f32_16x16x32_bf16 v[108:111], a[64:67], a[40:43], v[108:111]
ds_read_b64_tr_b16 a[78:79], v16 offset:25120
v_mfma_f32_16x16x32_bf16 v[108:111], a[68:71], a[44:47], v[108:111]
v_mfma_f32_16x16x32_bf16 v[112:115], a[64:67], a[48:51], v[112:115]
buffer_load_dwordx4 v8, s[16:19], s26 offen offset:3072 lds
v_mfma_f32_16x16x32_bf16 v[112:115], a[68:71], a[52:55], v[112:115]
v_mfma_f32_16x16x32_bf16 v[116:119], a[64:67], a[56:59], v[116:119]
v_mfma_f32_16x16x32_bf16 v[116:119], a[68:71], a[60:63], v[116:119]
s_waitcnt lgkmcnt(1)
v_mfma_f32_16x16x32_bf16 v[120:123], a[72:75], a[0:3], v[120:123]
s_waitcnt lgkmcnt(0)
v_mfma_f32_16x16x32_bf16 v[120:123], a[76:79], a[4:7], v[120:123]
ds_read_b64_tr_b16 a[64:65], v18
s_add_u32 m0, s30, 0x14200
v_mfma_f32_16x16x32_bf16 v[124:127], a[72:75], a[8:11], v[124:127]
buffer_load_dwordx4 v9, s[16:19], s26 offen lds
v_mfma_f32_16x16x32_bf16 v[124:127], a[76:79], a[12:15], v[124:127]
v_mfma_f32_16x16x32_bf16 v[128:131], a[72:75], a[16:19], v[128:131]
ds_read_b64_tr_b16 a[66:67], v18 offset:3584
v_mfma_f32_16x16x32_bf16 v[128:131], a[76:79], a[20:23], v[128:131]
v_mfma_f32_16x16x32_bf16 v[132:135], a[72:75], a[24:27], v[132:135]
v_mfma_f32_16x16x32_bf16 v[132:135], a[76:79], a[28:31], v[132:135]
ds_read_b64_tr_b16 a[68:69], v18 offset:7168
v_mfma_f32_16x16x32_bf16 v[136:139], a[72:75], a[32:35], v[136:139]
buffer_load_dwordx4 v10, s[16:19], s26 offen offset:1024 lds
v_mfma_f32_16x16x32_bf16 v[136:139], a[76:79], a[36:39], v[136:139]
v_mfma_f32_16x16x32_bf16 v[140:143], a[72:75], a[40:43], v[140:143]
ds_read_b64_tr_b16 a[70:71], v18 offset:10752
v_mfma_f32_16x16x32_bf16 v[140:143], a[76:79], a[44:47], v[140:143]
v_mfma_f32_16x16x32_bf16 v[144:147], a[72:75], a[48:51], v[144:147]
v_mfma_f32_16x16x32_bf16 v[144:147], a[76:79], a[52:55], v[144:147]
v_mfma_f32_16x16x32_bf16 v[148:151], a[72:75], a[56:59], v[148:151]
buffer_load_dwordx4 v11, s[16:19], s26 offen offset:2048 lds
v_mfma_f32_16x16x32_bf16 v[148:151], a[76:79], a[60:63], v[148:151]
s_waitcnt lgkmcnt(1)
v_mfma_f32_16x16x32_bf16 v[152:155], a[64:67], a[0:3], v[152:155]
s_waitcnt lgkmcnt(0)
v_mfma_f32_16x16x32_bf16 v[152:155], a[68:71], a[4:7], v[152:155]
ds_read_b64_tr_b16 a[72:73], v18 offset:14336
v_mfma_f32_16x16x32_bf16 v[156:159], a[64:67], a[8:11], v[156:159]
v_mfma_f32_16x16x32_bf16 v[156:159], a[68:71], a[12:15], v[156:159]
s_add_u32 m0, s28, 0x13200
v_mfma_f32_16x16x32_bf16 v[160:163], a[64:67], a[16:19], v[160:163]
ds_read_b64_tr_b16 a[74:75], v18 offset:17920
buffer_load_dwordx4 v0, s[8:11], s24 offen sc1 lds
v_mfma_f32_16x16x32_bf16 v[160:163], a[68:71], a[20:23], v[160:163]
v_mfma_f32_16x16x32_bf16 v[164:167], a[64:67], a[24:27], v[164:167]
v_mfma_f32_16x16x32_bf16 v[164:167], a[68:71], a[28:31], v[164:167]
ds_read_b64_tr_b16 a[76:77], v18 offset:21504
v_mfma_f32_16x16x32_bf16 v[168:171], a[64:67], a[32:35], v[168:171]
v_mfma_f32_16x16x32_bf16 v[168:171], a[68:71], a[36:39], v[168:171]
v_mfma_f32_16x16x32_bf16 v[172:175], a[64:67], a[40:43], v[172:175]
ds_read_b64_tr_b16 a[78:79], v18 offset:25088
buffer_load_dwordx4 v0, s[8:11], s24 offen offset:1024 sc1 lds
v_mfma_f32_16x16x32_bf16 v[172:175], a[68:71], a[44:47], v[172:175]
v_mfma_f32_16x16x32_bf16 v[176:179], a[64:67], a[48:51], v[176:179]
v_mfma_f32_16x16x32_bf16 v[176:179], a[68:71], a[52:55], v[176:179]
v_mfma_f32_16x16x32_bf16 v[180:183], a[64:67], a[56:59], v[180:183]
v_mfma_f32_16x16x32_bf16 v[180:183], a[68:71], a[60:63], v[180:183]
s_waitcnt lgkmcnt(1)
v_mfma_f32_16x16x32_bf16 v[184:187], a[72:75], a[0:3], v[184:187]
buffer_load_dwordx4 v0, s[8:11], s24 offen offset:2048 sc1 lds
s_waitcnt lgkmcnt(0)
v_mfma_f32_16x16x32_bf16 v[184:187], a[76:79], a[4:7], v[184:187]
ds_read_b64_tr_b16 a[64:65], v14 offset:96
v_mfma_f32_16x16x32_bf16 v[188:191], a[72:75], a[8:11], v[188:191]
v_mfma_f32_16x16x32_bf16 v[188:191], a[76:79], a[12:15], v[188:191]
v_mfma_f32_16x16x32_bf16 v[192:195], a[72:75], a[16:19], v[192:195]
ds_read_b64_tr_b16 a[66:67], v14 offset:608
v_mfma_f32_16x16x32_bf16 v[192:195], a[76:79], a[20:23], v[192:195]
v_mfma_f32_16x16x32_bf16 v[196:199], a[72:75], a[24:27], v[196:199]
buffer_load_dwordx4 v0, s[8:11], s24 offen offset:3072 sc1 lds
v_mfma_f32_16x16x32_bf16 v[196:199], a[76:79], a[28:31], v[196:199]
ds_read_b64_tr_b16 a[68:69], v14 offset:8544
v_mfma_f32_16x16x32_bf16 v[200:203], a[72:75], a[32:35], v[200:203]
v_mfma_f32_16x16x32_bf16 v[200:203], a[76:79], a[36:39], v[200:203]
v_mfma_f32_16x16x32_bf16 v[204:207], a[72:75], a[40:43], v[204:207]
ds_read_b64_tr_b16 a[70:71], v14 offset:9056
v_mfma_f32_16x16x32_bf16 v[204:207], a[76:79], a[44:47], v[204:207]
s_add_u32 m0, s28, 0x14200
s_add_u32 s34, s24, 0x1000
v_mfma_f32_16x16x32_bf16 v[208:211], a[72:75], a[48:51], v[208:211]
buffer_load_dwordx4 v0, s[8:11], s34 offen sc1 lds
v_mfma_f32_16x16x32_bf16 v[208:211], a[76:79], a[52:55], v[208:211]
v_mfma_f32_16x16x32_bf16 v[212:215], a[72:75], a[56:59], v[212:215]
v_mfma_f32_16x16x32_bf16 v[212:215], a[76:79], a[60:63], v[212:215]
s_waitcnt lgkmcnt(1)
v_mfma_f32_16x16x32_bf16 a[88:91], a[64:67], a[0:3], a[88:91]
s_waitcnt lgkmcnt(0)
v_mfma_f32_16x16x32_bf16 a[88:91], a[68:71], a[4:7], a[88:91]
ds_read_b64_tr_b16 a[72:73], v14 offset:128
v_mfma_f32_16x16x32_bf16 a[92:95], a[64:67], a[8:11], a[92:95]
buffer_load_dwordx4 v0, s[8:11], s34 offen offset:1024 sc1 lds
v_mfma_f32_16x16x32_bf16 a[92:95], a[68:71], a[12:15], a[92:95]
v_mfma_f32_16x16x32_bf16 a[96:99], a[64:67], a[16:19], a[96:99]
ds_read_b64_tr_b16 a[74:75], v14 offset:640
v_mfma_f32_16x16x32_bf16 a[96:99], a[68:71], a[20:23], a[96:99]
v_mfma_f32_16x16x32_bf16 a[100:103], a[64:67], a[24:27], a[100:103]
v_mfma_f32_16x16x32_bf16 a[100:103], a[68:71], a[28:31], a[100:103]
ds_read_b64_tr_b16 a[76:77], v14 offset:8576
v_mfma_f32_16x16x32_bf16 a[104:107], a[64:67], a[32:35], a[104:107]
buffer_load_dwordx4 v0, s[8:11], s34 offen offset:2048 sc1 lds
v_mfma_f32_16x16x32_bf16 a[104:107], a[68:71], a[36:39], a[104:107]
v_mfma_f32_16x16x32_bf16 a[108:111], a[64:67], a[40:43], a[108:111]
ds_read_b64_tr_b16 a[78:79], v14 offset:9088
v_mfma_f32_16x16x32_bf16 a[108:111], a[68:71], a[44:47], a[108:111]
v_mfma_f32_16x16x32_bf16 a[112:115], a[64:67], a[48:51], a[112:115]
v_mfma_f32_16x16x32_bf16 a[112:115], a[68:71], a[52:55], a[112:115]
v_mfma_f32_16x16x32_bf16 a[116:119], a[64:67], a[56:59], a[116:119]
buffer_load_dwordx4 v0, s[8:11], s34 offen offset:3072 sc1 lds
s_add_u32 s24, s24, 0x8000
s_add_u32 s25, s25, 0x80000
s_add_u32 s26, s26, 0x70000
v_mfma_f32_16x16x32_bf16 a[116:119], a[68:71], a[60:63], a[116:119]
s_waitcnt lgkmcnt(1)
v_mfma_f32_16x16x32_bf16 a[120:123], a[72:75], a[0:3], a[120:123]
s_waitcnt lgkmcnt(0)
v_mfma_f32_16x16x32_bf16 a[120:123], a[76:79], a[4:7], a[120:123]
ds_read_b64_tr_b16 a[64:65], v14 offset:160
v_mfma_f32_16x16x32_bf16 a[124:127], a[72:75], a[8:11], a[124:127]
v_mfma_f32_16x16x32_bf16 a[124:127], a[76:79], a[12:15], a[124:127]
v_mfma_f32_16x16x32_bf16 a[128:131], a[72:75], a[16:19], a[128:131]
ds_read_b64_tr_b16 a[66:67], v14 offset:672
v_mfma_f32_16x16x32_bf16 a[128:131], a[76:79], a[20:23], a[128:131]
s_getpc_b64 s[40:41]
CTbB_%=:
s_sub_u32 s40, s40, CTbB_%=-KSTART_%=
s_subb_u32 s41, s41, 0
v_mfma_f32_16x16x32_bf16 a[132:135], a[72:75], a[24:27], a[132:135]
s_lshr_b32 s45, s5, 3
v_mfma_f32_16x16x32_bf16 a[132:135], a[76:79], a[28:31], a[132:135]
ds_read_b64_tr_b16 a[68:69], v14 offset:8608
s_lshl_b32 s45, s45, 2
v_mfma_f32_16x16x32_bf16 a[136:139], a[72:75], a[32:35], a[136:139]
s_add_u32 s45, s45, s4
v_mfma_f32_16x16x32_bf16 a[136:139], a[76:79], a[36:39], a[136:139]
s_lshl_b32 s45, s45, 13
v_mfma_f32_16x16x32_bf16 a[140:143], a[72:75], a[40:43], a[140:143]
ds_read_b64_tr_b16 a[70:71], v14 offset:9120
s_cmp_lt_u32 s45, END_%=-KSTART_%=
s_cselect_b32 s44, 0x80, 0
v_mfma_f32_16x16x32_bf16 a[140:143], a[76:79], a[44:47], a[140:143]
s_min_u32 s45, s45, END_%=-KSTART_%=-8192
v_mfma_f32_16x16x32_bf16 a[144:147], a[72:75], a[48:51], a[144:147]
v_mbcnt_lo_u32_b32 v254, -1, 0
v_mfma_f32_16x16x32_bf16 a[144:147], a[76:79], a[52:55], a[144:147]
v_mbcnt_hi_u32_b32 v254, -1, v254
v_mfma_f32_16x16x32_bf16 a[148:151], a[72:75], a[56:59], a[148:151]
v_mul_u32_u24 v254, s44, v254
v_mfma_f32_16x16x32_bf16 a[148:151], a[76:79], a[60:63], a[148:151]
v_add_u32 v254, s45, v254
s_waitcnt lgkmcnt(1)
v_mfma_f32_16x16x32_bf16 a[152:155], a[64:67], a[0:3], a[152:155]
global_load_dword v255, v254, s[40:41]
s_waitcnt lgkmcnt(0)
v_mfma_f32_16x16x32_bf16 a[152:155], a[68:71], a[4:7], a[152:155]
ds_read_b64_tr_b16 a[72:73], v14 offset:192
v_mfma_f32_16x16x32_bf16 a[156:159], a[64:67], a[8:11], a[156:159]
v_mfma_f32_16x16x32_bf16 a[156:159], a[68:71], a[12:15], a[156:159]
v_mfma_f32_16x16x32_bf16 a[160:163], a[64:67], a[16:19], a[160:163]
ds_read_b64_tr_b16 a[74:75], v14 offset:704
v_mfma_f32_16x16x32_bf16 a[160:163], a[68:71], a[20:23], a[160:163]
v_mfma_f32_16x16x32_bf16 a[164:167], a[64:67], a[24:27], a[164:167]
v_mfma_f32_16x16x32_bf16 a[164:167], a[68:71], a[28:31], a[164:167]
ds_read_b64_tr_b16 a[76:77], v14 offset:8640
v_mfma_f32_16x16x32_bf16 a[168:171], a[64:67], a[32:35], a[168:171]
v_mfma_f32_16x16x32_bf16 a[168:171], a[68:71], a[36:39], a[168:171]
v_mfma_f32_16x16x32_bf16 a[172:175], a[64:67], a[40:43], a[172:175]
ds_read_b64_tr_b16 a[78:79], v14 offset:9152
v_mfma_f32_16x16x32_bf16 a[172:175], a[68:71], a[44:47], a[172:175]
v_mfma_f32_16x16x32_bf16 a[176:179], a[64:67], a[48:51], a[176:179]
v_mfma_f32_16x16x32_bf16 a[176:179], a[68:71], a[52:55], a[176:179]
v_mfma_f32_16x16x32_bf16 a[180:183], a[64:67], a[56:59], a[180:183]
v_mfma_f32_16x16x32_bf16 a[180:183], a[68:71], a[60:63], a[180:183]
s_waitcnt lgkmcnt(1)
v_mfma_f32_16x16x32_bf16 a[184:187], a[72:75], a[0:3], a[184:187]
s_waitcnt lgkmcnt(0)
v_mfma_f32_16x16x32_bf16 a[184:187], a[76:79], a[4:7], a[184:187]
ds_read_b64_tr_b16 a[64:65], v14 offset:224
v_mfma_f32_16x16x32_bf16 a[188:191], a[72:75], a[8:11], a[188:191]
ds_read_b64_tr_b16 a[66:67], v14 offset:736
v_mfma_f32_16x16x32_bf16 a[188:191], a[76:79], a[12:15], a[188:191]
ds_read_b64_tr_b16 a[68:69], v14 offset:8672
v_mfma_f32_16x16x32_bf16 a[192:195], a[72:75], a[16:19], a[192:195]
ds_read_b64_tr_b16 a[70:71], v14 offset:9184
v_mfma_f32_16x16x32_bf16 a[192:195], a[76:79], a[20:23], a[192:195]
v_mfma_f32_16x16x32_bf16 a[196:199], a[72:75], a[24:27], a[196:199]
v_mfma_f32_16x16x32_bf16 a[196:199], a[76:79], a[28:31], a[196:199]
v_mfma_f32_16x16x32_bf16 a[200:203], a[72:75], a[32:35], a[200:203]
v_mfma_f32_16x16x32_bf16 a[200:203], a[76:79], a[36:39], a[200:203]
v_mfma_f32_16x16x32_bf16 a[204:207], a[72:75], a[40:43], a[204:207]
v_mfma_f32_16x16x32_bf16 a[204:207], a[76:79], a[44:47], a[204:207]
v_mfma_f32_16x16x32_bf16 a[208:211], a[72:75], a[48:51], a[208:211]
v_mfma_f32_16x16x32_bf16 a[208:211], a[76:79], a[52:55], a[208:211]
v_mfma_f32_16x16x32_bf16 a[212:215], a[72:75], a[56:59], a[212:215]
v_mfma_f32_16x16x32_bf16 a[212:215], a[76:79], a[60:63], a[212:215]
s_waitcnt vmcnt(1) lgkmcnt(0)
s_barrier
v_mfma_f32_16x16x32_bf16 a[216:219], a[64:67], a[0:3], a[216:219]
ds_read_b64_tr_b16 a[72:73], v17
ds_read_b128 a[0:3], v13
v_mfma_f32_16x16x32_bf16 a[216:219], a[68:71], a[4:7], a[216:219]
ds_read_b64_tr_b16 a[74:75], v17 offset:3584
ds_read_b128 a[4:7], v13 offset:1024
v_mfma_f32_16x16x32_bf16 a[220:223], a[64:67], a[8:11], a[220:223]
ds_read_b64_tr_b16 a[76:77], v17 offset:7168
ds_read_b128 a[8:11], v13 offset:2048
v_mfma_f32_16x16x32_bf16 a[220:223], a[68:71], a[12:15], a[220:223]
ds_read_b64_tr_b16 a[78:79], v17 offset:10752
ds_read_b128 a[12:15], v13 offset:3072
v_mfma_f32_16x16x32_bf16 a[224:227], a[64:67], a[16:19], a[224:227]
ds_read_b128 a[16:19], v13 offset:4096
v_mfma_f32_16x16x32_bf16 a[224:227], a[68:71], a[20:23], a[224:227]
ds_read_b128 a[20:23], v13 offset:5120
v_mfma_f32_16x16x32_bf16 a[228:231], a[64:67], a[24:27], a[228:231]
ds_read_b128 a[24:27], v13 offset:6144
v_mfma_f32_16x16x32_bf16 a[228:231], a[68:71], a[28:31], a[228:231]
ds_read_b128 a[28:31], v13 offset:7168
v_mfma_f32_16x16x32_bf16 a[232:235], a[64:67], a[32:35], a[232:235]
ds_read_b128 a[32:35], v13 offset:8192
v_mfma_f32_16x16x32_bf16 a[232:235], a[68:71], a[36:39], a[232:235]
ds_read_b128 a[36:39], v13 offset:9216
v_mfma_f32_16x16x32_bf16 a[236:239], a[64:67], a[40:43], a[236:239]
ds_read_b128 a[40:43], v13 offset:10240
v_mfma_f32_16x16x32_bf16 a[236:239], a[68:71], a[44:47], a[236:239]
ds_read_b128 a[44:47], v13 offset:11264
v_mfma_f32_16x16x32_bf16 a[240:243], a[64:67], a[48:51], a[240:243]
ds_read_b128 a[48:51], v13 offset:12288
v_mfma_f32_16x16x32_bf16 a[240:243], a[68:71], a[52:55], a[240:243]
ds_read_b128 a[52:55], v13 offset:13312
v_mfma_f32_16x16x32_bf16 a[244:247], a[64:67], a[56:59], a[244:247]
ds_read_b128 a[56:59], v13 offset:14336
v_mfma_f32_16x16x32_bf16 a[244:247], a[68:71], a[60:63], a[244:247]
ds_read_b128 a[60:63], v13 offset:15360
s_waitcnt lgkmcnt(6)
v_mfma_f32_16x16x32_bf16 v[24:27], a[72:75], a[0:3], v[24:27]
v_mbcnt_lo_u32_b32 v20, -1, 0
v_mbcnt_hi_u32_b32 v20, -1, v20
)ASM"
R"ASM(v_mfma_f32_16x16x32_bf16 v[24:27], a[76:79], a[4:7], v[24:27]
ds_read_b64_tr_b16 a[64:65], v17 offset:14336
v_lshrrev_b32 v21, 4, v20
v_and_b32 v22, 15, v20
v_mfma_f32_16x16x32_bf16 v[28:31], a[72:75], a[8:11], v[28:31]
v_add_u32 v23, 0, v21
v_xor_b32 v23, v22, v23
v_mfma_f32_16x16x32_bf16 v[28:31], a[76:79], a[12:15], v[28:31]
v_lshlrev_b32 v23, 4, v23
v_lshl_add_u32 v1, v21, 8, v23
v_mfma_f32_16x16x32_bf16 v[32:35], a[72:75], a[16:19], v[32:35]
ds_read_b64_tr_b16 a[66:67], v17 offset:17920
v_add_u32 v23, 4, v21
v_xor_b32 v23, v22, v23
v_mfma_f32_16x16x32_bf16 v[32:35], a[76:79], a[20:23], v[32:35]
v_lshlrev_b32 v23, 4, v23
v_lshl_add_u32 v2, v21, 8, v23
v_mfma_f32_16x16x32_bf16 v[36:39], a[72:75], a[24:27], v[36:39]
v_add_u32 v23, 8, v21
v_xor_b32 v23, v22, v23
v_mfma_f32_16x16x32_bf16 v[36:39], a[76:79], a[28:31], v[36:39]
ds_read_b64_tr_b16 a[68:69], v17 offset:21504
v_lshlrev_b32 v23, 4, v23
v_lshl_add_u32 v3, v21, 8, v23
v_mfma_f32_16x16x32_bf16 v[40:43], a[72:75], a[32:35], v[40:43]
v_add_u32 v23, 12, v21
v_xor_b32 v23, v22, v23
v_mfma_f32_16x16x32_bf16 v[40:43], a[76:79], a[36:39], v[40:43]
v_lshlrev_b32 v23, 4, v23
v_lshl_add_u32 v4, v21, 8, v23
s_waitcnt lgkmcnt(2)
v_mfma_f32_16x16x32_bf16 v[44:47], a[72:75], a[40:43], v[44:47]
ds_read_b64_tr_b16 a[70:71], v17 offset:25088
v_lshlrev_b32 v23, 4, v22
v_lshl_add_u32 v5, v21, 11, v23
v_mfma_f32_16x16x32_bf16 v[44:47], a[76:79], a[44:47], v[44:47]
v_add_u32 v5, 0x960, v5
v_subrev_u32 v6, 0x320, v5
v_mfma_f32_16x16x32_bf16 v[48:51], a[72:75], a[48:51], v[48:51]
v_subrev_u32 v7, 0x640, v5
v_subrev_u32 v8, 0x960, v5
v_mfma_f32_16x16x32_bf16 v[48:51], a[76:79], a[52:55], v[48:51]
s_mov_b32 s8, s50
s_and_b32 s9, s51, 0xffff
v_mfma_f32_16x16x32_bf16 v[52:55], a[72:75], a[56:59], v[52:55]
s_mov_b32 s10, 0x200000
s_mov_b32 s11, 0x20000
v_mfma_f32_16x16x32_bf16 v[52:55], a[76:79], a[60:63], v[52:55]
s_mov_b32 s12, s52
s_and_b32 s13, s53, 0xffff
s_waitcnt lgkmcnt(1)
v_mfma_f32_16x16x32_bf16 v[56:59], a[64:67], a[0:3], v[56:59]
s_mov_b32 s14, 0x200000
s_mov_b32 s15, 0x20000
s_waitcnt lgkmcnt(0)
v_mfma_f32_16x16x32_bf16 v[56:59], a[68:71], a[4:7], v[56:59]
ds_read_b64_tr_b16 a[72:73], v17 offset:32
s_lshr_b32 s35, s7, 3
s_lshl_b32 s35, s35, 19
v_mfma_f32_16x16x32_bf16 v[60:63], a[64:67], a[8:11], v[60:63]
s_add_u32 s35, s35, 0x60000
s_lshl_b32 s36, s4, 12
v_mfma_f32_16x16x32_bf16 v[60:63], a[68:71], a[12:15], v[60:63]
s_add_u32 s24, s35, s36
s_lshl_b32 s36, s68, 13
v_mfma_f32_16x16x32_bf16 v[64:67], a[64:67], a[16:19], v[64:67]
ds_read_b64_tr_b16 a[74:75], v17 offset:3616
s_add_u32 s25, s35, s36
s_lshl_b32 s36, s69, 10
v_mfma_f32_16x16x32_bf16 v[64:67], a[68:71], a[20:23], v[64:67]
s_add_u32 s25, s25, s36
s_sub_u32 s25, s25, 0x960
v_mfma_f32_16x16x32_bf16 v[68:71], a[64:67], a[24:27], v[68:71]
s_lshl_b32 s28, s4, 12
s_add_u32 s28, s28, 0
v_mfma_f32_16x16x32_bf16 v[68:71], a[68:71], a[28:31], v[68:71]
ds_read_b64_tr_b16 a[76:77], v17 offset:7200
s_mul_i32 s29, s68, 0x2100
s_mul_i32 s36, s69, 0x1080
v_mfma_f32_16x16x32_bf16 v[72:75], a[64:67], a[32:35], v[72:75]
s_add_u32 s29, s29, s36
s_add_u32 s29, s29, 0x8000
v_mfma_f32_16x16x32_bf16 v[72:75], a[68:71], a[36:39], v[72:75]
v_mfma_f32_16x16x32_bf16 v[76:79], a[64:67], a[40:43], v[76:79]
ds_read_b64_tr_b16 a[78:79], v17 offset:10784
s_add_u32 m0, s28, 0
v_mfma_f32_16x16x32_bf16 v[76:79], a[68:71], a[44:47], v[76:79]
buffer_load_dwordx4 v1, s[8:11], s24 offen lds
v_mfma_f32_16x16x32_bf16 v[80:83], a[64:67], a[48:51], v[80:83]
v_mfma_f32_16x16x32_bf16 v[80:83], a[68:71], a[52:55], v[80:83]
v_mfma_f32_16x16x32_bf16 v[84:87], a[64:67], a[56:59], v[84:87]
v_mfma_f32_16x16x32_bf16 v[84:87], a[68:71], a[60:63], v[84:87]
s_waitcnt lgkmcnt(1)
v_mfma_f32_16x16x32_bf16 v[88:91], a[72:75], a[0:3], v[88:91]
s_waitcnt lgkmcnt(0)
v_mfma_f32_16x16x32_bf16 v[88:91], a[76:79], a[4:7], v[88:91]
ds_read_b64_tr_b16 a[64:65], v17 offset:14368
buffer_load_dwordx4 v2, s[8:11], s24 offen offset:1024 lds
v_mfma_f32_16x16x32_bf16 v[92:95], a[72:75], a[8:11], v[92:95]
v_mfma_f32_16x16x32_bf16 v[92:95], a[76:79], a[12:15], v[92:95]
v_mfma_f32_16x16x32_bf16 v[96:99], a[72:75], a[16:19], v[96:99]
ds_read_b64_tr_b16 a[66:67], v17 offset:17952
v_mfma_f32_16x16x32_bf16 v[96:99], a[76:79], a[20:23], v[96:99]
v_mfma_f32_16x16x32_bf16 v[100:103], a[72:75], a[24:27], v[100:103]
v_mfma_f32_16x16x32_bf16 v[100:103], a[76:79], a[28:31], v[100:103]
ds_read_b64_tr_b16 a[68:69], v17 offset:21536
buffer_load_dwordx4 v3, s[8:11], s24 offen offset:2048 lds
v_mfma_f32_16x16x32_bf16 v[104:107], a[72:75], a[32:35], v[104:107]
v_mfma_f32_16x16x32_bf16 v[104:107], a[76:79], a[36:39], v[104:107]
v_mfma_f32_16x16x32_bf16 v[108:111], a[72:75], a[40:43], v[108:111]
ds_read_b64_tr_b16 a[70:71], v17 offset:25120
v_mfma_f32_16x16x32_bf16 v[108:111], a[76:79], a[44:47], v[108:111]
v_mfma_f32_16x16x32_bf16 v[112:115], a[72:75], a[48:51], v[112:115]
v_mfma_f32_16x16x32_bf16 v[112:115], a[76:79], a[52:55], v[112:115]
buffer_load_dwordx4 v4, s[8:11], s24 offen offset:3072 lds
s_add_u32 s24, s24, 0x4000
v_mfma_f32_16x16x32_bf16 v[116:119], a[72:75], a[56:59], v[116:119]
v_mfma_f32_16x16x32_bf16 v[116:119], a[76:79], a[60:63], v[116:119]
s_waitcnt lgkmcnt(1)
v_mfma_f32_16x16x32_bf16 v[120:123], a[64:67], a[0:3], v[120:123]
s_waitcnt lgkmcnt(0)
v_mfma_f32_16x16x32_bf16 v[120:123], a[68:71], a[4:7], v[120:123]
ds_read_b64_tr_b16 a[72:73], v19
v_mfma_f32_16x16x32_bf16 v[124:127], a[64:67], a[8:11], v[124:127]
s_add_u32 m0, s29, 0
v_mfma_f32_16x16x32_bf16 v[124:127], a[68:71], a[12:15], v[124:127]
buffer_load_dwordx4 v5, s[12:15], s25 offen lds
v_mfma_f32_16x16x32_bf16 v[128:131], a[64:67], a[16:19], v[128:131]
ds_read_b64_tr_b16 a[74:75], v19 offset:3584
v_mfma_f32_16x16x32_bf16 v[128:131], a[68:71], a[20:23], v[128:131]
v_mfma_f32_16x16x32_bf16 v[132:135], a[64:67], a[24:27], v[132:135]
v_mfma_f32_16x16x32_bf16 v[132:135], a[68:71], a[28:31], v[132:135]
ds_read_b64_tr_b16 a[76:77], v19 offset:7168
v_mfma_f32_16x16x32_bf16 v[136:139], a[64:67], a[32:35], v[136:139]
v_mfma_f32_16x16x32_bf16 v[136:139], a[68:71], a[36:39], v[136:139]
buffer_load_dwordx4 v6, s[12:15], s25 offen offset:1056 lds
v_mfma_f32_16x16x32_bf16 v[140:143], a[64:67], a[40:43], v[140:143]
ds_read_b64_tr_b16 a[78:79], v19 offset:10752
v_mfma_f32_16x16x32_bf16 v[140:143], a[68:71], a[44:47], v[140:143]
v_mfma_f32_16x16x32_bf16 v[144:147], a[64:67], a[48:51], v[144:147]
v_mfma_f32_16x16x32_bf16 v[144:147], a[68:71], a[52:55], v[144:147]
v_mfma_f32_16x16x32_bf16 v[148:151], a[64:67], a[56:59], v[148:151]
v_mfma_f32_16x16x32_bf16 v[148:151], a[68:71], a[60:63], v[148:151]
buffer_load_dwordx4 v7, s[12:15], s25 offen offset:2112 lds
s_waitcnt lgkmcnt(1)
v_mfma_f32_16x16x32_bf16 v[152:155], a[72:75], a[0:3], v[152:155]
s_waitcnt lgkmcnt(0)
v_mfma_f32_16x16x32_bf16 v[152:155], a[76:79], a[4:7], v[152:155]
ds_read_b64_tr_b16 a[64:65], v19 offset:14336
v_mfma_f32_16x16x32_bf16 v[156:159], a[72:75], a[8:11], v[156:159]
v_mfma_f32_16x16x32_bf16 v[156:159], a[76:79], a[12:15], v[156:159]
v_mfma_f32_16x16x32_bf16 v[160:163], a[72:75], a[16:19], v[160:163]
ds_read_b64_tr_b16 a[66:67], v19 offset:17920
v_mfma_f32_16x16x32_bf16 v[160:163], a[76:79], a[20:23], v[160:163]
buffer_load_dwordx4 v8, s[12:15], s25 offen offset:3168 lds
s_add_u32 s25, s25, 0x4000
v_mfma_f32_16x16x32_bf16 v[164:167], a[72:75], a[24:27], v[164:167]
v_mfma_f32_16x16x32_bf16 v[164:167], a[76:79], a[28:31], v[164:167]
ds_read_b64_tr_b16 a[68:69], v19 offset:21504
v_mfma_f32_16x16x32_bf16 v[168:171], a[72:75], a[32:35], v[168:171]
v_mfma_f32_16x16x32_bf16 v[168:171], a[76:79], a[36:39], v[168:171]
v_mfma_f32_16x16x32_bf16 v[172:175], a[72:75], a[40:43], v[172:175]
ds_read_b64_tr_b16 a[70:71], v19 offset:25088
s_add_u32 m0, s28, 0x4000
v_mfma_f32_16x16x32_bf16 v[172:175], a[76:79], a[44:47], v[172:175]
buffer_load_dwordx4 v1, s[8:11], s24 offen lds
v_mfma_f32_16x16x32_bf16 v[176:179], a[72:75], a[48:51], v[176:179]
v_mfma_f32_16x16x32_bf16 v[176:179], a[76:79], a[52:55], v[176:179]
v_mfma_f32_16x16x32_bf16 v[180:183], a[72:75], a[56:59], v[180:183]
v_mfma_f32_16x16x32_bf16 v[180:183], a[76:79], a[60:63], v[180:183]
s_waitcnt lgkmcnt(1)
v_mfma_f32_16x16x32_bf16 v[184:187], a[64:67], a[0:3], v[184:187]
s_waitcnt lgkmcnt(0)
v_mfma_f32_16x16x32_bf16 v[184:187], a[68:71], a[4:7], v[184:187]
ds_read_b64_tr_b16 a[72:73], v15 offset:96
buffer_load_dwordx4 v2, s[8:11], s24 offen offset:1024 lds
v_mfma_f32_16x16x32_bf16 v[188:191], a[64:67], a[8:11], v[188:191]
v_mfma_f32_16x16x32_bf16 v[188:191], a[68:71], a[12:15], v[188:191]
v_mfma_f32_16x16x32_bf16 v[192:195], a[64:67], a[16:19], v[192:195]
ds_read_b64_tr_b16 a[74:75], v15 offset:608
v_mfma_f32_16x16x32_bf16 v[192:195], a[68:71], a[20:23], v[192:195]
v_mfma_f32_16x16x32_bf16 v[196:199], a[64:67], a[24:27], v[196:199]
v_mfma_f32_16x16x32_bf16 v[196:199], a[68:71], a[28:31], v[196:199]
ds_read_b64_tr_b16 a[76:77], v15 offset:8544
buffer_load_dwordx4 v3, s[8:11], s24 offen offset:2048 lds
v_mfma_f32_16x16x32_bf16 v[200:203], a[64:67], a[32:35], v[200:203]
v_mfma_f32_16x16x32_bf16 v[200:203], a[68:71], a[36:39], v[200:203]
v_mfma_f32_16x16x32_bf16 v[204:207], a[64:67], a[40:43], v[204:207]
ds_read_b64_tr_b16 a[78:79], v15 offset:9056
v_mfma_f32_16x16x32_bf16 v[204:207], a[68:71], a[44:47], v[204:207]
v_mfma_f32_16x16x32_bf16 v[208:211], a[64:67], a[48:51], v[208:211]
v_mfma_f32_16x16x32_bf16 v[208:211], a[68:71], a[52:55], v[208:211]
buffer_load_dwordx4 v4, s[8:11], s24 offen offset:3072 lds
s_add_u32 s24, s24, 0x4000
v_mfma_f32_16x16x32_bf16 v[212:215], a[64:67], a[56:59], v[212:215]
v_mfma_f32_16x16x32_bf16 v[212:215], a[68:71], a[60:63], v[212:215]
s_waitcnt lgkmcnt(1)
v_mfma_f32_16x16x32_bf16 a[88:91], a[72:75], a[0:3], a[88:91]
s_waitcnt lgkmcnt(0)
v_mfma_f32_16x16x32_bf16 a[88:91], a[76:79], a[4:7], a[88:91]
ds_read_b64_tr_b16 a[64:65], v15 offset:128
v_mfma_f32_16x16x32_bf16 a[92:95], a[72:75], a[8:11], a[92:95]
v_mfma_f32_16x16x32_bf16 a[92:95], a[76:79], a[12:15], a[92:95]
v_mfma_f32_16x16x32_bf16 a[96:99], a[72:75], a[16:19], a[96:99]
ds_read_b64_tr_b16 a[66:67], v15 offset:640
v_mfma_f32_16x16x32_bf16 a[96:99], a[76:79], a[20:23], a[96:99]
v_mfma_f32_16x16x32_bf16 a[100:103], a[72:75], a[24:27], a[100:103]
v_mfma_f32_16x16x32_bf16 a[100:103], a[76:79], a[28:31], a[100:103]
ds_read_b64_tr_b16 a[68:69], v15 offset:8576
v_mfma_f32_16x16x32_bf16 a[104:107], a[72:75], a[32:35], a[104:107]
v_mfma_f32_16x16x32_bf16 a[104:107], a[76:79], a[36:39], a[104:107]
v_mfma_f32_16x16x32_bf16 a[108:111], a[72:75], a[40:43], a[108:111]
ds_read_b64_tr_b16 a[70:71], v15 offset:9088
v_mfma_f32_16x16x32_bf16 a[108:111], a[76:79], a[44:47], a[108:111]
v_mfma_f32_16x16x32_bf16 a[112:115], a[72:75], a[48:51], a[112:115]
v_mfma_f32_16x16x32_bf16 a[112:115], a[76:79], a[52:55], a[112:115]
v_mfma_f32_16x16x32_bf16 a[116:119], a[72:75], a[56:59], a[116:119]
s_bfe_u32 s74, s5, 0x20001
v_mfma_f32_16x16x32_bf16 a[116:119], a[76:79], a[60:63], a[116:119]
s_and_b32 s41, s5, 1
s_waitcnt lgkmcnt(1)
v_mfma_f32_16x16x32_bf16 a[120:123], a[64:67], a[0:3], a[120:123]
s_lshl_b32 s41, s41, 2
s_waitcnt lgkmcnt(0)
v_mfma_f32_16x16x32_bf16 a[120:123], a[68:71], a[4:7], a[120:123]
ds_read_b64_tr_b16 a[72:73], v15 offset:160
s_bfe_u32 s42, s5, 0x20003
v_mfma_f32_16x16x32_bf16 a[124:127], a[64:67], a[8:11], a[124:127]
s_add_u32 s72, s41, s42
v_mfma_f32_16x16x32_bf16 a[124:127], a[68:71], a[12:15], a[124:127]
s_lshr_b32 s73, s5, 5
v_mfma_f32_16x16x32_bf16 a[128:131], a[64:67], a[16:19], a[128:131]
ds_read_b64_tr_b16 a[74:75], v15 offset:672
s_mov_b32 s16, s62
v_mfma_f32_16x16x32_bf16 a[128:131], a[68:71], a[20:23], a[128:131]
s_and_b32 s17, s63, 0xffff
v_mfma_f32_16x16x32_bf16 a[132:135], a[64:67], a[24:27], a[132:135]
s_mov_b32 s18, 0x700000
v_mfma_f32_16x16x32_bf16 a[132:135], a[68:71], a[28:31], a[132:135]
ds_read_b64_tr_b16 a[76:77], v15 offset:8608
s_mov_b32 s19, 0x20000
v_mfma_f32_16x16x32_bf16 a[136:139], a[64:67], a[32:35], a[136:139]
s_mov_b32 s20, s66
v_mfma_f32_16x16x32_bf16 a[136:139], a[68:71], a[36:39], a[136:139]
s_and_b32 s21, s67, 0xffff
v_mfma_f32_16x16x32_bf16 a[140:143], a[64:67], a[40:43], a[140:143]
ds_read_b64_tr_b16 a[78:79], v15 offset:9120
s_mov_b32 s22, 0x2300000
v_mfma_f32_16x16x32_bf16 a[140:143], a[68:71], a[44:47], a[140:143]
s_mov_b32 s23, 0x20000
v_mfma_f32_16x16x32_bf16 a[144:147], a[64:67], a[48:51], a[144:147]
s_lshl_b32 s40, s72, 1
v_mfma_f32_16x16x32_bf16 a[144:147], a[68:71], a[52:55], a[144:147]
s_add_u32 s40, s40, s68
v_mfma_f32_16x16x32_bf16 a[148:151], a[64:67], a[56:59], a[148:151]
s_mul_i32 s26, s40, 56
v_mfma_f32_16x16x32_bf16 a[148:151], a[68:71], a[60:63], a[148:151]
s_mul_i32 s39, s74, 14
s_waitcnt lgkmcnt(1)
v_mfma_f32_16x16x32_bf16 a[152:155], a[72:75], a[0:3], a[152:155]
s_add_u32 s26, s26, s39
s_waitcnt lgkmcnt(0)
v_mfma_f32_16x16x32_bf16 a[152:155], a[76:79], a[4:7], a[152:155]
ds_read_b64_tr_b16 a[64:65], v15 offset:192
s_lshl_b32 s26, s26, 14
v_mfma_f32_16x16x32_bf16 a[156:159], a[72:75], a[8:11], a[156:159]
s_lshl_b32 s39, s69, 13
v_mfma_f32_16x16x32_bf16 a[156:159], a[76:79], a[12:15], a[156:159]
s_add_u32 s26, s26, s39
v_mfma_f32_16x16x32_bf16 a[160:163], a[72:75], a[16:19], a[160:163]
ds_read_b64_tr_b16 a[66:67], v15 offset:704
s_add_u32 s26, s26, 0x500000
v_mfma_f32_16x16x32_bf16 a[160:163], a[76:79], a[20:23], a[160:163]
s_mul_i32 s27, s74, 0x1c0000
v_mfma_f32_16x16x32_bf16 a[164:167], a[72:75], a[24:27], a[164:167]
s_lshl_b32 s39, s68, 16
v_mfma_f32_16x16x32_bf16 a[164:167], a[76:79], a[28:31], a[164:167]
ds_read_b64_tr_b16 a[68:69], v15 offset:8640
s_add_u32 s27, s27, s39
v_mfma_f32_16x16x32_bf16 a[168:171], a[72:75], a[32:35], a[168:171]
s_lshl_b32 s39, s69, 13
v_mfma_f32_16x16x32_bf16 a[168:171], a[76:79], a[36:39], a[168:171]
s_add_u32 s27, s27, s39
v_mfma_f32_16x16x32_bf16 a[172:175], a[72:75], a[40:43], a[172:175]
ds_read_b64_tr_b16 a[70:71], v15 offset:9152
s_lshl_b32 s39, s73, 8
v_mfma_f32_16x16x32_bf16 a[172:175], a[76:79], a[44:47], a[172:175]
s_add_u32 s27, s27, s39
v_mfma_f32_16x16x32_bf16 a[176:179], a[72:75], a[48:51], a[176:179]
s_mul_i32 s31, s40, 64
v_mfma_f32_16x16x32_bf16 a[176:179], a[76:79], a[52:55], a[176:179]
s_mul_i32 s39, s74, 16
v_mfma_f32_16x16x32_bf16 a[180:183], a[72:75], a[56:59], a[180:183]
s_add_u32 s31, s31, s39
v_mfma_f32_16x16x32_bf16 a[180:183], a[76:79], a[60:63], a[180:183]
s_lshl_b32 s31, s31, 14
s_waitcnt lgkmcnt(1)
v_mfma_f32_16x16x32_bf16 a[184:187], a[64:67], a[0:3], a[184:187]
s_lshl_b32 s39, s69, 13
s_waitcnt lgkmcnt(0)
v_mfma_f32_16x16x32_bf16 a[184:187], a[68:71], a[4:7], a[184:187]
ds_read_b64_tr_b16 a[72:73], v15 offset:224
s_add_u32 s31, s31, s39
v_mfma_f32_16x16x32_bf16 a[188:191], a[64:67], a[8:11], a[188:191]
ds_read_b64_tr_b16 a[74:75], v15 offset:736
s_add_u32 s31, s31, 0x1300000
v_mfma_f32_16x16x32_bf16 a[188:191], a[68:71], a[12:15], a[188:191]
ds_read_b64_tr_b16 a[76:77], v15 offset:8672
s_mul_i32 s75, s74, 0x200000
v_mfma_f32_16x16x32_bf16 a[192:195], a[64:67], a[16:19], a[192:195]
ds_read_b64_tr_b16 a[78:79], v15 offset:9184
s_lshl_b32 s39, s68, 16
v_mfma_f32_16x16x32_bf16 a[192:195], a[68:71], a[20:23], a[192:195]
s_add_u32 s75, s75, s39
v_mfma_f32_16x16x32_bf16 a[196:199], a[64:67], a[24:27], a[196:199]
s_lshl_b32 s39, s69, 13
v_mfma_f32_16x16x32_bf16 a[196:199], a[68:71], a[28:31], a[196:199]
s_add_u32 s75, s75, s39
v_mfma_f32_16x16x32_bf16 a[200:203], a[64:67], a[32:35], a[200:203]
s_lshl_b32 s39, s73, 8
v_mfma_f32_16x16x32_bf16 a[200:203], a[68:71], a[36:39], a[200:203]
s_add_u32 s75, s75, s39
v_mfma_f32_16x16x32_bf16 a[204:207], a[64:67], a[40:43], a[204:207]
s_mul_i32 s30, s68, 0x2100
v_mfma_f32_16x16x32_bf16 a[204:207], a[68:71], a[44:47], a[204:207]
s_mul_i32 s39, s69, 0x1080
v_mfma_f32_16x16x32_bf16 a[208:211], a[64:67], a[48:51], a[208:211]
s_add_u32 s30, s30, s39
v_mfma_f32_16x16x32_bf16 a[208:211], a[68:71], a[52:55], a[208:211]
v_mfma_f32_16x16x32_bf16 a[212:215], a[64:67], a[56:59], a[212:215]
v_mfma_f32_16x16x32_bf16 a[212:215], a[68:71], a[60:63], a[212:215]
s_waitcnt lgkmcnt(0)
s_barrier
v_mfma_f32_16x16x32_bf16 a[216:219], a[72:75], a[0:3], a[216:219]
v_mfma_f32_16x16x32_bf16 a[216:219], a[76:79], a[4:7], a[216:219]
v_mfma_f32_16x16x32_bf16 a[220:223], a[72:75], a[8:11], a[220:223]
v_mfma_f32_16x16x32_bf16 a[220:223], a[76:79], a[12:15], a[220:223]
v_mfma_f32_16x16x32_bf16 a[224:227], a[72:75], a[16:19], a[224:227]
v_mfma_f32_16x16x32_bf16 a[224:227], a[76:79], a[20:23], a[224:227]
v_mfma_f32_16x16x32_bf16 a[228:231], a[72:75], a[24:27], a[228:231]
v_mfma_f32_16x16x32_bf16 a[228:231], a[76:79], a[28:31], a[228:231]
v_mfma_f32_16x16x32_bf16 a[232:235], a[72:75], a[32:35], a[232:235]
v_mfma_f32_16x16x32_bf16 a[232:235], a[76:79], a[36:39], a[232:235]
v_mfma_f32_16x16x32_bf16 a[236:239], a[72:75], a[40:43], a[236:239]
v_mfma_f32_16x16x32_bf16 a[236:239], a[76:79], a[44:47], a[236:239]
v_mfma_f32_16x16x32_bf16 a[240:243], a[72:75], a[48:51], a[240:243]
v_mfma_f32_16x16x32_bf16 a[240:243], a[76:79], a[52:55], a[240:243]
v_mfma_f32_16x16x32_bf16 a[244:247], a[72:75], a[56:59], a[244:247]
v_mfma_f32_16x16x32_bf16 a[244:247], a[76:79], a[60:63], a[244:247]
s_nop 7
v_mbcnt_lo_u32_b32 v9, -1, 0
v_mbcnt_hi_u32_b32 v9, -1, v9
v_lshrrev_b32 v12, 5, v9
v_lshlrev_b32 v12, 8, v12
v_and_b32 v13, 15, v9
v_lshl_add_u32 v12, v13, 4, v12
v_bfe_u32 v13, v9, 4, 1
v_lshl_add_u32 v12, v13, 3, v12
s_lshl_b32 s34, s68, 15
s_add_u32 s34, s34, 0x13200
v_add_u32 v10, s34, v12
s_mov_b32 s36, 0x3e0293ee
s_mov_b32 s37, 0x3e0293ee
v_accvgpr_read_b32 v12, a88
v_accvgpr_read_b32 v13, a89
v_accvgpr_read_b32 v14, a90
v_accvgpr_read_b32 v15, a91
v_pk_mul_f32 v[12:13], v[12:13], s[36:37]
v_pk_mul_f32 v[14:15], v[14:15], s[36:37]
v_cvt_pk_bf16_f32 v16, v12, v13
v_cvt_pk_bf16_f32 v17, v14, v15
ds_write_b64 v10, v[16:17] offset:1536
v_accvgpr_read_b32 v18, a92
v_accvgpr_read_b32 v19, a93
v_accvgpr_read_b32 v20, a94
v_accvgpr_read_b32 v21, a95
v_pk_mul_f32 v[18:19], v[18:19], s[36:37]
v_pk_mul_f32 v[20:21], v[20:21], s[36:37]
v_cvt_pk_bf16_f32 v22, v18, v19
v_cvt_pk_bf16_f32 v23, v20, v21
ds_write_b64 v10, v[22:23] offset:5632
v_accvgpr_read_b32 v12, a96
v_accvgpr_read_b32 v13, a97
v_accvgpr_read_b32 v14, a98
v_accvgpr_read_b32 v15, a99
v_pk_mul_f32 v[12:13], v[12:13], s[36:37]
v_pk_mul_f32 v[14:15], v[14:15], s[36:37]
v_cvt_pk_bf16_f32 v16, v12, v13
v_cvt_pk_bf16_f32 v17, v14, v15
ds_write_b64 v10, v[16:17] offset:9728
v_accvgpr_read_b32 v18, a100
v_accvgpr_read_b32 v19, a101
v_accvgpr_read_b32 v20, a102
v_accvgpr_read_b32 v21, a103
v_pk_mul_f32 v[18:19], v[18:19], s[36:37]
v_pk_mul_f32 v[20:21], v[20:21], s[36:37]
v_cvt_pk_bf16_f32 v22, v18, v19
v_cvt_pk_bf16_f32 v23, v20, v21
ds_write_b64 v10, v[22:23] offset:13824
v_accvgpr_read_b32 v12, a104
)ASM"
R"ASM(v_accvgpr_read_b32 v13, a105
v_accvgpr_read_b32 v14, a106
v_accvgpr_read_b32 v15, a107
v_pk_mul_f32 v[12:13], v[12:13], s[36:37]
v_pk_mul_f32 v[14:15], v[14:15], s[36:37]
v_cvt_pk_bf16_f32 v16, v12, v13
v_cvt_pk_bf16_f32 v17, v14, v15
ds_write_b64 v10, v[16:17] offset:17920
v_accvgpr_read_b32 v18, a108
v_accvgpr_read_b32 v19, a109
v_accvgpr_read_b32 v20, a110
v_accvgpr_read_b32 v21, a111
v_pk_mul_f32 v[18:19], v[18:19], s[36:37]
v_pk_mul_f32 v[20:21], v[20:21], s[36:37]
v_cvt_pk_bf16_f32 v22, v18, v19
v_cvt_pk_bf16_f32 v23, v20, v21
ds_write_b64 v10, v[22:23] offset:22016
v_accvgpr_read_b32 v12, a112
v_accvgpr_read_b32 v13, a113
v_accvgpr_read_b32 v14, a114
v_accvgpr_read_b32 v15, a115
v_pk_mul_f32 v[12:13], v[12:13], s[36:37]
v_pk_mul_f32 v[14:15], v[14:15], s[36:37]
v_cvt_pk_bf16_f32 v16, v12, v13
v_cvt_pk_bf16_f32 v17, v14, v15
ds_write_b64 v10, v[16:17] offset:26112
v_accvgpr_read_b32 v18, a116
v_accvgpr_read_b32 v19, a117
v_accvgpr_read_b32 v20, a118
v_accvgpr_read_b32 v21, a119
v_pk_mul_f32 v[18:19], v[18:19], s[36:37]
v_pk_mul_f32 v[20:21], v[20:21], s[36:37]
v_cvt_pk_bf16_f32 v22, v18, v19
v_cvt_pk_bf16_f32 v23, v20, v21
ds_write_b64 v10, v[22:23] offset:30208
v_accvgpr_read_b32 v12, a120
v_accvgpr_read_b32 v13, a121
v_accvgpr_read_b32 v14, a122
v_accvgpr_read_b32 v15, a123
v_pk_mul_f32 v[12:13], v[12:13], s[36:37]
v_pk_mul_f32 v[14:15], v[14:15], s[36:37]
v_cvt_pk_bf16_f32 v16, v12, v13
v_cvt_pk_bf16_f32 v17, v14, v15
ds_write_b64 v10, v[16:17] offset:2048
v_accvgpr_read_b32 v18, a124
v_accvgpr_read_b32 v19, a125
v_accvgpr_read_b32 v20, a126
v_accvgpr_read_b32 v21, a127
v_pk_mul_f32 v[18:19], v[18:19], s[36:37]
v_pk_mul_f32 v[20:21], v[20:21], s[36:37]
v_cvt_pk_bf16_f32 v22, v18, v19
v_cvt_pk_bf16_f32 v23, v20, v21
ds_write_b64 v10, v[22:23] offset:6144
v_accvgpr_read_b32 v12, a128
v_accvgpr_read_b32 v13, a129
v_accvgpr_read_b32 v14, a130
v_accvgpr_read_b32 v15, a131
v_pk_mul_f32 v[12:13], v[12:13], s[36:37]
v_pk_mul_f32 v[14:15], v[14:15], s[36:37]
v_cvt_pk_bf16_f32 v16, v12, v13
v_cvt_pk_bf16_f32 v17, v14, v15
ds_write_b64 v10, v[16:17] offset:10240
v_accvgpr_read_b32 v18, a132
v_accvgpr_read_b32 v19, a133
v_accvgpr_read_b32 v20, a134
v_accvgpr_read_b32 v21, a135
v_pk_mul_f32 v[18:19], v[18:19], s[36:37]
v_pk_mul_f32 v[20:21], v[20:21], s[36:37]
v_cvt_pk_bf16_f32 v22, v18, v19
v_cvt_pk_bf16_f32 v23, v20, v21
ds_write_b64 v10, v[22:23] offset:14336
v_accvgpr_read_b32 v12, a136
v_accvgpr_read_b32 v13, a137
v_accvgpr_read_b32 v14, a138
v_accvgpr_read_b32 v15, a139
v_pk_mul_f32 v[12:13], v[12:13], s[36:37]
v_pk_mul_f32 v[14:15], v[14:15], s[36:37]
v_cvt_pk_bf16_f32 v16, v12, v13
v_cvt_pk_bf16_f32 v17, v14, v15
ds_write_b64 v10, v[16:17] offset:18432
v_accvgpr_read_b32 v18, a140
v_accvgpr_read_b32 v19, a141
v_accvgpr_read_b32 v20, a142
v_accvgpr_read_b32 v21, a143
v_pk_mul_f32 v[18:19], v[18:19], s[36:37]
v_pk_mul_f32 v[20:21], v[20:21], s[36:37]
v_cvt_pk_bf16_f32 v22, v18, v19
v_cvt_pk_bf16_f32 v23, v20, v21
ds_write_b64 v10, v[22:23] offset:22528
v_accvgpr_read_b32 v12, a144
v_accvgpr_read_b32 v13, a145
v_accvgpr_read_b32 v14, a146
v_accvgpr_read_b32 v15, a147
v_pk_mul_f32 v[12:13], v[12:13], s[36:37]
v_pk_mul_f32 v[14:15], v[14:15], s[36:37]
v_cvt_pk_bf16_f32 v16, v12, v13
v_cvt_pk_bf16_f32 v17, v14, v15
ds_write_b64 v10, v[16:17] offset:26624
v_accvgpr_read_b32 v18, a148
v_accvgpr_read_b32 v19, a149
v_accvgpr_read_b32 v20, a150
v_accvgpr_read_b32 v21, a151
v_pk_mul_f32 v[18:19], v[18:19], s[36:37]
v_pk_mul_f32 v[20:21], v[20:21], s[36:37]
v_cvt_pk_bf16_f32 v22, v18, v19
v_cvt_pk_bf16_f32 v23, v20, v21
ds_write_b64 v10, v[22:23] offset:30720
v_accvgpr_read_b32 v12, a152
v_accvgpr_read_b32 v13, a153
v_accvgpr_read_b32 v14, a154
v_accvgpr_read_b32 v15, a155
v_pk_mul_f32 v[12:13], v[12:13], s[36:37]
v_pk_mul_f32 v[14:15], v[14:15], s[36:37]
v_cvt_pk_bf16_f32 v16, v12, v13
v_cvt_pk_bf16_f32 v17, v14, v15
ds_write_b64 v10, v[16:17] offset:2560
v_accvgpr_read_b32 v18, a156
v_accvgpr_read_b32 v19, a157
v_accvgpr_read_b32 v20, a158
v_accvgpr_read_b32 v21, a159
v_pk_mul_f32 v[18:19], v[18:19], s[36:37]
v_pk_mul_f32 v[20:21], v[20:21], s[36:37]
v_cvt_pk_bf16_f32 v22, v18, v19
v_cvt_pk_bf16_f32 v23, v20, v21
ds_write_b64 v10, v[22:23] offset:6656
v_accvgpr_read_b32 v12, a160
v_accvgpr_read_b32 v13, a161
v_accvgpr_read_b32 v14, a162
v_accvgpr_read_b32 v15, a163
v_pk_mul_f32 v[12:13], v[12:13], s[36:37]
v_pk_mul_f32 v[14:15], v[14:15], s[36:37]
v_cvt_pk_bf16_f32 v16, v12, v13
v_cvt_pk_bf16_f32 v17, v14, v15
ds_write_b64 v10, v[16:17] offset:10752
v_accvgpr_read_b32 v18, a164
v_accvgpr_read_b32 v19, a165
v_accvgpr_read_b32 v20, a166
v_accvgpr_read_b32 v21, a167
v_pk_mul_f32 v[18:19], v[18:19], s[36:37]
v_pk_mul_f32 v[20:21], v[20:21], s[36:37]
v_cvt_pk_bf16_f32 v22, v18, v19
v_cvt_pk_bf16_f32 v23, v20, v21
ds_write_b64 v10, v[22:23] offset:14848
v_accvgpr_read_b32 v12, a168
v_accvgpr_read_b32 v13, a169
v_accvgpr_read_b32 v14, a170
v_accvgpr_read_b32 v15, a171
v_pk_mul_f32 v[12:13], v[12:13], s[36:37]
v_pk_mul_f32 v[14:15], v[14:15], s[36:37]
v_cvt_pk_bf16_f32 v16, v12, v13
v_cvt_pk_bf16_f32 v17, v14, v15
ds_write_b64 v10, v[16:17] offset:18944
v_accvgpr_read_b32 v18, a172
v_accvgpr_read_b32 v19, a173
v_accvgpr_read_b32 v20, a174
v_accvgpr_read_b32 v21, a175
v_pk_mul_f32 v[18:19], v[18:19], s[36:37]
v_pk_mul_f32 v[20:21], v[20:21], s[36:37]
v_cvt_pk_bf16_f32 v22, v18, v19
v_cvt_pk_bf16_f32 v23, v20, v21
ds_write_b64 v10, v[22:23] offset:23040
v_accvgpr_read_b32 v12, a176
v_accvgpr_read_b32 v13, a177
v_accvgpr_read_b32 v14, a178
v_accvgpr_read_b32 v15, a179
v_pk_mul_f32 v[12:13], v[12:13], s[36:37]
v_pk_mul_f32 v[14:15], v[14:15], s[36:37]
v_cvt_pk_bf16_f32 v16, v12, v13
v_cvt_pk_bf16_f32 v17, v14, v15
ds_write_b64 v10, v[16:17] offset:27136
v_accvgpr_read_b32 v18, a180
v_accvgpr_read_b32 v19, a181
v_accvgpr_read_b32 v20, a182
v_accvgpr_read_b32 v21, a183
v_pk_mul_f32 v[18:19], v[18:19], s[36:37]
v_pk_mul_f32 v[20:21], v[20:21], s[36:37]
v_cvt_pk_bf16_f32 v22, v18, v19
v_cvt_pk_bf16_f32 v23, v20, v21
ds_write_b64 v10, v[22:23] offset:31232
v_accvgpr_read_b32 v12, a184
v_accvgpr_read_b32 v13, a185
v_accvgpr_read_b32 v14, a186
v_accvgpr_read_b32 v15, a187
v_pk_mul_f32 v[12:13], v[12:13], s[36:37]
v_pk_mul_f32 v[14:15], v[14:15], s[36:37]
v_cvt_pk_bf16_f32 v16, v12, v13
v_cvt_pk_bf16_f32 v17, v14, v15
ds_write_b64 v10, v[16:17] offset:3072
v_accvgpr_read_b32 v18, a188
v_accvgpr_read_b32 v19, a189
v_accvgpr_read_b32 v20, a190
v_accvgpr_read_b32 v21, a191
v_pk_mul_f32 v[18:19], v[18:19], s[36:37]
v_pk_mul_f32 v[20:21], v[20:21], s[36:37]
v_cvt_pk_bf16_f32 v22, v18, v19
v_cvt_pk_bf16_f32 v23, v20, v21
ds_write_b64 v10, v[22:23] offset:7168
v_accvgpr_read_b32 v12, a192
v_accvgpr_read_b32 v13, a193
v_accvgpr_read_b32 v14, a194
v_accvgpr_read_b32 v15, a195
v_pk_mul_f32 v[12:13], v[12:13], s[36:37]
v_pk_mul_f32 v[14:15], v[14:15], s[36:37]
v_cvt_pk_bf16_f32 v16, v12, v13
v_cvt_pk_bf16_f32 v17, v14, v15
ds_write_b64 v10, v[16:17] offset:11264
v_accvgpr_read_b32 v18, a196
v_accvgpr_read_b32 v19, a197
v_accvgpr_read_b32 v20, a198
v_accvgpr_read_b32 v21, a199
v_pk_mul_f32 v[18:19], v[18:19], s[36:37]
v_pk_mul_f32 v[20:21], v[20:21], s[36:37]
v_cvt_pk_bf16_f32 v22, v18, v19
v_cvt_pk_bf16_f32 v23, v20, v21
ds_write_b64 v10, v[22:23] offset:15360
v_accvgpr_read_b32 v12, a200
v_accvgpr_read_b32 v13, a201
v_accvgpr_read_b32 v14, a202
v_accvgpr_read_b32 v15, a203
v_pk_mul_f32 v[12:13], v[12:13], s[36:37]
v_pk_mul_f32 v[14:15], v[14:15], s[36:37]
v_cvt_pk_bf16_f32 v16, v12, v13
v_cvt_pk_bf16_f32 v17, v14, v15
ds_write_b64 v10, v[16:17] offset:19456
v_accvgpr_read_b32 v18, a204
v_accvgpr_read_b32 v19, a205
v_accvgpr_read_b32 v20, a206
v_accvgpr_read_b32 v21, a207
v_pk_mul_f32 v[18:19], v[18:19], s[36:37]
v_pk_mul_f32 v[20:21], v[20:21], s[36:37]
v_cvt_pk_bf16_f32 v22, v18, v19
v_cvt_pk_bf16_f32 v23, v20, v21
ds_write_b64 v10, v[22:23] offset:23552
v_accvgpr_read_b32 v12, a208
v_accvgpr_read_b32 v13, a209
v_accvgpr_read_b32 v14, a210
v_accvgpr_read_b32 v15, a211
v_pk_mul_f32 v[12:13], v[12:13], s[36:37]
v_pk_mul_f32 v[14:15], v[14:15], s[36:37]
v_cvt_pk_bf16_f32 v16, v12, v13
v_cvt_pk_bf16_f32 v17, v14, v15
ds_write_b64 v10, v[16:17] offset:27648
v_accvgpr_read_b32 v18, a212
v_accvgpr_read_b32 v19, a213
v_accvgpr_read_b32 v20, a214
v_accvgpr_read_b32 v21, a215
v_pk_mul_f32 v[18:19], v[18:19], s[36:37]
v_pk_mul_f32 v[20:21], v[20:21], s[36:37]
v_cvt_pk_bf16_f32 v22, v18, v19
v_cvt_pk_bf16_f32 v23, v20, v21
ds_write_b64 v10, v[22:23] offset:31744
v_accvgpr_read_b32 v12, a216
v_accvgpr_read_b32 v13, a217
v_accvgpr_read_b32 v14, a218
v_accvgpr_read_b32 v15, a219
v_pk_mul_f32 v[12:13], v[12:13], s[36:37]
v_pk_mul_f32 v[14:15], v[14:15], s[36:37]
v_cvt_pk_bf16_f32 v16, v12, v13
v_cvt_pk_bf16_f32 v17, v14, v15
ds_write_b64 v10, v[16:17] offset:3584
v_accvgpr_read_b32 v18, a220
v_accvgpr_read_b32 v19, a221
v_accvgpr_read_b32 v20, a222
v_accvgpr_read_b32 v21, a223
v_pk_mul_f32 v[18:19], v[18:19], s[36:37]
v_pk_mul_f32 v[20:21], v[20:21], s[36:37]
v_cvt_pk_bf16_f32 v22, v18, v19
v_cvt_pk_bf16_f32 v23, v20, v21
ds_write_b64 v10, v[22:23] offset:7680
v_accvgpr_read_b32 v12, a224
v_accvgpr_read_b32 v13, a225
v_accvgpr_read_b32 v14, a226
v_accvgpr_read_b32 v15, a227
v_pk_mul_f32 v[12:13], v[12:13], s[36:37]
v_pk_mul_f32 v[14:15], v[14:15], s[36:37]
v_cvt_pk_bf16_f32 v16, v12, v13
v_cvt_pk_bf16_f32 v17, v14, v15
ds_write_b64 v10, v[16:17] offset:11776
v_accvgpr_read_b32 v18, a228
v_accvgpr_read_b32 v19, a229
v_accvgpr_read_b32 v20, a230
v_accvgpr_read_b32 v21, a231
v_pk_mul_f32 v[18:19], v[18:19], s[36:37]
v_pk_mul_f32 v[20:21], v[20:21], s[36:37]
v_cvt_pk_bf16_f32 v22, v18, v19
v_cvt_pk_bf16_f32 v23, v20, v21
ds_write_b64 v10, v[22:23] offset:15872
v_accvgpr_read_b32 v12, a232
v_accvgpr_read_b32 v13, a233
v_accvgpr_read_b32 v14, a234
v_accvgpr_read_b32 v15, a235
v_pk_mul_f32 v[12:13], v[12:13], s[36:37]
v_pk_mul_f32 v[14:15], v[14:15], s[36:37]
v_cvt_pk_bf16_f32 v16, v12, v13
v_cvt_pk_bf16_f32 v17, v14, v15
ds_write_b64 v10, v[16:17] offset:19968
v_accvgpr_read_b32 v18, a236
v_accvgpr_read_b32 v19, a237
v_accvgpr_read_b32 v20, a238
v_accvgpr_read_b32 v21, a239
v_pk_mul_f32 v[18:19], v[18:19], s[36:37]
v_pk_mul_f32 v[20:21], v[20:21], s[36:37]
v_cvt_pk_bf16_f32 v22, v18, v19
v_cvt_pk_bf16_f32 v23, v20, v21
ds_write_b64 v10, v[22:23] offset:24064
v_accvgpr_read_b32 v12, a240
v_accvgpr_read_b32 v13, a241
v_accvgpr_read_b32 v14, a242
v_accvgpr_read_b32 v15, a243
v_pk_mul_f32 v[12:13], v[12:13], s[36:37]
v_pk_mul_f32 v[14:15], v[14:15], s[36:37]
v_cvt_pk_bf16_f32 v16, v12, v13
v_cvt_pk_bf16_f32 v17, v14, v15
ds_write_b64 v10, v[16:17] offset:28160
v_accvgpr_read_b32 v18, a244
v_accvgpr_read_b32 v19, a245
v_accvgpr_read_b32 v20, a246
v_accvgpr_read_b32 v21, a247
v_pk_mul_f32 v[18:19], v[18:19], s[36:37]
v_pk_mul_f32 v[20:21], v[20:21], s[36:37]
v_cvt_pk_bf16_f32 v22, v18, v19
v_cvt_pk_bf16_f32 v23, v20, v21
ds_write_b64 v10, v[22:23] offset:32256
v_lshlrev_b32 v0, 4, v9
s_lshl_b32 s38, s6, 1
s_add_u32 s38, s38, s68
s_mul_i32 s38, s38, 0xe0000
s_add_u32 s38, s38, 0x500000
s_mul_i32 s39, s7, 7
s_and_b32 s40, s7, 1
s_cmp_eq_u32 s40, 1
s_cselect_b32 s41, 1, 4
s_cselect_b32 s45, 0, 6
s_add_u32 s41, s41, s39
s_lshr_b32 s42, s41, 2
s_lshl_b32 s42, s42, 14
s_add_u32 s43, s38, s42
s_bfe_u32 s42, s41, 0x10001
s_lshl_b32 s42, s42, 10
s_add_u32 s43, s43, s42
s_add_u32 s45, s45, s39
s_lshr_b32 s42, s45, 2
s_lshl_b32 s42, s42, 14
s_add_u32 s44, s38, s42
s_bfe_u32 s42, s45, 0x10001
s_lshl_b32 s42, s42, 10
s_add_u32 s44, s44, s42
s_and_b32 s45, s45, 1
s_lshl_b32 s45, s45, 3
s_add_u32 s44, s44, s45
s_mov_b32 s36, 0xbfb8aa3b
s_mov_b32 s37, 0xbfb8aa3b
s_mov_b32 s46, 1.0
s_mov_b32 s47, 1.0
v_pk_mul_f32 v[180:181], v[180:181], s[36:37]
v_pk_mul_f32 v[182:183], v[182:183], s[36:37]
v_exp_f32 v180, v180
v_exp_f32 v181, v181
v_exp_f32 v182, v182
v_exp_f32 v183, v183
v_pk_add_f32 v[180:181], v[180:181], s[46:47]
v_pk_add_f32 v[182:183], v[182:183], s[46:47]
v_rcp_f32 v180, v180
v_rcp_f32 v181, v181
v_rcp_f32 v182, v182
v_rcp_f32 v183, v183
v_pk_mul_f32 v[180:181], v[180:181], v[212:213]
v_pk_mul_f32 v[182:183], v[182:183], v[214:215]
v_cvt_pk_bf16_f32 v12, v180, v181
v_cvt_pk_bf16_f32 v13, v182, v183
s_add_u32 s42, s44, 0x3800
buffer_store_dwordx2 v[12:13], v0, s[76:79], s42 offen sc1
v_pk_mul_f32 v[176:177], v[176:177], s[36:37]
v_pk_mul_f32 v[178:179], v[178:179], s[36:37]
v_exp_f32 v176, v176
v_exp_f32 v177, v177
v_exp_f32 v178, v178
v_exp_f32 v179, v179
v_pk_add_f32 v[176:177], v[176:177], s[46:47]
v_pk_add_f32 v[178:179], v[178:179], s[46:47]
v_rcp_f32 v176, v176
v_rcp_f32 v177, v177
v_rcp_f32 v178, v178
v_rcp_f32 v179, v179
v_pk_mul_f32 v[176:177], v[176:177], v[208:209]
v_pk_mul_f32 v[178:179], v[178:179], v[210:211]
v_cvt_pk_bf16_f32 v16, v176, v177
v_cvt_pk_bf16_f32 v17, v178, v179
s_add_u32 s42, s44, 0x3000
buffer_store_dwordx2 v[16:17], v0, s[76:79], s42 offen sc1
v_pk_mul_f32 v[172:173], v[172:173], s[36:37]
v_pk_mul_f32 v[174:175], v[174:175], s[36:37]
v_exp_f32 v172, v172
v_exp_f32 v173, v173
v_exp_f32 v174, v174
v_exp_f32 v175, v175
v_pk_add_f32 v[172:173], v[172:173], s[46:47]
v_pk_add_f32 v[174:175], v[174:175], s[46:47]
v_rcp_f32 v172, v172
v_rcp_f32 v173, v173
)ASM"
R"ASM(v_rcp_f32 v174, v174
v_rcp_f32 v175, v175
v_pk_mul_f32 v[172:173], v[172:173], v[204:205]
v_pk_mul_f32 v[174:175], v[174:175], v[206:207]
v_cvt_pk_bf16_f32 v12, v172, v173
v_cvt_pk_bf16_f32 v13, v174, v175
s_add_u32 s42, s44, 0x2800
buffer_store_dwordx2 v[12:13], v0, s[76:79], s42 offen sc1
v_pk_mul_f32 v[168:169], v[168:169], s[36:37]
v_pk_mul_f32 v[170:171], v[170:171], s[36:37]
v_exp_f32 v168, v168
v_exp_f32 v169, v169
v_exp_f32 v170, v170
v_exp_f32 v171, v171
v_pk_add_f32 v[168:169], v[168:169], s[46:47]
v_pk_add_f32 v[170:171], v[170:171], s[46:47]
v_rcp_f32 v168, v168
v_rcp_f32 v169, v169
v_rcp_f32 v170, v170
v_rcp_f32 v171, v171
v_pk_mul_f32 v[168:169], v[168:169], v[200:201]
v_pk_mul_f32 v[170:171], v[170:171], v[202:203]
v_cvt_pk_bf16_f32 v16, v168, v169
v_cvt_pk_bf16_f32 v17, v170, v171
s_add_u32 s42, s44, 0x2000
buffer_store_dwordx2 v[16:17], v0, s[76:79], s42 offen sc1
v_pk_mul_f32 v[164:165], v[164:165], s[36:37]
v_pk_mul_f32 v[166:167], v[166:167], s[36:37]
v_exp_f32 v164, v164
v_exp_f32 v165, v165
v_exp_f32 v166, v166
v_exp_f32 v167, v167
v_pk_add_f32 v[164:165], v[164:165], s[46:47]
v_pk_add_f32 v[166:167], v[166:167], s[46:47]
v_rcp_f32 v164, v164
v_rcp_f32 v165, v165
v_rcp_f32 v166, v166
v_rcp_f32 v167, v167
v_pk_mul_f32 v[164:165], v[164:165], v[196:197]
v_pk_mul_f32 v[166:167], v[166:167], v[198:199]
v_cvt_pk_bf16_f32 v12, v164, v165
v_cvt_pk_bf16_f32 v13, v166, v167
s_add_u32 s42, s44, 0x1800
buffer_store_dwordx2 v[12:13], v0, s[76:79], s42 offen sc1
v_pk_mul_f32 v[160:161], v[160:161], s[36:37]
v_pk_mul_f32 v[162:163], v[162:163], s[36:37]
v_exp_f32 v160, v160
v_exp_f32 v161, v161
v_exp_f32 v162, v162
v_exp_f32 v163, v163
v_pk_add_f32 v[160:161], v[160:161], s[46:47]
v_pk_add_f32 v[162:163], v[162:163], s[46:47]
v_rcp_f32 v160, v160
v_rcp_f32 v161, v161
v_rcp_f32 v162, v162
v_rcp_f32 v163, v163
v_pk_mul_f32 v[160:161], v[160:161], v[192:193]
v_pk_mul_f32 v[162:163], v[162:163], v[194:195]
v_cvt_pk_bf16_f32 v16, v160, v161
v_cvt_pk_bf16_f32 v17, v162, v163
s_add_u32 s42, s44, 0x1000
buffer_store_dwordx2 v[16:17], v0, s[76:79], s42 offen sc1
v_pk_mul_f32 v[156:157], v[156:157], s[36:37]
v_pk_mul_f32 v[158:159], v[158:159], s[36:37]
v_exp_f32 v156, v156
v_exp_f32 v157, v157
v_exp_f32 v158, v158
v_exp_f32 v159, v159
v_pk_add_f32 v[156:157], v[156:157], s[46:47]
v_pk_add_f32 v[158:159], v[158:159], s[46:47]
v_rcp_f32 v156, v156
v_rcp_f32 v157, v157
v_rcp_f32 v158, v158
v_rcp_f32 v159, v159
v_pk_mul_f32 v[156:157], v[156:157], v[188:189]
v_pk_mul_f32 v[158:159], v[158:159], v[190:191]
v_cvt_pk_bf16_f32 v12, v156, v157
v_cvt_pk_bf16_f32 v13, v158, v159
s_add_u32 s42, s44, 0x800
buffer_store_dwordx2 v[12:13], v0, s[76:79], s42 offen sc1
v_pk_mul_f32 v[152:153], v[152:153], s[36:37]
v_pk_mul_f32 v[154:155], v[154:155], s[36:37]
v_exp_f32 v152, v152
v_exp_f32 v153, v153
v_exp_f32 v154, v154
v_exp_f32 v155, v155
v_pk_add_f32 v[152:153], v[152:153], s[46:47]
v_pk_add_f32 v[154:155], v[154:155], s[46:47]
v_rcp_f32 v152, v152
v_rcp_f32 v153, v153
v_rcp_f32 v154, v154
v_rcp_f32 v155, v155
v_pk_mul_f32 v[152:153], v[152:153], v[184:185]
v_pk_mul_f32 v[154:155], v[154:155], v[186:187]
v_cvt_pk_bf16_f32 v16, v152, v153
v_cvt_pk_bf16_f32 v17, v154, v155
s_add_u32 s42, s44, 0
buffer_store_dwordx2 v[16:17], v0, s[76:79], s42 offen sc1
v_pk_mul_f32 v[24:25], v[24:25], s[36:37]
v_pk_mul_f32 v[26:27], v[26:27], s[36:37]
v_exp_f32 v24, v24
v_exp_f32 v25, v25
v_exp_f32 v26, v26
v_exp_f32 v27, v27
v_pk_add_f32 v[24:25], v[24:25], s[46:47]
v_pk_add_f32 v[26:27], v[26:27], s[46:47]
v_rcp_f32 v24, v24
v_rcp_f32 v25, v25
v_rcp_f32 v26, v26
v_rcp_f32 v27, v27
v_pk_mul_f32 v[24:25], v[24:25], v[56:57]
v_pk_mul_f32 v[26:27], v[26:27], v[58:59]
v_cvt_pk_bf16_f32 v192, v24, v25
v_cvt_pk_bf16_f32 v193, v26, v27
v_pk_mul_f32 v[88:89], v[88:89], s[36:37]
v_pk_mul_f32 v[90:91], v[90:91], s[36:37]
v_exp_f32 v88, v88
v_exp_f32 v89, v89
v_exp_f32 v90, v90
v_exp_f32 v91, v91
v_pk_add_f32 v[88:89], v[88:89], s[46:47]
v_pk_add_f32 v[90:91], v[90:91], s[46:47]
v_rcp_f32 v88, v88
v_rcp_f32 v89, v89
v_rcp_f32 v90, v90
v_rcp_f32 v91, v91
v_pk_mul_f32 v[88:89], v[88:89], v[120:121]
v_pk_mul_f32 v[90:91], v[90:91], v[122:123]
v_cvt_pk_bf16_f32 v194, v88, v89
v_cvt_pk_bf16_f32 v195, v90, v91
s_add_u32 s42, s43, 0
buffer_store_dwordx4 v[192:195], v0, s[76:79], s42 offen sc1
v_pk_mul_f32 v[28:29], v[28:29], s[36:37]
v_pk_mul_f32 v[30:31], v[30:31], s[36:37]
v_exp_f32 v28, v28
v_exp_f32 v29, v29
v_exp_f32 v30, v30
v_exp_f32 v31, v31
v_pk_add_f32 v[28:29], v[28:29], s[46:47]
v_pk_add_f32 v[30:31], v[30:31], s[46:47]
v_rcp_f32 v28, v28
v_rcp_f32 v29, v29
v_rcp_f32 v30, v30
v_rcp_f32 v31, v31
v_pk_mul_f32 v[28:29], v[28:29], v[60:61]
v_pk_mul_f32 v[30:31], v[30:31], v[62:63]
v_cvt_pk_bf16_f32 v196, v28, v29
v_cvt_pk_bf16_f32 v197, v30, v31
v_pk_mul_f32 v[92:93], v[92:93], s[36:37]
v_pk_mul_f32 v[94:95], v[94:95], s[36:37]
v_exp_f32 v92, v92
v_exp_f32 v93, v93
v_exp_f32 v94, v94
v_exp_f32 v95, v95
v_pk_add_f32 v[92:93], v[92:93], s[46:47]
v_pk_add_f32 v[94:95], v[94:95], s[46:47]
v_rcp_f32 v92, v92
v_rcp_f32 v93, v93
v_rcp_f32 v94, v94
v_rcp_f32 v95, v95
v_pk_mul_f32 v[92:93], v[92:93], v[124:125]
v_pk_mul_f32 v[94:95], v[94:95], v[126:127]
v_cvt_pk_bf16_f32 v198, v92, v93
v_cvt_pk_bf16_f32 v199, v94, v95
s_add_u32 s42, s43, 0x800
buffer_store_dwordx4 v[196:199], v0, s[76:79], s42 offen sc1
v_pk_mul_f32 v[32:33], v[32:33], s[36:37]
v_pk_mul_f32 v[34:35], v[34:35], s[36:37]
v_exp_f32 v32, v32
v_exp_f32 v33, v33
v_exp_f32 v34, v34
v_exp_f32 v35, v35
v_pk_add_f32 v[32:33], v[32:33], s[46:47]
v_pk_add_f32 v[34:35], v[34:35], s[46:47]
v_rcp_f32 v32, v32
v_rcp_f32 v33, v33
v_rcp_f32 v34, v34
v_rcp_f32 v35, v35
v_pk_mul_f32 v[32:33], v[32:33], v[64:65]
v_pk_mul_f32 v[34:35], v[34:35], v[66:67]
v_cvt_pk_bf16_f32 v200, v32, v33
v_cvt_pk_bf16_f32 v201, v34, v35
v_pk_mul_f32 v[96:97], v[96:97], s[36:37]
v_pk_mul_f32 v[98:99], v[98:99], s[36:37]
v_exp_f32 v96, v96
v_exp_f32 v97, v97
v_exp_f32 v98, v98
v_exp_f32 v99, v99
v_pk_add_f32 v[96:97], v[96:97], s[46:47]
v_pk_add_f32 v[98:99], v[98:99], s[46:47]
v_rcp_f32 v96, v96
v_rcp_f32 v97, v97
v_rcp_f32 v98, v98
v_rcp_f32 v99, v99
v_pk_mul_f32 v[96:97], v[96:97], v[128:129]
v_pk_mul_f32 v[98:99], v[98:99], v[130:131]
v_cvt_pk_bf16_f32 v202, v96, v97
v_cvt_pk_bf16_f32 v203, v98, v99
s_add_u32 s42, s43, 0x1000
buffer_store_dwordx4 v[200:203], v0, s[76:79], s42 offen sc1
v_pk_mul_f32 v[36:37], v[36:37], s[36:37]
v_pk_mul_f32 v[38:39], v[38:39], s[36:37]
v_exp_f32 v36, v36
v_exp_f32 v37, v37
v_exp_f32 v38, v38
v_exp_f32 v39, v39
v_pk_add_f32 v[36:37], v[36:37], s[46:47]
v_pk_add_f32 v[38:39], v[38:39], s[46:47]
v_rcp_f32 v36, v36
v_rcp_f32 v37, v37
v_rcp_f32 v38, v38
v_rcp_f32 v39, v39
v_pk_mul_f32 v[36:37], v[36:37], v[68:69]
v_pk_mul_f32 v[38:39], v[38:39], v[70:71]
v_cvt_pk_bf16_f32 v204, v36, v37
v_cvt_pk_bf16_f32 v205, v38, v39
v_pk_mul_f32 v[100:101], v[100:101], s[36:37]
v_pk_mul_f32 v[102:103], v[102:103], s[36:37]
v_exp_f32 v100, v100
v_exp_f32 v101, v101
v_exp_f32 v102, v102
v_exp_f32 v103, v103
v_pk_add_f32 v[100:101], v[100:101], s[46:47]
v_pk_add_f32 v[102:103], v[102:103], s[46:47]
v_rcp_f32 v100, v100
v_rcp_f32 v101, v101
v_rcp_f32 v102, v102
v_rcp_f32 v103, v103
v_pk_mul_f32 v[100:101], v[100:101], v[132:133]
v_pk_mul_f32 v[102:103], v[102:103], v[134:135]
v_cvt_pk_bf16_f32 v206, v100, v101
v_cvt_pk_bf16_f32 v207, v102, v103
s_add_u32 s42, s43, 0x1800
buffer_store_dwordx4 v[204:207], v0, s[76:79], s42 offen sc1
v_pk_mul_f32 v[40:41], v[40:41], s[36:37]
v_pk_mul_f32 v[42:43], v[42:43], s[36:37]
v_exp_f32 v40, v40
v_exp_f32 v41, v41
v_exp_f32 v42, v42
v_exp_f32 v43, v43
v_pk_add_f32 v[40:41], v[40:41], s[46:47]
v_pk_add_f32 v[42:43], v[42:43], s[46:47]
v_rcp_f32 v40, v40
v_rcp_f32 v41, v41
v_rcp_f32 v42, v42
v_rcp_f32 v43, v43
v_pk_mul_f32 v[40:41], v[40:41], v[72:73]
v_pk_mul_f32 v[42:43], v[42:43], v[74:75]
v_cvt_pk_bf16_f32 v208, v40, v41
v_cvt_pk_bf16_f32 v209, v42, v43
v_pk_mul_f32 v[104:105], v[104:105], s[36:37]
v_pk_mul_f32 v[106:107], v[106:107], s[36:37]
v_exp_f32 v104, v104
v_exp_f32 v105, v105
v_exp_f32 v106, v106
v_exp_f32 v107, v107
v_pk_add_f32 v[104:105], v[104:105], s[46:47]
v_pk_add_f32 v[106:107], v[106:107], s[46:47]
v_rcp_f32 v104, v104
v_rcp_f32 v105, v105
v_rcp_f32 v106, v106
v_rcp_f32 v107, v107
v_pk_mul_f32 v[104:105], v[104:105], v[136:137]
v_pk_mul_f32 v[106:107], v[106:107], v[138:139]
v_cvt_pk_bf16_f32 v210, v104, v105
v_cvt_pk_bf16_f32 v211, v106, v107
s_add_u32 s42, s43, 0x2000
buffer_store_dwordx4 v[208:211], v0, s[76:79], s42 offen sc1
v_pk_mul_f32 v[44:45], v[44:45], s[36:37]
v_pk_mul_f32 v[46:47], v[46:47], s[36:37]
v_exp_f32 v44, v44
v_exp_f32 v45, v45
v_exp_f32 v46, v46
v_exp_f32 v47, v47
v_pk_add_f32 v[44:45], v[44:45], s[46:47]
v_pk_add_f32 v[46:47], v[46:47], s[46:47]
v_rcp_f32 v44, v44
v_rcp_f32 v45, v45
v_rcp_f32 v46, v46
v_rcp_f32 v47, v47
v_pk_mul_f32 v[44:45], v[44:45], v[76:77]
v_pk_mul_f32 v[46:47], v[46:47], v[78:79]
v_cvt_pk_bf16_f32 v212, v44, v45
v_cvt_pk_bf16_f32 v213, v46, v47
v_pk_mul_f32 v[108:109], v[108:109], s[36:37]
v_pk_mul_f32 v[110:111], v[110:111], s[36:37]
v_exp_f32 v108, v108
v_exp_f32 v109, v109
v_exp_f32 v110, v110
v_exp_f32 v111, v111
v_pk_add_f32 v[108:109], v[108:109], s[46:47]
v_pk_add_f32 v[110:111], v[110:111], s[46:47]
v_rcp_f32 v108, v108
v_rcp_f32 v109, v109
v_rcp_f32 v110, v110
v_rcp_f32 v111, v111
v_pk_mul_f32 v[108:109], v[108:109], v[140:141]
v_pk_mul_f32 v[110:111], v[110:111], v[142:143]
v_cvt_pk_bf16_f32 v214, v108, v109
v_cvt_pk_bf16_f32 v215, v110, v111
s_add_u32 s42, s43, 0x2800
buffer_store_dwordx4 v[212:215], v0, s[76:79], s42 offen sc1
v_pk_mul_f32 v[48:49], v[48:49], s[36:37]
v_pk_mul_f32 v[50:51], v[50:51], s[36:37]
v_exp_f32 v48, v48
v_exp_f32 v49, v49
v_exp_f32 v50, v50
v_exp_f32 v51, v51
v_pk_add_f32 v[48:49], v[48:49], s[46:47]
v_pk_add_f32 v[50:51], v[50:51], s[46:47]
v_rcp_f32 v48, v48
v_rcp_f32 v49, v49
v_rcp_f32 v50, v50
v_rcp_f32 v51, v51
v_pk_mul_f32 v[48:49], v[48:49], v[80:81]
v_pk_mul_f32 v[50:51], v[50:51], v[82:83]
v_cvt_pk_bf16_f32 v216, v48, v49
v_cvt_pk_bf16_f32 v217, v50, v51
v_pk_mul_f32 v[112:113], v[112:113], s[36:37]
v_pk_mul_f32 v[114:115], v[114:115], s[36:37]
v_exp_f32 v112, v112
v_exp_f32 v113, v113
v_exp_f32 v114, v114
v_exp_f32 v115, v115
v_pk_add_f32 v[112:113], v[112:113], s[46:47]
v_pk_add_f32 v[114:115], v[114:115], s[46:47]
v_rcp_f32 v112, v112
v_rcp_f32 v113, v113
v_rcp_f32 v114, v114
v_rcp_f32 v115, v115
v_pk_mul_f32 v[112:113], v[112:113], v[144:145]
v_pk_mul_f32 v[114:115], v[114:115], v[146:147]
v_cvt_pk_bf16_f32 v218, v112, v113
v_cvt_pk_bf16_f32 v219, v114, v115
s_add_u32 s42, s43, 0x3000
buffer_store_dwordx4 v[216:219], v0, s[76:79], s42 offen sc1
v_pk_mul_f32 v[52:53], v[52:53], s[36:37]
v_pk_mul_f32 v[54:55], v[54:55], s[36:37]
v_exp_f32 v52, v52
v_exp_f32 v53, v53
v_exp_f32 v54, v54
v_exp_f32 v55, v55
v_pk_add_f32 v[52:53], v[52:53], s[46:47]
v_pk_add_f32 v[54:55], v[54:55], s[46:47]
v_rcp_f32 v52, v52
v_rcp_f32 v53, v53
v_rcp_f32 v54, v54
v_rcp_f32 v55, v55
v_pk_mul_f32 v[52:53], v[52:53], v[84:85]
v_pk_mul_f32 v[54:55], v[54:55], v[86:87]
v_cvt_pk_bf16_f32 v220, v52, v53
v_cvt_pk_bf16_f32 v221, v54, v55
v_pk_mul_f32 v[116:117], v[116:117], s[36:37]
v_pk_mul_f32 v[118:119], v[118:119], s[36:37]
v_exp_f32 v116, v116
v_exp_f32 v117, v117
v_exp_f32 v118, v118
v_exp_f32 v119, v119
v_pk_add_f32 v[116:117], v[116:117], s[46:47]
v_pk_add_f32 v[118:119], v[118:119], s[46:47]
v_rcp_f32 v116, v116
v_rcp_f32 v117, v117
v_rcp_f32 v118, v118
v_rcp_f32 v119, v119
v_pk_mul_f32 v[116:117], v[116:117], v[148:149]
v_pk_mul_f32 v[118:119], v[118:119], v[150:151]
v_cvt_pk_bf16_f32 v222, v116, v117
v_cvt_pk_bf16_f32 v223, v118, v119
s_add_u32 s42, s43, 0x3800
buffer_store_dwordx4 v[220:223], v0, s[76:79], s42 offen sc1
s_mov_b32 s97, s43
s_mov_b32 s96, 0x7ffffff0
s_waitcnt vmcnt(16) lgkmcnt(0)
JOIN_%=:
v_mbcnt_lo_u32_b32 v9, -1, 0
v_mbcnt_hi_u32_b32 v9, -1, v9
v_lshrrev_b32 v10, 4, v9
v_and_b32 v11, 15, v9
v_lshlrev_b32 v11, 4, v11
v_lshl_add_u32 v20, v10, 14, v11
v_add_u32 v21, 0x3e0, v20
v_add_u32 v22, 0x7c0, v20
v_add_u32 v23, 0xba0, v20
v_bfe_u32 v17, v9, 2, 2
v_and_b32 v18, 1, v10
v_lshl_add_u32 v18, v18, 2, v17
s_movk_i32 s34, 0x420
v_mul_u32_u24 v18, s34, v18
v_lshrrev_b32 v19, 1, v10
v_lshlrev_b32 v19, 8, v19
v_and_b32 v17, 3, v9
v_lshlrev_b32 v17, 3, v17
v_add3_u32 v188, v18, v19, v17
v_add_u32 v188, 0xb1c0, v188
v_add_u32 v189, 0xc600, v188
v_add_u32 v190, 0x18c00, v188
v_mbcnt_lo_u32_b32 v9, -1, 0
v_mbcnt_hi_u32_b32 v9, -1, v9
v_lshlrev_b32 v0, 4, v9
v_lshrrev_b32 v10, 4, v9
)ASM"
R"ASM(v_and_b32 v11, 15, v9
v_add_u32 v17, 0, v10
v_xor_b32 v17, v17, v11
v_lshlrev_b32 v17, 4, v17
v_lshl_add_u32 v12, v11, 8, v17
v_add_u32 v17, 4, v10
v_xor_b32 v17, v17, v11
v_lshlrev_b32 v17, 4, v17
v_lshl_add_u32 v13, v11, 8, v17
v_add_u32 v17, 8, v10
v_xor_b32 v17, v17, v11
v_lshlrev_b32 v17, 4, v17
v_lshl_add_u32 v14, v11, 8, v17
v_add_u32 v17, 12, v10
v_xor_b32 v17, v17, v11
v_lshlrev_b32 v17, 4, v17
v_lshl_add_u32 v15, v11, 8, v17
v_bfe_u32 v17, v9, 2, 2
v_and_b32 v18, 1, v10
v_lshl_add_u32 v18, v18, 2, v17
s_movk_i32 s34, 0x420
v_mul_u32_u24 v18, s34, v18
v_lshrrev_b32 v19, 1, v10
v_lshlrev_b32 v19, 8, v19
v_and_b32 v17, 3, v9
v_lshlrev_b32 v17, 3, v17
v_add3_u32 v16, v18, v19, v17
v_add_u32 v16, 0x8000, v16
v_mov_b32 v184, 0x3f803f80
v_mov_b32 v185, 0x3f803f80
v_mov_b32 v186, 0x3f803f80
v_mov_b32 v187, 0x3f803f80
s_lshl_b32 s34, s4, 14
s_add_u32 s34, s34, 0x13200
v_add_u32 v17, s34, v0
s_mov_b32 s71, 0
s_barrier
ds_read_b128 a[128:131], v17
ds_read_b128 a[132:135], v17 offset:1024
ds_read_b128 a[136:139], v17 offset:2048
ds_read_b128 a[140:143], v17 offset:3072
ds_read_b128 a[144:147], v17 offset:4096
ds_read_b128 a[148:151], v17 offset:5120
ds_read_b128 a[152:155], v17 offset:6144
ds_read_b128 a[156:159], v17 offset:7168
ds_read_b128 a[160:163], v17 offset:8192
ds_read_b128 a[164:167], v17 offset:9216
ds_read_b128 a[168:171], v17 offset:10240
ds_read_b128 a[172:175], v17 offset:11264
ds_read_b128 a[176:179], v17 offset:12288
ds_read_b128 a[180:183], v17 offset:13312
ds_read_b128 a[184:187], v17 offset:14336
ds_read_b128 a[188:191], v17 offset:15360
ATT_START_%=:
ds_read_b128 a[192:195], v12
ds_read_b128 a[196:199], v13
ds_read_b128 a[200:203], v14
ds_read_b128 a[204:207], v15
s_waitcnt lgkmcnt(3)
v_mfma_f32_16x16x32_bf16 v[24:27], a[192:195], a[128:131], 0
s_waitcnt lgkmcnt(2)
v_mfma_f32_16x16x32_bf16 v[24:27], a[196:199], a[132:135], v[24:27]
ds_read_b128 a[208:211], v12 offset:4096
s_waitcnt lgkmcnt(2)
v_mfma_f32_16x16x32_bf16 v[24:27], a[200:203], a[136:139], v[24:27]
s_waitcnt lgkmcnt(1)
v_mfma_f32_16x16x32_bf16 v[24:27], a[204:207], a[140:143], v[24:27]
v_mfma_f32_16x16x32_bf16 v[28:31], a[192:195], a[144:147], 0
ds_read_b128 a[212:215], v13 offset:4096
v_mfma_f32_16x16x32_bf16 v[28:31], a[196:199], a[148:151], v[28:31]
v_mfma_f32_16x16x32_bf16 v[28:31], a[200:203], a[152:155], v[28:31]
v_mfma_f32_16x16x32_bf16 v[28:31], a[204:207], a[156:159], v[28:31]
ds_read_b128 a[216:219], v14 offset:4096
v_mfma_f32_16x16x32_bf16 v[32:35], a[192:195], a[160:163], 0
v_mfma_f32_16x16x32_bf16 v[32:35], a[196:199], a[164:167], v[32:35]
v_mfma_f32_16x16x32_bf16 v[32:35], a[200:203], a[168:171], v[32:35]
ds_read_b128 a[220:223], v15 offset:4096
v_mfma_f32_16x16x32_bf16 v[32:35], a[204:207], a[172:175], v[32:35]
v_mfma_f32_16x16x32_bf16 v[36:39], a[192:195], a[176:179], 0
v_mfma_f32_16x16x32_bf16 v[36:39], a[196:199], a[180:183], v[36:39]
v_mfma_f32_16x16x32_bf16 v[36:39], a[200:203], a[184:187], v[36:39]
v_mfma_f32_16x16x32_bf16 v[36:39], a[204:207], a[188:191], v[36:39]
s_waitcnt lgkmcnt(1)
v_mfma_f32_16x16x32_bf16 v[40:43], a[208:211], a[128:131], 0
v_mfma_f32_16x16x32_bf16 v[40:43], a[212:215], a[132:135], v[40:43]
ds_read_b128 a[192:195], v12 offset:8192
v_mfma_f32_16x16x32_bf16 v[40:43], a[216:219], a[136:139], v[40:43]
s_waitcnt lgkmcnt(1)
v_mfma_f32_16x16x32_bf16 v[40:43], a[220:223], a[140:143], v[40:43]
v_mfma_f32_16x16x32_bf16 v[44:47], a[208:211], a[144:147], 0
ds_read_b128 a[196:199], v13 offset:8192
v_mfma_f32_16x16x32_bf16 v[44:47], a[212:215], a[148:151], v[44:47]
v_mfma_f32_16x16x32_bf16 v[44:47], a[216:219], a[152:155], v[44:47]
v_mfma_f32_16x16x32_bf16 v[44:47], a[220:223], a[156:159], v[44:47]
ds_read_b128 a[200:203], v14 offset:8192
v_mfma_f32_16x16x32_bf16 v[48:51], a[208:211], a[160:163], 0
v_mfma_f32_16x16x32_bf16 v[48:51], a[212:215], a[164:167], v[48:51]
v_mfma_f32_16x16x32_bf16 v[48:51], a[216:219], a[168:171], v[48:51]
ds_read_b128 a[204:207], v15 offset:8192
v_mfma_f32_16x16x32_bf16 v[48:51], a[220:223], a[172:175], v[48:51]
v_mfma_f32_16x16x32_bf16 v[52:55], a[208:211], a[176:179], 0
v_mfma_f32_16x16x32_bf16 v[52:55], a[212:215], a[180:183], v[52:55]
v_mfma_f32_16x16x32_bf16 v[52:55], a[216:219], a[184:187], v[52:55]
v_mfma_f32_16x16x32_bf16 v[52:55], a[220:223], a[188:191], v[52:55]
s_waitcnt lgkmcnt(1)
v_mfma_f32_16x16x32_bf16 v[56:59], a[192:195], a[128:131], 0
v_mfma_f32_16x16x32_bf16 v[56:59], a[196:199], a[132:135], v[56:59]
ds_read_b128 a[208:211], v12 offset:12288
v_mfma_f32_16x16x32_bf16 v[56:59], a[200:203], a[136:139], v[56:59]
s_waitcnt lgkmcnt(1)
v_mfma_f32_16x16x32_bf16 v[56:59], a[204:207], a[140:143], v[56:59]
v_mfma_f32_16x16x32_bf16 v[60:63], a[192:195], a[144:147], 0
ds_read_b128 a[212:215], v13 offset:12288
v_mfma_f32_16x16x32_bf16 v[60:63], a[196:199], a[148:151], v[60:63]
v_mfma_f32_16x16x32_bf16 v[60:63], a[200:203], a[152:155], v[60:63]
v_mfma_f32_16x16x32_bf16 v[60:63], a[204:207], a[156:159], v[60:63]
ds_read_b128 a[216:219], v14 offset:12288
v_mfma_f32_16x16x32_bf16 v[64:67], a[192:195], a[160:163], 0
v_mfma_f32_16x16x32_bf16 v[64:67], a[196:199], a[164:167], v[64:67]
v_mfma_f32_16x16x32_bf16 v[64:67], a[200:203], a[168:171], v[64:67]
ds_read_b128 a[220:223], v15 offset:12288
v_mfma_f32_16x16x32_bf16 v[64:67], a[204:207], a[172:175], v[64:67]
v_mfma_f32_16x16x32_bf16 v[68:71], a[192:195], a[176:179], 0
v_mfma_f32_16x16x32_bf16 v[68:71], a[196:199], a[180:183], v[68:71]
v_mfma_f32_16x16x32_bf16 v[68:71], a[200:203], a[184:187], v[68:71]
v_mfma_f32_16x16x32_bf16 v[68:71], a[204:207], a[188:191], v[68:71]
s_waitcnt lgkmcnt(1)
v_mfma_f32_16x16x32_bf16 v[72:75], a[208:211], a[128:131], 0
v_mfma_f32_16x16x32_bf16 v[72:75], a[212:215], a[132:135], v[72:75]
v_mfma_f32_16x16x32_bf16 v[72:75], a[216:219], a[136:139], v[72:75]
ds_read_b128 a[192:195], v12 offset:16384
s_waitcnt lgkmcnt(1)
v_mfma_f32_16x16x32_bf16 v[72:75], a[220:223], a[140:143], v[72:75]
v_mfma_f32_16x16x32_bf16 v[76:79], a[208:211], a[144:147], 0
v_mfma_f32_16x16x32_bf16 v[76:79], a[212:215], a[148:151], v[76:79]
ds_read_b128 a[196:199], v13 offset:16384
v_mfma_f32_16x16x32_bf16 v[76:79], a[216:219], a[152:155], v[76:79]
v_mfma_f32_16x16x32_bf16 v[76:79], a[220:223], a[156:159], v[76:79]
v_mfma_f32_16x16x32_bf16 v[80:83], a[208:211], a[160:163], 0
ds_read_b128 a[200:203], v14 offset:16384
v_mfma_f32_16x16x32_bf16 v[80:83], a[212:215], a[164:167], v[80:83]
v_mfma_f32_16x16x32_bf16 v[80:83], a[216:219], a[168:171], v[80:83]
v_mfma_f32_16x16x32_bf16 v[80:83], a[220:223], a[172:175], v[80:83]
ds_read_b128 a[204:207], v15 offset:16384
v_mfma_f32_16x16x32_bf16 v[84:87], a[208:211], a[176:179], 0
v_mfma_f32_16x16x32_bf16 v[84:87], a[212:215], a[180:183], v[84:87]
v_mfma_f32_16x16x32_bf16 v[84:87], a[216:219], a[184:187], v[84:87]
v_mfma_f32_16x16x32_bf16 v[84:87], a[220:223], a[188:191], v[84:87]
s_nop 7
s_cmp_lg_u32 s71, 0
s_cbranch_scc1 SKFmx_%=
v_max3_f32 v168, v24, v25, v26
v_max3_f32 v168, v168, v27, v40
v_max3_f32 v168, v168, v41, v42
v_max3_f32 v168, v168, v43, v56
v_max3_f32 v168, v168, v57, v58
v_max3_f32 v168, v168, v59, v72
v_max3_f32 v168, v168, v73, v74
v_max_f32 v168, v168, v75
v_max3_f32 v172, v28, v29, v30
v_max3_f32 v172, v172, v31, v44
v_max3_f32 v172, v172, v45, v46
v_max3_f32 v172, v172, v47, v60
v_max3_f32 v172, v172, v61, v62
v_max3_f32 v172, v172, v63, v76
v_max3_f32 v172, v172, v77, v78
v_max_f32 v172, v172, v79
v_max3_f32 v176, v32, v33, v34
v_max3_f32 v176, v176, v35, v48
v_max3_f32 v176, v176, v49, v50
v_max3_f32 v176, v176, v51, v64
v_max3_f32 v176, v176, v65, v66
v_max3_f32 v176, v176, v67, v80
v_max3_f32 v176, v176, v81, v82
v_max_f32 v176, v176, v83
v_max3_f32 v180, v36, v37, v38
v_max3_f32 v180, v180, v39, v52
v_max3_f32 v180, v180, v53, v54
v_max3_f32 v180, v180, v55, v68
v_max3_f32 v180, v180, v69, v70
v_max3_f32 v180, v180, v71, v84
v_max3_f32 v180, v180, v85, v86
v_max_f32 v180, v180, v87
v_mov_b32 v169, v168
v_mov_b32 v173, v172
v_mov_b32 v177, v176
v_mov_b32 v181, v180
v_permlane16_swap_b32 v168, v169
v_permlane16_swap_b32 v172, v173
v_permlane16_swap_b32 v176, v177
v_permlane16_swap_b32 v180, v181
v_max_f32 v168, v168, v169
v_mov_b32 v169, v168
v_max_f32 v172, v172, v173
v_mov_b32 v173, v172
v_max_f32 v176, v176, v177
v_mov_b32 v177, v176
v_max_f32 v180, v180, v181
v_mov_b32 v181, v180
v_permlane32_swap_b32 v168, v169
v_permlane32_swap_b32 v172, v173
v_permlane32_swap_b32 v176, v177
v_permlane32_swap_b32 v180, v181
v_max_f32 v168, v168, v169
v_max_f32 v172, v172, v173
v_max_f32 v176, v176, v177
v_max_f32 v180, v180, v181
v_sub_f32 v168, 0, v168
v_sub_f32 v172, 0, v172
v_sub_f32 v176, 0, v176
v_sub_f32 v180, 0, v180
v_mov_b32 v169, v168
v_mov_b32 v170, v168
v_mov_b32 v171, v168
v_mov_b32 v173, v172
v_mov_b32 v174, v172
v_mov_b32 v175, v172
v_mov_b32 v177, v176
v_mov_b32 v178, v176
v_mov_b32 v179, v176
v_mov_b32 v181, v180
v_mov_b32 v182, v180
v_mov_b32 v183, v180
SKFmx_%=:
v_add_f32 v24, v168, v24
v_add_f32 v25, v168, v25
v_add_f32 v26, v168, v26
v_add_f32 v27, v168, v27
v_add_f32 v40, v168, v40
v_add_f32 v41, v168, v41
v_add_f32 v42, v168, v42
v_add_f32 v43, v168, v43
v_add_f32 v28, v172, v28
v_add_f32 v29, v172, v29
v_add_f32 v30, v172, v30
v_add_f32 v31, v172, v31
v_add_f32 v44, v172, v44
v_add_f32 v45, v172, v45
v_add_f32 v46, v172, v46
v_add_f32 v47, v172, v47
v_add_f32 v32, v176, v32
v_add_f32 v33, v176, v33
v_add_f32 v34, v176, v34
v_add_f32 v35, v176, v35
v_add_f32 v48, v176, v48
v_add_f32 v49, v176, v49
v_add_f32 v50, v176, v50
v_add_f32 v51, v176, v51
v_add_f32 v36, v180, v36
v_add_f32 v37, v180, v37
v_add_f32 v38, v180, v38
v_add_f32 v39, v180, v39
v_add_f32 v52, v180, v52
v_add_f32 v53, v180, v53
v_add_f32 v54, v180, v54
v_add_f32 v55, v180, v55
v_exp_f32 v24, v24
v_exp_f32 v25, v25
v_exp_f32 v26, v26
v_exp_f32 v27, v27
v_exp_f32 v40, v40
v_exp_f32 v41, v41
v_exp_f32 v42, v42
v_exp_f32 v43, v43
v_exp_f32 v28, v28
v_exp_f32 v29, v29
v_exp_f32 v30, v30
v_exp_f32 v31, v31
v_exp_f32 v44, v44
v_exp_f32 v45, v45
v_exp_f32 v46, v46
v_exp_f32 v47, v47
v_exp_f32 v32, v32
v_exp_f32 v33, v33
v_exp_f32 v34, v34
v_exp_f32 v35, v35
v_exp_f32 v48, v48
v_exp_f32 v49, v49
v_exp_f32 v50, v50
v_exp_f32 v51, v51
v_exp_f32 v36, v36
v_exp_f32 v37, v37
v_exp_f32 v38, v38
v_exp_f32 v39, v39
v_exp_f32 v52, v52
v_exp_f32 v53, v53
v_exp_f32 v54, v54
v_exp_f32 v55, v55
v_cvt_pk_bf16_f32 v120, v24, v25
v_cvt_pk_bf16_f32 v121, v26, v27
v_cvt_pk_bf16_f32 v122, v40, v41
v_cvt_pk_bf16_f32 v123, v42, v43
v_cvt_pk_bf16_f32 v124, v28, v29
v_cvt_pk_bf16_f32 v125, v30, v31
v_cvt_pk_bf16_f32 v126, v44, v45
v_cvt_pk_bf16_f32 v127, v46, v47
v_cvt_pk_bf16_f32 v128, v32, v33
v_cvt_pk_bf16_f32 v129, v34, v35
v_cvt_pk_bf16_f32 v130, v48, v49
v_cvt_pk_bf16_f32 v131, v50, v51
v_cvt_pk_bf16_f32 v132, v36, v37
v_cvt_pk_bf16_f32 v133, v38, v39
v_cvt_pk_bf16_f32 v134, v52, v53
v_cvt_pk_bf16_f32 v135, v54, v55
s_waitcnt lgkmcnt(0)
s_barrier
s_cmp_lg_u32 s71, 0
s_cbranch_scc1 SKFpf_%=
s_cmp_eq_u32 s4, 0
s_cbranch_scc1 OOLwpf_%=
RETwpf_%=:
SKFpf_%=:
v_mfma_f32_16x16x32_bf16 v[24:27], a[192:195], a[128:131], v[168:171]
v_add_f32 v56, v168, v56
v_add_f32 v57, v168, v57
v_mfma_f32_16x16x32_bf16 v[24:27], a[196:199], a[132:135], v[24:27]
s_add_u32 m0, s28, 0
v_add_f32 v58, v168, v58
v_mfma_f32_16x16x32_bf16 v[24:27], a[200:203], a[136:139], v[24:27]
ds_read_b128 a[208:211], v12 offset:20480
v_add_f32 v59, v168, v59
v_mfma_f32_16x16x32_bf16 v[24:27], a[204:207], a[140:143], v[24:27]
buffer_load_dwordx4 v1, s[8:11], s24 offen lds
v_mfma_f32_16x16x32_bf16 v[28:31], a[192:195], a[144:147], v[172:175]
v_exp_f32 v56, v56
v_mfma_f32_16x16x32_bf16 v[28:31], a[196:199], a[148:151], v[28:31]
v_exp_f32 v57, v57
v_mfma_f32_16x16x32_bf16 v[28:31], a[200:203], a[152:155], v[28:31]
ds_read_b128 a[212:215], v13 offset:20480
v_exp_f32 v58, v58
v_mfma_f32_16x16x32_bf16 v[28:31], a[204:207], a[156:159], v[28:31]
v_exp_f32 v59, v59
v_mfma_f32_16x16x32_bf16 v[32:35], a[192:195], a[160:163], v[176:179]
v_add_f32 v72, v168, v72
v_cvt_pk_bf16_f32 v152, v56, v57
v_mfma_f32_16x16x32_bf16 v[32:35], a[196:199], a[164:167], v[32:35]
ds_read_b128 a[216:219], v14 offset:20480
buffer_load_dwordx4 v2, s[8:11], s24 offen offset:1024 lds
v_add_f32 v73, v168, v73
v_mfma_f32_16x16x32_bf16 v[32:35], a[200:203], a[168:171], v[32:35]
v_add_f32 v74, v168, v74
v_cvt_pk_bf16_f32 v153, v58, v59
v_mfma_f32_16x16x32_bf16 v[32:35], a[204:207], a[172:175], v[32:35]
v_add_f32 v75, v168, v75
ds_read_b128 a[220:223], v15 offset:20480
v_mfma_f32_16x16x32_bf16 v[36:39], a[192:195], a[176:179], v[180:183]
v_exp_f32 v72, v72
v_mfma_f32_16x16x32_bf16 v[36:39], a[196:199], a[180:183], v[36:39]
v_exp_f32 v73, v73
v_mfma_f32_16x16x32_bf16 v[36:39], a[200:203], a[184:187], v[36:39]
buffer_load_dwordx4 v3, s[8:11], s24 offen offset:2048 lds
v_exp_f32 v74, v74
v_mfma_f32_16x16x32_bf16 v[36:39], a[204:207], a[188:191], v[36:39]
v_exp_f32 v75, v75
s_waitcnt lgkmcnt(1)
v_mfma_f32_16x16x32_bf16 v[40:43], a[208:211], a[128:131], v[168:171]
v_add_f32 v60, v172, v60
v_cvt_pk_bf16_f32 v154, v72, v73
v_mfma_f32_16x16x32_bf16 v[40:43], a[212:215], a[132:135], v[40:43]
v_add_f32 v61, v172, v61
v_add_f32 v62, v172, v62
v_mfma_f32_16x16x32_bf16 v[40:43], a[216:219], a[136:139], v[40:43]
v_cvt_pk_bf16_f32 v155, v74, v75
ds_read_b128 a[192:195], v12 offset:24576
s_waitcnt lgkmcnt(1)
v_mfma_f32_16x16x32_bf16 v[40:43], a[220:223], a[140:143], v[40:43]
buffer_load_dwordx4 v4, s[8:11], s24 offen offset:3072 lds
s_add_u32 s24, s24, 0x4000
v_add_f32 v63, v172, v63
v_mfma_f32_16x16x32_bf16 v[44:47], a[208:211], a[144:147], v[172:175]
v_exp_f32 v60, v60
v_exp_f32 v61, v61
v_mfma_f32_16x16x32_bf16 v[44:47], a[212:215], a[148:151], v[44:47]
v_exp_f32 v62, v62
v_mfma_f32_16x16x32_bf16 v[44:47], a[216:219], a[152:155], v[44:47]
ds_read_b128 a[196:199], v13 offset:24576
s_add_u32 m0, s29, 0x4200
v_exp_f32 v63, v63
v_mfma_f32_16x16x32_bf16 v[44:47], a[220:223], a[156:159], v[44:47]
v_add_f32 v76, v172, v76
v_cvt_pk_bf16_f32 v156, v60, v61
v_mfma_f32_16x16x32_bf16 v[48:51], a[208:211], a[160:163], v[176:179]
buffer_load_dwordx4 v5, s[12:15], s25 offen lds
v_add_f32 v77, v172, v77
v_mfma_f32_16x16x32_bf16 v[48:51], a[212:215], a[164:167], v[48:51]
ds_read_b128 a[200:203], v14 offset:24576
v_add_f32 v78, v172, v78
v_cvt_pk_bf16_f32 v157, v62, v63
v_mfma_f32_16x16x32_bf16 v[48:51], a[216:219], a[168:171], v[48:51]
v_add_f32 v79, v172, v79
ds_read_b128 a[204:207], v15 offset:24576
v_mfma_f32_16x16x32_bf16 v[48:51], a[220:223], a[172:175], v[48:51]
v_exp_f32 v76, v76
v_mfma_f32_16x16x32_bf16 v[52:55], a[208:211], a[176:179], v[180:183]
v_exp_f32 v77, v77
v_mfma_f32_16x16x32_bf16 v[52:55], a[212:215], a[180:183], v[52:55]
buffer_load_dwordx4 v6, s[12:15], s25 offen offset:1056 lds
v_exp_f32 v78, v78
v_mfma_f32_16x16x32_bf16 v[52:55], a[216:219], a[184:187], v[52:55]
v_exp_f32 v79, v79
)ASM"
R"ASM(v_mfma_f32_16x16x32_bf16 v[52:55], a[220:223], a[188:191], v[52:55]
v_add_f32 v64, v176, v64
v_cvt_pk_bf16_f32 v158, v76, v77
s_waitcnt lgkmcnt(1)
v_mfma_f32_16x16x32_bf16 v[88:91], a[192:195], a[128:131], v[168:171]
v_add_f32 v65, v176, v65
v_add_f32 v66, v176, v66
v_mfma_f32_16x16x32_bf16 v[88:91], a[196:199], a[132:135], v[88:91]
v_cvt_pk_bf16_f32 v159, v78, v79
v_add_f32 v67, v176, v67
v_mfma_f32_16x16x32_bf16 v[88:91], a[200:203], a[136:139], v[88:91]
buffer_load_dwordx4 v7, s[12:15], s25 offen offset:2112 lds
s_waitcnt lgkmcnt(0)
v_mfma_f32_16x16x32_bf16 v[88:91], a[204:207], a[140:143], v[88:91]
ds_read_b128 a[208:211], v12 offset:28672
v_exp_f32 v64, v64
v_mfma_f32_16x16x32_bf16 v[92:95], a[192:195], a[144:147], v[172:175]
v_exp_f32 v65, v65
v_mfma_f32_16x16x32_bf16 v[92:95], a[196:199], a[148:151], v[92:95]
v_exp_f32 v66, v66
v_mfma_f32_16x16x32_bf16 v[92:95], a[200:203], a[152:155], v[92:95]
ds_read_b128 a[212:215], v13 offset:28672
v_exp_f32 v67, v67
v_mfma_f32_16x16x32_bf16 v[92:95], a[204:207], a[156:159], v[92:95]
buffer_load_dwordx4 v8, s[12:15], s25 offen offset:3168 lds
s_add_u32 s25, s25, 0x4000
v_add_f32 v80, v176, v80
v_cvt_pk_bf16_f32 v160, v64, v65
v_mfma_f32_16x16x32_bf16 v[96:99], a[192:195], a[160:163], v[176:179]
v_add_f32 v81, v176, v81
v_add_f32 v82, v176, v82
v_mfma_f32_16x16x32_bf16 v[96:99], a[196:199], a[164:167], v[96:99]
ds_read_b128 a[216:219], v14 offset:28672
v_add_f32 v83, v176, v83
v_cvt_pk_bf16_f32 v161, v66, v67
v_mfma_f32_16x16x32_bf16 v[96:99], a[200:203], a[168:171], v[96:99]
v_exp_f32 v80, v80
v_mfma_f32_16x16x32_bf16 v[96:99], a[204:207], a[172:175], v[96:99]
v_exp_f32 v81, v81
v_mfma_f32_16x16x32_bf16 v[100:103], a[192:195], a[176:179], v[180:183]
ds_read_b128 a[220:223], v15 offset:28672
v_exp_f32 v82, v82
v_mfma_f32_16x16x32_bf16 v[100:103], a[196:199], a[180:183], v[100:103]
v_exp_f32 v83, v83
v_mfma_f32_16x16x32_bf16 v[100:103], a[200:203], a[184:187], v[100:103]
ds_read_b64_tr_b16 a[224:225], v16
v_add_f32 v68, v180, v68
v_cvt_pk_bf16_f32 v162, v80, v81
v_mfma_f32_16x16x32_bf16 v[100:103], a[204:207], a[188:191], v[100:103]
v_add_f32 v69, v180, v69
ds_read_b64_tr_b16 a[226:227], v16 offset:512
s_waitcnt lgkmcnt(3)
v_mfma_f32_16x16x32_bf16 v[104:107], a[208:211], a[128:131], v[168:171]
v_add_f32 v70, v180, v70
v_cvt_pk_bf16_f32 v163, v82, v83
v_mfma_f32_16x16x32_bf16 v[104:107], a[212:215], a[132:135], v[104:107]
v_add_f32 v71, v180, v71
ds_read_b64_tr_b16 a[228:229], v16 offset:8448
v_mfma_f32_16x16x32_bf16 v[104:107], a[216:219], a[136:139], v[104:107]
v_exp_f32 v68, v68
s_waitcnt lgkmcnt(3)
v_mfma_f32_16x16x32_bf16 v[104:107], a[220:223], a[140:143], v[104:107]
v_exp_f32 v69, v69
v_mfma_f32_16x16x32_bf16 v[108:111], a[208:211], a[144:147], v[172:175]
ds_read_b64_tr_b16 a[230:231], v16 offset:8960
v_exp_f32 v70, v70
v_mfma_f32_16x16x32_bf16 v[108:111], a[212:215], a[148:151], v[108:111]
v_exp_f32 v71, v71
v_mfma_f32_16x16x32_bf16 v[108:111], a[216:219], a[152:155], v[108:111]
ds_read_b64_tr_b16 a[232:233], v16 offset:32
v_add_f32 v84, v180, v84
v_cvt_pk_bf16_f32 v164, v68, v69
v_mfma_f32_16x16x32_bf16 v[108:111], a[220:223], a[156:159], v[108:111]
v_add_f32 v85, v180, v85
ds_read_b64_tr_b16 a[234:235], v16 offset:544
v_mfma_f32_16x16x32_bf16 v[112:115], a[208:211], a[160:163], v[176:179]
v_add_f32 v86, v180, v86
v_cvt_pk_bf16_f32 v165, v70, v71
v_mfma_f32_16x16x32_bf16 v[112:115], a[212:215], a[164:167], v[112:115]
v_add_f32 v87, v180, v87
ds_read_b64_tr_b16 a[236:237], v16 offset:8480
v_mfma_f32_16x16x32_bf16 v[112:115], a[216:219], a[168:171], v[112:115]
v_exp_f32 v84, v84
v_mfma_f32_16x16x32_bf16 v[112:115], a[220:223], a[172:175], v[112:115]
v_exp_f32 v85, v85
v_mfma_f32_16x16x32_bf16 v[116:119], a[208:211], a[176:179], v[180:183]
ds_read_b64_tr_b16 a[238:239], v16 offset:8992
v_exp_f32 v86, v86
v_mfma_f32_16x16x32_bf16 v[116:119], a[212:215], a[180:183], v[116:119]
v_exp_f32 v87, v87
v_mfma_f32_16x16x32_bf16 v[116:119], a[216:219], a[184:187], v[116:119]
v_cvt_pk_bf16_f32 v166, v84, v85
v_cvt_pk_bf16_f32 v167, v86, v87
v_mfma_f32_16x16x32_bf16 v[116:119], a[220:223], a[188:191], v[116:119]
s_waitcnt lgkmcnt(1)
v_mfma_f32_16x16x32_bf16 a[0:3], a[224:227], v[120:123], 0
v_exp_f32 v24, v24
v_mfma_f32_16x16x32_bf16 a[0:3], a[228:231], v[152:155], a[0:3]
ds_read_b64_tr_b16 a[208:209], v16 offset:64
v_mfma_f32_16x16x32_bf16 a[4:7], a[224:227], v[124:127], 0
v_exp_f32 v25, v25
v_mfma_f32_16x16x32_bf16 a[4:7], a[228:231], v[156:159], a[4:7]
ds_read_b64_tr_b16 a[210:211], v16 offset:576
v_cvt_pk_bf16_f32 v136, v24, v25
v_mfma_f32_16x16x32_bf16 a[8:11], a[224:227], v[128:131], 0
v_exp_f32 v26, v26
v_mfma_f32_16x16x32_bf16 a[8:11], a[228:231], v[160:163], a[8:11]
ds_read_b64_tr_b16 a[212:213], v16 offset:8512
v_mfma_f32_16x16x32_bf16 a[12:15], a[224:227], v[132:135], 0
v_exp_f32 v27, v27
v_mfma_f32_16x16x32_bf16 a[12:15], a[228:231], v[164:167], a[12:15]
ds_read_b64_tr_b16 a[214:215], v16 offset:9024
v_cvt_pk_bf16_f32 v137, v26, v27
v_mfma_f32_16x16x32_bf16 a[240:243], v[184:187], v[120:123], 0
v_exp_f32 v40, v40
v_mfma_f32_16x16x32_bf16 a[16:19], a[232:235], v[120:123], 0
ds_read_b64_tr_b16 a[216:217], v16 offset:96
s_waitcnt lgkmcnt(3)
v_mfma_f32_16x16x32_bf16 a[16:19], a[236:239], v[152:155], a[16:19]
v_exp_f32 v41, v41
v_mfma_f32_16x16x32_bf16 a[20:23], a[232:235], v[124:127], 0
ds_read_b64_tr_b16 a[218:219], v16 offset:608
v_cvt_pk_bf16_f32 v138, v40, v41
v_mfma_f32_16x16x32_bf16 a[20:23], a[236:239], v[156:159], a[20:23]
v_exp_f32 v42, v42
v_mfma_f32_16x16x32_bf16 a[24:27], a[232:235], v[128:131], 0
ds_read_b64_tr_b16 a[220:221], v16 offset:8544
v_mfma_f32_16x16x32_bf16 a[24:27], a[236:239], v[160:163], a[24:27]
v_exp_f32 v43, v43
v_mfma_f32_16x16x32_bf16 a[28:31], a[232:235], v[132:135], 0
ds_read_b64_tr_b16 a[222:223], v16 offset:9056
v_cvt_pk_bf16_f32 v139, v42, v43
v_mfma_f32_16x16x32_bf16 a[28:31], a[236:239], v[164:167], a[28:31]
v_exp_f32 v28, v28
v_mfma_f32_16x16x32_bf16 a[240:243], v[184:187], v[152:155], a[240:243]
v_exp_f32 v29, v29
v_mfma_f32_16x16x32_bf16 a[32:35], a[208:211], v[120:123], 0
ds_read_b64_tr_b16 a[224:225], v16 offset:128
v_cvt_pk_bf16_f32 v140, v28, v29
s_waitcnt lgkmcnt(3)
v_mfma_f32_16x16x32_bf16 a[32:35], a[212:215], v[152:155], a[32:35]
v_exp_f32 v30, v30
v_mfma_f32_16x16x32_bf16 a[36:39], a[208:211], v[124:127], 0
ds_read_b64_tr_b16 a[226:227], v16 offset:640
v_mfma_f32_16x16x32_bf16 a[36:39], a[212:215], v[156:159], a[36:39]
v_exp_f32 v31, v31
v_mfma_f32_16x16x32_bf16 a[40:43], a[208:211], v[128:131], 0
ds_read_b64_tr_b16 a[228:229], v16 offset:8576
v_cvt_pk_bf16_f32 v141, v30, v31
v_mfma_f32_16x16x32_bf16 a[40:43], a[212:215], v[160:163], a[40:43]
v_exp_f32 v44, v44
v_mfma_f32_16x16x32_bf16 a[44:47], a[208:211], v[132:135], 0
ds_read_b64_tr_b16 a[230:231], v16 offset:9088
v_mfma_f32_16x16x32_bf16 a[44:47], a[212:215], v[164:167], a[44:47]
v_exp_f32 v45, v45
v_mfma_f32_16x16x32_bf16 a[244:247], v[184:187], v[124:127], 0
v_exp_f32 v46, v46
v_mfma_f32_16x16x32_bf16 a[48:51], a[216:219], v[120:123], 0
ds_read_b64_tr_b16 a[232:233], v16 offset:160
v_cvt_pk_bf16_f32 v142, v44, v45
s_waitcnt lgkmcnt(3)
v_mfma_f32_16x16x32_bf16 a[48:51], a[220:223], v[152:155], a[48:51]
v_exp_f32 v47, v47
v_mfma_f32_16x16x32_bf16 a[52:55], a[216:219], v[124:127], 0
ds_read_b64_tr_b16 a[234:235], v16 offset:672
v_cvt_pk_bf16_f32 v143, v46, v47
v_mfma_f32_16x16x32_bf16 a[52:55], a[220:223], v[156:159], a[52:55]
v_exp_f32 v32, v32
v_mfma_f32_16x16x32_bf16 a[56:59], a[216:219], v[128:131], 0
ds_read_b64_tr_b16 a[236:237], v16 offset:8608
v_mfma_f32_16x16x32_bf16 a[56:59], a[220:223], v[160:163], a[56:59]
v_exp_f32 v33, v33
v_mfma_f32_16x16x32_bf16 a[60:63], a[216:219], v[132:135], 0
ds_read_b64_tr_b16 a[238:239], v16 offset:9120
v_cvt_pk_bf16_f32 v144, v32, v33
v_mfma_f32_16x16x32_bf16 a[60:63], a[220:223], v[164:167], a[60:63]
v_exp_f32 v34, v34
v_mfma_f32_16x16x32_bf16 a[244:247], v[184:187], v[156:159], a[244:247]
v_mfma_f32_16x16x32_bf16 a[64:67], a[224:227], v[120:123], 0
ds_read_b64_tr_b16 a[208:209], v16 offset:192
s_waitcnt lgkmcnt(3)
v_mfma_f32_16x16x32_bf16 a[64:67], a[228:231], v[152:155], a[64:67]
v_exp_f32 v35, v35
v_mfma_f32_16x16x32_bf16 a[68:71], a[224:227], v[124:127], 0
ds_read_b64_tr_b16 a[210:211], v16 offset:704
v_cvt_pk_bf16_f32 v145, v34, v35
v_mfma_f32_16x16x32_bf16 a[68:71], a[228:231], v[156:159], a[68:71]
v_exp_f32 v48, v48
v_mfma_f32_16x16x32_bf16 a[72:75], a[224:227], v[128:131], 0
ds_read_b64_tr_b16 a[212:213], v16 offset:8640
v_mfma_f32_16x16x32_bf16 a[72:75], a[228:231], v[160:163], a[72:75]
v_exp_f32 v49, v49
v_mfma_f32_16x16x32_bf16 a[76:79], a[224:227], v[132:135], 0
ds_read_b64_tr_b16 a[214:215], v16 offset:9152
v_cvt_pk_bf16_f32 v146, v48, v49
v_mfma_f32_16x16x32_bf16 a[76:79], a[228:231], v[164:167], a[76:79]
v_exp_f32 v50, v50
v_mfma_f32_16x16x32_bf16 a[248:251], v[184:187], v[128:131], 0
v_mfma_f32_16x16x32_bf16 a[80:83], a[232:235], v[120:123], 0
ds_read_b64_tr_b16 a[216:217], v16 offset:224
s_waitcnt lgkmcnt(3)
v_mfma_f32_16x16x32_bf16 a[80:83], a[236:239], v[152:155], a[80:83]
v_exp_f32 v51, v51
v_mfma_f32_16x16x32_bf16 a[84:87], a[232:235], v[124:127], 0
ds_read_b64_tr_b16 a[218:219], v16 offset:736
v_cvt_pk_bf16_f32 v147, v50, v51
v_mfma_f32_16x16x32_bf16 a[84:87], a[236:239], v[156:159], a[84:87]
v_exp_f32 v36, v36
v_mfma_f32_16x16x32_bf16 a[88:91], a[232:235], v[128:131], 0
ds_read_b64_tr_b16 a[220:221], v16 offset:8672
v_mfma_f32_16x16x32_bf16 a[88:91], a[236:239], v[160:163], a[88:91]
v_exp_f32 v37, v37
v_mfma_f32_16x16x32_bf16 a[92:95], a[232:235], v[132:135], 0
ds_read_b64_tr_b16 a[222:223], v16 offset:9184
v_cvt_pk_bf16_f32 v148, v36, v37
v_mfma_f32_16x16x32_bf16 a[92:95], a[236:239], v[164:167], a[92:95]
v_exp_f32 v38, v38
v_mfma_f32_16x16x32_bf16 a[248:251], v[184:187], v[160:163], a[248:251]
v_mfma_f32_16x16x32_bf16 a[96:99], a[208:211], v[120:123], 0
v_exp_f32 v39, v39
s_waitcnt lgkmcnt(2)
v_mfma_f32_16x16x32_bf16 a[96:99], a[212:215], v[152:155], a[96:99]
v_cvt_pk_bf16_f32 v149, v38, v39
v_mfma_f32_16x16x32_bf16 a[100:103], a[208:211], v[124:127], 0
v_exp_f32 v52, v52
v_mfma_f32_16x16x32_bf16 a[100:103], a[212:215], v[156:159], a[100:103]
v_mfma_f32_16x16x32_bf16 a[104:107], a[208:211], v[128:131], 0
v_exp_f32 v53, v53
v_mfma_f32_16x16x32_bf16 a[104:107], a[212:215], v[160:163], a[104:107]
v_cvt_pk_bf16_f32 v150, v52, v53
v_mfma_f32_16x16x32_bf16 a[108:111], a[208:211], v[132:135], 0
v_exp_f32 v54, v54
v_mfma_f32_16x16x32_bf16 a[108:111], a[212:215], v[164:167], a[108:111]
v_mfma_f32_16x16x32_bf16 a[252:255], v[184:187], v[132:135], 0
s_waitcnt vmcnt(0) lgkmcnt(0)
s_barrier
v_mfma_f32_16x16x32_bf16 a[112:115], a[216:219], v[120:123], 0
ds_read_b128 a[192:195], v12
v_mfma_f32_16x16x32_bf16 a[112:115], a[220:223], v[152:155], a[112:115]
v_exp_f32 v55, v55
v_mfma_f32_16x16x32_bf16 a[116:119], a[216:219], v[124:127], 0
ds_read_b128 a[196:199], v13
v_cvt_pk_bf16_f32 v151, v54, v55
v_mfma_f32_16x16x32_bf16 a[116:119], a[220:223], v[156:159], a[116:119]
s_cmp_lg_u32 s71, 0
s_cbranch_scc1 SKFph_%=
s_cmp_eq_u32 s4, 0
s_cbranch_scc1 OOLwph_%=
RETwph_%=:
SKFph_%=:
v_mfma_f32_16x16x32_bf16 a[120:123], a[216:219], v[128:131], 0
ds_read_b128 a[200:203], v14
v_mfma_f32_16x16x32_bf16 a[120:123], a[220:223], v[160:163], a[120:123]
v_mfma_f32_16x16x32_bf16 a[124:127], a[216:219], v[132:135], 0
ds_read_b128 a[204:207], v15
v_mfma_f32_16x16x32_bf16 a[124:127], a[220:223], v[164:167], a[124:127]
v_mfma_f32_16x16x32_bf16 a[252:255], v[184:187], v[164:167], a[252:255]
s_cmp_eq_u32 s71, 0
s_cselect_b32 s98, 5, 0
s_cselect_b32 s18, 0x700000, 0
s_add_u32 s99, s30, 0x135c0
s_mov_b32 s70, 2
ATL_%=:
s_waitcnt lgkmcnt(0)
v_mfma_f32_16x16x32_bf16 v[24:27], a[192:195], a[128:131], v[168:171]
v_exp_f32 v88, v88
v_mfma_f32_16x16x32_bf16 v[24:27], a[196:199], a[132:135], v[24:27]
s_add_u32 m0, s28, 0x4000
ds_read_b128 a[208:211], v12 offset:4096
v_mfma_f32_16x16x32_bf16 v[24:27], a[200:203], a[136:139], v[24:27]
v_exp_f32 v89, v89
v_mfma_f32_16x16x32_bf16 v[24:27], a[204:207], a[140:143], v[24:27]
buffer_load_dwordx4 v1, s[8:11], s24 offen lds
v_mfma_f32_16x16x32_bf16 v[28:31], a[192:195], a[144:147], v[172:175]
v_exp_f32 v90, v90
v_mfma_f32_16x16x32_bf16 v[28:31], a[196:199], a[148:151], v[28:31]
ds_read_b128 a[212:215], v13 offset:4096
v_cvt_pk_bf16_f32 v152, v88, v89
v_mfma_f32_16x16x32_bf16 v[28:31], a[200:203], a[152:155], v[28:31]
v_exp_f32 v91, v91
v_mfma_f32_16x16x32_bf16 v[28:31], a[204:207], a[156:159], v[28:31]
ds_read_b128 a[216:219], v14 offset:4096
v_cvt_pk_bf16_f32 v153, v90, v91
v_mfma_f32_16x16x32_bf16 v[32:35], a[192:195], a[160:163], v[176:179]
buffer_load_dwordx4 v2, s[8:11], s24 offen offset:1024 lds
v_mfma_f32_16x16x32_bf16 v[32:35], a[196:199], a[164:167], v[32:35]
v_exp_f32 v104, v104
v_mfma_f32_16x16x32_bf16 v[32:35], a[200:203], a[168:171], v[32:35]
v_exp_f32 v105, v105
v_mfma_f32_16x16x32_bf16 v[32:35], a[204:207], a[172:175], v[32:35]
ds_read_b128 a[220:223], v15 offset:4096
v_cvt_pk_bf16_f32 v154, v104, v105
v_mfma_f32_16x16x32_bf16 v[36:39], a[192:195], a[176:179], v[180:183]
v_exp_f32 v106, v106
v_mfma_f32_16x16x32_bf16 v[36:39], a[196:199], a[180:183], v[36:39]
buffer_load_dwordx4 v3, s[8:11], s24 offen offset:2048 lds
v_mfma_f32_16x16x32_bf16 v[36:39], a[200:203], a[184:187], v[36:39]
v_exp_f32 v107, v107
v_mfma_f32_16x16x32_bf16 v[36:39], a[204:207], a[188:191], v[36:39]
v_exp_f32 v92, v92
s_waitcnt lgkmcnt(1)
v_mfma_f32_16x16x32_bf16 v[40:43], a[208:211], a[128:131], v[168:171]
v_cvt_pk_bf16_f32 v155, v106, v107
ds_read_b128 a[192:195], v12 offset:8192
v_mfma_f32_16x16x32_bf16 v[40:43], a[212:215], a[132:135], v[40:43]
buffer_load_dwordx4 v4, s[8:11], s24 offen offset:3072 lds
s_add_u32 s24, s24, 0x4000
v_mfma_f32_16x16x32_bf16 v[40:43], a[216:219], a[136:139], v[40:43]
v_exp_f32 v93, v93
s_waitcnt lgkmcnt(1)
v_mfma_f32_16x16x32_bf16 v[40:43], a[220:223], a[140:143], v[40:43]
v_exp_f32 v94, v94
v_mfma_f32_16x16x32_bf16 v[44:47], a[208:211], a[144:147], v[172:175]
ds_read_b128 a[196:199], v13 offset:8192
s_add_u32 m0, s29, 0
v_mfma_f32_16x16x32_bf16 v[44:47], a[212:215], a[148:151], v[44:47]
v_exp_f32 v95, v95
v_mfma_f32_16x16x32_bf16 v[44:47], a[216:219], a[152:155], v[44:47]
v_cvt_pk_bf16_f32 v156, v92, v93
ds_read_b128 a[200:203], v14 offset:8192
v_mfma_f32_16x16x32_bf16 v[44:47], a[220:223], a[156:159], v[44:47]
buffer_load_dwordx4 v5, s[12:15], s25 offen lds
v_mfma_f32_16x16x32_bf16 v[48:51], a[208:211], a[160:163], v[176:179]
v_exp_f32 v108, v108
v_mfma_f32_16x16x32_bf16 v[48:51], a[212:215], a[164:167], v[48:51]
v_cvt_pk_bf16_f32 v157, v94, v95
ds_read_b128 a[204:207], v15 offset:8192
v_mfma_f32_16x16x32_bf16 v[48:51], a[216:219], a[168:171], v[48:51]
v_exp_f32 v109, v109
v_mfma_f32_16x16x32_bf16 v[48:51], a[220:223], a[172:175], v[48:51]
v_exp_f32 v110, v110
v_mfma_f32_16x16x32_bf16 v[52:55], a[208:211], a[176:179], v[180:183]
buffer_load_dwordx4 v6, s[12:15], s25 offen offset:1056 lds
v_mfma_f32_16x16x32_bf16 v[52:55], a[212:215], a[180:183], v[52:55]
v_cvt_pk_bf16_f32 v158, v108, v109
v_mfma_f32_16x16x32_bf16 v[52:55], a[216:219], a[184:187], v[52:55]
v_exp_f32 v111, v111
v_mfma_f32_16x16x32_bf16 v[52:55], a[220:223], a[188:191], v[52:55]
v_exp_f32 v96, v96
s_waitcnt lgkmcnt(0)
v_mfma_f32_16x16x32_bf16 v[56:59], a[192:195], a[128:131], v[168:171]
v_cvt_pk_bf16_f32 v159, v110, v111
ds_read_b128 a[208:211], v12 offset:12288
v_mfma_f32_16x16x32_bf16 v[56:59], a[196:199], a[132:135], v[56:59]
buffer_load_dwordx4 v7, s[12:15], s25 offen offset:2112 lds
v_mfma_f32_16x16x32_bf16 v[56:59], a[200:203], a[136:139], v[56:59]
v_exp_f32 v97, v97
v_mfma_f32_16x16x32_bf16 v[56:59], a[204:207], a[140:143], v[56:59]
v_exp_f32 v98, v98
v_mfma_f32_16x16x32_bf16 v[60:63], a[192:195], a[144:147], v[172:175]
v_cvt_pk_bf16_f32 v160, v96, v97
ds_read_b128 a[212:215], v13 offset:12288
v_mfma_f32_16x16x32_bf16 v[60:63], a[196:199], a[148:151], v[60:63]
v_exp_f32 v99, v99
v_mfma_f32_16x16x32_bf16 v[60:63], a[200:203], a[152:155], v[60:63]
buffer_load_dwordx4 v8, s[12:15], s25 offen offset:3168 lds
s_add_u32 s25, s25, 0x4000
v_mfma_f32_16x16x32_bf16 v[60:63], a[204:207], a[156:159], v[60:63]
v_exp_f32 v112, v112
v_mfma_f32_16x16x32_bf16 v[64:67], a[192:195], a[160:163], v[176:179]
ds_read_b128 a[216:219], v14 offset:12288
v_cvt_pk_bf16_f32 v161, v98, v99
v_mfma_f32_16x16x32_bf16 v[64:67], a[196:199], a[164:167], v[64:67]
v_exp_f32 v113, v113
v_mfma_f32_16x16x32_bf16 v[64:67], a[200:203], a[168:171], v[64:67]
v_exp_f32 v114, v114
v_mfma_f32_16x16x32_bf16 v[64:67], a[204:207], a[172:175], v[64:67]
ds_read_b128 a[220:223], v15 offset:12288
v_cvt_pk_bf16_f32 v162, v112, v113
v_mfma_f32_16x16x32_bf16 v[68:71], a[192:195], a[176:179], v[180:183]
v_exp_f32 v115, v115
v_mfma_f32_16x16x32_bf16 v[68:71], a[196:199], a[180:183], v[68:71]
ds_read_b64_tr_b16 a[224:225], v16 offset:16896
ds_read_b64_tr_b16 a[226:227], v16 offset:17408
v_mfma_f32_16x16x32_bf16 v[68:71], a[200:203], a[184:187], v[68:71]
v_exp_f32 v100, v100
v_mfma_f32_16x16x32_bf16 v[68:71], a[204:207], a[188:191], v[68:71]
v_cvt_pk_bf16_f32 v163, v114, v115
ds_read_b64_tr_b16 a[228:229], v16 offset:25344
s_waitcnt lgkmcnt(4)
v_mfma_f32_16x16x32_bf16 v[72:75], a[208:211], a[128:131], v[168:171]
v_exp_f32 v101, v101
v_mfma_f32_16x16x32_bf16 v[72:75], a[212:215], a[132:135], v[72:75]
v_exp_f32 v102, v102
v_mfma_f32_16x16x32_bf16 v[72:75], a[216:219], a[136:139], v[72:75]
ds_read_b64_tr_b16 a[230:231], v16 offset:25856
v_cvt_pk_bf16_f32 v164, v100, v101
s_waitcnt lgkmcnt(4)
v_mfma_f32_16x16x32_bf16 v[72:75], a[220:223], a[140:143], v[72:75]
v_exp_f32 v103, v103
v_mfma_f32_16x16x32_bf16 v[76:79], a[208:211], a[144:147], v[172:175]
ds_read_b64_tr_b16 a[232:233], v16 offset:16928
v_cvt_pk_bf16_f32 v165, v102, v103
v_mfma_f32_16x16x32_bf16 v[76:79], a[212:215], a[148:151], v[76:79]
v_exp_f32 v116, v116
v_mfma_f32_16x16x32_bf16 v[76:79], a[216:219], a[152:155], v[76:79]
ds_read_b64_tr_b16 a[234:235], v16 offset:17440
v_mfma_f32_16x16x32_bf16 v[76:79], a[220:223], a[156:159], v[76:79]
v_exp_f32 v117, v117
v_mfma_f32_16x16x32_bf16 v[80:83], a[208:211], a[160:163], v[176:179]
)ASM"
R"ASM(ds_read_b64_tr_b16 a[236:237], v16 offset:25376
v_cvt_pk_bf16_f32 v166, v116, v117
v_mfma_f32_16x16x32_bf16 v[80:83], a[212:215], a[164:167], v[80:83]
v_exp_f32 v118, v118
v_mfma_f32_16x16x32_bf16 v[80:83], a[216:219], a[168:171], v[80:83]
ds_read_b64_tr_b16 a[238:239], v16 offset:25888
v_mfma_f32_16x16x32_bf16 v[80:83], a[220:223], a[172:175], v[80:83]
v_exp_f32 v119, v119
v_mfma_f32_16x16x32_bf16 v[84:87], a[208:211], a[176:179], v[180:183]
v_cvt_pk_bf16_f32 v167, v118, v119
v_mfma_f32_16x16x32_bf16 v[84:87], a[212:215], a[180:183], v[84:87]
v_mfma_f32_16x16x32_bf16 v[84:87], a[216:219], a[184:187], v[84:87]
v_mfma_f32_16x16x32_bf16 v[84:87], a[220:223], a[188:191], v[84:87]
s_waitcnt lgkmcnt(1)
v_mfma_f32_16x16x32_bf16 a[0:3], a[224:227], v[136:139], a[0:3]
v_exp_f32 v24, v24
v_mfma_f32_16x16x32_bf16 a[0:3], a[228:231], v[152:155], a[0:3]
ds_read_b64_tr_b16 a[208:209], v16 offset:16960
v_mfma_f32_16x16x32_bf16 a[4:7], a[224:227], v[140:143], a[4:7]
v_exp_f32 v25, v25
v_mfma_f32_16x16x32_bf16 a[4:7], a[228:231], v[156:159], a[4:7]
ds_read_b64_tr_b16 a[210:211], v16 offset:17472
v_cvt_pk_bf16_f32 v120, v24, v25
v_mfma_f32_16x16x32_bf16 a[8:11], a[224:227], v[144:147], a[8:11]
v_exp_f32 v26, v26
v_mfma_f32_16x16x32_bf16 a[8:11], a[228:231], v[160:163], a[8:11]
ds_read_b64_tr_b16 a[212:213], v16 offset:25408
v_mfma_f32_16x16x32_bf16 a[12:15], a[224:227], v[148:151], a[12:15]
v_exp_f32 v27, v27
v_mfma_f32_16x16x32_bf16 a[12:15], a[228:231], v[164:167], a[12:15]
ds_read_b64_tr_b16 a[214:215], v16 offset:25920
v_cvt_pk_bf16_f32 v121, v26, v27
v_mfma_f32_16x16x32_bf16 a[240:243], v[184:187], v[136:139], a[240:243]
v_exp_f32 v40, v40
v_mfma_f32_16x16x32_bf16 a[16:19], a[232:235], v[136:139], a[16:19]
ds_read_b64_tr_b16 a[216:217], v16 offset:16992
s_waitcnt lgkmcnt(3)
v_mfma_f32_16x16x32_bf16 a[16:19], a[236:239], v[152:155], a[16:19]
v_exp_f32 v41, v41
v_mfma_f32_16x16x32_bf16 a[20:23], a[232:235], v[140:143], a[20:23]
ds_read_b64_tr_b16 a[218:219], v16 offset:17504
v_cvt_pk_bf16_f32 v122, v40, v41
v_mfma_f32_16x16x32_bf16 a[20:23], a[236:239], v[156:159], a[20:23]
v_exp_f32 v42, v42
v_mfma_f32_16x16x32_bf16 a[24:27], a[232:235], v[144:147], a[24:27]
ds_read_b64_tr_b16 a[220:221], v16 offset:25440
v_mfma_f32_16x16x32_bf16 a[24:27], a[236:239], v[160:163], a[24:27]
v_exp_f32 v43, v43
v_mfma_f32_16x16x32_bf16 a[28:31], a[232:235], v[148:151], a[28:31]
ds_read_b64_tr_b16 a[222:223], v16 offset:25952
v_cvt_pk_bf16_f32 v123, v42, v43
v_mfma_f32_16x16x32_bf16 a[28:31], a[236:239], v[164:167], a[28:31]
v_exp_f32 v28, v28
v_mfma_f32_16x16x32_bf16 a[240:243], v[184:187], v[152:155], a[240:243]
v_exp_f32 v29, v29
v_mfma_f32_16x16x32_bf16 a[32:35], a[208:211], v[136:139], a[32:35]
ds_read_b64_tr_b16 a[224:225], v16 offset:17024
v_cvt_pk_bf16_f32 v124, v28, v29
s_waitcnt lgkmcnt(3)
v_mfma_f32_16x16x32_bf16 a[32:35], a[212:215], v[152:155], a[32:35]
v_exp_f32 v30, v30
v_mfma_f32_16x16x32_bf16 a[36:39], a[208:211], v[140:143], a[36:39]
ds_read_b64_tr_b16 a[226:227], v16 offset:17536
v_mfma_f32_16x16x32_bf16 a[36:39], a[212:215], v[156:159], a[36:39]
v_exp_f32 v31, v31
v_mfma_f32_16x16x32_bf16 a[40:43], a[208:211], v[144:147], a[40:43]
ds_read_b64_tr_b16 a[228:229], v16 offset:25472
v_cvt_pk_bf16_f32 v125, v30, v31
v_mfma_f32_16x16x32_bf16 a[40:43], a[212:215], v[160:163], a[40:43]
v_exp_f32 v44, v44
v_mfma_f32_16x16x32_bf16 a[44:47], a[208:211], v[148:151], a[44:47]
ds_read_b64_tr_b16 a[230:231], v16 offset:25984
v_mfma_f32_16x16x32_bf16 a[44:47], a[212:215], v[164:167], a[44:47]
v_exp_f32 v45, v45
v_mfma_f32_16x16x32_bf16 a[244:247], v[184:187], v[140:143], a[244:247]
v_exp_f32 v46, v46
v_mfma_f32_16x16x32_bf16 a[48:51], a[216:219], v[136:139], a[48:51]
ds_read_b64_tr_b16 a[232:233], v16 offset:17056
v_cvt_pk_bf16_f32 v126, v44, v45
s_waitcnt lgkmcnt(3)
v_mfma_f32_16x16x32_bf16 a[48:51], a[220:223], v[152:155], a[48:51]
v_exp_f32 v47, v47
v_mfma_f32_16x16x32_bf16 a[52:55], a[216:219], v[140:143], a[52:55]
ds_read_b64_tr_b16 a[234:235], v16 offset:17568
v_cvt_pk_bf16_f32 v127, v46, v47
v_mfma_f32_16x16x32_bf16 a[52:55], a[220:223], v[156:159], a[52:55]
v_exp_f32 v32, v32
v_mfma_f32_16x16x32_bf16 a[56:59], a[216:219], v[144:147], a[56:59]
ds_read_b64_tr_b16 a[236:237], v16 offset:25504
v_mfma_f32_16x16x32_bf16 a[56:59], a[220:223], v[160:163], a[56:59]
v_exp_f32 v33, v33
v_mfma_f32_16x16x32_bf16 a[60:63], a[216:219], v[148:151], a[60:63]
ds_read_b64_tr_b16 a[238:239], v16 offset:26016
v_cvt_pk_bf16_f32 v128, v32, v33
v_mfma_f32_16x16x32_bf16 a[60:63], a[220:223], v[164:167], a[60:63]
v_exp_f32 v34, v34
v_mfma_f32_16x16x32_bf16 a[244:247], v[184:187], v[156:159], a[244:247]
v_mfma_f32_16x16x32_bf16 a[64:67], a[224:227], v[136:139], a[64:67]
ds_read_b64_tr_b16 a[208:209], v16 offset:17088
s_waitcnt lgkmcnt(3)
v_mfma_f32_16x16x32_bf16 a[64:67], a[228:231], v[152:155], a[64:67]
v_exp_f32 v35, v35
v_mfma_f32_16x16x32_bf16 a[68:71], a[224:227], v[140:143], a[68:71]
ds_read_b64_tr_b16 a[210:211], v16 offset:17600
v_cvt_pk_bf16_f32 v129, v34, v35
v_mfma_f32_16x16x32_bf16 a[68:71], a[228:231], v[156:159], a[68:71]
v_exp_f32 v48, v48
v_mfma_f32_16x16x32_bf16 a[72:75], a[224:227], v[144:147], a[72:75]
ds_read_b64_tr_b16 a[212:213], v16 offset:25536
v_mfma_f32_16x16x32_bf16 a[72:75], a[228:231], v[160:163], a[72:75]
v_exp_f32 v49, v49
v_mfma_f32_16x16x32_bf16 a[76:79], a[224:227], v[148:151], a[76:79]
ds_read_b64_tr_b16 a[214:215], v16 offset:26048
v_cvt_pk_bf16_f32 v130, v48, v49
v_mfma_f32_16x16x32_bf16 a[76:79], a[228:231], v[164:167], a[76:79]
v_exp_f32 v50, v50
v_mfma_f32_16x16x32_bf16 a[248:251], v[184:187], v[144:147], a[248:251]
v_mfma_f32_16x16x32_bf16 a[80:83], a[232:235], v[136:139], a[80:83]
ds_read_b64_tr_b16 a[216:217], v16 offset:17120
s_waitcnt lgkmcnt(3)
v_mfma_f32_16x16x32_bf16 a[80:83], a[236:239], v[152:155], a[80:83]
v_exp_f32 v51, v51
v_mfma_f32_16x16x32_bf16 a[84:87], a[232:235], v[140:143], a[84:87]
ds_read_b64_tr_b16 a[218:219], v16 offset:17632
v_cvt_pk_bf16_f32 v131, v50, v51
v_mfma_f32_16x16x32_bf16 a[84:87], a[236:239], v[156:159], a[84:87]
v_exp_f32 v36, v36
v_mfma_f32_16x16x32_bf16 a[88:91], a[232:235], v[144:147], a[88:91]
ds_read_b64_tr_b16 a[220:221], v16 offset:25568
v_mfma_f32_16x16x32_bf16 a[88:91], a[236:239], v[160:163], a[88:91]
v_exp_f32 v37, v37
v_mfma_f32_16x16x32_bf16 a[92:95], a[232:235], v[148:151], a[92:95]
ds_read_b64_tr_b16 a[222:223], v16 offset:26080
v_cvt_pk_bf16_f32 v132, v36, v37
v_mfma_f32_16x16x32_bf16 a[92:95], a[236:239], v[164:167], a[92:95]
v_exp_f32 v38, v38
v_mfma_f32_16x16x32_bf16 a[248:251], v[184:187], v[160:163], a[248:251]
v_mfma_f32_16x16x32_bf16 a[96:99], a[208:211], v[136:139], a[96:99]
v_exp_f32 v39, v39
s_waitcnt lgkmcnt(2)
v_mfma_f32_16x16x32_bf16 a[96:99], a[212:215], v[152:155], a[96:99]
v_cvt_pk_bf16_f32 v133, v38, v39
v_mfma_f32_16x16x32_bf16 a[100:103], a[208:211], v[140:143], a[100:103]
v_exp_f32 v52, v52
v_mfma_f32_16x16x32_bf16 a[100:103], a[212:215], v[156:159], a[100:103]
v_mfma_f32_16x16x32_bf16 a[104:107], a[208:211], v[144:147], a[104:107]
v_exp_f32 v53, v53
v_mfma_f32_16x16x32_bf16 a[104:107], a[212:215], v[160:163], a[104:107]
v_cvt_pk_bf16_f32 v134, v52, v53
v_mfma_f32_16x16x32_bf16 a[108:111], a[208:211], v[148:151], a[108:111]
v_exp_f32 v54, v54
v_mfma_f32_16x16x32_bf16 a[108:111], a[212:215], v[164:167], a[108:111]
v_mfma_f32_16x16x32_bf16 a[252:255], v[184:187], v[148:151], a[252:255]
s_waitcnt vmcnt(0) lgkmcnt(0)
s_barrier
v_mfma_f32_16x16x32_bf16 a[112:115], a[216:219], v[136:139], a[112:115]
s_mov_b32 m0, s99
ds_read_b128 a[192:195], v12 offset:16384
v_mfma_f32_16x16x32_bf16 a[112:115], a[220:223], v[152:155], a[112:115]
buffer_load_dwordx4 v20, s[16:19], s27 offen lds
v_mfma_f32_16x16x32_bf16 a[116:119], a[216:219], v[140:143], a[116:119]
ds_read_b128 a[196:199], v13 offset:16384
v_mfma_f32_16x16x32_bf16 a[116:119], a[220:223], v[156:159], a[116:119]
buffer_load_dwordx4 v21, s[16:19], s27 offen offset:1056 lds
v_mfma_f32_16x16x32_bf16 a[120:123], a[216:219], v[144:147], a[120:123]
ds_read_b128 a[200:203], v14 offset:16384
v_mfma_f32_16x16x32_bf16 a[120:123], a[220:223], v[160:163], a[120:123]
buffer_load_dwordx4 v22, s[16:19], s27 offen offset:2112 lds
v_mfma_f32_16x16x32_bf16 a[124:127], a[216:219], v[148:151], a[124:127]
v_exp_f32 v55, v55
v_mfma_f32_16x16x32_bf16 a[124:127], a[220:223], v[164:167], a[124:127]
buffer_load_dwordx4 v23, s[16:19], s27 offen offset:3168 lds
s_add_u32 s27, s27, 0x20000
ds_read_b128 a[204:207], v15 offset:16384
v_mfma_f32_16x16x32_bf16 a[252:255], v[184:187], v[164:167], a[252:255]
s_add_u32 s99, s99, 0x4200
s_sub_u32 s98, s98, 1
v_cvt_pk_bf16_f32 v135, v54, v55
s_waitcnt lgkmcnt(2)
v_mfma_f32_16x16x32_bf16 v[24:27], a[192:195], a[128:131], v[168:171]
v_exp_f32 v56, v56
v_mfma_f32_16x16x32_bf16 v[24:27], a[196:199], a[132:135], v[24:27]
s_add_u32 m0, s28, 0
ds_read_b128 a[208:211], v12 offset:20480
s_waitcnt lgkmcnt(2)
v_mfma_f32_16x16x32_bf16 v[24:27], a[200:203], a[136:139], v[24:27]
v_exp_f32 v57, v57
s_waitcnt lgkmcnt(1)
v_mfma_f32_16x16x32_bf16 v[24:27], a[204:207], a[140:143], v[24:27]
buffer_load_dwordx4 v1, s[8:11], s24 offen lds
v_mfma_f32_16x16x32_bf16 v[28:31], a[192:195], a[144:147], v[172:175]
v_exp_f32 v58, v58
v_mfma_f32_16x16x32_bf16 v[28:31], a[196:199], a[148:151], v[28:31]
ds_read_b128 a[212:215], v13 offset:20480
v_cvt_pk_bf16_f32 v152, v56, v57
v_mfma_f32_16x16x32_bf16 v[28:31], a[200:203], a[152:155], v[28:31]
v_exp_f32 v59, v59
v_mfma_f32_16x16x32_bf16 v[28:31], a[204:207], a[156:159], v[28:31]
ds_read_b128 a[216:219], v14 offset:20480
v_cvt_pk_bf16_f32 v153, v58, v59
v_mfma_f32_16x16x32_bf16 v[32:35], a[192:195], a[160:163], v[176:179]
buffer_load_dwordx4 v2, s[8:11], s24 offen offset:1024 lds
v_mfma_f32_16x16x32_bf16 v[32:35], a[196:199], a[164:167], v[32:35]
v_exp_f32 v72, v72
v_mfma_f32_16x16x32_bf16 v[32:35], a[200:203], a[168:171], v[32:35]
v_exp_f32 v73, v73
v_mfma_f32_16x16x32_bf16 v[32:35], a[204:207], a[172:175], v[32:35]
ds_read_b128 a[220:223], v15 offset:20480
v_cvt_pk_bf16_f32 v154, v72, v73
v_mfma_f32_16x16x32_bf16 v[36:39], a[192:195], a[176:179], v[180:183]
v_exp_f32 v74, v74
v_mfma_f32_16x16x32_bf16 v[36:39], a[196:199], a[180:183], v[36:39]
buffer_load_dwordx4 v3, s[8:11], s24 offen offset:2048 lds
v_mfma_f32_16x16x32_bf16 v[36:39], a[200:203], a[184:187], v[36:39]
v_exp_f32 v75, v75
v_mfma_f32_16x16x32_bf16 v[36:39], a[204:207], a[188:191], v[36:39]
v_exp_f32 v60, v60
s_waitcnt lgkmcnt(1)
v_mfma_f32_16x16x32_bf16 v[40:43], a[208:211], a[128:131], v[168:171]
v_cvt_pk_bf16_f32 v155, v74, v75
ds_read_b128 a[192:195], v12 offset:24576
v_mfma_f32_16x16x32_bf16 v[40:43], a[212:215], a[132:135], v[40:43]
buffer_load_dwordx4 v4, s[8:11], s24 offen offset:3072 lds
s_add_u32 s24, s24, 0x4000
v_mfma_f32_16x16x32_bf16 v[40:43], a[216:219], a[136:139], v[40:43]
v_exp_f32 v61, v61
s_waitcnt lgkmcnt(1)
v_mfma_f32_16x16x32_bf16 v[40:43], a[220:223], a[140:143], v[40:43]
v_exp_f32 v62, v62
v_mfma_f32_16x16x32_bf16 v[44:47], a[208:211], a[144:147], v[172:175]
ds_read_b128 a[196:199], v13 offset:24576
s_add_u32 m0, s29, 0x4200
v_mfma_f32_16x16x32_bf16 v[44:47], a[212:215], a[148:151], v[44:47]
v_exp_f32 v63, v63
v_mfma_f32_16x16x32_bf16 v[44:47], a[216:219], a[152:155], v[44:47]
v_cvt_pk_bf16_f32 v156, v60, v61
ds_read_b128 a[200:203], v14 offset:24576
v_mfma_f32_16x16x32_bf16 v[44:47], a[220:223], a[156:159], v[44:47]
buffer_load_dwordx4 v5, s[12:15], s25 offen lds
v_mfma_f32_16x16x32_bf16 v[48:51], a[208:211], a[160:163], v[176:179]
v_exp_f32 v76, v76
v_mfma_f32_16x16x32_bf16 v[48:51], a[212:215], a[164:167], v[48:51]
v_cvt_pk_bf16_f32 v157, v62, v63
ds_read_b128 a[204:207], v15 offset:24576
v_mfma_f32_16x16x32_bf16 v[48:51], a[216:219], a[168:171], v[48:51]
v_exp_f32 v77, v77
v_mfma_f32_16x16x32_bf16 v[48:51], a[220:223], a[172:175], v[48:51]
v_exp_f32 v78, v78
v_mfma_f32_16x16x32_bf16 v[52:55], a[208:211], a[176:179], v[180:183]
buffer_load_dwordx4 v6, s[12:15], s25 offen offset:1056 lds
v_mfma_f32_16x16x32_bf16 v[52:55], a[212:215], a[180:183], v[52:55]
v_cvt_pk_bf16_f32 v158, v76, v77
v_mfma_f32_16x16x32_bf16 v[52:55], a[216:219], a[184:187], v[52:55]
v_exp_f32 v79, v79
v_mfma_f32_16x16x32_bf16 v[52:55], a[220:223], a[188:191], v[52:55]
v_exp_f32 v64, v64
s_waitcnt lgkmcnt(0)
v_mfma_f32_16x16x32_bf16 v[88:91], a[192:195], a[128:131], v[168:171]
v_cvt_pk_bf16_f32 v159, v78, v79
ds_read_b128 a[208:211], v12 offset:28672
v_mfma_f32_16x16x32_bf16 v[88:91], a[196:199], a[132:135], v[88:91]
buffer_load_dwordx4 v7, s[12:15], s25 offen offset:2112 lds
v_mfma_f32_16x16x32_bf16 v[88:91], a[200:203], a[136:139], v[88:91]
v_exp_f32 v65, v65
v_mfma_f32_16x16x32_bf16 v[88:91], a[204:207], a[140:143], v[88:91]
v_exp_f32 v66, v66
v_mfma_f32_16x16x32_bf16 v[92:95], a[192:195], a[144:147], v[172:175]
v_cvt_pk_bf16_f32 v160, v64, v65
ds_read_b128 a[212:215], v13 offset:28672
v_mfma_f32_16x16x32_bf16 v[92:95], a[196:199], a[148:151], v[92:95]
v_exp_f32 v67, v67
v_mfma_f32_16x16x32_bf16 v[92:95], a[200:203], a[152:155], v[92:95]
buffer_load_dwordx4 v8, s[12:15], s25 offen offset:3168 lds
s_add_u32 s25, s25, 0x4000
v_mfma_f32_16x16x32_bf16 v[92:95], a[204:207], a[156:159], v[92:95]
v_exp_f32 v80, v80
v_mfma_f32_16x16x32_bf16 v[96:99], a[192:195], a[160:163], v[176:179]
ds_read_b128 a[216:219], v14 offset:28672
v_cvt_pk_bf16_f32 v161, v66, v67
v_mfma_f32_16x16x32_bf16 v[96:99], a[196:199], a[164:167], v[96:99]
v_exp_f32 v81, v81
v_mfma_f32_16x16x32_bf16 v[96:99], a[200:203], a[168:171], v[96:99]
v_exp_f32 v82, v82
v_mfma_f32_16x16x32_bf16 v[96:99], a[204:207], a[172:175], v[96:99]
ds_read_b128 a[220:223], v15 offset:28672
v_cvt_pk_bf16_f32 v162, v80, v81
v_mfma_f32_16x16x32_bf16 v[100:103], a[192:195], a[176:179], v[180:183]
s_cmp_eq_u32 s70, 2
s_cbranch_scc1 OOLcct3_%=
RETcct3_%=:
v_mfma_f32_16x16x32_bf16 v[100:103], a[196:199], a[180:183], v[100:103]
v_exp_f32 v83, v83
v_mfma_f32_16x16x32_bf16 v[100:103], a[200:203], a[184:187], v[100:103]
ds_read_b64_tr_b16 a[224:225], v16
ds_read_b64_tr_b16 a[226:227], v16 offset:512
v_mfma_f32_16x16x32_bf16 v[100:103], a[204:207], a[188:191], v[100:103]
v_exp_f32 v68, v68
s_waitcnt lgkmcnt(3)
v_mfma_f32_16x16x32_bf16 v[104:107], a[208:211], a[128:131], v[168:171]
v_cvt_pk_bf16_f32 v163, v82, v83
ds_read_b64_tr_b16 a[228:229], v16 offset:8448
v_mfma_f32_16x16x32_bf16 v[104:107], a[212:215], a[132:135], v[104:107]
v_exp_f32 v69, v69
v_mfma_f32_16x16x32_bf16 v[104:107], a[216:219], a[136:139], v[104:107]
v_exp_f32 v70, v70
s_waitcnt lgkmcnt(3)
v_mfma_f32_16x16x32_bf16 v[104:107], a[220:223], a[140:143], v[104:107]
ds_read_b64_tr_b16 a[230:231], v16 offset:8960
v_cvt_pk_bf16_f32 v164, v68, v69
v_mfma_f32_16x16x32_bf16 v[108:111], a[208:211], a[144:147], v[172:175]
v_exp_f32 v71, v71
v_mfma_f32_16x16x32_bf16 v[108:111], a[212:215], a[148:151], v[108:111]
ds_read_b64_tr_b16 a[232:233], v16 offset:32
ds_read_b64_tr_b16 a[234:235], v16 offset:544
v_mfma_f32_16x16x32_bf16 v[108:111], a[216:219], a[152:155], v[108:111]
v_exp_f32 v84, v84
v_mfma_f32_16x16x32_bf16 v[108:111], a[220:223], a[156:159], v[108:111]
v_cvt_pk_bf16_f32 v165, v70, v71
ds_read_b64_tr_b16 a[236:237], v16 offset:8480
v_mfma_f32_16x16x32_bf16 v[112:115], a[208:211], a[160:163], v[176:179]
v_exp_f32 v85, v85
v_mfma_f32_16x16x32_bf16 v[112:115], a[212:215], a[164:167], v[112:115]
v_exp_f32 v86, v86
v_mfma_f32_16x16x32_bf16 v[112:115], a[216:219], a[168:171], v[112:115]
ds_read_b64_tr_b16 a[238:239], v16 offset:8992
v_cvt_pk_bf16_f32 v166, v84, v85
v_mfma_f32_16x16x32_bf16 v[112:115], a[220:223], a[172:175], v[112:115]
v_exp_f32 v87, v87
v_mfma_f32_16x16x32_bf16 v[116:119], a[208:211], a[176:179], v[180:183]
v_cvt_pk_bf16_f32 v167, v86, v87
v_mfma_f32_16x16x32_bf16 v[116:119], a[212:215], a[180:183], v[116:119]
v_mfma_f32_16x16x32_bf16 v[116:119], a[216:219], a[184:187], v[116:119]
v_mfma_f32_16x16x32_bf16 v[116:119], a[220:223], a[188:191], v[116:119]
s_waitcnt lgkmcnt(1)
v_mfma_f32_16x16x32_bf16 a[0:3], a[224:227], v[120:123], a[0:3]
v_exp_f32 v24, v24
v_mfma_f32_16x16x32_bf16 a[0:3], a[228:231], v[152:155], a[0:3]
ds_read_b64_tr_b16 a[208:209], v16 offset:64
v_mfma_f32_16x16x32_bf16 a[4:7], a[224:227], v[124:127], a[4:7]
v_exp_f32 v25, v25
v_mfma_f32_16x16x32_bf16 a[4:7], a[228:231], v[156:159], a[4:7]
ds_read_b64_tr_b16 a[210:211], v16 offset:576
v_cvt_pk_bf16_f32 v136, v24, v25
v_mfma_f32_16x16x32_bf16 a[8:11], a[224:227], v[128:131], a[8:11]
v_exp_f32 v26, v26
v_mfma_f32_16x16x32_bf16 a[8:11], a[228:231], v[160:163], a[8:11]
ds_read_b64_tr_b16 a[212:213], v16 offset:8512
v_mfma_f32_16x16x32_bf16 a[12:15], a[224:227], v[132:135], a[12:15]
v_exp_f32 v27, v27
v_mfma_f32_16x16x32_bf16 a[12:15], a[228:231], v[164:167], a[12:15]
ds_read_b64_tr_b16 a[214:215], v16 offset:9024
v_cvt_pk_bf16_f32 v137, v26, v27
v_mfma_f32_16x16x32_bf16 a[240:243], v[184:187], v[120:123], a[240:243]
v_exp_f32 v40, v40
v_mfma_f32_16x16x32_bf16 a[16:19], a[232:235], v[120:123], a[16:19]
ds_read_b64_tr_b16 a[216:217], v16 offset:96
s_waitcnt lgkmcnt(3)
v_mfma_f32_16x16x32_bf16 a[16:19], a[236:239], v[152:155], a[16:19]
v_exp_f32 v41, v41
v_mfma_f32_16x16x32_bf16 a[20:23], a[232:235], v[124:127], a[20:23]
ds_read_b64_tr_b16 a[218:219], v16 offset:608
v_cvt_pk_bf16_f32 v138, v40, v41
v_mfma_f32_16x16x32_bf16 a[20:23], a[236:239], v[156:159], a[20:23]
v_exp_f32 v42, v42
v_mfma_f32_16x16x32_bf16 a[24:27], a[232:235], v[128:131], a[24:27]
ds_read_b64_tr_b16 a[220:221], v16 offset:8544
v_mfma_f32_16x16x32_bf16 a[24:27], a[236:239], v[160:163], a[24:27]
v_exp_f32 v43, v43
v_mfma_f32_16x16x32_bf16 a[28:31], a[232:235], v[132:135], a[28:31]
ds_read_b64_tr_b16 a[222:223], v16 offset:9056
v_cvt_pk_bf16_f32 v139, v42, v43
v_mfma_f32_16x16x32_bf16 a[28:31], a[236:239], v[164:167], a[28:31]
v_exp_f32 v28, v28
v_mfma_f32_16x16x32_bf16 a[240:243], v[184:187], v[152:155], a[240:243]
v_exp_f32 v29, v29
v_mfma_f32_16x16x32_bf16 a[32:35], a[208:211], v[120:123], a[32:35]
ds_read_b64_tr_b16 a[224:225], v16 offset:128
v_cvt_pk_bf16_f32 v140, v28, v29
s_waitcnt lgkmcnt(3)
v_mfma_f32_16x16x32_bf16 a[32:35], a[212:215], v[152:155], a[32:35]
v_exp_f32 v30, v30
v_mfma_f32_16x16x32_bf16 a[36:39], a[208:211], v[124:127], a[36:39]
ds_read_b64_tr_b16 a[226:227], v16 offset:640
v_mfma_f32_16x16x32_bf16 a[36:39], a[212:215], v[156:159], a[36:39]
v_exp_f32 v31, v31
v_mfma_f32_16x16x32_bf16 a[40:43], a[208:211], v[128:131], a[40:43]
ds_read_b64_tr_b16 a[228:229], v16 offset:8576
v_cvt_pk_bf16_f32 v141, v30, v31
v_mfma_f32_16x16x32_bf16 a[40:43], a[212:215], v[160:163], a[40:43]
v_exp_f32 v44, v44
v_mfma_f32_16x16x32_bf16 a[44:47], a[208:211], v[132:135], a[44:47]
ds_read_b64_tr_b16 a[230:231], v16 offset:9088
v_mfma_f32_16x16x32_bf16 a[44:47], a[212:215], v[164:167], a[44:47]
v_exp_f32 v45, v45
v_mfma_f32_16x16x32_bf16 a[244:247], v[184:187], v[124:127], a[244:247]
v_exp_f32 v46, v46
v_mfma_f32_16x16x32_bf16 a[48:51], a[216:219], v[120:123], a[48:51]
ds_read_b64_tr_b16 a[232:233], v16 offset:160
v_cvt_pk_bf16_f32 v142, v44, v45
s_waitcnt lgkmcnt(3)
)ASM"
R"ASM(v_mfma_f32_16x16x32_bf16 a[48:51], a[220:223], v[152:155], a[48:51]
v_exp_f32 v47, v47
v_mfma_f32_16x16x32_bf16 a[52:55], a[216:219], v[124:127], a[52:55]
ds_read_b64_tr_b16 a[234:235], v16 offset:672
v_cvt_pk_bf16_f32 v143, v46, v47
v_mfma_f32_16x16x32_bf16 a[52:55], a[220:223], v[156:159], a[52:55]
v_exp_f32 v32, v32
v_mfma_f32_16x16x32_bf16 a[56:59], a[216:219], v[128:131], a[56:59]
ds_read_b64_tr_b16 a[236:237], v16 offset:8608
v_mfma_f32_16x16x32_bf16 a[56:59], a[220:223], v[160:163], a[56:59]
v_exp_f32 v33, v33
v_mfma_f32_16x16x32_bf16 a[60:63], a[216:219], v[132:135], a[60:63]
ds_read_b64_tr_b16 a[238:239], v16 offset:9120
v_cvt_pk_bf16_f32 v144, v32, v33
v_mfma_f32_16x16x32_bf16 a[60:63], a[220:223], v[164:167], a[60:63]
v_exp_f32 v34, v34
v_mfma_f32_16x16x32_bf16 a[244:247], v[184:187], v[156:159], a[244:247]
v_mfma_f32_16x16x32_bf16 a[64:67], a[224:227], v[120:123], a[64:67]
ds_read_b64_tr_b16 a[208:209], v16 offset:192
s_waitcnt lgkmcnt(3)
v_mfma_f32_16x16x32_bf16 a[64:67], a[228:231], v[152:155], a[64:67]
v_exp_f32 v35, v35
v_mfma_f32_16x16x32_bf16 a[68:71], a[224:227], v[124:127], a[68:71]
ds_read_b64_tr_b16 a[210:211], v16 offset:704
v_cvt_pk_bf16_f32 v145, v34, v35
v_mfma_f32_16x16x32_bf16 a[68:71], a[228:231], v[156:159], a[68:71]
v_exp_f32 v48, v48
v_mfma_f32_16x16x32_bf16 a[72:75], a[224:227], v[128:131], a[72:75]
ds_read_b64_tr_b16 a[212:213], v16 offset:8640
v_mfma_f32_16x16x32_bf16 a[72:75], a[228:231], v[160:163], a[72:75]
v_exp_f32 v49, v49
v_mfma_f32_16x16x32_bf16 a[76:79], a[224:227], v[132:135], a[76:79]
ds_read_b64_tr_b16 a[214:215], v16 offset:9152
v_cvt_pk_bf16_f32 v146, v48, v49
v_mfma_f32_16x16x32_bf16 a[76:79], a[228:231], v[164:167], a[76:79]
v_exp_f32 v50, v50
v_mfma_f32_16x16x32_bf16 a[248:251], v[184:187], v[128:131], a[248:251]
v_mfma_f32_16x16x32_bf16 a[80:83], a[232:235], v[120:123], a[80:83]
ds_read_b64_tr_b16 a[216:217], v16 offset:224
s_waitcnt lgkmcnt(3)
v_mfma_f32_16x16x32_bf16 a[80:83], a[236:239], v[152:155], a[80:83]
v_exp_f32 v51, v51
v_mfma_f32_16x16x32_bf16 a[84:87], a[232:235], v[124:127], a[84:87]
ds_read_b64_tr_b16 a[218:219], v16 offset:736
v_cvt_pk_bf16_f32 v147, v50, v51
v_mfma_f32_16x16x32_bf16 a[84:87], a[236:239], v[156:159], a[84:87]
v_exp_f32 v36, v36
v_mfma_f32_16x16x32_bf16 a[88:91], a[232:235], v[128:131], a[88:91]
ds_read_b64_tr_b16 a[220:221], v16 offset:8672
v_mfma_f32_16x16x32_bf16 a[88:91], a[236:239], v[160:163], a[88:91]
v_exp_f32 v37, v37
v_mfma_f32_16x16x32_bf16 a[92:95], a[232:235], v[132:135], a[92:95]
ds_read_b64_tr_b16 a[222:223], v16 offset:9184
v_cvt_pk_bf16_f32 v148, v36, v37
v_mfma_f32_16x16x32_bf16 a[92:95], a[236:239], v[164:167], a[92:95]
v_exp_f32 v38, v38
v_mfma_f32_16x16x32_bf16 a[248:251], v[184:187], v[160:163], a[248:251]
v_mfma_f32_16x16x32_bf16 a[96:99], a[208:211], v[120:123], a[96:99]
v_exp_f32 v39, v39
s_waitcnt lgkmcnt(2)
v_mfma_f32_16x16x32_bf16 a[96:99], a[212:215], v[152:155], a[96:99]
v_cvt_pk_bf16_f32 v149, v38, v39
v_mfma_f32_16x16x32_bf16 a[100:103], a[208:211], v[124:127], a[100:103]
v_exp_f32 v52, v52
v_mfma_f32_16x16x32_bf16 a[100:103], a[212:215], v[156:159], a[100:103]
v_mfma_f32_16x16x32_bf16 a[104:107], a[208:211], v[128:131], a[104:107]
v_exp_f32 v53, v53
v_mfma_f32_16x16x32_bf16 a[104:107], a[212:215], v[160:163], a[104:107]
v_cvt_pk_bf16_f32 v150, v52, v53
v_mfma_f32_16x16x32_bf16 a[108:111], a[208:211], v[132:135], a[108:111]
v_exp_f32 v54, v54
v_mfma_f32_16x16x32_bf16 a[108:111], a[212:215], v[164:167], a[108:111]
v_mfma_f32_16x16x32_bf16 a[252:255], v[184:187], v[132:135], a[252:255]
s_waitcnt vmcnt(0) lgkmcnt(0)
s_barrier
v_mfma_f32_16x16x32_bf16 a[112:115], a[216:219], v[120:123], a[112:115]
s_mov_b32 m0, s99
ds_read_b128 a[192:195], v12
v_mfma_f32_16x16x32_bf16 a[112:115], a[220:223], v[152:155], a[112:115]
buffer_load_dwordx4 v20, s[16:19], s27 offen lds
v_mfma_f32_16x16x32_bf16 a[116:119], a[216:219], v[124:127], a[116:119]
ds_read_b128 a[196:199], v13
v_mfma_f32_16x16x32_bf16 a[116:119], a[220:223], v[156:159], a[116:119]
buffer_load_dwordx4 v21, s[16:19], s27 offen offset:1056 lds
v_mfma_f32_16x16x32_bf16 a[120:123], a[216:219], v[128:131], a[120:123]
ds_read_b128 a[200:203], v14
v_mfma_f32_16x16x32_bf16 a[120:123], a[220:223], v[160:163], a[120:123]
buffer_load_dwordx4 v22, s[16:19], s27 offen offset:2112 lds
v_mfma_f32_16x16x32_bf16 a[124:127], a[216:219], v[132:135], a[124:127]
v_exp_f32 v55, v55
v_mfma_f32_16x16x32_bf16 a[124:127], a[220:223], v[164:167], a[124:127]
buffer_load_dwordx4 v23, s[16:19], s27 offen offset:3168 lds
s_add_u32 s27, s27, 0x20000
ds_read_b128 a[204:207], v15
v_mfma_f32_16x16x32_bf16 a[252:255], v[184:187], v[164:167], a[252:255]
s_add_u32 s99, s99, 0x4200
s_sub_u32 s98, s98, 1
v_cvt_pk_bf16_f32 v151, v54, v55
s_sub_u32 s70, s70, 1
s_cmp_lg_u32 s70, 0
s_cbranch_scc1 ATL_%=
s_waitcnt lgkmcnt(0)
v_mfma_f32_16x16x32_bf16 v[24:27], a[192:195], a[128:131], v[168:171]
v_exp_f32 v88, v88
v_mfma_f32_16x16x32_bf16 v[24:27], a[196:199], a[132:135], v[24:27]
s_add_u32 m0, s28, 0x4000
ds_read_b128 a[208:211], v12 offset:4096
v_mfma_f32_16x16x32_bf16 v[24:27], a[200:203], a[136:139], v[24:27]
v_exp_f32 v89, v89
v_mfma_f32_16x16x32_bf16 v[24:27], a[204:207], a[140:143], v[24:27]
buffer_load_dwordx4 v1, s[8:11], s24 offen lds
v_mfma_f32_16x16x32_bf16 v[28:31], a[192:195], a[144:147], v[172:175]
v_exp_f32 v90, v90
v_mfma_f32_16x16x32_bf16 v[28:31], a[196:199], a[148:151], v[28:31]
ds_read_b128 a[212:215], v13 offset:4096
v_cvt_pk_bf16_f32 v152, v88, v89
v_mfma_f32_16x16x32_bf16 v[28:31], a[200:203], a[152:155], v[28:31]
v_exp_f32 v91, v91
v_mfma_f32_16x16x32_bf16 v[28:31], a[204:207], a[156:159], v[28:31]
ds_read_b128 a[216:219], v14 offset:4096
v_cvt_pk_bf16_f32 v153, v90, v91
v_mfma_f32_16x16x32_bf16 v[32:35], a[192:195], a[160:163], v[176:179]
buffer_load_dwordx4 v2, s[8:11], s24 offen offset:1024 lds
v_mfma_f32_16x16x32_bf16 v[32:35], a[196:199], a[164:167], v[32:35]
v_exp_f32 v104, v104
v_mfma_f32_16x16x32_bf16 v[32:35], a[200:203], a[168:171], v[32:35]
v_exp_f32 v105, v105
v_mfma_f32_16x16x32_bf16 v[32:35], a[204:207], a[172:175], v[32:35]
ds_read_b128 a[220:223], v15 offset:4096
v_cvt_pk_bf16_f32 v154, v104, v105
v_mfma_f32_16x16x32_bf16 v[36:39], a[192:195], a[176:179], v[180:183]
v_exp_f32 v106, v106
v_mfma_f32_16x16x32_bf16 v[36:39], a[196:199], a[180:183], v[36:39]
buffer_load_dwordx4 v3, s[8:11], s24 offen offset:2048 lds
v_mfma_f32_16x16x32_bf16 v[36:39], a[200:203], a[184:187], v[36:39]
v_exp_f32 v107, v107
v_mfma_f32_16x16x32_bf16 v[36:39], a[204:207], a[188:191], v[36:39]
v_exp_f32 v92, v92
s_waitcnt lgkmcnt(1)
v_mfma_f32_16x16x32_bf16 v[40:43], a[208:211], a[128:131], v[168:171]
v_cvt_pk_bf16_f32 v155, v106, v107
ds_read_b128 a[192:195], v12 offset:8192
v_mfma_f32_16x16x32_bf16 v[40:43], a[212:215], a[132:135], v[40:43]
buffer_load_dwordx4 v4, s[8:11], s24 offen offset:3072 lds
s_add_u32 s24, s24, 0x4000
v_mfma_f32_16x16x32_bf16 v[40:43], a[216:219], a[136:139], v[40:43]
v_exp_f32 v93, v93
s_waitcnt lgkmcnt(1)
v_mfma_f32_16x16x32_bf16 v[40:43], a[220:223], a[140:143], v[40:43]
v_exp_f32 v94, v94
v_mfma_f32_16x16x32_bf16 v[44:47], a[208:211], a[144:147], v[172:175]
ds_read_b128 a[196:199], v13 offset:8192
s_add_u32 m0, s29, 0
v_mfma_f32_16x16x32_bf16 v[44:47], a[212:215], a[148:151], v[44:47]
v_exp_f32 v95, v95
v_mfma_f32_16x16x32_bf16 v[44:47], a[216:219], a[152:155], v[44:47]
v_cvt_pk_bf16_f32 v156, v92, v93
ds_read_b128 a[200:203], v14 offset:8192
v_mfma_f32_16x16x32_bf16 v[44:47], a[220:223], a[156:159], v[44:47]
buffer_load_dwordx4 v5, s[12:15], s25 offen lds
v_mfma_f32_16x16x32_bf16 v[48:51], a[208:211], a[160:163], v[176:179]
v_exp_f32 v108, v108
v_mfma_f32_16x16x32_bf16 v[48:51], a[212:215], a[164:167], v[48:51]
v_cvt_pk_bf16_f32 v157, v94, v95
ds_read_b128 a[204:207], v15 offset:8192
v_mfma_f32_16x16x32_bf16 v[48:51], a[216:219], a[168:171], v[48:51]
v_exp_f32 v109, v109
v_mfma_f32_16x16x32_bf16 v[48:51], a[220:223], a[172:175], v[48:51]
v_exp_f32 v110, v110
v_mfma_f32_16x16x32_bf16 v[52:55], a[208:211], a[176:179], v[180:183]
buffer_load_dwordx4 v6, s[12:15], s25 offen offset:1056 lds
v_mfma_f32_16x16x32_bf16 v[52:55], a[212:215], a[180:183], v[52:55]
v_cvt_pk_bf16_f32 v158, v108, v109
v_mfma_f32_16x16x32_bf16 v[52:55], a[216:219], a[184:187], v[52:55]
v_exp_f32 v111, v111
v_mfma_f32_16x16x32_bf16 v[52:55], a[220:223], a[188:191], v[52:55]
v_exp_f32 v96, v96
s_waitcnt lgkmcnt(0)
v_mfma_f32_16x16x32_bf16 v[56:59], a[192:195], a[128:131], v[168:171]
v_cvt_pk_bf16_f32 v159, v110, v111
ds_read_b128 a[208:211], v12 offset:12288
v_mfma_f32_16x16x32_bf16 v[56:59], a[196:199], a[132:135], v[56:59]
buffer_load_dwordx4 v7, s[12:15], s25 offen offset:2112 lds
v_mfma_f32_16x16x32_bf16 v[56:59], a[200:203], a[136:139], v[56:59]
v_exp_f32 v97, v97
v_mfma_f32_16x16x32_bf16 v[56:59], a[204:207], a[140:143], v[56:59]
v_exp_f32 v98, v98
v_mfma_f32_16x16x32_bf16 v[60:63], a[192:195], a[144:147], v[172:175]
v_cvt_pk_bf16_f32 v160, v96, v97
ds_read_b128 a[212:215], v13 offset:12288
v_mfma_f32_16x16x32_bf16 v[60:63], a[196:199], a[148:151], v[60:63]
v_exp_f32 v99, v99
v_mfma_f32_16x16x32_bf16 v[60:63], a[200:203], a[152:155], v[60:63]
buffer_load_dwordx4 v8, s[12:15], s25 offen offset:3168 lds
s_add_u32 s25, s25, 0x4000
v_mfma_f32_16x16x32_bf16 v[60:63], a[204:207], a[156:159], v[60:63]
v_exp_f32 v112, v112
v_mfma_f32_16x16x32_bf16 v[64:67], a[192:195], a[160:163], v[176:179]
s_cmp_lg_u32 s71, 0
s_cbranch_scc1 SKFhi_%=
s_lshl_b32 s46, s72, 2
s_add_u32 s46, s46, s74
s_lshl_b32 s46, s46, 9
s_add_u32 s46, s46, 0x4000
v_mbcnt_lo_u32_b32 v17, -1, 0
v_mbcnt_hi_u32_b32 v17, -1, v17
v_min_u32 v17, 7, v17
v_lshlrev_b32 v17, 6, v17
buffer_load_dword v191, v17, s[76:79], s46 offen sc1
SKFhi_%=:
v_mfma_f32_16x16x32_bf16 v[64:67], a[196:199], a[164:167], v[64:67]
ds_read_b128 a[216:219], v14 offset:12288
v_exp_f32 v113, v113
v_cvt_pk_bf16_f32 v161, v98, v99
v_mfma_f32_16x16x32_bf16 v[64:67], a[200:203], a[168:171], v[64:67]
v_exp_f32 v114, v114
v_mfma_f32_16x16x32_bf16 v[64:67], a[204:207], a[172:175], v[64:67]
ds_read_b128 a[220:223], v15 offset:12288
v_cvt_pk_bf16_f32 v162, v112, v113
v_mfma_f32_16x16x32_bf16 v[68:71], a[192:195], a[176:179], v[180:183]
v_exp_f32 v115, v115
v_mfma_f32_16x16x32_bf16 v[68:71], a[196:199], a[180:183], v[68:71]
ds_read_b64_tr_b16 a[224:225], v16 offset:16896
ds_read_b64_tr_b16 a[226:227], v16 offset:17408
v_mfma_f32_16x16x32_bf16 v[68:71], a[200:203], a[184:187], v[68:71]
v_exp_f32 v100, v100
v_mfma_f32_16x16x32_bf16 v[68:71], a[204:207], a[188:191], v[68:71]
v_cvt_pk_bf16_f32 v163, v114, v115
ds_read_b64_tr_b16 a[228:229], v16 offset:25344
s_waitcnt lgkmcnt(4)
v_mfma_f32_16x16x32_bf16 v[72:75], a[208:211], a[128:131], v[168:171]
v_exp_f32 v101, v101
v_mfma_f32_16x16x32_bf16 v[72:75], a[212:215], a[132:135], v[72:75]
v_exp_f32 v102, v102
v_mfma_f32_16x16x32_bf16 v[72:75], a[216:219], a[136:139], v[72:75]
ds_read_b64_tr_b16 a[230:231], v16 offset:25856
v_cvt_pk_bf16_f32 v164, v100, v101
s_waitcnt lgkmcnt(4)
v_mfma_f32_16x16x32_bf16 v[72:75], a[220:223], a[140:143], v[72:75]
v_exp_f32 v103, v103
v_mfma_f32_16x16x32_bf16 v[76:79], a[208:211], a[144:147], v[172:175]
ds_read_b64_tr_b16 a[232:233], v16 offset:16928
v_cvt_pk_bf16_f32 v165, v102, v103
v_mfma_f32_16x16x32_bf16 v[76:79], a[212:215], a[148:151], v[76:79]
v_exp_f32 v116, v116
v_mfma_f32_16x16x32_bf16 v[76:79], a[216:219], a[152:155], v[76:79]
ds_read_b64_tr_b16 a[234:235], v16 offset:17440
v_mfma_f32_16x16x32_bf16 v[76:79], a[220:223], a[156:159], v[76:79]
v_exp_f32 v117, v117
v_mfma_f32_16x16x32_bf16 v[80:83], a[208:211], a[160:163], v[176:179]
ds_read_b64_tr_b16 a[236:237], v16 offset:25376
v_cvt_pk_bf16_f32 v166, v116, v117
v_mfma_f32_16x16x32_bf16 v[80:83], a[212:215], a[164:167], v[80:83]
v_exp_f32 v118, v118
v_mfma_f32_16x16x32_bf16 v[80:83], a[216:219], a[168:171], v[80:83]
ds_read_b64_tr_b16 a[238:239], v16 offset:25888
v_mfma_f32_16x16x32_bf16 v[80:83], a[220:223], a[172:175], v[80:83]
v_exp_f32 v119, v119
v_mfma_f32_16x16x32_bf16 v[84:87], a[208:211], a[176:179], v[180:183]
v_cvt_pk_bf16_f32 v167, v118, v119
v_mfma_f32_16x16x32_bf16 v[84:87], a[212:215], a[180:183], v[84:87]
v_mfma_f32_16x16x32_bf16 v[84:87], a[216:219], a[184:187], v[84:87]
v_mfma_f32_16x16x32_bf16 v[84:87], a[220:223], a[188:191], v[84:87]
s_cmp_eq_u32 s71, 0
s_cselect_b32 s98, 1, 0
s_waitcnt lgkmcnt(1)
v_mfma_f32_16x16x32_bf16 a[0:3], a[224:227], v[136:139], a[0:3]
v_exp_f32 v24, v24
v_mfma_f32_16x16x32_bf16 a[0:3], a[228:231], v[152:155], a[0:3]
ds_read_b64_tr_b16 a[208:209], v16 offset:16960
v_mfma_f32_16x16x32_bf16 a[4:7], a[224:227], v[140:143], a[4:7]
v_exp_f32 v25, v25
v_mfma_f32_16x16x32_bf16 a[4:7], a[228:231], v[156:159], a[4:7]
ds_read_b64_tr_b16 a[210:211], v16 offset:17472
v_cvt_pk_bf16_f32 v120, v24, v25
v_mfma_f32_16x16x32_bf16 a[8:11], a[224:227], v[144:147], a[8:11]
v_exp_f32 v26, v26
v_mfma_f32_16x16x32_bf16 a[8:11], a[228:231], v[160:163], a[8:11]
ds_read_b64_tr_b16 a[212:213], v16 offset:25408
v_mfma_f32_16x16x32_bf16 a[12:15], a[224:227], v[148:151], a[12:15]
v_exp_f32 v27, v27
v_mfma_f32_16x16x32_bf16 a[12:15], a[228:231], v[164:167], a[12:15]
ds_read_b64_tr_b16 a[214:215], v16 offset:25920
v_cvt_pk_bf16_f32 v121, v26, v27
v_mfma_f32_16x16x32_bf16 a[240:243], v[184:187], v[136:139], a[240:243]
v_exp_f32 v40, v40
v_mfma_f32_16x16x32_bf16 a[16:19], a[232:235], v[136:139], a[16:19]
ds_read_b64_tr_b16 a[216:217], v16 offset:16992
s_waitcnt lgkmcnt(3)
v_mfma_f32_16x16x32_bf16 a[16:19], a[236:239], v[152:155], a[16:19]
v_exp_f32 v41, v41
v_mfma_f32_16x16x32_bf16 a[20:23], a[232:235], v[140:143], a[20:23]
ds_read_b64_tr_b16 a[218:219], v16 offset:17504
v_cvt_pk_bf16_f32 v122, v40, v41
v_mfma_f32_16x16x32_bf16 a[20:23], a[236:239], v[156:159], a[20:23]
v_exp_f32 v42, v42
v_mfma_f32_16x16x32_bf16 a[24:27], a[232:235], v[144:147], a[24:27]
ds_read_b64_tr_b16 a[220:221], v16 offset:25440
v_mfma_f32_16x16x32_bf16 a[24:27], a[236:239], v[160:163], a[24:27]
v_exp_f32 v43, v43
v_mfma_f32_16x16x32_bf16 a[28:31], a[232:235], v[148:151], a[28:31]
ds_read_b64_tr_b16 a[222:223], v16 offset:25952
v_cvt_pk_bf16_f32 v123, v42, v43
v_mfma_f32_16x16x32_bf16 a[28:31], a[236:239], v[164:167], a[28:31]
v_exp_f32 v28, v28
v_mfma_f32_16x16x32_bf16 a[240:243], v[184:187], v[152:155], a[240:243]
v_exp_f32 v29, v29
v_mfma_f32_16x16x32_bf16 a[32:35], a[208:211], v[136:139], a[32:35]
ds_read_b64_tr_b16 a[224:225], v16 offset:17024
v_cvt_pk_bf16_f32 v124, v28, v29
s_waitcnt lgkmcnt(3)
v_mfma_f32_16x16x32_bf16 a[32:35], a[212:215], v[152:155], a[32:35]
v_exp_f32 v30, v30
v_mfma_f32_16x16x32_bf16 a[36:39], a[208:211], v[140:143], a[36:39]
ds_read_b64_tr_b16 a[226:227], v16 offset:17536
v_mfma_f32_16x16x32_bf16 a[36:39], a[212:215], v[156:159], a[36:39]
v_exp_f32 v31, v31
v_mfma_f32_16x16x32_bf16 a[40:43], a[208:211], v[144:147], a[40:43]
ds_read_b64_tr_b16 a[228:229], v16 offset:25472
v_cvt_pk_bf16_f32 v125, v30, v31
v_mfma_f32_16x16x32_bf16 a[40:43], a[212:215], v[160:163], a[40:43]
v_exp_f32 v44, v44
v_mfma_f32_16x16x32_bf16 a[44:47], a[208:211], v[148:151], a[44:47]
ds_read_b64_tr_b16 a[230:231], v16 offset:25984
v_mfma_f32_16x16x32_bf16 a[44:47], a[212:215], v[164:167], a[44:47]
v_exp_f32 v45, v45
v_mfma_f32_16x16x32_bf16 a[244:247], v[184:187], v[140:143], a[244:247]
v_exp_f32 v46, v46
v_mfma_f32_16x16x32_bf16 a[48:51], a[216:219], v[136:139], a[48:51]
ds_read_b64_tr_b16 a[232:233], v16 offset:17056
v_cvt_pk_bf16_f32 v126, v44, v45
s_waitcnt lgkmcnt(3)
v_mfma_f32_16x16x32_bf16 a[48:51], a[220:223], v[152:155], a[48:51]
v_exp_f32 v47, v47
v_mfma_f32_16x16x32_bf16 a[52:55], a[216:219], v[140:143], a[52:55]
ds_read_b64_tr_b16 a[234:235], v16 offset:17568
v_cvt_pk_bf16_f32 v127, v46, v47
v_mfma_f32_16x16x32_bf16 a[52:55], a[220:223], v[156:159], a[52:55]
v_exp_f32 v32, v32
v_mfma_f32_16x16x32_bf16 a[56:59], a[216:219], v[144:147], a[56:59]
ds_read_b64_tr_b16 a[236:237], v16 offset:25504
v_mfma_f32_16x16x32_bf16 a[56:59], a[220:223], v[160:163], a[56:59]
v_exp_f32 v33, v33
v_mfma_f32_16x16x32_bf16 a[60:63], a[216:219], v[148:151], a[60:63]
ds_read_b64_tr_b16 a[238:239], v16 offset:26016
v_cvt_pk_bf16_f32 v128, v32, v33
v_mfma_f32_16x16x32_bf16 a[60:63], a[220:223], v[164:167], a[60:63]
v_exp_f32 v34, v34
v_mfma_f32_16x16x32_bf16 a[244:247], v[184:187], v[156:159], a[244:247]
v_mfma_f32_16x16x32_bf16 a[64:67], a[224:227], v[136:139], a[64:67]
ds_read_b64_tr_b16 a[208:209], v16 offset:17088
s_waitcnt lgkmcnt(3)
v_mfma_f32_16x16x32_bf16 a[64:67], a[228:231], v[152:155], a[64:67]
v_exp_f32 v35, v35
v_mfma_f32_16x16x32_bf16 a[68:71], a[224:227], v[140:143], a[68:71]
ds_read_b64_tr_b16 a[210:211], v16 offset:17600
v_cvt_pk_bf16_f32 v129, v34, v35
v_mfma_f32_16x16x32_bf16 a[68:71], a[228:231], v[156:159], a[68:71]
v_exp_f32 v48, v48
v_mfma_f32_16x16x32_bf16 a[72:75], a[224:227], v[144:147], a[72:75]
ds_read_b64_tr_b16 a[212:213], v16 offset:25536
v_mfma_f32_16x16x32_bf16 a[72:75], a[228:231], v[160:163], a[72:75]
v_exp_f32 v49, v49
v_mfma_f32_16x16x32_bf16 a[76:79], a[224:227], v[148:151], a[76:79]
ds_read_b64_tr_b16 a[214:215], v16 offset:26048
v_cvt_pk_bf16_f32 v130, v48, v49
v_mfma_f32_16x16x32_bf16 a[76:79], a[228:231], v[164:167], a[76:79]
v_exp_f32 v50, v50
v_mfma_f32_16x16x32_bf16 a[248:251], v[184:187], v[144:147], a[248:251]
v_mfma_f32_16x16x32_bf16 a[80:83], a[232:235], v[136:139], a[80:83]
ds_read_b64_tr_b16 a[216:217], v16 offset:17120
s_waitcnt lgkmcnt(3)
v_mfma_f32_16x16x32_bf16 a[80:83], a[236:239], v[152:155], a[80:83]
v_exp_f32 v51, v51
v_mfma_f32_16x16x32_bf16 a[84:87], a[232:235], v[140:143], a[84:87]
ds_read_b64_tr_b16 a[218:219], v16 offset:17632
v_cvt_pk_bf16_f32 v131, v50, v51
v_mfma_f32_16x16x32_bf16 a[84:87], a[236:239], v[156:159], a[84:87]
v_exp_f32 v36, v36
v_mfma_f32_16x16x32_bf16 a[88:91], a[232:235], v[144:147], a[88:91]
ds_read_b64_tr_b16 a[220:221], v16 offset:25568
v_mfma_f32_16x16x32_bf16 a[88:91], a[236:239], v[160:163], a[88:91]
v_exp_f32 v37, v37
v_mfma_f32_16x16x32_bf16 a[92:95], a[232:235], v[148:151], a[92:95]
ds_read_b64_tr_b16 a[222:223], v16 offset:26080
v_cvt_pk_bf16_f32 v132, v36, v37
v_mfma_f32_16x16x32_bf16 a[92:95], a[236:239], v[164:167], a[92:95]
v_exp_f32 v38, v38
v_mfma_f32_16x16x32_bf16 a[248:251], v[184:187], v[160:163], a[248:251]
v_mfma_f32_16x16x32_bf16 a[96:99], a[208:211], v[136:139], a[96:99]
v_exp_f32 v39, v39
s_waitcnt lgkmcnt(2)
v_mfma_f32_16x16x32_bf16 a[96:99], a[212:215], v[152:155], a[96:99]
v_cvt_pk_bf16_f32 v133, v38, v39
v_mfma_f32_16x16x32_bf16 a[100:103], a[208:211], v[140:143], a[100:103]
v_exp_f32 v52, v52
v_mfma_f32_16x16x32_bf16 a[100:103], a[212:215], v[156:159], a[100:103]
v_mfma_f32_16x16x32_bf16 a[104:107], a[208:211], v[144:147], a[104:107]
v_exp_f32 v53, v53
v_mfma_f32_16x16x32_bf16 a[104:107], a[212:215], v[160:163], a[104:107]
)ASM"
R"ASM(v_cvt_pk_bf16_f32 v134, v52, v53
v_mfma_f32_16x16x32_bf16 a[108:111], a[208:211], v[148:151], a[108:111]
v_exp_f32 v54, v54
v_mfma_f32_16x16x32_bf16 a[108:111], a[212:215], v[164:167], a[108:111]
v_mfma_f32_16x16x32_bf16 a[252:255], v[184:187], v[148:151], a[252:255]
s_waitcnt vmcnt(0) lgkmcnt(0)
s_barrier
v_mfma_f32_16x16x32_bf16 a[112:115], a[216:219], v[136:139], a[112:115]
ds_read_b128 a[192:195], v12 offset:16384
v_mfma_f32_16x16x32_bf16 a[112:115], a[220:223], v[152:155], a[112:115]
s_cmp_eq_u32 s98, 0
s_cbranch_scc1 SKWP5_%=
s_mov_b32 m0, s99
s_nop 0
buffer_load_dwordx4 v20, s[16:19], s27 offen lds
buffer_load_dwordx4 v21, s[16:19], s27 offen offset:1056 lds
buffer_load_dwordx4 v22, s[16:19], s27 offen offset:2112 lds
buffer_load_dwordx4 v23, s[16:19], s27 offen offset:3168 lds
s_add_u32 s27, s27, 0x20000
s_add_u32 s99, s99, 0x4200
s_sub_u32 s98, s98, 1
SKWP5_%=:
v_mfma_f32_16x16x32_bf16 a[116:119], a[216:219], v[140:143], a[116:119]
ds_read_b128 a[196:199], v13 offset:16384
v_mfma_f32_16x16x32_bf16 a[116:119], a[220:223], v[156:159], a[116:119]
v_exp_f32 v55, v55
v_mfma_f32_16x16x32_bf16 a[120:123], a[216:219], v[144:147], a[120:123]
ds_read_b128 a[200:203], v14 offset:16384
v_cvt_pk_bf16_f32 v135, v54, v55
v_mfma_f32_16x16x32_bf16 a[120:123], a[220:223], v[160:163], a[120:123]
v_mfma_f32_16x16x32_bf16 a[124:127], a[216:219], v[148:151], a[124:127]
ds_read_b128 a[204:207], v15 offset:16384
v_mfma_f32_16x16x32_bf16 a[124:127], a[220:223], v[164:167], a[124:127]
s_cmp_lg_u32 s71, 0
s_cbranch_scc1 SKFhc_%=
v_cmp_ne_u32 vcc, s33, v191
s_nop 1
s_cmp_lg_u64 vcc, 0
s_cbranch_scc1 OOLphc_%=
RETphc_%=:
SKFhc_%=:
v_mfma_f32_16x16x32_bf16 a[252:255], v[184:187], v[164:167], a[252:255]
s_add_u32 s39, s74, 0
s_and_b32 s39, s39, 3
s_lshl_b32 s39, s39, 11
v_add_u32 v1, s39, v0
s_add_u32 s39, s74, 1
s_and_b32 s39, s39, 3
s_lshl_b32 s39, s39, 11
v_add_u32 v2, s39, v0
s_add_u32 s39, s74, 2
s_and_b32 s39, s39, 3
s_lshl_b32 s39, s39, 11
v_add_u32 v3, s39, v0
s_add_u32 s39, s74, 3
s_and_b32 s39, s39, 3
s_lshl_b32 s39, s39, 11
v_add_u32 v4, s39, v0
s_waitcnt lgkmcnt(2)
v_mfma_f32_16x16x32_bf16 v[24:27], a[192:195], a[128:131], v[168:171]
v_exp_f32 v56, v56
v_mfma_f32_16x16x32_bf16 v[24:27], a[196:199], a[132:135], v[24:27]
s_add_u32 m0, s29, 0x4200
ds_read_b128 a[208:211], v12 offset:20480
s_waitcnt lgkmcnt(2)
v_mfma_f32_16x16x32_bf16 v[24:27], a[200:203], a[136:139], v[24:27]
v_exp_f32 v57, v57
s_waitcnt lgkmcnt(1)
v_mfma_f32_16x16x32_bf16 v[24:27], a[204:207], a[140:143], v[24:27]
buffer_load_dwordx4 v5, s[12:15], s25 offen lds
v_mfma_f32_16x16x32_bf16 v[28:31], a[192:195], a[144:147], v[172:175]
v_exp_f32 v58, v58
v_mfma_f32_16x16x32_bf16 v[28:31], a[196:199], a[148:151], v[28:31]
ds_read_b128 a[212:215], v13 offset:20480
v_cvt_pk_bf16_f32 v152, v56, v57
v_mfma_f32_16x16x32_bf16 v[28:31], a[200:203], a[152:155], v[28:31]
v_exp_f32 v59, v59
v_mfma_f32_16x16x32_bf16 v[28:31], a[204:207], a[156:159], v[28:31]
ds_read_b128 a[216:219], v14 offset:20480
v_cvt_pk_bf16_f32 v153, v58, v59
v_mfma_f32_16x16x32_bf16 v[32:35], a[192:195], a[160:163], v[176:179]
buffer_load_dwordx4 v6, s[12:15], s25 offen offset:1056 lds
v_mfma_f32_16x16x32_bf16 v[32:35], a[196:199], a[164:167], v[32:35]
v_exp_f32 v72, v72
v_mfma_f32_16x16x32_bf16 v[32:35], a[200:203], a[168:171], v[32:35]
v_exp_f32 v73, v73
v_mfma_f32_16x16x32_bf16 v[32:35], a[204:207], a[172:175], v[32:35]
ds_read_b128 a[220:223], v15 offset:20480
v_cvt_pk_bf16_f32 v154, v72, v73
v_mfma_f32_16x16x32_bf16 v[36:39], a[192:195], a[176:179], v[180:183]
v_exp_f32 v74, v74
v_mfma_f32_16x16x32_bf16 v[36:39], a[196:199], a[180:183], v[36:39]
buffer_load_dwordx4 v7, s[12:15], s25 offen offset:2112 lds
v_mfma_f32_16x16x32_bf16 v[36:39], a[200:203], a[184:187], v[36:39]
v_exp_f32 v75, v75
v_mfma_f32_16x16x32_bf16 v[36:39], a[204:207], a[188:191], v[36:39]
v_exp_f32 v60, v60
s_waitcnt lgkmcnt(1)
v_mfma_f32_16x16x32_bf16 v[40:43], a[208:211], a[128:131], v[168:171]
v_cvt_pk_bf16_f32 v155, v74, v75
ds_read_b128 a[192:195], v12 offset:24576
v_mfma_f32_16x16x32_bf16 v[40:43], a[212:215], a[132:135], v[40:43]
buffer_load_dwordx4 v8, s[12:15], s25 offen offset:3168 lds
s_add_u32 s25, s25, 0x4000
v_mfma_f32_16x16x32_bf16 v[40:43], a[216:219], a[136:139], v[40:43]
v_exp_f32 v61, v61
s_waitcnt lgkmcnt(1)
v_mfma_f32_16x16x32_bf16 v[40:43], a[220:223], a[140:143], v[40:43]
v_exp_f32 v62, v62
v_mfma_f32_16x16x32_bf16 v[44:47], a[208:211], a[144:147], v[172:175]
ds_read_b128 a[196:199], v13 offset:24576
v_cvt_pk_bf16_f32 v156, v60, v61
v_mfma_f32_16x16x32_bf16 v[44:47], a[212:215], a[148:151], v[44:47]
v_exp_f32 v63, v63
v_mfma_f32_16x16x32_bf16 v[44:47], a[216:219], a[152:155], v[44:47]
v_exp_f32 v76, v76
v_mfma_f32_16x16x32_bf16 v[44:47], a[220:223], a[156:159], v[44:47]
ds_read_b128 a[200:203], v14 offset:24576
v_cvt_pk_bf16_f32 v157, v62, v63
v_mfma_f32_16x16x32_bf16 v[48:51], a[208:211], a[160:163], v[176:179]
v_exp_f32 v77, v77
v_mfma_f32_16x16x32_bf16 v[48:51], a[212:215], a[164:167], v[48:51]
ds_read_b128 a[204:207], v15 offset:24576
v_cvt_pk_bf16_f32 v158, v76, v77
v_mfma_f32_16x16x32_bf16 v[48:51], a[216:219], a[168:171], v[48:51]
v_exp_f32 v78, v78
v_mfma_f32_16x16x32_bf16 v[48:51], a[220:223], a[172:175], v[48:51]
v_exp_f32 v79, v79
v_mfma_f32_16x16x32_bf16 v[52:55], a[208:211], a[176:179], v[180:183]
v_exp_f32 v64, v64
v_mfma_f32_16x16x32_bf16 v[52:55], a[212:215], a[180:183], v[52:55]
v_cvt_pk_bf16_f32 v159, v78, v79
v_mfma_f32_16x16x32_bf16 v[52:55], a[216:219], a[184:187], v[52:55]
s_cmp_lg_u32 s71, 0
s_cbranch_scc1 SKFa00_%=
buffer_load_dwordx4 v[192:195], v1, s[20:23], s26 offen sc1
buffer_load_dwordx4 v[196:199], v1, s[20:23], s26 offen offset:1024 sc1
SKFa00_%=:
v_mfma_f32_16x16x32_bf16 v[52:55], a[220:223], a[188:191], v[52:55]
v_exp_f32 v65, v65
s_waitcnt lgkmcnt(0)
v_mfma_f32_16x16x32_bf16 v[88:91], a[192:195], a[128:131], v[168:171]
ds_read_b128 a[208:211], v12 offset:28672
v_cvt_pk_bf16_f32 v160, v64, v65
v_mfma_f32_16x16x32_bf16 v[88:91], a[196:199], a[132:135], v[88:91]
v_exp_f32 v66, v66
v_mfma_f32_16x16x32_bf16 v[88:91], a[200:203], a[136:139], v[88:91]
s_cmp_lg_u32 s71, 0
s_cbranch_scc1 SKFa01_%=
buffer_load_dwordx4 v[200:203], v2, s[20:23], s26 offen sc1
buffer_load_dwordx4 v[204:207], v2, s[20:23], s26 offen offset:1024 sc1
SKFa01_%=:
v_mfma_f32_16x16x32_bf16 v[88:91], a[204:207], a[140:143], v[88:91]
ds_read_b128 a[212:215], v13 offset:28672
v_mfma_f32_16x16x32_bf16 v[92:95], a[192:195], a[144:147], v[172:175]
v_exp_f32 v67, v67
v_mfma_f32_16x16x32_bf16 v[92:95], a[196:199], a[148:151], v[92:95]
v_exp_f32 v80, v80
v_mfma_f32_16x16x32_bf16 v[92:95], a[200:203], a[152:155], v[92:95]
s_cmp_lg_u32 s71, 0
s_cbranch_scc1 SKFa02_%=
buffer_load_dwordx4 v[208:211], v3, s[20:23], s26 offen sc1
buffer_load_dwordx4 v[212:215], v3, s[20:23], s26 offen offset:1024 sc1
SKFa02_%=:
v_mfma_f32_16x16x32_bf16 v[92:95], a[204:207], a[156:159], v[92:95]
ds_read_b128 a[216:219], v14 offset:28672
v_cvt_pk_bf16_f32 v161, v66, v67
v_mfma_f32_16x16x32_bf16 v[96:99], a[192:195], a[160:163], v[176:179]
v_exp_f32 v81, v81
v_mfma_f32_16x16x32_bf16 v[96:99], a[196:199], a[164:167], v[96:99]
v_exp_f32 v82, v82
v_mfma_f32_16x16x32_bf16 v[96:99], a[200:203], a[168:171], v[96:99]
s_cmp_lg_u32 s71, 0
s_cbranch_scc1 SKFa03_%=
buffer_load_dwordx4 v[216:219], v4, s[20:23], s26 offen sc1
buffer_load_dwordx4 v[220:223], v4, s[20:23], s26 offen offset:1024 sc1
s_add_u32 s26, s26, 0x4000
SKFa03_%=:
v_mfma_f32_16x16x32_bf16 v[96:99], a[204:207], a[172:175], v[96:99]
ds_read_b128 a[220:223], v15 offset:28672
v_cvt_pk_bf16_f32 v162, v80, v81
v_mfma_f32_16x16x32_bf16 v[100:103], a[192:195], a[176:179], v[180:183]
v_exp_f32 v83, v83
v_mfma_f32_16x16x32_bf16 v[100:103], a[196:199], a[180:183], v[100:103]
ds_read_b64_tr_b16 a[224:225], v16
ds_read_b64_tr_b16 a[226:227], v16 offset:512
v_mfma_f32_16x16x32_bf16 v[100:103], a[200:203], a[184:187], v[100:103]
v_exp_f32 v68, v68
v_mfma_f32_16x16x32_bf16 v[100:103], a[204:207], a[188:191], v[100:103]
v_cvt_pk_bf16_f32 v163, v82, v83
ds_read_b64_tr_b16 a[228:229], v16 offset:8448
s_waitcnt lgkmcnt(4)
v_mfma_f32_16x16x32_bf16 v[104:107], a[208:211], a[128:131], v[168:171]
v_exp_f32 v69, v69
v_mfma_f32_16x16x32_bf16 v[104:107], a[212:215], a[132:135], v[104:107]
v_exp_f32 v70, v70
v_mfma_f32_16x16x32_bf16 v[104:107], a[216:219], a[136:139], v[104:107]
ds_read_b64_tr_b16 a[230:231], v16 offset:8960
v_cvt_pk_bf16_f32 v164, v68, v69
s_waitcnt lgkmcnt(4)
v_mfma_f32_16x16x32_bf16 v[104:107], a[220:223], a[140:143], v[104:107]
v_exp_f32 v71, v71
v_mfma_f32_16x16x32_bf16 v[108:111], a[208:211], a[144:147], v[172:175]
ds_read_b64_tr_b16 a[232:233], v16 offset:32
v_cvt_pk_bf16_f32 v165, v70, v71
v_mfma_f32_16x16x32_bf16 v[108:111], a[212:215], a[148:151], v[108:111]
v_exp_f32 v84, v84
v_mfma_f32_16x16x32_bf16 v[108:111], a[216:219], a[152:155], v[108:111]
ds_read_b64_tr_b16 a[234:235], v16 offset:544
v_mfma_f32_16x16x32_bf16 v[108:111], a[220:223], a[156:159], v[108:111]
v_exp_f32 v85, v85
v_mfma_f32_16x16x32_bf16 v[112:115], a[208:211], a[160:163], v[176:179]
ds_read_b64_tr_b16 a[236:237], v16 offset:8480
v_cvt_pk_bf16_f32 v166, v84, v85
v_mfma_f32_16x16x32_bf16 v[112:115], a[212:215], a[164:167], v[112:115]
v_exp_f32 v86, v86
v_mfma_f32_16x16x32_bf16 v[112:115], a[216:219], a[168:171], v[112:115]
ds_read_b64_tr_b16 a[238:239], v16 offset:8992
v_mfma_f32_16x16x32_bf16 v[112:115], a[220:223], a[172:175], v[112:115]
v_exp_f32 v87, v87
v_mfma_f32_16x16x32_bf16 v[116:119], a[208:211], a[176:179], v[180:183]
v_cvt_pk_bf16_f32 v167, v86, v87
v_mfma_f32_16x16x32_bf16 v[116:119], a[212:215], a[180:183], v[116:119]
v_mfma_f32_16x16x32_bf16 v[116:119], a[216:219], a[184:187], v[116:119]
v_mfma_f32_16x16x32_bf16 v[116:119], a[220:223], a[188:191], v[116:119]
s_waitcnt lgkmcnt(1)
v_mfma_f32_16x16x32_bf16 a[0:3], a[224:227], v[120:123], a[0:3]
v_exp_f32 v24, v24
v_mfma_f32_16x16x32_bf16 a[0:3], a[228:231], v[152:155], a[0:3]
ds_read_b64_tr_b16 a[208:209], v16 offset:64
v_mfma_f32_16x16x32_bf16 a[4:7], a[224:227], v[124:127], a[4:7]
v_exp_f32 v25, v25
v_mfma_f32_16x16x32_bf16 a[4:7], a[228:231], v[156:159], a[4:7]
ds_read_b64_tr_b16 a[210:211], v16 offset:576
v_cvt_pk_bf16_f32 v136, v24, v25
v_mfma_f32_16x16x32_bf16 a[8:11], a[224:227], v[128:131], a[8:11]
v_exp_f32 v26, v26
v_mfma_f32_16x16x32_bf16 a[8:11], a[228:231], v[160:163], a[8:11]
ds_read_b64_tr_b16 a[212:213], v16 offset:8512
v_mfma_f32_16x16x32_bf16 a[12:15], a[224:227], v[132:135], a[12:15]
s_cmp_lg_u32 s71, 0
s_cbranch_scc1 SKFa10_%=
buffer_load_dwordx4 v[224:227], v1, s[20:23], s26 offen sc1
buffer_load_dwordx4 v[228:231], v1, s[20:23], s26 offen offset:1024 sc1
SKFa10_%=:
v_mfma_f32_16x16x32_bf16 a[12:15], a[228:231], v[164:167], a[12:15]
v_exp_f32 v27, v27
v_mfma_f32_16x16x32_bf16 a[240:243], v[184:187], v[120:123], a[240:243]
ds_read_b64_tr_b16 a[214:215], v16 offset:9024
v_cvt_pk_bf16_f32 v137, v26, v27
v_mfma_f32_16x16x32_bf16 a[16:19], a[232:235], v[120:123], a[16:19]
v_exp_f32 v40, v40
s_waitcnt lgkmcnt(2)
v_mfma_f32_16x16x32_bf16 a[16:19], a[236:239], v[152:155], a[16:19]
ds_read_b64_tr_b16 a[216:217], v16 offset:96
v_mfma_f32_16x16x32_bf16 a[20:23], a[232:235], v[124:127], a[20:23]
v_exp_f32 v41, v41
v_mfma_f32_16x16x32_bf16 a[20:23], a[236:239], v[156:159], a[20:23]
ds_read_b64_tr_b16 a[218:219], v16 offset:608
v_cvt_pk_bf16_f32 v138, v40, v41
v_mfma_f32_16x16x32_bf16 a[24:27], a[232:235], v[128:131], a[24:27]
v_exp_f32 v42, v42
v_mfma_f32_16x16x32_bf16 a[24:27], a[236:239], v[160:163], a[24:27]
ds_read_b64_tr_b16 a[220:221], v16 offset:8544
v_mfma_f32_16x16x32_bf16 a[28:31], a[232:235], v[132:135], a[28:31]
v_exp_f32 v43, v43
v_mfma_f32_16x16x32_bf16 a[28:31], a[236:239], v[164:167], a[28:31]
s_cmp_lg_u32 s71, 0
s_cbranch_scc1 SKFa11_%=
buffer_load_dwordx4 v[232:235], v2, s[20:23], s26 offen sc1
buffer_load_dwordx4 v[236:239], v2, s[20:23], s26 offen offset:1024 sc1
SKFa11_%=:
v_mfma_f32_16x16x32_bf16 a[240:243], v[184:187], v[152:155], a[240:243]
ds_read_b64_tr_b16 a[222:223], v16 offset:9056
v_cvt_pk_bf16_f32 v139, v42, v43
v_mfma_f32_16x16x32_bf16 a[32:35], a[208:211], v[120:123], a[32:35]
v_exp_f32 v28, v28
s_waitcnt lgkmcnt(2)
v_mfma_f32_16x16x32_bf16 a[32:35], a[212:215], v[152:155], a[32:35]
ds_read_b64_tr_b16 a[224:225], v16 offset:128
v_mfma_f32_16x16x32_bf16 a[36:39], a[208:211], v[124:127], a[36:39]
v_exp_f32 v29, v29
v_mfma_f32_16x16x32_bf16 a[36:39], a[212:215], v[156:159], a[36:39]
ds_read_b64_tr_b16 a[226:227], v16 offset:640
v_cvt_pk_bf16_f32 v140, v28, v29
v_mfma_f32_16x16x32_bf16 a[40:43], a[208:211], v[128:131], a[40:43]
v_exp_f32 v30, v30
v_mfma_f32_16x16x32_bf16 a[40:43], a[212:215], v[160:163], a[40:43]
ds_read_b64_tr_b16 a[228:229], v16 offset:8576
v_mfma_f32_16x16x32_bf16 a[44:47], a[208:211], v[132:135], a[44:47]
v_exp_f32 v31, v31
v_mfma_f32_16x16x32_bf16 a[44:47], a[212:215], v[164:167], a[44:47]
ds_read_b64_tr_b16 a[230:231], v16 offset:9088
v_cvt_pk_bf16_f32 v141, v30, v31
v_mfma_f32_16x16x32_bf16 a[244:247], v[184:187], v[124:127], a[244:247]
s_cmp_lg_u32 s71, 0
s_cbranch_scc1 SKFa12_%=
buffer_load_dwordx4 v[240:243], v3, s[20:23], s26 offen sc1
buffer_load_dwordx4 v[244:247], v3, s[20:23], s26 offen offset:1024 sc1
SKFa12_%=:
v_mfma_f32_16x16x32_bf16 a[48:51], a[216:219], v[120:123], a[48:51]
v_exp_f32 v44, v44
s_waitcnt lgkmcnt(2)
v_mfma_f32_16x16x32_bf16 a[48:51], a[220:223], v[152:155], a[48:51]
ds_read_b64_tr_b16 a[232:233], v16 offset:160
v_mfma_f32_16x16x32_bf16 a[52:55], a[216:219], v[124:127], a[52:55]
v_exp_f32 v45, v45
v_mfma_f32_16x16x32_bf16 a[52:55], a[220:223], v[156:159], a[52:55]
ds_read_b64_tr_b16 a[234:235], v16 offset:672
v_cvt_pk_bf16_f32 v142, v44, v45
v_mfma_f32_16x16x32_bf16 a[56:59], a[216:219], v[128:131], a[56:59]
v_exp_f32 v46, v46
v_mfma_f32_16x16x32_bf16 a[56:59], a[220:223], v[160:163], a[56:59]
ds_read_b64_tr_b16 a[236:237], v16 offset:8608
v_mfma_f32_16x16x32_bf16 a[60:63], a[216:219], v[132:135], a[60:63]
v_exp_f32 v47, v47
v_mfma_f32_16x16x32_bf16 a[60:63], a[220:223], v[164:167], a[60:63]
ds_read_b64_tr_b16 a[238:239], v16 offset:9120
v_cvt_pk_bf16_f32 v143, v46, v47
v_mfma_f32_16x16x32_bf16 a[244:247], v[184:187], v[156:159], a[244:247]
v_exp_f32 v32, v32
v_mfma_f32_16x16x32_bf16 a[64:67], a[224:227], v[120:123], a[64:67]
s_cmp_lg_u32 s71, 0
s_cbranch_scc1 SKFa13_%=
buffer_load_dwordx4 v[248:251], v4, s[20:23], s26 offen sc1
buffer_load_dwordx4 v[252:255], v4, s[20:23], s26 offen offset:1024 sc1
s_add_u32 s26, s26, 0x4000
SKFa13_%=:
s_waitcnt lgkmcnt(2)
v_mfma_f32_16x16x32_bf16 a[64:67], a[228:231], v[152:155], a[64:67]
v_exp_f32 v33, v33
v_mfma_f32_16x16x32_bf16 a[68:71], a[224:227], v[124:127], a[68:71]
ds_read_b64_tr_b16 a[208:209], v16 offset:192
ds_read_b64_tr_b16 a[210:211], v16 offset:704
v_mfma_f32_16x16x32_bf16 a[68:71], a[228:231], v[156:159], a[68:71]
v_exp_f32 v34, v34
v_mfma_f32_16x16x32_bf16 a[72:75], a[224:227], v[128:131], a[72:75]
v_cvt_pk_bf16_f32 v144, v32, v33
ds_read_b64_tr_b16 a[212:213], v16 offset:8640
v_mfma_f32_16x16x32_bf16 a[72:75], a[228:231], v[160:163], a[72:75]
v_exp_f32 v35, v35
v_mfma_f32_16x16x32_bf16 a[76:79], a[224:227], v[132:135], a[76:79]
ds_read_b64_tr_b16 a[214:215], v16 offset:9152
v_cvt_pk_bf16_f32 v145, v34, v35
v_mfma_f32_16x16x32_bf16 a[76:79], a[228:231], v[164:167], a[76:79]
v_exp_f32 v48, v48
v_mfma_f32_16x16x32_bf16 a[248:251], v[184:187], v[128:131], a[248:251]
v_exp_f32 v49, v49
v_mfma_f32_16x16x32_bf16 a[80:83], a[232:235], v[120:123], a[80:83]
ds_read_b64_tr_b16 a[216:217], v16 offset:224
v_cvt_pk_bf16_f32 v146, v48, v49
s_waitcnt lgkmcnt(3)
v_mfma_f32_16x16x32_bf16 a[80:83], a[236:239], v[152:155], a[80:83]
v_exp_f32 v50, v50
v_mfma_f32_16x16x32_bf16 a[84:87], a[232:235], v[124:127], a[84:87]
ds_read_b64_tr_b16 a[218:219], v16 offset:736
v_mfma_f32_16x16x32_bf16 a[84:87], a[236:239], v[156:159], a[84:87]
v_exp_f32 v51, v51
v_mfma_f32_16x16x32_bf16 a[88:91], a[232:235], v[128:131], a[88:91]
ds_read_b64_tr_b16 a[220:221], v16 offset:8672
v_cvt_pk_bf16_f32 v147, v50, v51
v_mfma_f32_16x16x32_bf16 a[88:91], a[236:239], v[160:163], a[88:91]
v_exp_f32 v36, v36
v_mfma_f32_16x16x32_bf16 a[92:95], a[232:235], v[132:135], a[92:95]
ds_read_b64_tr_b16 a[222:223], v16 offset:9184
v_mfma_f32_16x16x32_bf16 a[92:95], a[236:239], v[164:167], a[92:95]
v_exp_f32 v37, v37
v_mfma_f32_16x16x32_bf16 a[248:251], v[184:187], v[160:163], a[248:251]
v_exp_f32 v38, v38
v_mfma_f32_16x16x32_bf16 a[96:99], a[208:211], v[120:123], a[96:99]
v_cvt_pk_bf16_f32 v148, v36, v37
s_waitcnt lgkmcnt(2)
v_mfma_f32_16x16x32_bf16 a[96:99], a[212:215], v[152:155], a[96:99]
v_exp_f32 v39, v39
v_mfma_f32_16x16x32_bf16 a[100:103], a[208:211], v[124:127], a[100:103]
v_exp_f32 v52, v52
v_mfma_f32_16x16x32_bf16 a[100:103], a[212:215], v[156:159], a[100:103]
v_cvt_pk_bf16_f32 v149, v38, v39
v_mfma_f32_16x16x32_bf16 a[104:107], a[208:211], v[128:131], a[104:107]
v_exp_f32 v53, v53
v_mfma_f32_16x16x32_bf16 a[104:107], a[212:215], v[160:163], a[104:107]
v_cvt_pk_bf16_f32 v150, v52, v53
v_mfma_f32_16x16x32_bf16 a[108:111], a[208:211], v[132:135], a[108:111]
v_exp_f32 v54, v54
v_mfma_f32_16x16x32_bf16 a[108:111], a[212:215], v[164:167], a[108:111]
v_mfma_f32_16x16x32_bf16 a[252:255], v[184:187], v[132:135], a[252:255]
s_waitcnt vmcnt(16) lgkmcnt(0)
s_cmp_lg_u32 s71, 0
s_cbranch_scc1 OOLfw6_%=
RETfw6_%=:
s_barrier
v_mfma_f32_16x16x32_bf16 a[112:115], a[216:219], v[120:123], a[112:115]
v_exp_f32 v55, v55
v_mfma_f32_16x16x32_bf16 a[112:115], a[220:223], v[152:155], a[112:115]
v_cvt_pk_bf16_f32 v151, v54, v55
v_mfma_f32_16x16x32_bf16 a[116:119], a[216:219], v[124:127], a[116:119]
v_mfma_f32_16x16x32_bf16 a[116:119], a[220:223], v[156:159], a[116:119]
v_mfma_f32_16x16x32_bf16 a[120:123], a[216:219], v[128:131], a[120:123]
v_mfma_f32_16x16x32_bf16 a[120:123], a[220:223], v[160:163], a[120:123]
v_mfma_f32_16x16x32_bf16 a[124:127], a[216:219], v[132:135], a[124:127]
)ASM"
R"ASM(v_mfma_f32_16x16x32_bf16 a[124:127], a[220:223], v[164:167], a[124:127]
v_mfma_f32_16x16x32_bf16 a[252:255], v[184:187], v[164:167], a[252:255]
ds_read_b64_tr_b16 a[192:193], v16 offset:16896
ds_read_b64_tr_b16 a[194:195], v16 offset:17408
ds_read_b64_tr_b16 a[196:197], v16 offset:16928
ds_read_b64_tr_b16 a[198:199], v16 offset:17440
ds_read_b64_tr_b16 a[200:201], v16 offset:16960
ds_read_b64_tr_b16 a[202:203], v16 offset:17472
ds_read_b64_tr_b16 a[204:205], v16 offset:16992
ds_read_b64_tr_b16 a[206:207], v16 offset:17504
s_waitcnt lgkmcnt(6)
v_mfma_f32_16x16x32_bf16 a[0:3], a[192:195], v[136:139], a[0:3]
v_exp_f32 v88, v88
v_mfma_f32_16x16x32_bf16 a[4:7], a[192:195], v[140:143], a[4:7]
ds_read_b64_tr_b16 a[208:209], v16 offset:17024
v_exp_f32 v89, v89
v_mfma_f32_16x16x32_bf16 a[8:11], a[192:195], v[144:147], a[8:11]
ds_read_b64_tr_b16 a[210:211], v16 offset:17536
v_exp_f32 v90, v90
v_mfma_f32_16x16x32_bf16 a[12:15], a[192:195], v[148:151], a[12:15]
v_exp_f32 v91, v91
v_cvt_pk_bf16_f32 v152, v88, v89
s_waitcnt lgkmcnt(6)
v_mfma_f32_16x16x32_bf16 a[16:19], a[196:199], v[136:139], a[16:19]
v_exp_f32 v104, v104
v_mfma_f32_16x16x32_bf16 a[20:23], a[196:199], v[140:143], a[20:23]
ds_read_b64_tr_b16 a[212:213], v16 offset:17056
v_exp_f32 v105, v105
v_cvt_pk_bf16_f32 v153, v90, v91
v_mfma_f32_16x16x32_bf16 a[24:27], a[196:199], v[144:147], a[24:27]
ds_read_b64_tr_b16 a[214:215], v16 offset:17568
v_exp_f32 v106, v106
v_mfma_f32_16x16x32_bf16 a[28:31], a[196:199], v[148:151], a[28:31]
v_exp_f32 v107, v107
v_cvt_pk_bf16_f32 v154, v104, v105
v_mfma_f32_16x16x32_bf16 a[240:243], v[184:187], v[136:139], a[240:243]
v_exp_f32 v92, v92
s_cmp_lg_u32 s71, 0
s_cbranch_scc1 SKFap2a_%=
buffer_load_dwordx4 v[24:27], v1, s[20:23], s26 offen sc1
buffer_load_dwordx4 v[28:31], v1, s[20:23], s26 offen offset:1024 sc1
buffer_load_dwordx4 v[32:35], v2, s[20:23], s26 offen sc1
buffer_load_dwordx4 v[36:39], v2, s[20:23], s26 offen offset:1024 sc1
SKFap2a_%=:
s_waitcnt lgkmcnt(2)
v_mfma_f32_16x16x32_bf16 a[32:35], a[200:203], v[136:139], a[32:35]
v_exp_f32 v93, v93
v_cvt_pk_bf16_f32 v155, v106, v107
v_mfma_f32_16x16x32_bf16 a[36:39], a[200:203], v[140:143], a[36:39]
ds_read_b64_tr_b16 a[216:217], v16 offset:17088
v_exp_f32 v94, v94
v_mfma_f32_16x16x32_bf16 a[40:43], a[200:203], v[144:147], a[40:43]
ds_read_b64_tr_b16 a[218:219], v16 offset:17600
v_exp_f32 v95, v95
v_cvt_pk_bf16_f32 v156, v92, v93
v_mfma_f32_16x16x32_bf16 a[44:47], a[200:203], v[148:151], a[44:47]
v_exp_f32 v108, v108
v_mfma_f32_16x16x32_bf16 a[48:51], a[204:207], v[136:139], a[48:51]
v_exp_f32 v109, v109
v_cvt_pk_bf16_f32 v157, v94, v95
v_mfma_f32_16x16x32_bf16 a[52:55], a[204:207], v[140:143], a[52:55]
ds_read_b64_tr_b16 a[220:221], v16 offset:17120
v_exp_f32 v110, v110
v_mfma_f32_16x16x32_bf16 a[56:59], a[204:207], v[144:147], a[56:59]
ds_read_b64_tr_b16 a[222:223], v16 offset:17632
v_exp_f32 v111, v111
v_cvt_pk_bf16_f32 v158, v108, v109
v_mfma_f32_16x16x32_bf16 a[60:63], a[204:207], v[148:151], a[60:63]
v_exp_f32 v96, v96
v_mfma_f32_16x16x32_bf16 a[244:247], v[184:187], v[140:143], a[244:247]
v_exp_f32 v97, v97
v_cvt_pk_bf16_f32 v159, v110, v111
v_mfma_f32_16x16x32_bf16 a[64:67], a[208:211], v[136:139], a[64:67]
v_exp_f32 v98, v98
v_mfma_f32_16x16x32_bf16 a[68:71], a[208:211], v[140:143], a[68:71]
ds_read_b64_tr_b16 a[192:193], v16 offset:25344
v_exp_f32 v99, v99
v_cvt_pk_bf16_f32 v160, v96, v97
v_mfma_f32_16x16x32_bf16 a[72:75], a[208:211], v[144:147], a[72:75]
ds_read_b64_tr_b16 a[194:195], v16 offset:25856
v_exp_f32 v112, v112
v_mfma_f32_16x16x32_bf16 a[76:79], a[208:211], v[148:151], a[76:79]
v_exp_f32 v113, v113
v_cvt_pk_bf16_f32 v161, v98, v99
s_waitcnt lgkmcnt(2)
v_mfma_f32_16x16x32_bf16 a[80:83], a[212:215], v[136:139], a[80:83]
v_exp_f32 v114, v114
s_cmp_lg_u32 s71, 0
s_cbranch_scc1 SKFap2b_%=
buffer_load_dwordx4 v[40:43], v3, s[20:23], s26 offen sc1
buffer_load_dwordx4 v[44:47], v3, s[20:23], s26 offen offset:1024 sc1
buffer_load_dwordx4 v[48:51], v4, s[20:23], s26 offen sc1
buffer_load_dwordx4 v[52:55], v4, s[20:23], s26 offen offset:1024 sc1
s_add_u32 s26, s26, 0x4000
SKFap2b_%=:
v_mfma_f32_16x16x32_bf16 a[84:87], a[212:215], v[140:143], a[84:87]
ds_read_b64_tr_b16 a[196:197], v16 offset:25376
v_exp_f32 v115, v115
v_cvt_pk_bf16_f32 v162, v112, v113
v_mfma_f32_16x16x32_bf16 a[88:91], a[212:215], v[144:147], a[88:91]
ds_read_b64_tr_b16 a[198:199], v16 offset:25888
v_exp_f32 v100, v100
v_mfma_f32_16x16x32_bf16 a[92:95], a[212:215], v[148:151], a[92:95]
v_exp_f32 v101, v101
v_cvt_pk_bf16_f32 v163, v114, v115
v_mfma_f32_16x16x32_bf16 a[248:251], v[184:187], v[144:147], a[248:251]
v_exp_f32 v102, v102
v_mfma_f32_16x16x32_bf16 a[96:99], a[216:219], v[136:139], a[96:99]
v_exp_f32 v103, v103
v_cvt_pk_bf16_f32 v164, v100, v101
v_mfma_f32_16x16x32_bf16 a[100:103], a[216:219], v[140:143], a[100:103]
ds_read_b64_tr_b16 a[200:201], v16 offset:25408
v_exp_f32 v116, v116
v_mfma_f32_16x16x32_bf16 a[104:107], a[216:219], v[144:147], a[104:107]
ds_read_b64_tr_b16 a[202:203], v16 offset:25920
v_exp_f32 v117, v117
v_cvt_pk_bf16_f32 v165, v102, v103
v_mfma_f32_16x16x32_bf16 a[108:111], a[216:219], v[148:151], a[108:111]
v_exp_f32 v118, v118
v_mfma_f32_16x16x32_bf16 a[112:115], a[220:223], v[136:139], a[112:115]
v_exp_f32 v119, v119
v_cvt_pk_bf16_f32 v166, v116, v117
v_mfma_f32_16x16x32_bf16 a[116:119], a[220:223], v[140:143], a[116:119]
ds_read_b64_tr_b16 a[204:205], v16 offset:25440
v_mfma_f32_16x16x32_bf16 a[120:123], a[220:223], v[144:147], a[120:123]
ds_read_b64_tr_b16 a[206:207], v16 offset:25952
v_cvt_pk_bf16_f32 v167, v118, v119
v_mfma_f32_16x16x32_bf16 a[124:127], a[220:223], v[148:151], a[124:127]
v_mfma_f32_16x16x32_bf16 a[252:255], v[184:187], v[148:151], a[252:255]
s_waitcnt lgkmcnt(2)
v_mfma_f32_16x16x32_bf16 a[0:3], a[192:195], v[152:155], a[0:3]
v_mfma_f32_16x16x32_bf16 a[4:7], a[192:195], v[156:159], a[4:7]
ds_read_b64_tr_b16 a[208:209], v16 offset:25472
v_mfma_f32_16x16x32_bf16 a[8:11], a[192:195], v[160:163], a[8:11]
ds_read_b64_tr_b16 a[210:211], v16 offset:25984
s_cmp_lg_u32 s71, 0
s_cbranch_scc1 SKFap3a_%=
buffer_load_dwordx4 v[56:59], v1, s[20:23], s26 offen sc1
buffer_load_dwordx4 v[60:63], v1, s[20:23], s26 offen offset:1024 sc1
buffer_load_dwordx4 v[64:67], v2, s[20:23], s26 offen sc1
buffer_load_dwordx4 v[68:71], v2, s[20:23], s26 offen offset:1024 sc1
SKFap3a_%=:
v_mfma_f32_16x16x32_bf16 a[12:15], a[192:195], v[164:167], a[12:15]
v_mfma_f32_16x16x32_bf16 a[16:19], a[196:199], v[152:155], a[16:19]
v_mfma_f32_16x16x32_bf16 a[20:23], a[196:199], v[156:159], a[20:23]
ds_read_b64_tr_b16 a[212:213], v16 offset:25504
v_mfma_f32_16x16x32_bf16 a[24:27], a[196:199], v[160:163], a[24:27]
ds_read_b64_tr_b16 a[214:215], v16 offset:26016
v_mfma_f32_16x16x32_bf16 a[28:31], a[196:199], v[164:167], a[28:31]
v_mfma_f32_16x16x32_bf16 a[240:243], v[184:187], v[152:155], a[240:243]
v_mfma_f32_16x16x32_bf16 a[32:35], a[200:203], v[152:155], a[32:35]
v_mfma_f32_16x16x32_bf16 a[36:39], a[200:203], v[156:159], a[36:39]
ds_read_b64_tr_b16 a[216:217], v16 offset:25536
v_mfma_f32_16x16x32_bf16 a[40:43], a[200:203], v[160:163], a[40:43]
ds_read_b64_tr_b16 a[218:219], v16 offset:26048
v_mfma_f32_16x16x32_bf16 a[44:47], a[200:203], v[164:167], a[44:47]
s_waitcnt lgkmcnt(2)
v_mfma_f32_16x16x32_bf16 a[48:51], a[204:207], v[152:155], a[48:51]
v_mfma_f32_16x16x32_bf16 a[52:55], a[204:207], v[156:159], a[52:55]
ds_read_b64_tr_b16 a[220:221], v16 offset:25568
v_mfma_f32_16x16x32_bf16 a[56:59], a[204:207], v[160:163], a[56:59]
ds_read_b64_tr_b16 a[222:223], v16 offset:26080
v_mfma_f32_16x16x32_bf16 a[60:63], a[204:207], v[164:167], a[60:63]
s_cmp_lg_u32 s71, 0
s_cbranch_scc1 SKFap3b_%=
buffer_load_dwordx4 v[72:75], v3, s[20:23], s26 offen sc1
buffer_load_dwordx4 v[76:79], v3, s[20:23], s26 offen offset:1024 sc1
buffer_load_dwordx4 v[80:83], v4, s[20:23], s26 offen sc1
buffer_load_dwordx4 v[84:87], v4, s[20:23], s26 offen offset:1024 sc1
s_add_u32 s26, s26, 0x4000
SKFap3b_%=:
v_mfma_f32_16x16x32_bf16 a[244:247], v[184:187], v[156:159], a[244:247]
v_mfma_f32_16x16x32_bf16 a[64:67], a[208:211], v[152:155], a[64:67]
v_mfma_f32_16x16x32_bf16 a[68:71], a[208:211], v[156:159], a[68:71]
v_mfma_f32_16x16x32_bf16 a[72:75], a[208:211], v[160:163], a[72:75]
v_mfma_f32_16x16x32_bf16 a[76:79], a[208:211], v[164:167], a[76:79]
v_mfma_f32_16x16x32_bf16 a[80:83], a[212:215], v[152:155], a[80:83]
v_mfma_f32_16x16x32_bf16 a[84:87], a[212:215], v[156:159], a[84:87]
v_mfma_f32_16x16x32_bf16 a[88:91], a[212:215], v[160:163], a[88:91]
v_mfma_f32_16x16x32_bf16 a[92:95], a[212:215], v[164:167], a[92:95]
v_mfma_f32_16x16x32_bf16 a[248:251], v[184:187], v[160:163], a[248:251]
s_waitcnt lgkmcnt(0)
v_mfma_f32_16x16x32_bf16 a[96:99], a[216:219], v[152:155], a[96:99]
v_mfma_f32_16x16x32_bf16 a[100:103], a[216:219], v[156:159], a[100:103]
v_mfma_f32_16x16x32_bf16 a[104:107], a[216:219], v[160:163], a[104:107]
v_mfma_f32_16x16x32_bf16 a[108:111], a[216:219], v[164:167], a[108:111]
v_mfma_f32_16x16x32_bf16 a[112:115], a[220:223], v[152:155], a[112:115]
v_mfma_f32_16x16x32_bf16 a[116:119], a[220:223], v[156:159], a[116:119]
v_mfma_f32_16x16x32_bf16 a[120:123], a[220:223], v[160:163], a[120:123]
v_mfma_f32_16x16x32_bf16 a[124:127], a[220:223], v[164:167], a[124:127]
v_mfma_f32_16x16x32_bf16 a[252:255], v[184:187], v[164:167], a[252:255]
s_nop 7
s_nop 1
v_accvgpr_read_b32 v136, a240
v_accvgpr_read_b32 v137, a244
v_accvgpr_read_b32 v138, a248
v_accvgpr_read_b32 v139, a252
s_mov_b32 s34, 0x5d800000
v_max3_f32 v140, v136, v137, v138
v_max_f32 v140, v140, v139
v_add_f32 v141, v136, v137
v_add_f32 v142, v138, v139
v_add_f32 v141, v141, v142
v_cmp_ngt_f32 vcc, s34, v141
s_nop 1
s_cmp_lg_u64 vcc, 0
s_cselect_b32 s35, 1, 0
s_lshl_b32 s36, s4, 2
s_add_u32 s36, s36, 0x27fc0
v_mov_b32 v140, s36
v_mov_b32 v141, s35
ds_write_b32 v140, v141
s_waitcnt lgkmcnt(0)
s_barrier
v_mov_b32 v140, 0x27fc0
ds_read_b128 v[144:147], v140
s_waitcnt lgkmcnt(0)
v_or_b32 v144, v144, v145
v_or_b32 v146, v146, v147
v_or_b32 v144, v144, v146
s_nop 0
v_readfirstlane_b32 s35, v144
s_nop 3
s_andn2_b32 s35, s35, s71
s_cmp_lg_u32 s35, 0
s_cbranch_scc1 FALLBACK_%=
ATT_OK_%=:
s_add_u32 s39, s74, 0
s_and_b32 s39, s39, 3
s_lshl_b32 s39, s39, 11
v_add_u32 v1, s39, v0
s_add_u32 s39, s74, 1
s_and_b32 s39, s39, 3
s_lshl_b32 s39, s39, 11
v_add_u32 v2, s39, v0
s_add_u32 s39, s74, 2
s_and_b32 s39, s39, 3
s_lshl_b32 s39, s39, 11
v_add_u32 v3, s39, v0
s_add_u32 s39, s74, 3
s_and_b32 s39, s39, 3
s_lshl_b32 s39, s39, 11
v_add_u32 v4, s39, v0
s_cmp_lg_u32 s71, 0
s_cbranch_scc0 P2FAST_%=
s_waitcnt vmcnt(0)
v_mbcnt_lo_u32_b32 v9, -1, 0
v_mbcnt_hi_u32_b32 v9, -1, v9
v_lshrrev_b32 v10, 4, v9
v_and_b32 v11, 15, v9
v_lshlrev_b32 v11, 4, v11
v_lshl_add_u32 v20, v10, 14, v11
v_add_u32 v21, 0x3e0, v20
v_add_u32 v22, 0x7c0, v20
v_add_u32 v23, 0xba0, v20
v_bfe_u32 v17, v9, 2, 2
v_and_b32 v18, 1, v10
v_lshl_add_u32 v18, v18, 2, v17
s_movk_i32 s34, 0x420
v_mul_u32_u24 v18, s34, v18
v_lshrrev_b32 v19, 1, v10
v_lshlrev_b32 v19, 8, v19
v_and_b32 v17, 3, v9
v_lshlrev_b32 v17, 3, v17
v_add3_u32 v188, v18, v19, v17
v_add_u32 v188, 0xb1c0, v188
v_add_u32 v189, 0xc600, v188
v_add_u32 v190, 0x18c00, v188
s_mov_b32 s16, s62
s_and_b32 s17, s63, 0xffff
s_mov_b32 s18, 0x700000
s_mov_b32 s19, 0x20000
s_mov_b32 s20, s66
s_and_b32 s21, s67, 0xffff
s_mov_b32 s22, 0x2300000
s_mov_b32 s23, 0x20000
s_lshl_b32 s40, s72, 1
s_add_u32 s40, s40, s68
s_mul_i32 s26, s40, 56
s_mul_i32 s39, s74, 14
s_add_u32 s26, s26, s39
s_lshl_b32 s26, s26, 14
s_lshl_b32 s39, s69, 13
s_add_u32 s26, s26, s39
s_add_u32 s26, s26, 0x500000
s_mul_i32 s27, s74, 0x1c0000
s_lshl_b32 s39, s68, 16
s_add_u32 s27, s27, s39
s_lshl_b32 s39, s69, 13
s_add_u32 s27, s27, s39
s_lshl_b32 s39, s73, 8
s_add_u32 s27, s27, s39
s_mul_i32 s31, s40, 64
s_mul_i32 s39, s74, 16
s_add_u32 s31, s31, s39
s_lshl_b32 s31, s31, 14
s_lshl_b32 s39, s69, 13
s_add_u32 s31, s31, s39
s_add_u32 s31, s31, 0x1300000
s_mul_i32 s75, s74, 0x200000
s_lshl_b32 s39, s68, 16
s_add_u32 s75, s75, s39
s_lshl_b32 s39, s69, 13
s_add_u32 s75, s75, s39
s_lshl_b32 s39, s73, 8
s_add_u32 s75, s75, s39
s_add_u32 m0, s30, 0x135c0
s_nop 0
buffer_load_dwordx4 v20, s[16:19], s27 offen lds
buffer_load_dwordx4 v21, s[16:19], s27 offen offset:1056 lds
buffer_load_dwordx4 v22, s[16:19], s27 offen offset:2112 lds
buffer_load_dwordx4 v23, s[16:19], s27 offen offset:3168 lds
s_add_u32 s27, s27, 0x20000
s_add_u32 m0, s30, 0x177c0
s_nop 0
buffer_load_dwordx4 v20, s[16:19], s27 offen lds
buffer_load_dwordx4 v21, s[16:19], s27 offen offset:1056 lds
buffer_load_dwordx4 v22, s[16:19], s27 offen offset:2112 lds
buffer_load_dwordx4 v23, s[16:19], s27 offen offset:3168 lds
s_add_u32 s27, s27, 0x20000
s_add_u32 m0, s30, 0x1b9c0
s_nop 0
buffer_load_dwordx4 v20, s[16:19], s27 offen lds
buffer_load_dwordx4 v21, s[16:19], s27 offen offset:1056 lds
buffer_load_dwordx4 v22, s[16:19], s27 offen offset:2112 lds
buffer_load_dwordx4 v23, s[16:19], s27 offen offset:3168 lds
s_add_u32 s27, s27, 0x20000
s_add_u32 m0, s30, 0x1fbc0
s_nop 0
buffer_load_dwordx4 v20, s[16:19], s27 offen lds
buffer_load_dwordx4 v21, s[16:19], s27 offen offset:1056 lds
buffer_load_dwordx4 v22, s[16:19], s27 offen offset:2112 lds
buffer_load_dwordx4 v23, s[16:19], s27 offen offset:3168 lds
s_add_u32 s27, s27, 0x20000
s_add_u32 m0, s30, 0x23dc0
s_nop 0
buffer_load_dwordx4 v20, s[16:19], s27 offen lds
buffer_load_dwordx4 v21, s[16:19], s27 offen offset:1056 lds
buffer_load_dwordx4 v22, s[16:19], s27 offen offset:2112 lds
buffer_load_dwordx4 v23, s[16:19], s27 offen offset:3168 lds
s_add_u32 s27, s27, 0x20000
buffer_load_dwordx4 v[192:195], v1, s[20:23], s26 offen sc1
buffer_load_dwordx4 v[196:199], v1, s[20:23], s26 offen offset:1024 sc1
buffer_load_dwordx4 v[200:203], v2, s[20:23], s26 offen sc1
buffer_load_dwordx4 v[204:207], v2, s[20:23], s26 offen offset:1024 sc1
buffer_load_dwordx4 v[208:211], v3, s[20:23], s26 offen sc1
buffer_load_dwordx4 v[212:215], v3, s[20:23], s26 offen offset:1024 sc1
buffer_load_dwordx4 v[216:219], v4, s[20:23], s26 offen sc1
buffer_load_dwordx4 v[220:223], v4, s[20:23], s26 offen offset:1024 sc1
s_add_u32 s26, s26, 0x4000
buffer_load_dwordx4 v[224:227], v1, s[20:23], s26 offen sc1
buffer_load_dwordx4 v[228:231], v1, s[20:23], s26 offen offset:1024 sc1
buffer_load_dwordx4 v[232:235], v2, s[20:23], s26 offen sc1
buffer_load_dwordx4 v[236:239], v2, s[20:23], s26 offen offset:1024 sc1
buffer_load_dwordx4 v[240:243], v3, s[20:23], s26 offen sc1
buffer_load_dwordx4 v[244:247], v3, s[20:23], s26 offen offset:1024 sc1
buffer_load_dwordx4 v[248:251], v4, s[20:23], s26 offen sc1
buffer_load_dwordx4 v[252:255], v4, s[20:23], s26 offen offset:1024 sc1
s_add_u32 s26, s26, 0x4000
buffer_load_dwordx4 v[24:27], v1, s[20:23], s26 offen sc1
buffer_load_dwordx4 v[28:31], v1, s[20:23], s26 offen offset:1024 sc1
buffer_load_dwordx4 v[32:35], v2, s[20:23], s26 offen sc1
buffer_load_dwordx4 v[36:39], v2, s[20:23], s26 offen offset:1024 sc1
buffer_load_dwordx4 v[40:43], v3, s[20:23], s26 offen sc1
buffer_load_dwordx4 v[44:47], v3, s[20:23], s26 offen offset:1024 sc1
buffer_load_dwordx4 v[48:51], v4, s[20:23], s26 offen sc1
buffer_load_dwordx4 v[52:55], v4, s[20:23], s26 offen offset:1024 sc1
s_add_u32 s26, s26, 0x4000
buffer_load_dwordx4 v[56:59], v1, s[20:23], s26 offen sc1
buffer_load_dwordx4 v[60:63], v1, s[20:23], s26 offen offset:1024 sc1
buffer_load_dwordx4 v[64:67], v2, s[20:23], s26 offen sc1
buffer_load_dwordx4 v[68:71], v2, s[20:23], s26 offen offset:1024 sc1
buffer_load_dwordx4 v[72:75], v3, s[20:23], s26 offen sc1
buffer_load_dwordx4 v[76:79], v3, s[20:23], s26 offen offset:1024 sc1
buffer_load_dwordx4 v[80:83], v4, s[20:23], s26 offen sc1
buffer_load_dwordx4 v[84:87], v4, s[20:23], s26 offen offset:1024 sc1
s_add_u32 s26, s26, 0x4000
s_waitcnt vmcnt(0)
s_barrier
P2FAST_%=:
v_rcp_f32 v136, v136
v_rcp_f32 v137, v137
v_rcp_f32 v138, v138
v_rcp_f32 v139, v139
s_lshl_b32 s34, s6, 1
s_add_u32 s34, s34, s68
s_lshl_b32 s34, s34, 6
s_lshl_b32 s35, s7, 1
s_add_u32 s34, s34, s35
s_lshl_b32 s34, s34, 14
s_lshl_b32 s35, s69, 13
s_add_u32 s34, s34, s35
s_add_u32 s34, s34, 0x1300000
v_accvgpr_read_b32 v148, a0
v_accvgpr_read_b32 v149, a1
v_accvgpr_read_b32 v150, a2
v_accvgpr_read_b32 v151, a3
v_accvgpr_read_b32 v152, a16
v_accvgpr_read_b32 v153, a17
v_accvgpr_read_b32 v154, a18
)ASM"
R"ASM(v_accvgpr_read_b32 v155, a19
v_mul_f32 v148, v136, v148
v_mul_f32 v149, v136, v149
v_mul_f32 v150, v136, v150
v_mul_f32 v151, v136, v151
v_mul_f32 v152, v136, v152
v_mul_f32 v153, v136, v153
v_mul_f32 v154, v136, v154
v_mul_f32 v155, v136, v155
v_cvt_pk_bf16_f32 v156, v148, v149
v_cvt_pk_bf16_f32 v157, v150, v151
v_cvt_pk_bf16_f32 v158, v152, v153
v_cvt_pk_bf16_f32 v159, v154, v155
s_add_u32 s35, s34, 0
buffer_store_dwordx4 v[156:159], v0, s[76:79], s35 offen sc1
v_accvgpr_read_b32 v160, a4
v_accvgpr_read_b32 v161, a5
v_accvgpr_read_b32 v162, a6
v_accvgpr_read_b32 v163, a7
v_accvgpr_read_b32 v164, a20
v_accvgpr_read_b32 v165, a21
v_accvgpr_read_b32 v166, a22
v_accvgpr_read_b32 v167, a23
v_mul_f32 v160, v137, v160
v_mul_f32 v161, v137, v161
v_mul_f32 v162, v137, v162
v_mul_f32 v163, v137, v163
v_mul_f32 v164, v137, v164
v_mul_f32 v165, v137, v165
v_mul_f32 v166, v137, v166
v_mul_f32 v167, v137, v167
v_cvt_pk_bf16_f32 v168, v160, v161
v_cvt_pk_bf16_f32 v169, v162, v163
v_cvt_pk_bf16_f32 v170, v164, v165
v_cvt_pk_bf16_f32 v171, v166, v167
s_add_u32 s35, s34, 0x800
buffer_store_dwordx4 v[168:171], v0, s[76:79], s35 offen sc1
v_accvgpr_read_b32 v148, a8
v_accvgpr_read_b32 v149, a9
v_accvgpr_read_b32 v150, a10
v_accvgpr_read_b32 v151, a11
v_accvgpr_read_b32 v152, a24
v_accvgpr_read_b32 v153, a25
v_accvgpr_read_b32 v154, a26
v_accvgpr_read_b32 v155, a27
v_mul_f32 v148, v138, v148
v_mul_f32 v149, v138, v149
v_mul_f32 v150, v138, v150
v_mul_f32 v151, v138, v151
v_mul_f32 v152, v138, v152
v_mul_f32 v153, v138, v153
v_mul_f32 v154, v138, v154
v_mul_f32 v155, v138, v155
v_cvt_pk_bf16_f32 v156, v148, v149
v_cvt_pk_bf16_f32 v157, v150, v151
v_cvt_pk_bf16_f32 v158, v152, v153
v_cvt_pk_bf16_f32 v159, v154, v155
s_add_u32 s35, s34, 0x1000
buffer_store_dwordx4 v[156:159], v0, s[76:79], s35 offen sc1
v_accvgpr_read_b32 v160, a12
v_accvgpr_read_b32 v161, a13
v_accvgpr_read_b32 v162, a14
v_accvgpr_read_b32 v163, a15
v_accvgpr_read_b32 v164, a28
v_accvgpr_read_b32 v165, a29
v_accvgpr_read_b32 v166, a30
v_accvgpr_read_b32 v167, a31
v_mul_f32 v160, v139, v160
v_mul_f32 v161, v139, v161
v_mul_f32 v162, v139, v162
v_mul_f32 v163, v139, v163
v_mul_f32 v164, v139, v164
v_mul_f32 v165, v139, v165
v_mul_f32 v166, v139, v166
v_mul_f32 v167, v139, v167
v_cvt_pk_bf16_f32 v168, v160, v161
v_cvt_pk_bf16_f32 v169, v162, v163
v_cvt_pk_bf16_f32 v170, v164, v165
v_cvt_pk_bf16_f32 v171, v166, v167
s_add_u32 s35, s34, 0x1800
buffer_store_dwordx4 v[168:171], v0, s[76:79], s35 offen sc1
v_accvgpr_read_b32 v148, a32
v_accvgpr_read_b32 v149, a33
v_accvgpr_read_b32 v150, a34
v_accvgpr_read_b32 v151, a35
v_accvgpr_read_b32 v152, a48
v_accvgpr_read_b32 v153, a49
v_accvgpr_read_b32 v154, a50
v_accvgpr_read_b32 v155, a51
v_mul_f32 v148, v136, v148
v_mul_f32 v149, v136, v149
v_mul_f32 v150, v136, v150
v_mul_f32 v151, v136, v151
v_mul_f32 v152, v136, v152
v_mul_f32 v153, v136, v153
v_mul_f32 v154, v136, v154
v_mul_f32 v155, v136, v155
v_cvt_pk_bf16_f32 v156, v148, v149
v_cvt_pk_bf16_f32 v157, v150, v151
v_cvt_pk_bf16_f32 v158, v152, v153
v_cvt_pk_bf16_f32 v159, v154, v155
s_add_u32 s35, s34, 0x400
buffer_store_dwordx4 v[156:159], v0, s[76:79], s35 offen sc1
v_accvgpr_read_b32 v160, a36
v_accvgpr_read_b32 v161, a37
v_accvgpr_read_b32 v162, a38
v_accvgpr_read_b32 v163, a39
v_accvgpr_read_b32 v164, a52
v_accvgpr_read_b32 v165, a53
v_accvgpr_read_b32 v166, a54
v_accvgpr_read_b32 v167, a55
v_mul_f32 v160, v137, v160
v_mul_f32 v161, v137, v161
v_mul_f32 v162, v137, v162
v_mul_f32 v163, v137, v163
v_mul_f32 v164, v137, v164
v_mul_f32 v165, v137, v165
v_mul_f32 v166, v137, v166
v_mul_f32 v167, v137, v167
v_cvt_pk_bf16_f32 v168, v160, v161
v_cvt_pk_bf16_f32 v169, v162, v163
v_cvt_pk_bf16_f32 v170, v164, v165
v_cvt_pk_bf16_f32 v171, v166, v167
s_add_u32 s35, s34, 0xc00
buffer_store_dwordx4 v[168:171], v0, s[76:79], s35 offen sc1
v_accvgpr_read_b32 v148, a40
v_accvgpr_read_b32 v149, a41
v_accvgpr_read_b32 v150, a42
v_accvgpr_read_b32 v151, a43
v_accvgpr_read_b32 v152, a56
v_accvgpr_read_b32 v153, a57
v_accvgpr_read_b32 v154, a58
v_accvgpr_read_b32 v155, a59
v_mul_f32 v148, v138, v148
v_mul_f32 v149, v138, v149
v_mul_f32 v150, v138, v150
v_mul_f32 v151, v138, v151
v_mul_f32 v152, v138, v152
v_mul_f32 v153, v138, v153
v_mul_f32 v154, v138, v154
v_mul_f32 v155, v138, v155
v_cvt_pk_bf16_f32 v156, v148, v149
v_cvt_pk_bf16_f32 v157, v150, v151
v_cvt_pk_bf16_f32 v158, v152, v153
v_cvt_pk_bf16_f32 v159, v154, v155
s_add_u32 s35, s34, 0x1400
buffer_store_dwordx4 v[156:159], v0, s[76:79], s35 offen sc1
v_accvgpr_read_b32 v160, a44
v_accvgpr_read_b32 v161, a45
v_accvgpr_read_b32 v162, a46
v_accvgpr_read_b32 v163, a47
v_accvgpr_read_b32 v164, a60
v_accvgpr_read_b32 v165, a61
v_accvgpr_read_b32 v166, a62
v_accvgpr_read_b32 v167, a63
v_mul_f32 v160, v139, v160
v_mul_f32 v161, v139, v161
v_mul_f32 v162, v139, v162
v_mul_f32 v163, v139, v163
v_mul_f32 v164, v139, v164
v_mul_f32 v165, v139, v165
v_mul_f32 v166, v139, v166
v_mul_f32 v167, v139, v167
v_cvt_pk_bf16_f32 v168, v160, v161
v_cvt_pk_bf16_f32 v169, v162, v163
v_cvt_pk_bf16_f32 v170, v164, v165
v_cvt_pk_bf16_f32 v171, v166, v167
s_add_u32 s35, s34, 0x1c00
buffer_store_dwordx4 v[168:171], v0, s[76:79], s35 offen sc1
v_accvgpr_read_b32 v148, a64
v_accvgpr_read_b32 v149, a65
v_accvgpr_read_b32 v150, a66
v_accvgpr_read_b32 v151, a67
v_accvgpr_read_b32 v152, a80
v_accvgpr_read_b32 v153, a81
v_accvgpr_read_b32 v154, a82
v_accvgpr_read_b32 v155, a83
v_mul_f32 v148, v136, v148
v_mul_f32 v149, v136, v149
v_mul_f32 v150, v136, v150
v_mul_f32 v151, v136, v151
v_mul_f32 v152, v136, v152
v_mul_f32 v153, v136, v153
v_mul_f32 v154, v136, v154
v_mul_f32 v155, v136, v155
v_cvt_pk_bf16_f32 v156, v148, v149
v_cvt_pk_bf16_f32 v157, v150, v151
v_cvt_pk_bf16_f32 v158, v152, v153
v_cvt_pk_bf16_f32 v159, v154, v155
s_add_u32 s35, s34, 0x4000
buffer_store_dwordx4 v[156:159], v0, s[76:79], s35 offen sc1
v_accvgpr_read_b32 v160, a68
v_accvgpr_read_b32 v161, a69
v_accvgpr_read_b32 v162, a70
v_accvgpr_read_b32 v163, a71
v_accvgpr_read_b32 v164, a84
v_accvgpr_read_b32 v165, a85
v_accvgpr_read_b32 v166, a86
v_accvgpr_read_b32 v167, a87
v_mul_f32 v160, v137, v160
v_mul_f32 v161, v137, v161
v_mul_f32 v162, v137, v162
v_mul_f32 v163, v137, v163
v_mul_f32 v164, v137, v164
v_mul_f32 v165, v137, v165
v_mul_f32 v166, v137, v166
v_mul_f32 v167, v137, v167
v_cvt_pk_bf16_f32 v168, v160, v161
v_cvt_pk_bf16_f32 v169, v162, v163
v_cvt_pk_bf16_f32 v170, v164, v165
v_cvt_pk_bf16_f32 v171, v166, v167
s_add_u32 s35, s34, 0x4800
buffer_store_dwordx4 v[168:171], v0, s[76:79], s35 offen sc1
v_accvgpr_read_b32 v148, a72
v_accvgpr_read_b32 v149, a73
v_accvgpr_read_b32 v150, a74
v_accvgpr_read_b32 v151, a75
v_accvgpr_read_b32 v152, a88
v_accvgpr_read_b32 v153, a89
v_accvgpr_read_b32 v154, a90
v_accvgpr_read_b32 v155, a91
v_mul_f32 v148, v138, v148
v_mul_f32 v149, v138, v149
v_mul_f32 v150, v138, v150
v_mul_f32 v151, v138, v151
v_mul_f32 v152, v138, v152
v_mul_f32 v153, v138, v153
v_mul_f32 v154, v138, v154
v_mul_f32 v155, v138, v155
v_cvt_pk_bf16_f32 v156, v148, v149
v_cvt_pk_bf16_f32 v157, v150, v151
v_cvt_pk_bf16_f32 v158, v152, v153
v_cvt_pk_bf16_f32 v159, v154, v155
s_add_u32 s35, s34, 0x5000
buffer_store_dwordx4 v[156:159], v0, s[76:79], s35 offen sc1
v_accvgpr_read_b32 v160, a76
v_accvgpr_read_b32 v161, a77
v_accvgpr_read_b32 v162, a78
v_accvgpr_read_b32 v163, a79
v_accvgpr_read_b32 v164, a92
v_accvgpr_read_b32 v165, a93
v_accvgpr_read_b32 v166, a94
v_accvgpr_read_b32 v167, a95
v_mul_f32 v160, v139, v160
v_mul_f32 v161, v139, v161
v_mul_f32 v162, v139, v162
v_mul_f32 v163, v139, v163
v_mul_f32 v164, v139, v164
v_mul_f32 v165, v139, v165
v_mul_f32 v166, v139, v166
v_mul_f32 v167, v139, v167
v_cvt_pk_bf16_f32 v168, v160, v161
v_cvt_pk_bf16_f32 v169, v162, v163
v_cvt_pk_bf16_f32 v170, v164, v165
v_cvt_pk_bf16_f32 v171, v166, v167
s_add_u32 s35, s34, 0x5800
buffer_store_dwordx4 v[168:171], v0, s[76:79], s35 offen sc1
v_accvgpr_read_b32 v148, a96
v_accvgpr_read_b32 v149, a97
v_accvgpr_read_b32 v150, a98
v_accvgpr_read_b32 v151, a99
v_accvgpr_read_b32 v152, a112
v_accvgpr_read_b32 v153, a113
v_accvgpr_read_b32 v154, a114
v_accvgpr_read_b32 v155, a115
v_mul_f32 v148, v136, v148
v_mul_f32 v149, v136, v149
v_mul_f32 v150, v136, v150
v_mul_f32 v151, v136, v151
v_mul_f32 v152, v136, v152
v_mul_f32 v153, v136, v153
v_mul_f32 v154, v136, v154
v_mul_f32 v155, v136, v155
v_cvt_pk_bf16_f32 v156, v148, v149
v_cvt_pk_bf16_f32 v157, v150, v151
v_cvt_pk_bf16_f32 v158, v152, v153
v_cvt_pk_bf16_f32 v159, v154, v155
s_add_u32 s35, s34, 0x4400
buffer_store_dwordx4 v[156:159], v0, s[76:79], s35 offen sc1
v_accvgpr_read_b32 v160, a100
v_accvgpr_read_b32 v161, a101
v_accvgpr_read_b32 v162, a102
v_accvgpr_read_b32 v163, a103
v_accvgpr_read_b32 v164, a116
v_accvgpr_read_b32 v165, a117
v_accvgpr_read_b32 v166, a118
v_accvgpr_read_b32 v167, a119
v_mul_f32 v160, v137, v160
v_mul_f32 v161, v137, v161
v_mul_f32 v162, v137, v162
v_mul_f32 v163, v137, v163
v_mul_f32 v164, v137, v164
v_mul_f32 v165, v137, v165
v_mul_f32 v166, v137, v166
v_mul_f32 v167, v137, v167
v_cvt_pk_bf16_f32 v168, v160, v161
v_cvt_pk_bf16_f32 v169, v162, v163
v_cvt_pk_bf16_f32 v170, v164, v165
v_cvt_pk_bf16_f32 v171, v166, v167
s_add_u32 s35, s34, 0x4c00
buffer_store_dwordx4 v[168:171], v0, s[76:79], s35 offen sc1
v_accvgpr_read_b32 v148, a104
v_accvgpr_read_b32 v149, a105
v_accvgpr_read_b32 v150, a106
v_accvgpr_read_b32 v151, a107
v_accvgpr_read_b32 v152, a120
v_accvgpr_read_b32 v153, a121
v_accvgpr_read_b32 v154, a122
v_accvgpr_read_b32 v155, a123
v_mul_f32 v148, v138, v148
v_mul_f32 v149, v138, v149
v_mul_f32 v150, v138, v150
v_mul_f32 v151, v138, v151
v_mul_f32 v152, v138, v152
v_mul_f32 v153, v138, v153
v_mul_f32 v154, v138, v154
v_mul_f32 v155, v138, v155
v_cvt_pk_bf16_f32 v156, v148, v149
v_cvt_pk_bf16_f32 v157, v150, v151
v_cvt_pk_bf16_f32 v158, v152, v153
v_cvt_pk_bf16_f32 v159, v154, v155
s_add_u32 s35, s34, 0x5400
buffer_store_dwordx4 v[156:159], v0, s[76:79], s35 offen sc1
v_accvgpr_read_b32 v160, a108
v_accvgpr_read_b32 v161, a109
v_accvgpr_read_b32 v162, a110
v_accvgpr_read_b32 v163, a111
v_accvgpr_read_b32 v164, a124
v_accvgpr_read_b32 v165, a125
v_accvgpr_read_b32 v166, a126
v_accvgpr_read_b32 v167, a127
v_mul_f32 v160, v139, v160
v_mul_f32 v161, v139, v161
v_mul_f32 v162, v139, v162
v_mul_f32 v163, v139, v163
v_mul_f32 v164, v139, v164
v_mul_f32 v165, v139, v165
v_mul_f32 v166, v139, v166
v_mul_f32 v167, v139, v167
v_cvt_pk_bf16_f32 v168, v160, v161
v_cvt_pk_bf16_f32 v169, v162, v163
v_cvt_pk_bf16_f32 v170, v164, v165
v_cvt_pk_bf16_f32 v171, v166, v167
s_add_u32 s35, s34, 0x5c00
buffer_store_dwordx4 v[168:171], v0, s[76:79], s35 offen sc1
s_mov_b32 s97, s34
ds_read_b64_tr_b16 v[120:121], v188 offset:33792
ds_read_b64_tr_b16 v[122:123], v188 offset:34304
ds_read_b64_tr_b16 v[124:125], v188 offset:42240
ds_read_b64_tr_b16 v[126:127], v188 offset:42752
ds_read_b64_tr_b16 v[128:129], v188 offset:33824
ds_read_b64_tr_b16 v[130:131], v188 offset:34336
ds_read_b64_tr_b16 v[132:133], v188 offset:42272
ds_read_b64_tr_b16 v[134:135], v188 offset:42784
ds_read_b64_tr_b16 v[136:137], v188 offset:33856
ds_read_b64_tr_b16 v[138:139], v188 offset:34368
ds_read_b64_tr_b16 v[140:141], v188 offset:42304
ds_read_b64_tr_b16 v[142:143], v188 offset:42816
ds_read_b64_tr_b16 v[144:145], v188 offset:33888
ds_read_b64_tr_b16 v[146:147], v188 offset:34400
ds_read_b64_tr_b16 v[148:149], v188 offset:42336
ds_read_b64_tr_b16 v[150:151], v188 offset:42848
ds_read_b64_tr_b16 v[152:153], v188 offset:33920
ds_read_b64_tr_b16 v[154:155], v188 offset:34432
ds_read_b64_tr_b16 v[156:157], v188 offset:42368
ds_read_b64_tr_b16 v[158:159], v188 offset:42880
ds_read_b64_tr_b16 v[160:161], v188 offset:33952
ds_read_b64_tr_b16 v[162:163], v188 offset:34464
ds_read_b64_tr_b16 v[164:165], v188 offset:42400
ds_read_b64_tr_b16 v[166:167], v188 offset:42912
ds_read_b64_tr_b16 v[168:169], v188 offset:33984
ds_read_b64_tr_b16 v[170:171], v188 offset:34496
ds_read_b64_tr_b16 v[172:173], v188 offset:42432
ds_read_b64_tr_b16 v[174:175], v188 offset:42944
s_waitcnt vmcnt(40)
s_barrier
s_waitcnt lgkmcnt(14)
v_mfma_f32_16x16x32_bf16 a[128:131], v[120:123], v[192:195], 0
ds_read_b64_tr_b16 v[176:177], v188 offset:34016
s_add_u32 m0, s30, 0xb1c0
v_mfma_f32_16x16x32_bf16 a[128:131], v[124:127], v[196:199], a[128:131]
buffer_load_dwordx4 v20, s[16:19], s27 offen lds
v_mfma_f32_16x16x32_bf16 a[132:135], v[120:123], v[200:203], 0
ds_read_b64_tr_b16 v[178:179], v188 offset:34528
v_mfma_f32_16x16x32_bf16 a[132:135], v[124:127], v[204:207], a[132:135]
v_mfma_f32_16x16x32_bf16 a[136:139], v[120:123], v[208:211], 0
ds_read_b64_tr_b16 v[180:181], v188 offset:42464
v_mfma_f32_16x16x32_bf16 a[136:139], v[124:127], v[212:215], a[136:139]
buffer_load_dwordx4 v21, s[16:19], s27 offen offset:1056 lds
v_mfma_f32_16x16x32_bf16 a[140:143], v[120:123], v[216:219], 0
ds_read_b64_tr_b16 v[182:183], v188 offset:42976
v_mfma_f32_16x16x32_bf16 a[140:143], v[124:127], v[220:223], a[140:143]
v_mfma_f32_16x16x32_bf16 a[144:147], v[128:131], v[192:195], 0
ds_read_b64_tr_b16 v[120:121], v189
v_mfma_f32_16x16x32_bf16 a[144:147], v[132:135], v[196:199], a[144:147]
v_mfma_f32_16x16x32_bf16 a[148:151], v[128:131], v[200:203], 0
ds_read_b64_tr_b16 v[122:123], v189 offset:512
v_mfma_f32_16x16x32_bf16 a[148:151], v[132:135], v[204:207], a[148:151]
buffer_load_dwordx4 v22, s[16:19], s27 offen offset:2112 lds
v_mfma_f32_16x16x32_bf16 a[152:155], v[128:131], v[208:211], 0
)ASM"
R"ASM(ds_read_b64_tr_b16 v[124:125], v189 offset:8448
v_mfma_f32_16x16x32_bf16 a[152:155], v[132:135], v[212:215], a[152:155]
v_mfma_f32_16x16x32_bf16 a[156:159], v[128:131], v[216:219], 0
ds_read_b64_tr_b16 v[126:127], v189 offset:8960
v_mfma_f32_16x16x32_bf16 a[156:159], v[132:135], v[220:223], a[156:159]
buffer_load_dwordx4 v23, s[16:19], s27 offen offset:3168 lds
s_add_u32 s27, s27, 0x20000
v_mfma_f32_16x16x32_bf16 a[160:163], v[136:139], v[192:195], 0
ds_read_b64_tr_b16 v[128:129], v189 offset:32
v_mfma_f32_16x16x32_bf16 a[160:163], v[140:143], v[196:199], a[160:163]
v_mfma_f32_16x16x32_bf16 a[164:167], v[136:139], v[200:203], 0
ds_read_b64_tr_b16 v[130:131], v189 offset:544
v_mfma_f32_16x16x32_bf16 a[164:167], v[140:143], v[204:207], a[164:167]
v_mfma_f32_16x16x32_bf16 a[168:171], v[136:139], v[208:211], 0
ds_read_b64_tr_b16 v[132:133], v189 offset:8480
v_mfma_f32_16x16x32_bf16 a[168:171], v[140:143], v[212:215], a[168:171]
buffer_load_dwordx4 v[88:91], v1, s[20:23], s26 offen sc1
v_mfma_f32_16x16x32_bf16 a[172:175], v[136:139], v[216:219], 0
ds_read_b64_tr_b16 v[134:135], v189 offset:8992
v_mfma_f32_16x16x32_bf16 a[172:175], v[140:143], v[220:223], a[172:175]
v_mfma_f32_16x16x32_bf16 a[176:179], v[144:147], v[192:195], 0
ds_read_b64_tr_b16 v[136:137], v189 offset:64
s_waitcnt lgkmcnt(3)
v_mfma_f32_16x16x32_bf16 a[176:179], v[148:151], v[196:199], a[176:179]
buffer_load_dwordx4 v[92:95], v1, s[20:23], s26 offen offset:1024 sc1
v_mfma_f32_16x16x32_bf16 a[180:183], v[144:147], v[200:203], 0
ds_read_b64_tr_b16 v[138:139], v189 offset:576
v_mfma_f32_16x16x32_bf16 a[180:183], v[148:151], v[204:207], a[180:183]
v_mfma_f32_16x16x32_bf16 a[184:187], v[144:147], v[208:211], 0
ds_read_b64_tr_b16 v[140:141], v189 offset:8512
v_mfma_f32_16x16x32_bf16 a[184:187], v[148:151], v[212:215], a[184:187]
v_mfma_f32_16x16x32_bf16 a[188:191], v[144:147], v[216:219], 0
ds_read_b64_tr_b16 v[142:143], v189 offset:9024
v_mfma_f32_16x16x32_bf16 a[188:191], v[148:151], v[220:223], a[188:191]
buffer_load_dwordx4 v[96:99], v2, s[20:23], s26 offen sc1
v_mfma_f32_16x16x32_bf16 a[192:195], v[152:155], v[192:195], 0
ds_read_b64_tr_b16 v[144:145], v189 offset:96
v_mfma_f32_16x16x32_bf16 a[192:195], v[156:159], v[196:199], a[192:195]
v_mfma_f32_16x16x32_bf16 a[196:199], v[152:155], v[200:203], 0
ds_read_b64_tr_b16 v[146:147], v189 offset:608
v_mfma_f32_16x16x32_bf16 a[196:199], v[156:159], v[204:207], a[196:199]
buffer_load_dwordx4 v[100:103], v2, s[20:23], s26 offen offset:1024 sc1
v_mfma_f32_16x16x32_bf16 a[200:203], v[152:155], v[208:211], 0
ds_read_b64_tr_b16 v[148:149], v189 offset:8544
v_mfma_f32_16x16x32_bf16 a[200:203], v[156:159], v[212:215], a[200:203]
v_mfma_f32_16x16x32_bf16 a[204:207], v[152:155], v[216:219], 0
ds_read_b64_tr_b16 v[150:151], v189 offset:9056
v_mfma_f32_16x16x32_bf16 a[204:207], v[156:159], v[220:223], a[204:207]
v_mfma_f32_16x16x32_bf16 a[208:211], v[160:163], v[192:195], 0
ds_read_b64_tr_b16 v[152:153], v189 offset:128
v_mfma_f32_16x16x32_bf16 a[208:211], v[164:167], v[196:199], a[208:211]
buffer_load_dwordx4 v[104:107], v3, s[20:23], s26 offen sc1
v_mfma_f32_16x16x32_bf16 a[212:215], v[160:163], v[200:203], 0
ds_read_b64_tr_b16 v[154:155], v189 offset:640
v_mfma_f32_16x16x32_bf16 a[212:215], v[164:167], v[204:207], a[212:215]
v_mfma_f32_16x16x32_bf16 a[216:219], v[160:163], v[208:211], 0
ds_read_b64_tr_b16 v[156:157], v189 offset:8576
v_mfma_f32_16x16x32_bf16 a[216:219], v[164:167], v[212:215], a[216:219]
buffer_load_dwordx4 v[108:111], v3, s[20:23], s26 offen offset:1024 sc1
v_mfma_f32_16x16x32_bf16 a[220:223], v[160:163], v[216:219], 0
ds_read_b64_tr_b16 v[158:159], v189 offset:9088
v_mfma_f32_16x16x32_bf16 a[220:223], v[164:167], v[220:223], a[220:223]
v_mfma_f32_16x16x32_bf16 a[224:227], v[168:171], v[192:195], 0
ds_read_b64_tr_b16 v[160:161], v189 offset:160
v_mfma_f32_16x16x32_bf16 a[224:227], v[172:175], v[196:199], a[224:227]
v_mfma_f32_16x16x32_bf16 a[228:231], v[168:171], v[200:203], 0
ds_read_b64_tr_b16 v[162:163], v189 offset:672
v_mfma_f32_16x16x32_bf16 a[228:231], v[172:175], v[204:207], a[228:231]
buffer_load_dwordx4 v[112:115], v4, s[20:23], s26 offen sc1
v_mfma_f32_16x16x32_bf16 a[232:235], v[168:171], v[208:211], 0
ds_read_b64_tr_b16 v[164:165], v189 offset:8608
v_mfma_f32_16x16x32_bf16 a[232:235], v[172:175], v[212:215], a[232:235]
v_mfma_f32_16x16x32_bf16 a[236:239], v[168:171], v[216:219], 0
ds_read_b64_tr_b16 v[166:167], v189 offset:9120
v_mfma_f32_16x16x32_bf16 a[236:239], v[172:175], v[220:223], a[236:239]
buffer_load_dwordx4 v[116:119], v4, s[20:23], s26 offen offset:1024 sc1
s_add_u32 s26, s26, 0x4000
v_mfma_f32_16x16x32_bf16 a[240:243], v[176:179], v[192:195], 0
ds_read_b64_tr_b16 v[168:169], v189 offset:192
v_mfma_f32_16x16x32_bf16 a[240:243], v[180:183], v[196:199], a[240:243]
v_mfma_f32_16x16x32_bf16 a[244:247], v[176:179], v[200:203], 0
ds_read_b64_tr_b16 v[170:171], v189 offset:704
v_mfma_f32_16x16x32_bf16 a[244:247], v[180:183], v[204:207], a[244:247]
v_mfma_f32_16x16x32_bf16 a[248:251], v[176:179], v[208:211], 0
ds_read_b64_tr_b16 v[172:173], v189 offset:8640
v_mfma_f32_16x16x32_bf16 a[248:251], v[180:183], v[212:215], a[248:251]
v_mfma_f32_16x16x32_bf16 a[252:255], v[176:179], v[216:219], 0
ds_read_b64_tr_b16 v[174:175], v189 offset:9152
v_mfma_f32_16x16x32_bf16 a[252:255], v[180:183], v[220:223], a[252:255]
s_waitcnt vmcnt(44)
v_mfma_f32_16x16x32_bf16 a[128:131], v[120:123], v[224:227], a[128:131]
ds_read_b64_tr_b16 v[176:177], v189 offset:224
s_add_u32 m0, s30, 0xf3c0
v_mfma_f32_16x16x32_bf16 a[128:131], v[124:127], v[228:231], a[128:131]
buffer_load_dwordx4 v20, s[16:19], s27 offen lds
v_mfma_f32_16x16x32_bf16 a[132:135], v[120:123], v[232:235], a[132:135]
ds_read_b64_tr_b16 v[178:179], v189 offset:736
v_mfma_f32_16x16x32_bf16 a[132:135], v[124:127], v[236:239], a[132:135]
v_mfma_f32_16x16x32_bf16 a[136:139], v[120:123], v[240:243], a[136:139]
ds_read_b64_tr_b16 v[180:181], v189 offset:8672
v_mfma_f32_16x16x32_bf16 a[136:139], v[124:127], v[244:247], a[136:139]
buffer_load_dwordx4 v21, s[16:19], s27 offen offset:1056 lds
v_mfma_f32_16x16x32_bf16 a[140:143], v[120:123], v[248:251], a[140:143]
ds_read_b64_tr_b16 v[182:183], v189 offset:9184
v_mfma_f32_16x16x32_bf16 a[140:143], v[124:127], v[252:255], a[140:143]
v_mfma_f32_16x16x32_bf16 a[144:147], v[128:131], v[224:227], a[144:147]
ds_read_b64_tr_b16 v[120:121], v189 offset:16896
s_waitcnt lgkmcnt(3)
v_mfma_f32_16x16x32_bf16 a[144:147], v[132:135], v[228:231], a[144:147]
v_mfma_f32_16x16x32_bf16 a[148:151], v[128:131], v[232:235], a[148:151]
ds_read_b64_tr_b16 v[122:123], v189 offset:17408
v_mfma_f32_16x16x32_bf16 a[148:151], v[132:135], v[236:239], a[148:151]
buffer_load_dwordx4 v22, s[16:19], s27 offen offset:2112 lds
v_mfma_f32_16x16x32_bf16 a[152:155], v[128:131], v[240:243], a[152:155]
ds_read_b64_tr_b16 v[124:125], v189 offset:25344
v_mfma_f32_16x16x32_bf16 a[152:155], v[132:135], v[244:247], a[152:155]
v_mfma_f32_16x16x32_bf16 a[156:159], v[128:131], v[248:251], a[156:159]
ds_read_b64_tr_b16 v[126:127], v189 offset:25856
v_mfma_f32_16x16x32_bf16 a[156:159], v[132:135], v[252:255], a[156:159]
buffer_load_dwordx4 v23, s[16:19], s27 offen offset:3168 lds
s_add_u32 s27, s27, 0x20000
v_mfma_f32_16x16x32_bf16 a[160:163], v[136:139], v[224:227], a[160:163]
ds_read_b64_tr_b16 v[128:129], v189 offset:16928
v_mfma_f32_16x16x32_bf16 a[160:163], v[140:143], v[228:231], a[160:163]
v_mfma_f32_16x16x32_bf16 a[164:167], v[136:139], v[232:235], a[164:167]
ds_read_b64_tr_b16 v[130:131], v189 offset:17440
v_mfma_f32_16x16x32_bf16 a[164:167], v[140:143], v[236:239], a[164:167]
v_mfma_f32_16x16x32_bf16 a[168:171], v[136:139], v[240:243], a[168:171]
ds_read_b64_tr_b16 v[132:133], v189 offset:25376
v_mfma_f32_16x16x32_bf16 a[168:171], v[140:143], v[244:247], a[168:171]
buffer_load_dwordx4 v[192:195], v1, s[20:23], s26 offen sc1
v_mfma_f32_16x16x32_bf16 a[172:175], v[136:139], v[248:251], a[172:175]
ds_read_b64_tr_b16 v[134:135], v189 offset:25888
v_mfma_f32_16x16x32_bf16 a[172:175], v[140:143], v[252:255], a[172:175]
v_mfma_f32_16x16x32_bf16 a[176:179], v[144:147], v[224:227], a[176:179]
ds_read_b64_tr_b16 v[136:137], v189 offset:16960
v_mfma_f32_16x16x32_bf16 a[176:179], v[148:151], v[228:231], a[176:179]
buffer_load_dwordx4 v[196:199], v1, s[20:23], s26 offen offset:1024 sc1
v_mfma_f32_16x16x32_bf16 a[180:183], v[144:147], v[232:235], a[180:183]
ds_read_b64_tr_b16 v[138:139], v189 offset:17472
v_mfma_f32_16x16x32_bf16 a[180:183], v[148:151], v[236:239], a[180:183]
v_mfma_f32_16x16x32_bf16 a[184:187], v[144:147], v[240:243], a[184:187]
ds_read_b64_tr_b16 v[140:141], v189 offset:25408
v_mfma_f32_16x16x32_bf16 a[184:187], v[148:151], v[244:247], a[184:187]
v_mfma_f32_16x16x32_bf16 a[188:191], v[144:147], v[248:251], a[188:191]
ds_read_b64_tr_b16 v[142:143], v189 offset:25920
v_mfma_f32_16x16x32_bf16 a[188:191], v[148:151], v[252:255], a[188:191]
buffer_load_dwordx4 v[200:203], v2, s[20:23], s26 offen sc1
v_mfma_f32_16x16x32_bf16 a[192:195], v[152:155], v[224:227], a[192:195]
ds_read_b64_tr_b16 v[144:145], v189 offset:16992
v_mfma_f32_16x16x32_bf16 a[192:195], v[156:159], v[228:231], a[192:195]
v_mfma_f32_16x16x32_bf16 a[196:199], v[152:155], v[232:235], a[196:199]
ds_read_b64_tr_b16 v[146:147], v189 offset:17504
v_mfma_f32_16x16x32_bf16 a[196:199], v[156:159], v[236:239], a[196:199]
buffer_load_dwordx4 v[204:207], v2, s[20:23], s26 offen offset:1024 sc1
v_mfma_f32_16x16x32_bf16 a[200:203], v[152:155], v[240:243], a[200:203]
ds_read_b64_tr_b16 v[148:149], v189 offset:25440
v_mfma_f32_16x16x32_bf16 a[200:203], v[156:159], v[244:247], a[200:203]
v_mfma_f32_16x16x32_bf16 a[204:207], v[152:155], v[248:251], a[204:207]
ds_read_b64_tr_b16 v[150:151], v189 offset:25952
v_mfma_f32_16x16x32_bf16 a[204:207], v[156:159], v[252:255], a[204:207]
v_mfma_f32_16x16x32_bf16 a[208:211], v[160:163], v[224:227], a[208:211]
ds_read_b64_tr_b16 v[152:153], v189 offset:17024
v_mfma_f32_16x16x32_bf16 a[208:211], v[164:167], v[228:231], a[208:211]
buffer_load_dwordx4 v[208:211], v3, s[20:23], s26 offen sc1
v_mfma_f32_16x16x32_bf16 a[212:215], v[160:163], v[232:235], a[212:215]
ds_read_b64_tr_b16 v[154:155], v189 offset:17536
v_mfma_f32_16x16x32_bf16 a[212:215], v[164:167], v[236:239], a[212:215]
v_mfma_f32_16x16x32_bf16 a[216:219], v[160:163], v[240:243], a[216:219]
ds_read_b64_tr_b16 v[156:157], v189 offset:25472
v_mfma_f32_16x16x32_bf16 a[216:219], v[164:167], v[244:247], a[216:219]
buffer_load_dwordx4 v[212:215], v3, s[20:23], s26 offen offset:1024 sc1
v_mfma_f32_16x16x32_bf16 a[220:223], v[160:163], v[248:251], a[220:223]
ds_read_b64_tr_b16 v[158:159], v189 offset:25984
v_mfma_f32_16x16x32_bf16 a[220:223], v[164:167], v[252:255], a[220:223]
v_mfma_f32_16x16x32_bf16 a[224:227], v[168:171], v[224:227], a[224:227]
ds_read_b64_tr_b16 v[160:161], v189 offset:17056
v_mfma_f32_16x16x32_bf16 a[224:227], v[172:175], v[228:231], a[224:227]
v_mfma_f32_16x16x32_bf16 a[228:231], v[168:171], v[232:235], a[228:231]
ds_read_b64_tr_b16 v[162:163], v189 offset:17568
v_mfma_f32_16x16x32_bf16 a[228:231], v[172:175], v[236:239], a[228:231]
buffer_load_dwordx4 v[216:219], v4, s[20:23], s26 offen sc1
v_mfma_f32_16x16x32_bf16 a[232:235], v[168:171], v[240:243], a[232:235]
ds_read_b64_tr_b16 v[164:165], v189 offset:25504
v_mfma_f32_16x16x32_bf16 a[232:235], v[172:175], v[244:247], a[232:235]
v_mfma_f32_16x16x32_bf16 a[236:239], v[168:171], v[248:251], a[236:239]
ds_read_b64_tr_b16 v[166:167], v189 offset:26016
v_mfma_f32_16x16x32_bf16 a[236:239], v[172:175], v[252:255], a[236:239]
buffer_load_dwordx4 v[220:223], v4, s[20:23], s26 offen offset:1024 sc1
s_add_u32 s26, s26, 0x4000
v_mfma_f32_16x16x32_bf16 a[240:243], v[176:179], v[224:227], a[240:243]
ds_read_b64_tr_b16 v[168:169], v189 offset:17088
s_waitcnt lgkmcnt(3)
v_mfma_f32_16x16x32_bf16 a[240:243], v[180:183], v[228:231], a[240:243]
v_mfma_f32_16x16x32_bf16 a[244:247], v[176:179], v[232:235], a[244:247]
ds_read_b64_tr_b16 v[170:171], v189 offset:17600
v_mfma_f32_16x16x32_bf16 a[244:247], v[180:183], v[236:239], a[244:247]
v_mfma_f32_16x16x32_bf16 a[248:251], v[176:179], v[240:243], a[248:251]
ds_read_b64_tr_b16 v[172:173], v189 offset:25536
v_mfma_f32_16x16x32_bf16 a[248:251], v[180:183], v[244:247], a[248:251]
v_mfma_f32_16x16x32_bf16 a[252:255], v[176:179], v[248:251], a[252:255]
ds_read_b64_tr_b16 v[174:175], v189 offset:26048
v_mfma_f32_16x16x32_bf16 a[252:255], v[180:183], v[252:255], a[252:255]
s_waitcnt vmcnt(48)
s_barrier
v_mfma_f32_16x16x32_bf16 a[128:131], v[120:123], v[24:27], a[128:131]
ds_read_b64_tr_b16 v[176:177], v189 offset:17120
s_add_u32 m0, s30, 0x135c0
v_mfma_f32_16x16x32_bf16 a[128:131], v[124:127], v[28:31], a[128:131]
buffer_load_dwordx4 v20, s[16:19], s27 offen lds
v_mfma_f32_16x16x32_bf16 a[132:135], v[120:123], v[32:35], a[132:135]
ds_read_b64_tr_b16 v[178:179], v189 offset:17632
v_mfma_f32_16x16x32_bf16 a[132:135], v[124:127], v[36:39], a[132:135]
v_mfma_f32_16x16x32_bf16 a[136:139], v[120:123], v[40:43], a[136:139]
ds_read_b64_tr_b16 v[180:181], v189 offset:25568
v_mfma_f32_16x16x32_bf16 a[136:139], v[124:127], v[44:47], a[136:139]
buffer_load_dwordx4 v21, s[16:19], s27 offen offset:1056 lds
v_mfma_f32_16x16x32_bf16 a[140:143], v[120:123], v[48:51], a[140:143]
ds_read_b64_tr_b16 v[182:183], v189 offset:26080
v_mfma_f32_16x16x32_bf16 a[140:143], v[124:127], v[52:55], a[140:143]
v_mfma_f32_16x16x32_bf16 a[144:147], v[128:131], v[24:27], a[144:147]
ds_read_b64_tr_b16 v[120:121], v189 offset:33792
v_mfma_f32_16x16x32_bf16 a[144:147], v[132:135], v[28:31], a[144:147]
v_mfma_f32_16x16x32_bf16 a[148:151], v[128:131], v[32:35], a[148:151]
ds_read_b64_tr_b16 v[122:123], v189 offset:34304
v_mfma_f32_16x16x32_bf16 a[148:151], v[132:135], v[36:39], a[148:151]
buffer_load_dwordx4 v22, s[16:19], s27 offen offset:2112 lds
v_mfma_f32_16x16x32_bf16 a[152:155], v[128:131], v[40:43], a[152:155]
ds_read_b64_tr_b16 v[124:125], v189 offset:42240
v_mfma_f32_16x16x32_bf16 a[152:155], v[132:135], v[44:47], a[152:155]
v_mfma_f32_16x16x32_bf16 a[156:159], v[128:131], v[48:51], a[156:159]
ds_read_b64_tr_b16 v[126:127], v189 offset:42752
v_mfma_f32_16x16x32_bf16 a[156:159], v[132:135], v[52:55], a[156:159]
buffer_load_dwordx4 v23, s[16:19], s27 offen offset:3168 lds
s_add_u32 s27, s27, 0x20000
v_mfma_f32_16x16x32_bf16 a[160:163], v[136:139], v[24:27], a[160:163]
ds_read_b64_tr_b16 v[128:129], v189 offset:33824
v_mfma_f32_16x16x32_bf16 a[160:163], v[140:143], v[28:31], a[160:163]
v_mfma_f32_16x16x32_bf16 a[164:167], v[136:139], v[32:35], a[164:167]
ds_read_b64_tr_b16 v[130:131], v189 offset:34336
v_mfma_f32_16x16x32_bf16 a[164:167], v[140:143], v[36:39], a[164:167]
v_mfma_f32_16x16x32_bf16 a[168:171], v[136:139], v[40:43], a[168:171]
ds_read_b64_tr_b16 v[132:133], v189 offset:42272
v_mfma_f32_16x16x32_bf16 a[168:171], v[140:143], v[44:47], a[168:171]
buffer_load_dwordx4 v[224:227], v1, s[20:23], s26 offen sc1
v_mfma_f32_16x16x32_bf16 a[172:175], v[136:139], v[48:51], a[172:175]
ds_read_b64_tr_b16 v[134:135], v189 offset:42784
v_mfma_f32_16x16x32_bf16 a[172:175], v[140:143], v[52:55], a[172:175]
v_mfma_f32_16x16x32_bf16 a[176:179], v[144:147], v[24:27], a[176:179]
ds_read_b64_tr_b16 v[136:137], v189 offset:33856
v_mfma_f32_16x16x32_bf16 a[176:179], v[148:151], v[28:31], a[176:179]
buffer_load_dwordx4 v[228:231], v1, s[20:23], s26 offen offset:1024 sc1
v_mfma_f32_16x16x32_bf16 a[180:183], v[144:147], v[32:35], a[180:183]
ds_read_b64_tr_b16 v[138:139], v189 offset:34368
v_mfma_f32_16x16x32_bf16 a[180:183], v[148:151], v[36:39], a[180:183]
v_mfma_f32_16x16x32_bf16 a[184:187], v[144:147], v[40:43], a[184:187]
ds_read_b64_tr_b16 v[140:141], v189 offset:42304
v_mfma_f32_16x16x32_bf16 a[184:187], v[148:151], v[44:47], a[184:187]
v_mfma_f32_16x16x32_bf16 a[188:191], v[144:147], v[48:51], a[188:191]
ds_read_b64_tr_b16 v[142:143], v189 offset:42816
v_mfma_f32_16x16x32_bf16 a[188:191], v[148:151], v[52:55], a[188:191]
buffer_load_dwordx4 v[232:235], v2, s[20:23], s26 offen sc1
v_mfma_f32_16x16x32_bf16 a[192:195], v[152:155], v[24:27], a[192:195]
ds_read_b64_tr_b16 v[144:145], v189 offset:33888
v_mfma_f32_16x16x32_bf16 a[192:195], v[156:159], v[28:31], a[192:195]
v_mfma_f32_16x16x32_bf16 a[196:199], v[152:155], v[32:35], a[196:199]
ds_read_b64_tr_b16 v[146:147], v189 offset:34400
v_mfma_f32_16x16x32_bf16 a[196:199], v[156:159], v[36:39], a[196:199]
buffer_load_dwordx4 v[236:239], v2, s[20:23], s26 offen offset:1024 sc1
v_mfma_f32_16x16x32_bf16 a[200:203], v[152:155], v[40:43], a[200:203]
ds_read_b64_tr_b16 v[148:149], v189 offset:42336
v_mfma_f32_16x16x32_bf16 a[200:203], v[156:159], v[44:47], a[200:203]
v_mfma_f32_16x16x32_bf16 a[204:207], v[152:155], v[48:51], a[204:207]
ds_read_b64_tr_b16 v[150:151], v189 offset:42848
v_mfma_f32_16x16x32_bf16 a[204:207], v[156:159], v[52:55], a[204:207]
v_mfma_f32_16x16x32_bf16 a[208:211], v[160:163], v[24:27], a[208:211]
ds_read_b64_tr_b16 v[152:153], v189 offset:33920
s_waitcnt lgkmcnt(3)
v_mfma_f32_16x16x32_bf16 a[208:211], v[164:167], v[28:31], a[208:211]
buffer_load_dwordx4 v[240:243], v3, s[20:23], s26 offen sc1
v_mfma_f32_16x16x32_bf16 a[212:215], v[160:163], v[32:35], a[212:215]
ds_read_b64_tr_b16 v[154:155], v189 offset:34432
v_mfma_f32_16x16x32_bf16 a[212:215], v[164:167], v[36:39], a[212:215]
v_mfma_f32_16x16x32_bf16 a[216:219], v[160:163], v[40:43], a[216:219]
ds_read_b64_tr_b16 v[156:157], v189 offset:42368
v_mfma_f32_16x16x32_bf16 a[216:219], v[164:167], v[44:47], a[216:219]
buffer_load_dwordx4 v[244:247], v3, s[20:23], s26 offen offset:1024 sc1
v_mfma_f32_16x16x32_bf16 a[220:223], v[160:163], v[48:51], a[220:223]
ds_read_b64_tr_b16 v[158:159], v189 offset:42880
v_mfma_f32_16x16x32_bf16 a[220:223], v[164:167], v[52:55], a[220:223]
v_mfma_f32_16x16x32_bf16 a[224:227], v[168:171], v[24:27], a[224:227]
ds_read_b64_tr_b16 v[160:161], v189 offset:33952
v_mfma_f32_16x16x32_bf16 a[224:227], v[172:175], v[28:31], a[224:227]
v_mfma_f32_16x16x32_bf16 a[228:231], v[168:171], v[32:35], a[228:231]
ds_read_b64_tr_b16 v[162:163], v189 offset:34464
v_mfma_f32_16x16x32_bf16 a[228:231], v[172:175], v[36:39], a[228:231]
buffer_load_dwordx4 v[248:251], v4, s[20:23], s26 offen sc1
v_mfma_f32_16x16x32_bf16 a[232:235], v[168:171], v[40:43], a[232:235]
ds_read_b64_tr_b16 v[164:165], v189 offset:42400
v_mfma_f32_16x16x32_bf16 a[232:235], v[172:175], v[44:47], a[232:235]
v_mfma_f32_16x16x32_bf16 a[236:239], v[168:171], v[48:51], a[236:239]
ds_read_b64_tr_b16 v[166:167], v189 offset:42912
v_mfma_f32_16x16x32_bf16 a[236:239], v[172:175], v[52:55], a[236:239]
buffer_load_dwordx4 v[252:255], v4, s[20:23], s26 offen offset:1024 sc1
s_add_u32 s26, s26, 0x4000
v_mfma_f32_16x16x32_bf16 a[240:243], v[176:179], v[24:27], a[240:243]
ds_read_b64_tr_b16 v[168:169], v189 offset:33984
v_mfma_f32_16x16x32_bf16 a[240:243], v[180:183], v[28:31], a[240:243]
v_mfma_f32_16x16x32_bf16 a[244:247], v[176:179], v[32:35], a[244:247]
ds_read_b64_tr_b16 v[170:171], v189 offset:34496
v_mfma_f32_16x16x32_bf16 a[244:247], v[180:183], v[36:39], a[244:247]
v_mfma_f32_16x16x32_bf16 a[248:251], v[176:179], v[40:43], a[248:251]
ds_read_b64_tr_b16 v[172:173], v189 offset:42432
v_mfma_f32_16x16x32_bf16 a[248:251], v[180:183], v[44:47], a[248:251]
v_mfma_f32_16x16x32_bf16 a[252:255], v[176:179], v[48:51], a[252:255]
ds_read_b64_tr_b16 v[174:175], v189 offset:42944
v_mfma_f32_16x16x32_bf16 a[252:255], v[180:183], v[52:55], a[252:255]
s_waitcnt vmcnt(32)
s_barrier
v_mfma_f32_16x16x32_bf16 a[128:131], v[120:123], v[56:59], a[128:131]
ds_read_b64_tr_b16 v[176:177], v189 offset:34016
s_add_u32 m0, s30, 0x177c0
v_mfma_f32_16x16x32_bf16 a[128:131], v[124:127], v[60:63], a[128:131]
buffer_load_dwordx4 v20, s[16:19], s27 offen lds
v_mfma_f32_16x16x32_bf16 a[132:135], v[120:123], v[64:67], a[132:135]
ds_read_b64_tr_b16 v[178:179], v189 offset:34528
v_mfma_f32_16x16x32_bf16 a[132:135], v[124:127], v[68:71], a[132:135]
v_mfma_f32_16x16x32_bf16 a[136:139], v[120:123], v[72:75], a[136:139]
ds_read_b64_tr_b16 v[180:181], v189 offset:42464
v_mfma_f32_16x16x32_bf16 a[136:139], v[124:127], v[76:79], a[136:139]
buffer_load_dwordx4 v21, s[16:19], s27 offen offset:1056 lds
v_mfma_f32_16x16x32_bf16 a[140:143], v[120:123], v[80:83], a[140:143]
ds_read_b64_tr_b16 v[182:183], v189 offset:42976
v_mfma_f32_16x16x32_bf16 a[140:143], v[124:127], v[84:87], a[140:143]
v_mfma_f32_16x16x32_bf16 a[144:147], v[128:131], v[56:59], a[144:147]
ds_read_b64_tr_b16 v[120:121], v190
v_mfma_f32_16x16x32_bf16 a[144:147], v[132:135], v[60:63], a[144:147]
v_mfma_f32_16x16x32_bf16 a[148:151], v[128:131], v[64:67], a[148:151]
ds_read_b64_tr_b16 v[122:123], v190 offset:512
v_mfma_f32_16x16x32_bf16 a[148:151], v[132:135], v[68:71], a[148:151]
buffer_load_dwordx4 v22, s[16:19], s27 offen offset:2112 lds
v_mfma_f32_16x16x32_bf16 a[152:155], v[128:131], v[72:75], a[152:155]
ds_read_b64_tr_b16 v[124:125], v190 offset:8448
v_mfma_f32_16x16x32_bf16 a[152:155], v[132:135], v[76:79], a[152:155]
v_mfma_f32_16x16x32_bf16 a[156:159], v[128:131], v[80:83], a[156:159]
ds_read_b64_tr_b16 v[126:127], v190 offset:8960
v_mfma_f32_16x16x32_bf16 a[156:159], v[132:135], v[84:87], a[156:159]
buffer_load_dwordx4 v23, s[16:19], s27 offen offset:3168 lds
s_add_u32 s27, s27, 0x20000
v_mfma_f32_16x16x32_bf16 a[160:163], v[136:139], v[56:59], a[160:163]
ds_read_b64_tr_b16 v[128:129], v190 offset:32
v_mfma_f32_16x16x32_bf16 a[160:163], v[140:143], v[60:63], a[160:163]
v_mfma_f32_16x16x32_bf16 a[164:167], v[136:139], v[64:67], a[164:167]
ds_read_b64_tr_b16 v[130:131], v190 offset:544
v_mfma_f32_16x16x32_bf16 a[164:167], v[140:143], v[68:71], a[164:167]
v_mfma_f32_16x16x32_bf16 a[168:171], v[136:139], v[72:75], a[168:171]
ds_read_b64_tr_b16 v[132:133], v190 offset:8480
v_mfma_f32_16x16x32_bf16 a[168:171], v[140:143], v[76:79], a[168:171]
buffer_load_dwordx4 v[24:27], v1, s[20:23], s26 offen sc1
v_mfma_f32_16x16x32_bf16 a[172:175], v[136:139], v[80:83], a[172:175]
ds_read_b64_tr_b16 v[134:135], v190 offset:8992
v_mfma_f32_16x16x32_bf16 a[172:175], v[140:143], v[84:87], a[172:175]
v_mfma_f32_16x16x32_bf16 a[176:179], v[144:147], v[56:59], a[176:179]
ds_read_b64_tr_b16 v[136:137], v190 offset:64
s_waitcnt lgkmcnt(3)
v_mfma_f32_16x16x32_bf16 a[176:179], v[148:151], v[60:63], a[176:179]
buffer_load_dwordx4 v[28:31], v1, s[20:23], s26 offen offset:1024 sc1
v_mfma_f32_16x16x32_bf16 a[180:183], v[144:147], v[64:67], a[180:183]
ds_read_b64_tr_b16 v[138:139], v190 offset:576
v_mfma_f32_16x16x32_bf16 a[180:183], v[148:151], v[68:71], a[180:183]
v_mfma_f32_16x16x32_bf16 a[184:187], v[144:147], v[72:75], a[184:187]
ds_read_b64_tr_b16 v[140:141], v190 offset:8512
v_mfma_f32_16x16x32_bf16 a[184:187], v[148:151], v[76:79], a[184:187]
v_mfma_f32_16x16x32_bf16 a[188:191], v[144:147], v[80:83], a[188:191]
s_cmp_eq_u32 s4, 0
s_cbranch_scc1 OOLwop_%=
RETwop_%=:
v_mfma_f32_16x16x32_bf16 a[188:191], v[148:151], v[84:87], a[188:191]
ds_read_b64_tr_b16 v[142:143], v190 offset:9024
v_mfma_f32_16x16x32_bf16 a[192:195], v[152:155], v[56:59], a[192:195]
ds_read_b64_tr_b16 v[144:145], v190 offset:96
v_mfma_f32_16x16x32_bf16 a[192:195], v[156:159], v[60:63], a[192:195]
buffer_load_dwordx4 v[32:35], v2, s[20:23], s26 offen sc1
v_mfma_f32_16x16x32_bf16 a[196:199], v[152:155], v[64:67], a[196:199]
ds_read_b64_tr_b16 v[146:147], v190 offset:608
v_mfma_f32_16x16x32_bf16 a[196:199], v[156:159], v[68:71], a[196:199]
buffer_load_dwordx4 v[36:39], v2, s[20:23], s26 offen offset:1024 sc1
v_mfma_f32_16x16x32_bf16 a[200:203], v[152:155], v[72:75], a[200:203]
ds_read_b64_tr_b16 v[148:149], v190 offset:8544
v_mfma_f32_16x16x32_bf16 a[200:203], v[156:159], v[76:79], a[200:203]
v_mfma_f32_16x16x32_bf16 a[204:207], v[152:155], v[80:83], a[204:207]
ds_read_b64_tr_b16 v[150:151], v190 offset:9056
v_mfma_f32_16x16x32_bf16 a[204:207], v[156:159], v[84:87], a[204:207]
v_mfma_f32_16x16x32_bf16 a[208:211], v[160:163], v[56:59], a[208:211]
ds_read_b64_tr_b16 v[152:153], v190 offset:128
v_mfma_f32_16x16x32_bf16 a[208:211], v[164:167], v[60:63], a[208:211]
buffer_load_dwordx4 v[40:43], v3, s[20:23], s26 offen sc1
v_mfma_f32_16x16x32_bf16 a[212:215], v[160:163], v[64:67], a[212:215]
ds_read_b64_tr_b16 v[154:155], v190 offset:640
v_mfma_f32_16x16x32_bf16 a[212:215], v[164:167], v[68:71], a[212:215]
)ASM"
R"ASM(v_mfma_f32_16x16x32_bf16 a[216:219], v[160:163], v[72:75], a[216:219]
ds_read_b64_tr_b16 v[156:157], v190 offset:8576
v_mfma_f32_16x16x32_bf16 a[216:219], v[164:167], v[76:79], a[216:219]
buffer_load_dwordx4 v[44:47], v3, s[20:23], s26 offen offset:1024 sc1
v_mfma_f32_16x16x32_bf16 a[220:223], v[160:163], v[80:83], a[220:223]
ds_read_b64_tr_b16 v[158:159], v190 offset:9088
v_mfma_f32_16x16x32_bf16 a[220:223], v[164:167], v[84:87], a[220:223]
v_mfma_f32_16x16x32_bf16 a[224:227], v[168:171], v[56:59], a[224:227]
ds_read_b64_tr_b16 v[160:161], v190 offset:160
v_mfma_f32_16x16x32_bf16 a[224:227], v[172:175], v[60:63], a[224:227]
v_mfma_f32_16x16x32_bf16 a[228:231], v[168:171], v[64:67], a[228:231]
ds_read_b64_tr_b16 v[162:163], v190 offset:672
v_mfma_f32_16x16x32_bf16 a[228:231], v[172:175], v[68:71], a[228:231]
buffer_load_dwordx4 v[48:51], v4, s[20:23], s26 offen sc1
v_mfma_f32_16x16x32_bf16 a[232:235], v[168:171], v[72:75], a[232:235]
ds_read_b64_tr_b16 v[164:165], v190 offset:8608
v_mfma_f32_16x16x32_bf16 a[232:235], v[172:175], v[76:79], a[232:235]
v_mfma_f32_16x16x32_bf16 a[236:239], v[168:171], v[80:83], a[236:239]
ds_read_b64_tr_b16 v[166:167], v190 offset:9120
v_mfma_f32_16x16x32_bf16 a[236:239], v[172:175], v[84:87], a[236:239]
buffer_load_dwordx4 v[52:55], v4, s[20:23], s26 offen offset:1024 sc1
s_add_u32 s26, s26, 0x4000
v_mfma_f32_16x16x32_bf16 a[240:243], v[176:179], v[56:59], a[240:243]
ds_read_b64_tr_b16 v[168:169], v190 offset:192
v_mfma_f32_16x16x32_bf16 a[240:243], v[180:183], v[60:63], a[240:243]
v_mfma_f32_16x16x32_bf16 a[244:247], v[176:179], v[64:67], a[244:247]
ds_read_b64_tr_b16 v[170:171], v190 offset:704
v_mfma_f32_16x16x32_bf16 a[244:247], v[180:183], v[68:71], a[244:247]
v_mfma_f32_16x16x32_bf16 a[248:251], v[176:179], v[72:75], a[248:251]
ds_read_b64_tr_b16 v[172:173], v190 offset:8640
v_mfma_f32_16x16x32_bf16 a[248:251], v[180:183], v[76:79], a[248:251]
v_mfma_f32_16x16x32_bf16 a[252:255], v[176:179], v[80:83], a[252:255]
ds_read_b64_tr_b16 v[174:175], v190 offset:9152
v_mfma_f32_16x16x32_bf16 a[252:255], v[180:183], v[84:87], a[252:255]
s_waitcnt vmcnt(32)
s_barrier
v_mfma_f32_16x16x32_bf16 a[128:131], v[120:123], v[88:91], a[128:131]
ds_read_b64_tr_b16 v[176:177], v190 offset:224
s_add_u32 m0, s30, 0x1b9c0
v_mfma_f32_16x16x32_bf16 a[128:131], v[124:127], v[92:95], a[128:131]
buffer_load_dwordx4 v20, s[16:19], s27 offen lds
v_mfma_f32_16x16x32_bf16 a[132:135], v[120:123], v[96:99], a[132:135]
ds_read_b64_tr_b16 v[178:179], v190 offset:736
v_mfma_f32_16x16x32_bf16 a[132:135], v[124:127], v[100:103], a[132:135]
v_mfma_f32_16x16x32_bf16 a[136:139], v[120:123], v[104:107], a[136:139]
ds_read_b64_tr_b16 v[180:181], v190 offset:8672
v_mfma_f32_16x16x32_bf16 a[136:139], v[124:127], v[108:111], a[136:139]
buffer_load_dwordx4 v21, s[16:19], s27 offen offset:1056 lds
v_mfma_f32_16x16x32_bf16 a[140:143], v[120:123], v[112:115], a[140:143]
ds_read_b64_tr_b16 v[182:183], v190 offset:9184
v_mfma_f32_16x16x32_bf16 a[140:143], v[124:127], v[116:119], a[140:143]
v_mfma_f32_16x16x32_bf16 a[144:147], v[128:131], v[88:91], a[144:147]
ds_read_b64_tr_b16 v[120:121], v188
s_waitcnt lgkmcnt(3)
v_mfma_f32_16x16x32_bf16 a[144:147], v[132:135], v[92:95], a[144:147]
v_mfma_f32_16x16x32_bf16 a[148:151], v[128:131], v[96:99], a[148:151]
ds_read_b64_tr_b16 v[122:123], v188 offset:512
v_mfma_f32_16x16x32_bf16 a[148:151], v[132:135], v[100:103], a[148:151]
buffer_load_dwordx4 v22, s[16:19], s27 offen offset:2112 lds
v_mfma_f32_16x16x32_bf16 a[152:155], v[128:131], v[104:107], a[152:155]
ds_read_b64_tr_b16 v[124:125], v188 offset:8448
v_mfma_f32_16x16x32_bf16 a[152:155], v[132:135], v[108:111], a[152:155]
v_mfma_f32_16x16x32_bf16 a[156:159], v[128:131], v[112:115], a[156:159]
ds_read_b64_tr_b16 v[126:127], v188 offset:8960
v_mfma_f32_16x16x32_bf16 a[156:159], v[132:135], v[116:119], a[156:159]
buffer_load_dwordx4 v23, s[16:19], s27 offen offset:3168 lds
s_add_u32 s27, s27, 0x20000
v_mfma_f32_16x16x32_bf16 a[160:163], v[136:139], v[88:91], a[160:163]
ds_read_b64_tr_b16 v[128:129], v188 offset:32
v_mfma_f32_16x16x32_bf16 a[160:163], v[140:143], v[92:95], a[160:163]
v_mfma_f32_16x16x32_bf16 a[164:167], v[136:139], v[96:99], a[164:167]
ds_read_b64_tr_b16 v[130:131], v188 offset:544
v_mfma_f32_16x16x32_bf16 a[164:167], v[140:143], v[100:103], a[164:167]
v_mfma_f32_16x16x32_bf16 a[168:171], v[136:139], v[104:107], a[168:171]
ds_read_b64_tr_b16 v[132:133], v188 offset:8480
v_mfma_f32_16x16x32_bf16 a[168:171], v[140:143], v[108:111], a[168:171]
buffer_load_dwordx4 v[56:59], v1, s[20:23], s26 offen sc1
v_mfma_f32_16x16x32_bf16 a[172:175], v[136:139], v[112:115], a[172:175]
ds_read_b64_tr_b16 v[134:135], v188 offset:8992
v_mfma_f32_16x16x32_bf16 a[172:175], v[140:143], v[116:119], a[172:175]
v_mfma_f32_16x16x32_bf16 a[176:179], v[144:147], v[88:91], a[176:179]
ds_read_b64_tr_b16 v[136:137], v188 offset:64
v_mfma_f32_16x16x32_bf16 a[176:179], v[148:151], v[92:95], a[176:179]
buffer_load_dwordx4 v[60:63], v1, s[20:23], s26 offen offset:1024 sc1
v_mfma_f32_16x16x32_bf16 a[180:183], v[144:147], v[96:99], a[180:183]
ds_read_b64_tr_b16 v[138:139], v188 offset:576
v_mfma_f32_16x16x32_bf16 a[180:183], v[148:151], v[100:103], a[180:183]
v_mfma_f32_16x16x32_bf16 a[184:187], v[144:147], v[104:107], a[184:187]
ds_read_b64_tr_b16 v[140:141], v188 offset:8512
v_mfma_f32_16x16x32_bf16 a[184:187], v[148:151], v[108:111], a[184:187]
v_mfma_f32_16x16x32_bf16 a[188:191], v[144:147], v[112:115], a[188:191]
ds_read_b64_tr_b16 v[142:143], v188 offset:9024
v_mfma_f32_16x16x32_bf16 a[188:191], v[148:151], v[116:119], a[188:191]
buffer_load_dwordx4 v[64:67], v2, s[20:23], s26 offen sc1
v_mfma_f32_16x16x32_bf16 a[192:195], v[152:155], v[88:91], a[192:195]
ds_read_b64_tr_b16 v[144:145], v188 offset:96
v_mfma_f32_16x16x32_bf16 a[192:195], v[156:159], v[92:95], a[192:195]
v_mfma_f32_16x16x32_bf16 a[196:199], v[152:155], v[96:99], a[196:199]
ds_read_b64_tr_b16 v[146:147], v188 offset:608
v_mfma_f32_16x16x32_bf16 a[196:199], v[156:159], v[100:103], a[196:199]
buffer_load_dwordx4 v[68:71], v2, s[20:23], s26 offen offset:1024 sc1
v_mfma_f32_16x16x32_bf16 a[200:203], v[152:155], v[104:107], a[200:203]
ds_read_b64_tr_b16 v[148:149], v188 offset:8544
v_mfma_f32_16x16x32_bf16 a[200:203], v[156:159], v[108:111], a[200:203]
v_mfma_f32_16x16x32_bf16 a[204:207], v[152:155], v[112:115], a[204:207]
ds_read_b64_tr_b16 v[150:151], v188 offset:9056
v_mfma_f32_16x16x32_bf16 a[204:207], v[156:159], v[116:119], a[204:207]
v_mfma_f32_16x16x32_bf16 a[208:211], v[160:163], v[88:91], a[208:211]
ds_read_b64_tr_b16 v[152:153], v188 offset:128
v_mfma_f32_16x16x32_bf16 a[208:211], v[164:167], v[92:95], a[208:211]
buffer_load_dwordx4 v[72:75], v3, s[20:23], s26 offen sc1
v_mfma_f32_16x16x32_bf16 a[212:215], v[160:163], v[96:99], a[212:215]
ds_read_b64_tr_b16 v[154:155], v188 offset:640
v_mfma_f32_16x16x32_bf16 a[212:215], v[164:167], v[100:103], a[212:215]
v_mfma_f32_16x16x32_bf16 a[216:219], v[160:163], v[104:107], a[216:219]
ds_read_b64_tr_b16 v[156:157], v188 offset:8576
v_mfma_f32_16x16x32_bf16 a[216:219], v[164:167], v[108:111], a[216:219]
buffer_load_dwordx4 v[76:79], v3, s[20:23], s26 offen offset:1024 sc1
v_mfma_f32_16x16x32_bf16 a[220:223], v[160:163], v[112:115], a[220:223]
ds_read_b64_tr_b16 v[158:159], v188 offset:9088
v_mfma_f32_16x16x32_bf16 a[220:223], v[164:167], v[116:119], a[220:223]
v_mfma_f32_16x16x32_bf16 a[224:227], v[168:171], v[88:91], a[224:227]
ds_read_b64_tr_b16 v[160:161], v188 offset:160
v_mfma_f32_16x16x32_bf16 a[224:227], v[172:175], v[92:95], a[224:227]
v_mfma_f32_16x16x32_bf16 a[228:231], v[168:171], v[96:99], a[228:231]
ds_read_b64_tr_b16 v[162:163], v188 offset:672
v_mfma_f32_16x16x32_bf16 a[228:231], v[172:175], v[100:103], a[228:231]
buffer_load_dwordx4 v[80:83], v4, s[20:23], s26 offen sc1
v_mfma_f32_16x16x32_bf16 a[232:235], v[168:171], v[104:107], a[232:235]
ds_read_b64_tr_b16 v[164:165], v188 offset:8608
v_mfma_f32_16x16x32_bf16 a[232:235], v[172:175], v[108:111], a[232:235]
v_mfma_f32_16x16x32_bf16 a[236:239], v[168:171], v[112:115], a[236:239]
ds_read_b64_tr_b16 v[166:167], v188 offset:9120
v_mfma_f32_16x16x32_bf16 a[236:239], v[172:175], v[116:119], a[236:239]
buffer_load_dwordx4 v[84:87], v4, s[20:23], s26 offen offset:1024 sc1
s_add_u32 s26, s26, 0x4000
v_mfma_f32_16x16x32_bf16 a[240:243], v[176:179], v[88:91], a[240:243]
ds_read_b64_tr_b16 v[168:169], v188 offset:192
s_waitcnt lgkmcnt(3)
v_mfma_f32_16x16x32_bf16 a[240:243], v[180:183], v[92:95], a[240:243]
v_mfma_f32_16x16x32_bf16 a[244:247], v[176:179], v[96:99], a[244:247]
ds_read_b64_tr_b16 v[170:171], v188 offset:704
v_mfma_f32_16x16x32_bf16 a[244:247], v[180:183], v[100:103], a[244:247]
v_mfma_f32_16x16x32_bf16 a[248:251], v[176:179], v[104:107], a[248:251]
ds_read_b64_tr_b16 v[172:173], v188 offset:8640
v_mfma_f32_16x16x32_bf16 a[248:251], v[180:183], v[108:111], a[248:251]
v_mfma_f32_16x16x32_bf16 a[252:255], v[176:179], v[112:115], a[252:255]
ds_read_b64_tr_b16 v[174:175], v188 offset:9152
v_mfma_f32_16x16x32_bf16 a[252:255], v[180:183], v[116:119], a[252:255]
s_waitcnt vmcnt(36)
v_mfma_f32_16x16x32_bf16 a[128:131], v[120:123], v[192:195], a[128:131]
ds_read_b64_tr_b16 v[176:177], v188 offset:224
s_add_u32 m0, s30, 0x1fbc0
v_mfma_f32_16x16x32_bf16 a[128:131], v[124:127], v[196:199], a[128:131]
buffer_load_dwordx4 v20, s[16:19], s27 offen lds
v_mfma_f32_16x16x32_bf16 a[132:135], v[120:123], v[200:203], a[132:135]
ds_read_b64_tr_b16 v[178:179], v188 offset:736
v_mfma_f32_16x16x32_bf16 a[132:135], v[124:127], v[204:207], a[132:135]
v_mfma_f32_16x16x32_bf16 a[136:139], v[120:123], v[208:211], a[136:139]
ds_read_b64_tr_b16 v[180:181], v188 offset:8672
v_mfma_f32_16x16x32_bf16 a[136:139], v[124:127], v[212:215], a[136:139]
buffer_load_dwordx4 v21, s[16:19], s27 offen offset:1056 lds
v_mfma_f32_16x16x32_bf16 a[140:143], v[120:123], v[216:219], a[140:143]
ds_read_b64_tr_b16 v[182:183], v188 offset:9184
v_mfma_f32_16x16x32_bf16 a[140:143], v[124:127], v[220:223], a[140:143]
v_mfma_f32_16x16x32_bf16 a[144:147], v[128:131], v[192:195], a[144:147]
ds_read_b64_tr_b16 v[120:121], v188 offset:16896
v_mfma_f32_16x16x32_bf16 a[144:147], v[132:135], v[196:199], a[144:147]
v_mfma_f32_16x16x32_bf16 a[148:151], v[128:131], v[200:203], a[148:151]
ds_read_b64_tr_b16 v[122:123], v188 offset:17408
v_mfma_f32_16x16x32_bf16 a[148:151], v[132:135], v[204:207], a[148:151]
buffer_load_dwordx4 v22, s[16:19], s27 offen offset:2112 lds
v_mfma_f32_16x16x32_bf16 a[152:155], v[128:131], v[208:211], a[152:155]
ds_read_b64_tr_b16 v[124:125], v188 offset:25344
v_mfma_f32_16x16x32_bf16 a[152:155], v[132:135], v[212:215], a[152:155]
v_mfma_f32_16x16x32_bf16 a[156:159], v[128:131], v[216:219], a[156:159]
ds_read_b64_tr_b16 v[126:127], v188 offset:25856
v_mfma_f32_16x16x32_bf16 a[156:159], v[132:135], v[220:223], a[156:159]
buffer_load_dwordx4 v23, s[16:19], s27 offen offset:3168 lds
s_add_u32 s27, s27, 0x20000
v_mfma_f32_16x16x32_bf16 a[160:163], v[136:139], v[192:195], a[160:163]
ds_read_b64_tr_b16 v[128:129], v188 offset:16928
v_mfma_f32_16x16x32_bf16 a[160:163], v[140:143], v[196:199], a[160:163]
v_mfma_f32_16x16x32_bf16 a[164:167], v[136:139], v[200:203], a[164:167]
ds_read_b64_tr_b16 v[130:131], v188 offset:17440
v_mfma_f32_16x16x32_bf16 a[164:167], v[140:143], v[204:207], a[164:167]
v_mfma_f32_16x16x32_bf16 a[168:171], v[136:139], v[208:211], a[168:171]
ds_read_b64_tr_b16 v[132:133], v188 offset:25376
v_mfma_f32_16x16x32_bf16 a[168:171], v[140:143], v[212:215], a[168:171]
buffer_load_dwordx4 v[88:91], v1, s[20:23], s26 offen sc1
v_mfma_f32_16x16x32_bf16 a[172:175], v[136:139], v[216:219], a[172:175]
ds_read_b64_tr_b16 v[134:135], v188 offset:25888
v_mfma_f32_16x16x32_bf16 a[172:175], v[140:143], v[220:223], a[172:175]
v_mfma_f32_16x16x32_bf16 a[176:179], v[144:147], v[192:195], a[176:179]
ds_read_b64_tr_b16 v[136:137], v188 offset:16960
v_mfma_f32_16x16x32_bf16 a[176:179], v[148:151], v[196:199], a[176:179]
buffer_load_dwordx4 v[92:95], v1, s[20:23], s26 offen offset:1024 sc1
v_mfma_f32_16x16x32_bf16 a[180:183], v[144:147], v[200:203], a[180:183]
ds_read_b64_tr_b16 v[138:139], v188 offset:17472
v_mfma_f32_16x16x32_bf16 a[180:183], v[148:151], v[204:207], a[180:183]
v_mfma_f32_16x16x32_bf16 a[184:187], v[144:147], v[208:211], a[184:187]
ds_read_b64_tr_b16 v[140:141], v188 offset:25408
v_mfma_f32_16x16x32_bf16 a[184:187], v[148:151], v[212:215], a[184:187]
v_mfma_f32_16x16x32_bf16 a[188:191], v[144:147], v[216:219], a[188:191]
ds_read_b64_tr_b16 v[142:143], v188 offset:25920
v_mfma_f32_16x16x32_bf16 a[188:191], v[148:151], v[220:223], a[188:191]
buffer_load_dwordx4 v[96:99], v2, s[20:23], s26 offen sc1
v_mfma_f32_16x16x32_bf16 a[192:195], v[152:155], v[192:195], a[192:195]
ds_read_b64_tr_b16 v[144:145], v188 offset:16992
v_mfma_f32_16x16x32_bf16 a[192:195], v[156:159], v[196:199], a[192:195]
v_mfma_f32_16x16x32_bf16 a[196:199], v[152:155], v[200:203], a[196:199]
ds_read_b64_tr_b16 v[146:147], v188 offset:17504
v_mfma_f32_16x16x32_bf16 a[196:199], v[156:159], v[204:207], a[196:199]
buffer_load_dwordx4 v[100:103], v2, s[20:23], s26 offen offset:1024 sc1
v_mfma_f32_16x16x32_bf16 a[200:203], v[152:155], v[208:211], a[200:203]
ds_read_b64_tr_b16 v[148:149], v188 offset:25440
v_mfma_f32_16x16x32_bf16 a[200:203], v[156:159], v[212:215], a[200:203]
v_mfma_f32_16x16x32_bf16 a[204:207], v[152:155], v[216:219], a[204:207]
ds_read_b64_tr_b16 v[150:151], v188 offset:25952
v_mfma_f32_16x16x32_bf16 a[204:207], v[156:159], v[220:223], a[204:207]
v_mfma_f32_16x16x32_bf16 a[208:211], v[160:163], v[192:195], a[208:211]
ds_read_b64_tr_b16 v[152:153], v188 offset:17024
s_waitcnt lgkmcnt(3)
v_mfma_f32_16x16x32_bf16 a[208:211], v[164:167], v[196:199], a[208:211]
buffer_load_dwordx4 v[104:107], v3, s[20:23], s26 offen sc1
v_mfma_f32_16x16x32_bf16 a[212:215], v[160:163], v[200:203], a[212:215]
ds_read_b64_tr_b16 v[154:155], v188 offset:17536
v_mfma_f32_16x16x32_bf16 a[212:215], v[164:167], v[204:207], a[212:215]
v_mfma_f32_16x16x32_bf16 a[216:219], v[160:163], v[208:211], a[216:219]
ds_read_b64_tr_b16 v[156:157], v188 offset:25472
v_mfma_f32_16x16x32_bf16 a[216:219], v[164:167], v[212:215], a[216:219]
buffer_load_dwordx4 v[108:111], v3, s[20:23], s26 offen offset:1024 sc1
v_mfma_f32_16x16x32_bf16 a[220:223], v[160:163], v[216:219], a[220:223]
ds_read_b64_tr_b16 v[158:159], v188 offset:25984
v_mfma_f32_16x16x32_bf16 a[220:223], v[164:167], v[220:223], a[220:223]
v_mfma_f32_16x16x32_bf16 a[224:227], v[168:171], v[192:195], a[224:227]
ds_read_b64_tr_b16 v[160:161], v188 offset:17056
v_mfma_f32_16x16x32_bf16 a[224:227], v[172:175], v[196:199], a[224:227]
v_mfma_f32_16x16x32_bf16 a[228:231], v[168:171], v[200:203], a[228:231]
ds_read_b64_tr_b16 v[162:163], v188 offset:17568
v_mfma_f32_16x16x32_bf16 a[228:231], v[172:175], v[204:207], a[228:231]
buffer_load_dwordx4 v[112:115], v4, s[20:23], s26 offen sc1
v_mfma_f32_16x16x32_bf16 a[232:235], v[168:171], v[208:211], a[232:235]
ds_read_b64_tr_b16 v[164:165], v188 offset:25504
v_mfma_f32_16x16x32_bf16 a[232:235], v[172:175], v[212:215], a[232:235]
v_mfma_f32_16x16x32_bf16 a[236:239], v[168:171], v[216:219], a[236:239]
ds_read_b64_tr_b16 v[166:167], v188 offset:26016
v_mfma_f32_16x16x32_bf16 a[236:239], v[172:175], v[220:223], a[236:239]
buffer_load_dwordx4 v[116:119], v4, s[20:23], s26 offen offset:1024 sc1
s_add_u32 s26, s26, 0x4000
v_mfma_f32_16x16x32_bf16 a[240:243], v[176:179], v[192:195], a[240:243]
ds_read_b64_tr_b16 v[168:169], v188 offset:17088
v_mfma_f32_16x16x32_bf16 a[240:243], v[180:183], v[196:199], a[240:243]
v_mfma_f32_16x16x32_bf16 a[244:247], v[176:179], v[200:203], a[244:247]
ds_read_b64_tr_b16 v[170:171], v188 offset:17600
v_mfma_f32_16x16x32_bf16 a[244:247], v[180:183], v[204:207], a[244:247]
v_mfma_f32_16x16x32_bf16 a[248:251], v[176:179], v[208:211], a[248:251]
ds_read_b64_tr_b16 v[172:173], v188 offset:25536
v_mfma_f32_16x16x32_bf16 a[248:251], v[180:183], v[212:215], a[248:251]
v_mfma_f32_16x16x32_bf16 a[252:255], v[176:179], v[216:219], a[252:255]
ds_read_b64_tr_b16 v[174:175], v188 offset:26048
v_mfma_f32_16x16x32_bf16 a[252:255], v[180:183], v[220:223], a[252:255]
s_waitcnt vmcnt(32)
s_barrier
v_mfma_f32_16x16x32_bf16 a[128:131], v[120:123], v[224:227], a[128:131]
ds_read_b64_tr_b16 v[176:177], v188 offset:17120
s_add_u32 m0, s30, 0x23dc0
v_mfma_f32_16x16x32_bf16 a[128:131], v[124:127], v[228:231], a[128:131]
buffer_load_dwordx4 v20, s[16:19], s27 offen lds
v_mfma_f32_16x16x32_bf16 a[132:135], v[120:123], v[232:235], a[132:135]
ds_read_b64_tr_b16 v[178:179], v188 offset:17632
v_mfma_f32_16x16x32_bf16 a[132:135], v[124:127], v[236:239], a[132:135]
v_mfma_f32_16x16x32_bf16 a[136:139], v[120:123], v[240:243], a[136:139]
ds_read_b64_tr_b16 v[180:181], v188 offset:25568
v_mfma_f32_16x16x32_bf16 a[136:139], v[124:127], v[244:247], a[136:139]
buffer_load_dwordx4 v21, s[16:19], s27 offen offset:1056 lds
v_mfma_f32_16x16x32_bf16 a[140:143], v[120:123], v[248:251], a[140:143]
ds_read_b64_tr_b16 v[182:183], v188 offset:26080
v_mfma_f32_16x16x32_bf16 a[140:143], v[124:127], v[252:255], a[140:143]
v_mfma_f32_16x16x32_bf16 a[144:147], v[128:131], v[224:227], a[144:147]
ds_read_b64_tr_b16 v[120:121], v188 offset:33792
v_mfma_f32_16x16x32_bf16 a[144:147], v[132:135], v[228:231], a[144:147]
v_mfma_f32_16x16x32_bf16 a[148:151], v[128:131], v[232:235], a[148:151]
ds_read_b64_tr_b16 v[122:123], v188 offset:34304
v_mfma_f32_16x16x32_bf16 a[148:151], v[132:135], v[236:239], a[148:151]
buffer_load_dwordx4 v22, s[16:19], s27 offen offset:2112 lds
v_mfma_f32_16x16x32_bf16 a[152:155], v[128:131], v[240:243], a[152:155]
ds_read_b64_tr_b16 v[124:125], v188 offset:42240
v_mfma_f32_16x16x32_bf16 a[152:155], v[132:135], v[244:247], a[152:155]
v_mfma_f32_16x16x32_bf16 a[156:159], v[128:131], v[248:251], a[156:159]
ds_read_b64_tr_b16 v[126:127], v188 offset:42752
v_mfma_f32_16x16x32_bf16 a[156:159], v[132:135], v[252:255], a[156:159]
buffer_load_dwordx4 v23, s[16:19], s27 offen offset:3168 lds
s_add_u32 s27, s27, 0x20000
v_mfma_f32_16x16x32_bf16 a[160:163], v[136:139], v[224:227], a[160:163]
ds_read_b64_tr_b16 v[128:129], v188 offset:33824
v_mfma_f32_16x16x32_bf16 a[160:163], v[140:143], v[228:231], a[160:163]
v_mfma_f32_16x16x32_bf16 a[164:167], v[136:139], v[232:235], a[164:167]
ds_read_b64_tr_b16 v[130:131], v188 offset:34336
v_mfma_f32_16x16x32_bf16 a[164:167], v[140:143], v[236:239], a[164:167]
v_mfma_f32_16x16x32_bf16 a[168:171], v[136:139], v[240:243], a[168:171]
ds_read_b64_tr_b16 v[132:133], v188 offset:42272
v_mfma_f32_16x16x32_bf16 a[168:171], v[140:143], v[244:247], a[168:171]
buffer_load_dwordx4 v[192:195], v1, s[20:23], s26 offen sc1
v_mfma_f32_16x16x32_bf16 a[172:175], v[136:139], v[248:251], a[172:175]
ds_read_b64_tr_b16 v[134:135], v188 offset:42784
v_mfma_f32_16x16x32_bf16 a[172:175], v[140:143], v[252:255], a[172:175]
v_mfma_f32_16x16x32_bf16 a[176:179], v[144:147], v[224:227], a[176:179]
ds_read_b64_tr_b16 v[136:137], v188 offset:33856
s_waitcnt lgkmcnt(3)
v_mfma_f32_16x16x32_bf16 a[176:179], v[148:151], v[228:231], a[176:179]
buffer_load_dwordx4 v[196:199], v1, s[20:23], s26 offen offset:1024 sc1
v_mfma_f32_16x16x32_bf16 a[180:183], v[144:147], v[232:235], a[180:183]
ds_read_b64_tr_b16 v[138:139], v188 offset:34368
v_mfma_f32_16x16x32_bf16 a[180:183], v[148:151], v[236:239], a[180:183]
v_mfma_f32_16x16x32_bf16 a[184:187], v[144:147], v[240:243], a[184:187]
ds_read_b64_tr_b16 v[140:141], v188 offset:42304
v_mfma_f32_16x16x32_bf16 a[184:187], v[148:151], v[244:247], a[184:187]
v_mfma_f32_16x16x32_bf16 a[188:191], v[144:147], v[248:251], a[188:191]
ds_read_b64_tr_b16 v[142:143], v188 offset:42816
v_mfma_f32_16x16x32_bf16 a[188:191], v[148:151], v[252:255], a[188:191]
buffer_load_dwordx4 v[200:203], v2, s[20:23], s26 offen sc1
v_mfma_f32_16x16x32_bf16 a[192:195], v[152:155], v[224:227], a[192:195]
ds_read_b64_tr_b16 v[144:145], v188 offset:33888
v_mfma_f32_16x16x32_bf16 a[192:195], v[156:159], v[228:231], a[192:195]
v_mfma_f32_16x16x32_bf16 a[196:199], v[152:155], v[232:235], a[196:199]
ds_read_b64_tr_b16 v[146:147], v188 offset:34400
v_mfma_f32_16x16x32_bf16 a[196:199], v[156:159], v[236:239], a[196:199]
buffer_load_dwordx4 v[204:207], v2, s[20:23], s26 offen offset:1024 sc1
v_mfma_f32_16x16x32_bf16 a[200:203], v[152:155], v[240:243], a[200:203]
ds_read_b64_tr_b16 v[148:149], v188 offset:42336
v_mfma_f32_16x16x32_bf16 a[200:203], v[156:159], v[244:247], a[200:203]
v_mfma_f32_16x16x32_bf16 a[204:207], v[152:155], v[248:251], a[204:207]
ds_read_b64_tr_b16 v[150:151], v188 offset:42848
v_mfma_f32_16x16x32_bf16 a[204:207], v[156:159], v[252:255], a[204:207]
v_mfma_f32_16x16x32_bf16 a[208:211], v[160:163], v[224:227], a[208:211]
ds_read_b64_tr_b16 v[152:153], v188 offset:33920
v_mfma_f32_16x16x32_bf16 a[208:211], v[164:167], v[228:231], a[208:211]
buffer_load_dwordx4 v[208:211], v3, s[20:23], s26 offen sc1
v_mfma_f32_16x16x32_bf16 a[212:215], v[160:163], v[232:235], a[212:215]
ds_read_b64_tr_b16 v[154:155], v188 offset:34432
v_mfma_f32_16x16x32_bf16 a[212:215], v[164:167], v[236:239], a[212:215]
v_mfma_f32_16x16x32_bf16 a[216:219], v[160:163], v[240:243], a[216:219]
ds_read_b64_tr_b16 v[156:157], v188 offset:42368
v_mfma_f32_16x16x32_bf16 a[216:219], v[164:167], v[244:247], a[216:219]
buffer_load_dwordx4 v[212:215], v3, s[20:23], s26 offen offset:1024 sc1
v_mfma_f32_16x16x32_bf16 a[220:223], v[160:163], v[248:251], a[220:223]
ds_read_b64_tr_b16 v[158:159], v188 offset:42880
v_mfma_f32_16x16x32_bf16 a[220:223], v[164:167], v[252:255], a[220:223]
v_mfma_f32_16x16x32_bf16 a[224:227], v[168:171], v[224:227], a[224:227]
ds_read_b64_tr_b16 v[160:161], v188 offset:33952
v_mfma_f32_16x16x32_bf16 a[224:227], v[172:175], v[228:231], a[224:227]
v_mfma_f32_16x16x32_bf16 a[228:231], v[168:171], v[232:235], a[228:231]
ds_read_b64_tr_b16 v[162:163], v188 offset:34464
v_mfma_f32_16x16x32_bf16 a[228:231], v[172:175], v[236:239], a[228:231]
buffer_load_dwordx4 v[216:219], v4, s[20:23], s26 offen sc1
v_mfma_f32_16x16x32_bf16 a[232:235], v[168:171], v[240:243], a[232:235]
ds_read_b64_tr_b16 v[164:165], v188 offset:42400
v_mfma_f32_16x16x32_bf16 a[232:235], v[172:175], v[244:247], a[232:235]
v_mfma_f32_16x16x32_bf16 a[236:239], v[168:171], v[248:251], a[236:239]
ds_read_b64_tr_b16 v[166:167], v188 offset:42912
v_mfma_f32_16x16x32_bf16 a[236:239], v[172:175], v[252:255], a[236:239]
buffer_load_dwordx4 v[220:223], v4, s[20:23], s26 offen offset:1024 sc1
s_add_u32 s26, s26, 0x4000
v_mfma_f32_16x16x32_bf16 a[240:243], v[176:179], v[224:227], a[240:243]
ds_read_b64_tr_b16 v[168:169], v188 offset:33984
v_mfma_f32_16x16x32_bf16 a[240:243], v[180:183], v[228:231], a[240:243]
v_mfma_f32_16x16x32_bf16 a[244:247], v[176:179], v[232:235], a[244:247]
ds_read_b64_tr_b16 v[170:171], v188 offset:34496
v_mfma_f32_16x16x32_bf16 a[244:247], v[180:183], v[236:239], a[244:247]
v_mfma_f32_16x16x32_bf16 a[248:251], v[176:179], v[240:243], a[248:251]
ds_read_b64_tr_b16 v[172:173], v188 offset:42432
v_mfma_f32_16x16x32_bf16 a[248:251], v[180:183], v[244:247], a[248:251]
v_mfma_f32_16x16x32_bf16 a[252:255], v[176:179], v[248:251], a[252:255]
ds_read_b64_tr_b16 v[174:175], v188 offset:42944
v_mfma_f32_16x16x32_bf16 a[252:255], v[180:183], v[252:255], a[252:255]
s_waitcnt vmcnt(36)
v_mfma_f32_16x16x32_bf16 a[128:131], v[120:123], v[24:27], a[128:131]
ds_read_b64_tr_b16 v[176:177], v188 offset:34016
s_add_u32 m0, s30, 0xb1c0
v_mfma_f32_16x16x32_bf16 a[128:131], v[124:127], v[28:31], a[128:131]
buffer_load_dwordx4 v20, s[16:19], s27 offen lds
v_mfma_f32_16x16x32_bf16 a[132:135], v[120:123], v[32:35], a[132:135]
ds_read_b64_tr_b16 v[178:179], v188 offset:34528
v_mfma_f32_16x16x32_bf16 a[132:135], v[124:127], v[36:39], a[132:135]
v_mfma_f32_16x16x32_bf16 a[136:139], v[120:123], v[40:43], a[136:139]
ds_read_b64_tr_b16 v[180:181], v188 offset:42464
v_mfma_f32_16x16x32_bf16 a[136:139], v[124:127], v[44:47], a[136:139]
buffer_load_dwordx4 v21, s[16:19], s27 offen offset:1056 lds
v_mfma_f32_16x16x32_bf16 a[140:143], v[120:123], v[48:51], a[140:143]
ds_read_b64_tr_b16 v[182:183], v188 offset:42976
v_mfma_f32_16x16x32_bf16 a[140:143], v[124:127], v[52:55], a[140:143]
v_mfma_f32_16x16x32_bf16 a[144:147], v[128:131], v[24:27], a[144:147]
ds_read_b64_tr_b16 v[120:121], v189
s_waitcnt lgkmcnt(3)
v_mfma_f32_16x16x32_bf16 a[144:147], v[132:135], v[28:31], a[144:147]
v_mfma_f32_16x16x32_bf16 a[148:151], v[128:131], v[32:35], a[148:151]
ds_read_b64_tr_b16 v[122:123], v189 offset:512
v_mfma_f32_16x16x32_bf16 a[148:151], v[132:135], v[36:39], a[148:151]
buffer_load_dwordx4 v22, s[16:19], s27 offen offset:2112 lds
)ASM"
R"ASM(v_mfma_f32_16x16x32_bf16 a[152:155], v[128:131], v[40:43], a[152:155]
ds_read_b64_tr_b16 v[124:125], v189 offset:8448
v_mfma_f32_16x16x32_bf16 a[152:155], v[132:135], v[44:47], a[152:155]
v_mfma_f32_16x16x32_bf16 a[156:159], v[128:131], v[48:51], a[156:159]
ds_read_b64_tr_b16 v[126:127], v189 offset:8960
v_mfma_f32_16x16x32_bf16 a[156:159], v[132:135], v[52:55], a[156:159]
buffer_load_dwordx4 v23, s[16:19], s27 offen offset:3168 lds
s_add_u32 s27, s27, 0x20000
v_mfma_f32_16x16x32_bf16 a[160:163], v[136:139], v[24:27], a[160:163]
ds_read_b64_tr_b16 v[128:129], v189 offset:32
v_mfma_f32_16x16x32_bf16 a[160:163], v[140:143], v[28:31], a[160:163]
v_mfma_f32_16x16x32_bf16 a[164:167], v[136:139], v[32:35], a[164:167]
ds_read_b64_tr_b16 v[130:131], v189 offset:544
v_mfma_f32_16x16x32_bf16 a[164:167], v[140:143], v[36:39], a[164:167]
v_mfma_f32_16x16x32_bf16 a[168:171], v[136:139], v[40:43], a[168:171]
ds_read_b64_tr_b16 v[132:133], v189 offset:8480
v_mfma_f32_16x16x32_bf16 a[168:171], v[140:143], v[44:47], a[168:171]
buffer_load_dwordx4 v[224:227], v1, s[20:23], s26 offen sc1
v_mfma_f32_16x16x32_bf16 a[172:175], v[136:139], v[48:51], a[172:175]
ds_read_b64_tr_b16 v[134:135], v189 offset:8992
v_mfma_f32_16x16x32_bf16 a[172:175], v[140:143], v[52:55], a[172:175]
v_mfma_f32_16x16x32_bf16 a[176:179], v[144:147], v[24:27], a[176:179]
ds_read_b64_tr_b16 v[136:137], v189 offset:64
v_mfma_f32_16x16x32_bf16 a[176:179], v[148:151], v[28:31], a[176:179]
buffer_load_dwordx4 v[228:231], v1, s[20:23], s26 offen offset:1024 sc1
v_mfma_f32_16x16x32_bf16 a[180:183], v[144:147], v[32:35], a[180:183]
ds_read_b64_tr_b16 v[138:139], v189 offset:576
v_mfma_f32_16x16x32_bf16 a[180:183], v[148:151], v[36:39], a[180:183]
v_mfma_f32_16x16x32_bf16 a[184:187], v[144:147], v[40:43], a[184:187]
ds_read_b64_tr_b16 v[140:141], v189 offset:8512
v_mfma_f32_16x16x32_bf16 a[184:187], v[148:151], v[44:47], a[184:187]
v_mfma_f32_16x16x32_bf16 a[188:191], v[144:147], v[48:51], a[188:191]
ds_read_b64_tr_b16 v[142:143], v189 offset:9024
v_mfma_f32_16x16x32_bf16 a[188:191], v[148:151], v[52:55], a[188:191]
buffer_load_dwordx4 v[232:235], v2, s[20:23], s26 offen sc1
v_mfma_f32_16x16x32_bf16 a[192:195], v[152:155], v[24:27], a[192:195]
ds_read_b64_tr_b16 v[144:145], v189 offset:96
v_mfma_f32_16x16x32_bf16 a[192:195], v[156:159], v[28:31], a[192:195]
v_mfma_f32_16x16x32_bf16 a[196:199], v[152:155], v[32:35], a[196:199]
ds_read_b64_tr_b16 v[146:147], v189 offset:608
v_mfma_f32_16x16x32_bf16 a[196:199], v[156:159], v[36:39], a[196:199]
buffer_load_dwordx4 v[236:239], v2, s[20:23], s26 offen offset:1024 sc1
v_mfma_f32_16x16x32_bf16 a[200:203], v[152:155], v[40:43], a[200:203]
ds_read_b64_tr_b16 v[148:149], v189 offset:8544
v_mfma_f32_16x16x32_bf16 a[200:203], v[156:159], v[44:47], a[200:203]
v_mfma_f32_16x16x32_bf16 a[204:207], v[152:155], v[48:51], a[204:207]
ds_read_b64_tr_b16 v[150:151], v189 offset:9056
v_mfma_f32_16x16x32_bf16 a[204:207], v[156:159], v[52:55], a[204:207]
v_mfma_f32_16x16x32_bf16 a[208:211], v[160:163], v[24:27], a[208:211]
ds_read_b64_tr_b16 v[152:153], v189 offset:128
v_mfma_f32_16x16x32_bf16 a[208:211], v[164:167], v[28:31], a[208:211]
buffer_load_dwordx4 v[240:243], v3, s[20:23], s26 offen sc1
v_mfma_f32_16x16x32_bf16 a[212:215], v[160:163], v[32:35], a[212:215]
ds_read_b64_tr_b16 v[154:155], v189 offset:640
v_mfma_f32_16x16x32_bf16 a[212:215], v[164:167], v[36:39], a[212:215]
v_mfma_f32_16x16x32_bf16 a[216:219], v[160:163], v[40:43], a[216:219]
ds_read_b64_tr_b16 v[156:157], v189 offset:8576
v_mfma_f32_16x16x32_bf16 a[216:219], v[164:167], v[44:47], a[216:219]
buffer_load_dwordx4 v[244:247], v3, s[20:23], s26 offen offset:1024 sc1
v_mfma_f32_16x16x32_bf16 a[220:223], v[160:163], v[48:51], a[220:223]
ds_read_b64_tr_b16 v[158:159], v189 offset:9088
v_mfma_f32_16x16x32_bf16 a[220:223], v[164:167], v[52:55], a[220:223]
v_mfma_f32_16x16x32_bf16 a[224:227], v[168:171], v[24:27], a[224:227]
ds_read_b64_tr_b16 v[160:161], v189 offset:160
v_mfma_f32_16x16x32_bf16 a[224:227], v[172:175], v[28:31], a[224:227]
v_mfma_f32_16x16x32_bf16 a[228:231], v[168:171], v[32:35], a[228:231]
s_lshl_b32 s46, s72, 2
s_add_u32 s46, s46, s74
s_lshl_b32 s46, s46, 9
s_add_u32 s46, s46, 0x8000
v_mbcnt_lo_u32_b32 v17, -1, 0
v_mbcnt_hi_u32_b32 v17, -1, v17
v_min_u32 v17, 7, v17
v_lshlrev_b32 v17, 6, v17
buffer_load_dword v184, v17, s[76:79], s46 offen sc1
v_mfma_f32_16x16x32_bf16 a[228:231], v[172:175], v[36:39], a[228:231]
ds_read_b64_tr_b16 v[162:163], v189 offset:672
v_mfma_f32_16x16x32_bf16 a[232:235], v[168:171], v[40:43], a[232:235]
ds_read_b64_tr_b16 v[164:165], v189 offset:8608
v_mfma_f32_16x16x32_bf16 a[232:235], v[172:175], v[44:47], a[232:235]
buffer_load_dwordx4 v[248:251], v4, s[20:23], s26 offen sc1
v_mfma_f32_16x16x32_bf16 a[236:239], v[168:171], v[48:51], a[236:239]
s_add_u32 s46, s74, 1
s_and_b32 s46, s46, 3
s_lshl_b32 s46, s46, 1
s_and_b32 s35, s5, 0xf9
s_or_b32 s46, s46, s35
s_lshl_b32 s46, s46, 6
s_add_u32 s46, s46, 0x14000
v_mov_b32 v17, 0
buffer_load_dword v185, v17, s[76:79], s46 offen sc1
v_mfma_f32_16x16x32_bf16 a[236:239], v[172:175], v[52:55], a[236:239]
ds_read_b64_tr_b16 v[166:167], v189 offset:9120
v_mfma_f32_16x16x32_bf16 a[240:243], v[176:179], v[24:27], a[240:243]
ds_read_b64_tr_b16 v[168:169], v189 offset:192
s_waitcnt lgkmcnt(4)
v_mfma_f32_16x16x32_bf16 a[240:243], v[180:183], v[28:31], a[240:243]
buffer_load_dwordx4 v[252:255], v4, s[20:23], s26 offen offset:1024 sc1
s_add_u32 s26, s26, 0x4000
v_mfma_f32_16x16x32_bf16 a[244:247], v[176:179], v[32:35], a[244:247]
s_add_u32 s46, s74, 2
s_and_b32 s46, s46, 3
s_lshl_b32 s46, s46, 1
s_and_b32 s35, s5, 0xf9
s_or_b32 s46, s46, s35
s_lshl_b32 s46, s46, 6
s_add_u32 s46, s46, 0x14000
v_mov_b32 v17, 0
buffer_load_dword v186, v17, s[76:79], s46 offen sc1
v_mfma_f32_16x16x32_bf16 a[244:247], v[180:183], v[36:39], a[244:247]
ds_read_b64_tr_b16 v[170:171], v189 offset:704
v_mfma_f32_16x16x32_bf16 a[248:251], v[176:179], v[40:43], a[248:251]
ds_read_b64_tr_b16 v[172:173], v189 offset:8640
v_mfma_f32_16x16x32_bf16 a[248:251], v[180:183], v[44:47], a[248:251]
v_mfma_f32_16x16x32_bf16 a[252:255], v[176:179], v[48:51], a[252:255]
s_add_u32 s46, s74, 3
s_and_b32 s46, s46, 3
s_lshl_b32 s46, s46, 1
s_and_b32 s35, s5, 0xf9
s_or_b32 s46, s46, s35
s_lshl_b32 s46, s46, 6
s_add_u32 s46, s46, 0x14000
v_mov_b32 v17, 0
buffer_load_dword v187, v17, s[76:79], s46 offen sc1
v_mfma_f32_16x16x32_bf16 a[252:255], v[180:183], v[52:55], a[252:255]
ds_read_b64_tr_b16 v[174:175], v189 offset:9152
s_waitcnt vmcnt(36)
s_barrier
v_mfma_f32_16x16x32_bf16 a[128:131], v[120:123], v[56:59], a[128:131]
ds_read_b64_tr_b16 v[176:177], v189 offset:224
s_add_u32 m0, s30, 0xf3c0
v_mfma_f32_16x16x32_bf16 a[128:131], v[124:127], v[60:63], a[128:131]
buffer_load_dwordx4 v20, s[16:19], s27 offen lds
v_mfma_f32_16x16x32_bf16 a[132:135], v[120:123], v[64:67], a[132:135]
ds_read_b64_tr_b16 v[178:179], v189 offset:736
v_mfma_f32_16x16x32_bf16 a[132:135], v[124:127], v[68:71], a[132:135]
v_mfma_f32_16x16x32_bf16 a[136:139], v[120:123], v[72:75], a[136:139]
ds_read_b64_tr_b16 v[180:181], v189 offset:8672
v_mfma_f32_16x16x32_bf16 a[136:139], v[124:127], v[76:79], a[136:139]
buffer_load_dwordx4 v21, s[16:19], s27 offen offset:1056 lds
v_mfma_f32_16x16x32_bf16 a[140:143], v[120:123], v[80:83], a[140:143]
ds_read_b64_tr_b16 v[182:183], v189 offset:9184
v_mfma_f32_16x16x32_bf16 a[140:143], v[124:127], v[84:87], a[140:143]
v_mfma_f32_16x16x32_bf16 a[144:147], v[128:131], v[56:59], a[144:147]
ds_read_b64_tr_b16 v[120:121], v189 offset:16896
v_mfma_f32_16x16x32_bf16 a[144:147], v[132:135], v[60:63], a[144:147]
v_mfma_f32_16x16x32_bf16 a[148:151], v[128:131], v[64:67], a[148:151]
ds_read_b64_tr_b16 v[122:123], v189 offset:17408
v_mfma_f32_16x16x32_bf16 a[148:151], v[132:135], v[68:71], a[148:151]
buffer_load_dwordx4 v22, s[16:19], s27 offen offset:2112 lds
v_mfma_f32_16x16x32_bf16 a[152:155], v[128:131], v[72:75], a[152:155]
ds_read_b64_tr_b16 v[124:125], v189 offset:25344
v_mfma_f32_16x16x32_bf16 a[152:155], v[132:135], v[76:79], a[152:155]
v_mfma_f32_16x16x32_bf16 a[156:159], v[128:131], v[80:83], a[156:159]
ds_read_b64_tr_b16 v[126:127], v189 offset:25856
v_mfma_f32_16x16x32_bf16 a[156:159], v[132:135], v[84:87], a[156:159]
buffer_load_dwordx4 v23, s[16:19], s27 offen offset:3168 lds
s_add_u32 s27, s27, 0x20000
v_mfma_f32_16x16x32_bf16 a[160:163], v[136:139], v[56:59], a[160:163]
ds_read_b64_tr_b16 v[128:129], v189 offset:16928
v_mfma_f32_16x16x32_bf16 a[160:163], v[140:143], v[60:63], a[160:163]
v_mfma_f32_16x16x32_bf16 a[164:167], v[136:139], v[64:67], a[164:167]
ds_read_b64_tr_b16 v[130:131], v189 offset:17440
v_mfma_f32_16x16x32_bf16 a[164:167], v[140:143], v[68:71], a[164:167]
v_mfma_f32_16x16x32_bf16 a[168:171], v[136:139], v[72:75], a[168:171]
ds_read_b64_tr_b16 v[132:133], v189 offset:25376
v_mfma_f32_16x16x32_bf16 a[168:171], v[140:143], v[76:79], a[168:171]
buffer_load_dwordx4 v[24:27], v1, s[20:23], s26 offen sc1
v_mfma_f32_16x16x32_bf16 a[172:175], v[136:139], v[80:83], a[172:175]
ds_read_b64_tr_b16 v[134:135], v189 offset:25888
v_mfma_f32_16x16x32_bf16 a[172:175], v[140:143], v[84:87], a[172:175]
v_mfma_f32_16x16x32_bf16 a[176:179], v[144:147], v[56:59], a[176:179]
ds_read_b64_tr_b16 v[136:137], v189 offset:16960
v_mfma_f32_16x16x32_bf16 a[176:179], v[148:151], v[60:63], a[176:179]
buffer_load_dwordx4 v[28:31], v1, s[20:23], s26 offen offset:1024 sc1
v_mfma_f32_16x16x32_bf16 a[180:183], v[144:147], v[64:67], a[180:183]
ds_read_b64_tr_b16 v[138:139], v189 offset:17472
v_mfma_f32_16x16x32_bf16 a[180:183], v[148:151], v[68:71], a[180:183]
v_mfma_f32_16x16x32_bf16 a[184:187], v[144:147], v[72:75], a[184:187]
ds_read_b64_tr_b16 v[140:141], v189 offset:25408
v_mfma_f32_16x16x32_bf16 a[184:187], v[148:151], v[76:79], a[184:187]
v_mfma_f32_16x16x32_bf16 a[188:191], v[144:147], v[80:83], a[188:191]
ds_read_b64_tr_b16 v[142:143], v189 offset:25920
v_mfma_f32_16x16x32_bf16 a[188:191], v[148:151], v[84:87], a[188:191]
buffer_load_dwordx4 v[32:35], v2, s[20:23], s26 offen sc1
v_mfma_f32_16x16x32_bf16 a[192:195], v[152:155], v[56:59], a[192:195]
ds_read_b64_tr_b16 v[144:145], v189 offset:16992
v_mfma_f32_16x16x32_bf16 a[192:195], v[156:159], v[60:63], a[192:195]
v_mfma_f32_16x16x32_bf16 a[196:199], v[152:155], v[64:67], a[196:199]
ds_read_b64_tr_b16 v[146:147], v189 offset:17504
v_mfma_f32_16x16x32_bf16 a[196:199], v[156:159], v[68:71], a[196:199]
buffer_load_dwordx4 v[36:39], v2, s[20:23], s26 offen offset:1024 sc1
v_mfma_f32_16x16x32_bf16 a[200:203], v[152:155], v[72:75], a[200:203]
ds_read_b64_tr_b16 v[148:149], v189 offset:25440
v_mfma_f32_16x16x32_bf16 a[200:203], v[156:159], v[76:79], a[200:203]
v_mfma_f32_16x16x32_bf16 a[204:207], v[152:155], v[80:83], a[204:207]
ds_read_b64_tr_b16 v[150:151], v189 offset:25952
v_mfma_f32_16x16x32_bf16 a[204:207], v[156:159], v[84:87], a[204:207]
s_waitcnt lgkmcnt(3)
v_mfma_f32_16x16x32_bf16 a[208:211], v[160:163], v[56:59], a[208:211]
ds_read_b64_tr_b16 v[152:153], v189 offset:17024
v_mfma_f32_16x16x32_bf16 a[208:211], v[164:167], v[60:63], a[208:211]
buffer_load_dwordx4 v[40:43], v3, s[20:23], s26 offen sc1
v_mfma_f32_16x16x32_bf16 a[212:215], v[160:163], v[64:67], a[212:215]
ds_read_b64_tr_b16 v[154:155], v189 offset:17536
v_mfma_f32_16x16x32_bf16 a[212:215], v[164:167], v[68:71], a[212:215]
v_mfma_f32_16x16x32_bf16 a[216:219], v[160:163], v[72:75], a[216:219]
ds_read_b64_tr_b16 v[156:157], v189 offset:25472
v_mfma_f32_16x16x32_bf16 a[216:219], v[164:167], v[76:79], a[216:219]
buffer_load_dwordx4 v[44:47], v3, s[20:23], s26 offen offset:1024 sc1
v_mfma_f32_16x16x32_bf16 a[220:223], v[160:163], v[80:83], a[220:223]
ds_read_b64_tr_b16 v[158:159], v189 offset:25984
v_mfma_f32_16x16x32_bf16 a[220:223], v[164:167], v[84:87], a[220:223]
v_mfma_f32_16x16x32_bf16 a[224:227], v[168:171], v[56:59], a[224:227]
ds_read_b64_tr_b16 v[160:161], v189 offset:17056
v_mfma_f32_16x16x32_bf16 a[224:227], v[172:175], v[60:63], a[224:227]
v_mfma_f32_16x16x32_bf16 a[228:231], v[168:171], v[64:67], a[228:231]
ds_read_b64_tr_b16 v[162:163], v189 offset:17568
v_mfma_f32_16x16x32_bf16 a[228:231], v[172:175], v[68:71], a[228:231]
buffer_load_dwordx4 v[48:51], v4, s[20:23], s26 offen sc1
v_mfma_f32_16x16x32_bf16 a[232:235], v[168:171], v[72:75], a[232:235]
ds_read_b64_tr_b16 v[164:165], v189 offset:25504
v_mfma_f32_16x16x32_bf16 a[232:235], v[172:175], v[76:79], a[232:235]
v_mfma_f32_16x16x32_bf16 a[236:239], v[168:171], v[80:83], a[236:239]
ds_read_b64_tr_b16 v[166:167], v189 offset:26016
v_mfma_f32_16x16x32_bf16 a[236:239], v[172:175], v[84:87], a[236:239]
buffer_load_dwordx4 v[52:55], v4, s[20:23], s26 offen offset:1024 sc1
s_add_u32 s26, s26, 0x4000
v_mfma_f32_16x16x32_bf16 a[240:243], v[176:179], v[56:59], a[240:243]
ds_read_b64_tr_b16 v[168:169], v189 offset:17088
v_mfma_f32_16x16x32_bf16 a[240:243], v[180:183], v[60:63], a[240:243]
v_mfma_f32_16x16x32_bf16 a[244:247], v[176:179], v[64:67], a[244:247]
ds_read_b64_tr_b16 v[170:171], v189 offset:17600
v_mfma_f32_16x16x32_bf16 a[244:247], v[180:183], v[68:71], a[244:247]
v_mfma_f32_16x16x32_bf16 a[248:251], v[176:179], v[72:75], a[248:251]
ds_read_b64_tr_b16 v[172:173], v189 offset:25536
v_mfma_f32_16x16x32_bf16 a[248:251], v[180:183], v[76:79], a[248:251]
v_mfma_f32_16x16x32_bf16 a[252:255], v[176:179], v[80:83], a[252:255]
ds_read_b64_tr_b16 v[174:175], v189 offset:26048
v_mfma_f32_16x16x32_bf16 a[252:255], v[180:183], v[84:87], a[252:255]
s_waitcnt vmcnt(40)
s_mov_b32 s27, s75
s_mov_b32 s16, s56
s_and_b32 s17, s57, 0xffff
s_mov_b32 s18, 0x800000
v_mfma_f32_16x16x32_bf16 a[128:131], v[120:123], v[88:91], a[128:131]
ds_read_b64_tr_b16 v[176:177], v189 offset:17120
s_add_u32 m0, s30, 0x135c0
v_mfma_f32_16x16x32_bf16 a[128:131], v[124:127], v[92:95], a[128:131]
buffer_load_dwordx4 v20, s[16:19], s27 offen lds
v_mfma_f32_16x16x32_bf16 a[132:135], v[120:123], v[96:99], a[132:135]
ds_read_b64_tr_b16 v[178:179], v189 offset:17632
v_mfma_f32_16x16x32_bf16 a[132:135], v[124:127], v[100:103], a[132:135]
v_mfma_f32_16x16x32_bf16 a[136:139], v[120:123], v[104:107], a[136:139]
ds_read_b64_tr_b16 v[180:181], v189 offset:25568
v_mfma_f32_16x16x32_bf16 a[136:139], v[124:127], v[108:111], a[136:139]
buffer_load_dwordx4 v21, s[16:19], s27 offen offset:1056 lds
v_mfma_f32_16x16x32_bf16 a[140:143], v[120:123], v[112:115], a[140:143]
ds_read_b64_tr_b16 v[182:183], v189 offset:26080
v_mfma_f32_16x16x32_bf16 a[140:143], v[124:127], v[116:119], a[140:143]
v_mfma_f32_16x16x32_bf16 a[144:147], v[128:131], v[88:91], a[144:147]
ds_read_b64_tr_b16 v[120:121], v189 offset:33792
v_mfma_f32_16x16x32_bf16 a[144:147], v[132:135], v[92:95], a[144:147]
v_mfma_f32_16x16x32_bf16 a[148:151], v[128:131], v[96:99], a[148:151]
ds_read_b64_tr_b16 v[122:123], v189 offset:34304
v_mfma_f32_16x16x32_bf16 a[148:151], v[132:135], v[100:103], a[148:151]
buffer_load_dwordx4 v22, s[16:19], s27 offen offset:2112 lds
v_mfma_f32_16x16x32_bf16 a[152:155], v[128:131], v[104:107], a[152:155]
ds_read_b64_tr_b16 v[124:125], v189 offset:42240
v_mfma_f32_16x16x32_bf16 a[152:155], v[132:135], v[108:111], a[152:155]
v_mfma_f32_16x16x32_bf16 a[156:159], v[128:131], v[112:115], a[156:159]
ds_read_b64_tr_b16 v[126:127], v189 offset:42752
v_mfma_f32_16x16x32_bf16 a[156:159], v[132:135], v[116:119], a[156:159]
buffer_load_dwordx4 v23, s[16:19], s27 offen offset:3168 lds
s_add_u32 s27, s27, 0x20000
v_mfma_f32_16x16x32_bf16 a[160:163], v[136:139], v[88:91], a[160:163]
ds_read_b64_tr_b16 v[128:129], v189 offset:33824
v_mfma_f32_16x16x32_bf16 a[160:163], v[140:143], v[92:95], a[160:163]
v_mfma_f32_16x16x32_bf16 a[164:167], v[136:139], v[96:99], a[164:167]
ds_read_b64_tr_b16 v[130:131], v189 offset:34336
v_mfma_f32_16x16x32_bf16 a[164:167], v[140:143], v[100:103], a[164:167]
v_mfma_f32_16x16x32_bf16 a[168:171], v[136:139], v[104:107], a[168:171]
ds_read_b64_tr_b16 v[132:133], v189 offset:42272
v_mfma_f32_16x16x32_bf16 a[168:171], v[140:143], v[108:111], a[168:171]
buffer_load_dwordx4 v[56:59], v1, s[20:23], s26 offen sc1
v_mfma_f32_16x16x32_bf16 a[172:175], v[136:139], v[112:115], a[172:175]
ds_read_b64_tr_b16 v[134:135], v189 offset:42784
v_mfma_f32_16x16x32_bf16 a[172:175], v[140:143], v[116:119], a[172:175]
s_waitcnt lgkmcnt(3)
v_mfma_f32_16x16x32_bf16 a[176:179], v[144:147], v[88:91], a[176:179]
ds_read_b64_tr_b16 v[136:137], v189 offset:33856
v_mfma_f32_16x16x32_bf16 a[176:179], v[148:151], v[92:95], a[176:179]
buffer_load_dwordx4 v[60:63], v1, s[20:23], s26 offen offset:1024 sc1
v_mfma_f32_16x16x32_bf16 a[180:183], v[144:147], v[96:99], a[180:183]
ds_read_b64_tr_b16 v[138:139], v189 offset:34368
v_mfma_f32_16x16x32_bf16 a[180:183], v[148:151], v[100:103], a[180:183]
v_mfma_f32_16x16x32_bf16 a[184:187], v[144:147], v[104:107], a[184:187]
ds_read_b64_tr_b16 v[140:141], v189 offset:42304
v_mfma_f32_16x16x32_bf16 a[184:187], v[148:151], v[108:111], a[184:187]
v_mfma_f32_16x16x32_bf16 a[188:191], v[144:147], v[112:115], a[188:191]
ds_read_b64_tr_b16 v[142:143], v189 offset:42816
v_mfma_f32_16x16x32_bf16 a[188:191], v[148:151], v[116:119], a[188:191]
buffer_load_dwordx4 v[64:67], v2, s[20:23], s26 offen sc1
v_mfma_f32_16x16x32_bf16 a[192:195], v[152:155], v[88:91], a[192:195]
ds_read_b64_tr_b16 v[144:145], v189 offset:33888
v_mfma_f32_16x16x32_bf16 a[192:195], v[156:159], v[92:95], a[192:195]
v_mfma_f32_16x16x32_bf16 a[196:199], v[152:155], v[96:99], a[196:199]
ds_read_b64_tr_b16 v[146:147], v189 offset:34400
v_mfma_f32_16x16x32_bf16 a[196:199], v[156:159], v[100:103], a[196:199]
buffer_load_dwordx4 v[68:71], v2, s[20:23], s26 offen offset:1024 sc1
v_mfma_f32_16x16x32_bf16 a[200:203], v[152:155], v[104:107], a[200:203]
ds_read_b64_tr_b16 v[148:149], v189 offset:42336
v_mfma_f32_16x16x32_bf16 a[200:203], v[156:159], v[108:111], a[200:203]
v_mfma_f32_16x16x32_bf16 a[204:207], v[152:155], v[112:115], a[204:207]
ds_read_b64_tr_b16 v[150:151], v189 offset:42848
v_mfma_f32_16x16x32_bf16 a[204:207], v[156:159], v[116:119], a[204:207]
v_mfma_f32_16x16x32_bf16 a[208:211], v[160:163], v[88:91], a[208:211]
ds_read_b64_tr_b16 v[152:153], v189 offset:33920
v_mfma_f32_16x16x32_bf16 a[208:211], v[164:167], v[92:95], a[208:211]
buffer_load_dwordx4 v[72:75], v3, s[20:23], s26 offen sc1
v_mfma_f32_16x16x32_bf16 a[212:215], v[160:163], v[96:99], a[212:215]
ds_read_b64_tr_b16 v[154:155], v189 offset:34432
v_mfma_f32_16x16x32_bf16 a[212:215], v[164:167], v[100:103], a[212:215]
v_mfma_f32_16x16x32_bf16 a[216:219], v[160:163], v[104:107], a[216:219]
ds_read_b64_tr_b16 v[156:157], v189 offset:42368
v_mfma_f32_16x16x32_bf16 a[216:219], v[164:167], v[108:111], a[216:219]
buffer_load_dwordx4 v[76:79], v3, s[20:23], s26 offen offset:1024 sc1
v_mfma_f32_16x16x32_bf16 a[220:223], v[160:163], v[112:115], a[220:223]
ds_read_b64_tr_b16 v[158:159], v189 offset:42880
v_mfma_f32_16x16x32_bf16 a[220:223], v[164:167], v[116:119], a[220:223]
v_mfma_f32_16x16x32_bf16 a[224:227], v[168:171], v[88:91], a[224:227]
ds_read_b64_tr_b16 v[160:161], v189 offset:33952
v_mfma_f32_16x16x32_bf16 a[224:227], v[172:175], v[92:95], a[224:227]
v_mfma_f32_16x16x32_bf16 a[228:231], v[168:171], v[96:99], a[228:231]
ds_read_b64_tr_b16 v[162:163], v189 offset:34464
v_mfma_f32_16x16x32_bf16 a[228:231], v[172:175], v[100:103], a[228:231]
buffer_load_dwordx4 v[80:83], v4, s[20:23], s26 offen sc1
v_mfma_f32_16x16x32_bf16 a[232:235], v[168:171], v[104:107], a[232:235]
ds_read_b64_tr_b16 v[164:165], v189 offset:42400
v_mfma_f32_16x16x32_bf16 a[232:235], v[172:175], v[108:111], a[232:235]
v_mfma_f32_16x16x32_bf16 a[236:239], v[168:171], v[112:115], a[236:239]
ds_read_b64_tr_b16 v[166:167], v189 offset:42912
v_mfma_f32_16x16x32_bf16 a[236:239], v[172:175], v[116:119], a[236:239]
buffer_load_dwordx4 v[84:87], v4, s[20:23], s26 offen offset:1024 sc1
s_add_u32 s26, s26, 0x4000
v_mfma_f32_16x16x32_bf16 a[240:243], v[176:179], v[88:91], a[240:243]
ds_read_b64_tr_b16 v[168:169], v189 offset:33984
v_mfma_f32_16x16x32_bf16 a[240:243], v[180:183], v[92:95], a[240:243]
v_mfma_f32_16x16x32_bf16 a[244:247], v[176:179], v[96:99], a[244:247]
ds_read_b64_tr_b16 v[170:171], v189 offset:34496
v_mfma_f32_16x16x32_bf16 a[244:247], v[180:183], v[100:103], a[244:247]
v_mfma_f32_16x16x32_bf16 a[248:251], v[176:179], v[104:107], a[248:251]
ds_read_b64_tr_b16 v[172:173], v189 offset:42432
v_mfma_f32_16x16x32_bf16 a[248:251], v[180:183], v[108:111], a[248:251]
v_mfma_f32_16x16x32_bf16 a[252:255], v[176:179], v[112:115], a[252:255]
ds_read_b64_tr_b16 v[174:175], v189 offset:42944
v_mfma_f32_16x16x32_bf16 a[252:255], v[180:183], v[116:119], a[252:255]
s_waitcnt vmcnt(24)
s_barrier
v_cmp_ne_u32 vcc, s33, v184
s_nop 1
s_cmp_lg_u64 vcc, 0
s_cbranch_scc1 OOLpoc_%=
RETpoc_%=:
v_cmp_ne_u32 vcc, s33, v185
s_nop 1
s_cmp_lg_u64 vcc, 0
s_cbranch_scc1 OOLppc1_%=
RETppc1_%=:
v_cmp_ne_u32 vcc, s33, v186
s_nop 1
s_cmp_lg_u64 vcc, 0
s_cbranch_scc1 OOLppc2_%=
RETppc2_%=:
v_cmp_ne_u32 vcc, s33, v187
s_nop 1
s_cmp_lg_u64 vcc, 0
s_cbranch_scc1 OOLppc3_%=
RETppc3_%=:
s_mov_b32 s26, s31
v_mfma_f32_16x16x32_bf16 a[128:131], v[120:123], v[192:195], a[128:131]
ds_read_b64_tr_b16 v[176:177], v189 offset:34016
s_add_u32 m0, s30, 0x177c0
v_mfma_f32_16x16x32_bf16 a[128:131], v[124:127], v[196:199], a[128:131]
buffer_load_dwordx4 v20, s[16:19], s27 offen lds
v_mfma_f32_16x16x32_bf16 a[132:135], v[120:123], v[200:203], a[132:135]
ds_read_b64_tr_b16 v[178:179], v189 offset:34528
v_mfma_f32_16x16x32_bf16 a[132:135], v[124:127], v[204:207], a[132:135]
v_mfma_f32_16x16x32_bf16 a[136:139], v[120:123], v[208:211], a[136:139]
ds_read_b64_tr_b16 v[180:181], v189 offset:42464
v_mfma_f32_16x16x32_bf16 a[136:139], v[124:127], v[212:215], a[136:139]
buffer_load_dwordx4 v21, s[16:19], s27 offen offset:1056 lds
v_mfma_f32_16x16x32_bf16 a[140:143], v[120:123], v[216:219], a[140:143]
ds_read_b64_tr_b16 v[182:183], v189 offset:42976
v_mfma_f32_16x16x32_bf16 a[140:143], v[124:127], v[220:223], a[140:143]
s_waitcnt lgkmcnt(3)
v_mfma_f32_16x16x32_bf16 a[144:147], v[128:131], v[192:195], a[144:147]
ds_read_b64_tr_b16 v[120:121], v190
v_mfma_f32_16x16x32_bf16 a[144:147], v[132:135], v[196:199], a[144:147]
v_mfma_f32_16x16x32_bf16 a[148:151], v[128:131], v[200:203], a[148:151]
)ASM"
R"ASM(ds_read_b64_tr_b16 v[122:123], v190 offset:512
v_mfma_f32_16x16x32_bf16 a[148:151], v[132:135], v[204:207], a[148:151]
buffer_load_dwordx4 v22, s[16:19], s27 offen offset:2112 lds
v_mfma_f32_16x16x32_bf16 a[152:155], v[128:131], v[208:211], a[152:155]
ds_read_b64_tr_b16 v[124:125], v190 offset:8448
v_mfma_f32_16x16x32_bf16 a[152:155], v[132:135], v[212:215], a[152:155]
v_mfma_f32_16x16x32_bf16 a[156:159], v[128:131], v[216:219], a[156:159]
ds_read_b64_tr_b16 v[126:127], v190 offset:8960
v_mfma_f32_16x16x32_bf16 a[156:159], v[132:135], v[220:223], a[156:159]
buffer_load_dwordx4 v23, s[16:19], s27 offen offset:3168 lds
s_add_u32 s27, s27, 0x20000
v_mfma_f32_16x16x32_bf16 a[160:163], v[136:139], v[192:195], a[160:163]
ds_read_b64_tr_b16 v[128:129], v190 offset:32
v_mfma_f32_16x16x32_bf16 a[160:163], v[140:143], v[196:199], a[160:163]
v_mfma_f32_16x16x32_bf16 a[164:167], v[136:139], v[200:203], a[164:167]
ds_read_b64_tr_b16 v[130:131], v190 offset:544
v_mfma_f32_16x16x32_bf16 a[164:167], v[140:143], v[204:207], a[164:167]
v_mfma_f32_16x16x32_bf16 a[168:171], v[136:139], v[208:211], a[168:171]
ds_read_b64_tr_b16 v[132:133], v190 offset:8480
v_mfma_f32_16x16x32_bf16 a[168:171], v[140:143], v[212:215], a[168:171]
buffer_load_dwordx4 v[88:91], v1, s[20:23], s26 offen sc1
v_mfma_f32_16x16x32_bf16 a[172:175], v[136:139], v[216:219], a[172:175]
ds_read_b64_tr_b16 v[134:135], v190 offset:8992
v_mfma_f32_16x16x32_bf16 a[172:175], v[140:143], v[220:223], a[172:175]
v_mfma_f32_16x16x32_bf16 a[176:179], v[144:147], v[192:195], a[176:179]
ds_read_b64_tr_b16 v[136:137], v190 offset:64
v_mfma_f32_16x16x32_bf16 a[176:179], v[148:151], v[196:199], a[176:179]
buffer_load_dwordx4 v[92:95], v1, s[20:23], s26 offen offset:1024 sc1
v_mfma_f32_16x16x32_bf16 a[180:183], v[144:147], v[200:203], a[180:183]
ds_read_b64_tr_b16 v[138:139], v190 offset:576
v_mfma_f32_16x16x32_bf16 a[180:183], v[148:151], v[204:207], a[180:183]
v_mfma_f32_16x16x32_bf16 a[184:187], v[144:147], v[208:211], a[184:187]
ds_read_b64_tr_b16 v[140:141], v190 offset:8512
v_mfma_f32_16x16x32_bf16 a[184:187], v[148:151], v[212:215], a[184:187]
v_mfma_f32_16x16x32_bf16 a[188:191], v[144:147], v[216:219], a[188:191]
ds_read_b64_tr_b16 v[142:143], v190 offset:9024
v_mfma_f32_16x16x32_bf16 a[188:191], v[148:151], v[220:223], a[188:191]
buffer_load_dwordx4 v[96:99], v2, s[20:23], s26 offen sc1
v_mfma_f32_16x16x32_bf16 a[192:195], v[152:155], v[192:195], a[192:195]
ds_read_b64_tr_b16 v[144:145], v190 offset:96
v_mfma_f32_16x16x32_bf16 a[192:195], v[156:159], v[196:199], a[192:195]
v_mfma_f32_16x16x32_bf16 a[196:199], v[152:155], v[200:203], a[196:199]
ds_read_b64_tr_b16 v[146:147], v190 offset:608
v_mfma_f32_16x16x32_bf16 a[196:199], v[156:159], v[204:207], a[196:199]
buffer_load_dwordx4 v[100:103], v2, s[20:23], s26 offen offset:1024 sc1
v_mfma_f32_16x16x32_bf16 a[200:203], v[152:155], v[208:211], a[200:203]
ds_read_b64_tr_b16 v[148:149], v190 offset:8544
v_mfma_f32_16x16x32_bf16 a[200:203], v[156:159], v[212:215], a[200:203]
v_mfma_f32_16x16x32_bf16 a[204:207], v[152:155], v[216:219], a[204:207]
ds_read_b64_tr_b16 v[150:151], v190 offset:9056
v_mfma_f32_16x16x32_bf16 a[204:207], v[156:159], v[220:223], a[204:207]
v_mfma_f32_16x16x32_bf16 a[208:211], v[160:163], v[192:195], a[208:211]
ds_read_b64_tr_b16 v[152:153], v190 offset:128
v_mfma_f32_16x16x32_bf16 a[208:211], v[164:167], v[196:199], a[208:211]
buffer_load_dwordx4 v[104:107], v3, s[20:23], s26 offen sc1
v_mfma_f32_16x16x32_bf16 a[212:215], v[160:163], v[200:203], a[212:215]
ds_read_b64_tr_b16 v[154:155], v190 offset:640
v_mfma_f32_16x16x32_bf16 a[212:215], v[164:167], v[204:207], a[212:215]
v_mfma_f32_16x16x32_bf16 a[216:219], v[160:163], v[208:211], a[216:219]
ds_read_b64_tr_b16 v[156:157], v190 offset:8576
v_mfma_f32_16x16x32_bf16 a[216:219], v[164:167], v[212:215], a[216:219]
buffer_load_dwordx4 v[108:111], v3, s[20:23], s26 offen offset:1024 sc1
v_mfma_f32_16x16x32_bf16 a[220:223], v[160:163], v[216:219], a[220:223]
ds_read_b64_tr_b16 v[158:159], v190 offset:9088
v_mfma_f32_16x16x32_bf16 a[220:223], v[164:167], v[220:223], a[220:223]
v_mfma_f32_16x16x32_bf16 a[224:227], v[168:171], v[192:195], a[224:227]
ds_read_b64_tr_b16 v[160:161], v190 offset:160
v_mfma_f32_16x16x32_bf16 a[224:227], v[172:175], v[196:199], a[224:227]
v_mfma_f32_16x16x32_bf16 a[228:231], v[168:171], v[200:203], a[228:231]
ds_read_b64_tr_b16 v[162:163], v190 offset:672
v_mfma_f32_16x16x32_bf16 a[228:231], v[172:175], v[204:207], a[228:231]
buffer_load_dwordx4 v[112:115], v4, s[20:23], s26 offen sc1
v_mfma_f32_16x16x32_bf16 a[232:235], v[168:171], v[208:211], a[232:235]
ds_read_b64_tr_b16 v[164:165], v190 offset:8608
v_mfma_f32_16x16x32_bf16 a[232:235], v[172:175], v[212:215], a[232:235]
v_mfma_f32_16x16x32_bf16 a[236:239], v[168:171], v[216:219], a[236:239]
ds_read_b64_tr_b16 v[166:167], v190 offset:9120
v_mfma_f32_16x16x32_bf16 a[236:239], v[172:175], v[220:223], a[236:239]
buffer_load_dwordx4 v[116:119], v4, s[20:23], s26 offen offset:1024 sc1
s_add_u32 s26, s26, 0x4000
s_waitcnt lgkmcnt(3)
v_mfma_f32_16x16x32_bf16 a[240:243], v[176:179], v[192:195], a[240:243]
ds_read_b64_tr_b16 v[168:169], v190 offset:192
v_mfma_f32_16x16x32_bf16 a[240:243], v[180:183], v[196:199], a[240:243]
v_mfma_f32_16x16x32_bf16 a[244:247], v[176:179], v[200:203], a[244:247]
ds_read_b64_tr_b16 v[170:171], v190 offset:704
v_mfma_f32_16x16x32_bf16 a[244:247], v[180:183], v[204:207], a[244:247]
v_mfma_f32_16x16x32_bf16 a[248:251], v[176:179], v[208:211], a[248:251]
ds_read_b64_tr_b16 v[172:173], v190 offset:8640
v_mfma_f32_16x16x32_bf16 a[248:251], v[180:183], v[212:215], a[248:251]
v_mfma_f32_16x16x32_bf16 a[252:255], v[176:179], v[216:219], a[252:255]
ds_read_b64_tr_b16 v[174:175], v190 offset:9152
v_mfma_f32_16x16x32_bf16 a[252:255], v[180:183], v[220:223], a[252:255]
s_waitcnt vmcnt(38)
v_mfma_f32_16x16x32_bf16 a[128:131], v[120:123], v[224:227], a[128:131]
ds_read_b64_tr_b16 v[176:177], v190 offset:224
s_add_u32 m0, s30, 0x1b9c0
v_mfma_f32_16x16x32_bf16 a[128:131], v[124:127], v[228:231], a[128:131]
buffer_load_dwordx4 v20, s[16:19], s27 offen lds
v_mfma_f32_16x16x32_bf16 a[132:135], v[120:123], v[232:235], a[132:135]
ds_read_b64_tr_b16 v[178:179], v190 offset:736
v_mfma_f32_16x16x32_bf16 a[132:135], v[124:127], v[236:239], a[132:135]
v_mfma_f32_16x16x32_bf16 a[136:139], v[120:123], v[240:243], a[136:139]
ds_read_b64_tr_b16 v[180:181], v190 offset:8672
v_mfma_f32_16x16x32_bf16 a[136:139], v[124:127], v[244:247], a[136:139]
buffer_load_dwordx4 v21, s[16:19], s27 offen offset:1056 lds
v_mfma_f32_16x16x32_bf16 a[140:143], v[120:123], v[248:251], a[140:143]
ds_read_b64_tr_b16 v[182:183], v190 offset:9184
v_mfma_f32_16x16x32_bf16 a[140:143], v[124:127], v[252:255], a[140:143]
v_mfma_f32_16x16x32_bf16 a[144:147], v[128:131], v[224:227], a[144:147]
ds_read_b64_tr_b16 v[120:121], v188
v_mfma_f32_16x16x32_bf16 a[144:147], v[132:135], v[228:231], a[144:147]
v_mfma_f32_16x16x32_bf16 a[148:151], v[128:131], v[232:235], a[148:151]
ds_read_b64_tr_b16 v[122:123], v188 offset:512
v_mfma_f32_16x16x32_bf16 a[148:151], v[132:135], v[236:239], a[148:151]
buffer_load_dwordx4 v22, s[16:19], s27 offen offset:2112 lds
v_mfma_f32_16x16x32_bf16 a[152:155], v[128:131], v[240:243], a[152:155]
ds_read_b64_tr_b16 v[124:125], v188 offset:8448
v_mfma_f32_16x16x32_bf16 a[152:155], v[132:135], v[244:247], a[152:155]
v_mfma_f32_16x16x32_bf16 a[156:159], v[128:131], v[248:251], a[156:159]
ds_read_b64_tr_b16 v[126:127], v188 offset:8960
v_mfma_f32_16x16x32_bf16 a[156:159], v[132:135], v[252:255], a[156:159]
buffer_load_dwordx4 v23, s[16:19], s27 offen offset:3168 lds
s_add_u32 s27, s27, 0x20000
v_mfma_f32_16x16x32_bf16 a[160:163], v[136:139], v[224:227], a[160:163]
ds_read_b64_tr_b16 v[128:129], v188 offset:32
v_mfma_f32_16x16x32_bf16 a[160:163], v[140:143], v[228:231], a[160:163]
v_mfma_f32_16x16x32_bf16 a[164:167], v[136:139], v[232:235], a[164:167]
ds_read_b64_tr_b16 v[130:131], v188 offset:544
v_mfma_f32_16x16x32_bf16 a[164:167], v[140:143], v[236:239], a[164:167]
v_mfma_f32_16x16x32_bf16 a[168:171], v[136:139], v[240:243], a[168:171]
ds_read_b64_tr_b16 v[132:133], v188 offset:8480
v_mfma_f32_16x16x32_bf16 a[168:171], v[140:143], v[244:247], a[168:171]
buffer_load_dwordx4 v[192:195], v1, s[20:23], s26 offen sc1
v_mfma_f32_16x16x32_bf16 a[172:175], v[136:139], v[248:251], a[172:175]
ds_read_b64_tr_b16 v[134:135], v188 offset:8992
v_mfma_f32_16x16x32_bf16 a[172:175], v[140:143], v[252:255], a[172:175]
v_mfma_f32_16x16x32_bf16 a[176:179], v[144:147], v[224:227], a[176:179]
ds_read_b64_tr_b16 v[136:137], v188 offset:64
v_mfma_f32_16x16x32_bf16 a[176:179], v[148:151], v[228:231], a[176:179]
buffer_load_dwordx4 v[196:199], v1, s[20:23], s26 offen offset:1024 sc1
v_mfma_f32_16x16x32_bf16 a[180:183], v[144:147], v[232:235], a[180:183]
ds_read_b64_tr_b16 v[138:139], v188 offset:576
v_mfma_f32_16x16x32_bf16 a[180:183], v[148:151], v[236:239], a[180:183]
v_mfma_f32_16x16x32_bf16 a[184:187], v[144:147], v[240:243], a[184:187]
ds_read_b64_tr_b16 v[140:141], v188 offset:8512
v_mfma_f32_16x16x32_bf16 a[184:187], v[148:151], v[244:247], a[184:187]
v_mfma_f32_16x16x32_bf16 a[188:191], v[144:147], v[248:251], a[188:191]
ds_read_b64_tr_b16 v[142:143], v188 offset:9024
v_mfma_f32_16x16x32_bf16 a[188:191], v[148:151], v[252:255], a[188:191]
buffer_load_dwordx4 v[200:203], v2, s[20:23], s26 offen sc1
v_mfma_f32_16x16x32_bf16 a[192:195], v[152:155], v[224:227], a[192:195]
ds_read_b64_tr_b16 v[144:145], v188 offset:96
v_mfma_f32_16x16x32_bf16 a[192:195], v[156:159], v[228:231], a[192:195]
v_mfma_f32_16x16x32_bf16 a[196:199], v[152:155], v[232:235], a[196:199]
ds_read_b64_tr_b16 v[146:147], v188 offset:608
v_mfma_f32_16x16x32_bf16 a[196:199], v[156:159], v[236:239], a[196:199]
buffer_load_dwordx4 v[204:207], v2, s[20:23], s26 offen offset:1024 sc1
v_mfma_f32_16x16x32_bf16 a[200:203], v[152:155], v[240:243], a[200:203]
ds_read_b64_tr_b16 v[148:149], v188 offset:8544
v_mfma_f32_16x16x32_bf16 a[200:203], v[156:159], v[244:247], a[200:203]
v_mfma_f32_16x16x32_bf16 a[204:207], v[152:155], v[248:251], a[204:207]
ds_read_b64_tr_b16 v[150:151], v188 offset:9056
v_mfma_f32_16x16x32_bf16 a[204:207], v[156:159], v[252:255], a[204:207]
s_waitcnt lgkmcnt(3)
v_mfma_f32_16x16x32_bf16 a[208:211], v[160:163], v[224:227], a[208:211]
ds_read_b64_tr_b16 v[152:153], v188 offset:128
v_mfma_f32_16x16x32_bf16 a[208:211], v[164:167], v[228:231], a[208:211]
buffer_load_dwordx4 v[208:211], v3, s[20:23], s26 offen sc1
v_mfma_f32_16x16x32_bf16 a[212:215], v[160:163], v[232:235], a[212:215]
ds_read_b64_tr_b16 v[154:155], v188 offset:640
v_mfma_f32_16x16x32_bf16 a[212:215], v[164:167], v[236:239], a[212:215]
v_mfma_f32_16x16x32_bf16 a[216:219], v[160:163], v[240:243], a[216:219]
ds_read_b64_tr_b16 v[156:157], v188 offset:8576
v_mfma_f32_16x16x32_bf16 a[216:219], v[164:167], v[244:247], a[216:219]
buffer_load_dwordx4 v[212:215], v3, s[20:23], s26 offen offset:1024 sc1
v_mfma_f32_16x16x32_bf16 a[220:223], v[160:163], v[248:251], a[220:223]
ds_read_b64_tr_b16 v[158:159], v188 offset:9088
v_mfma_f32_16x16x32_bf16 a[220:223], v[164:167], v[252:255], a[220:223]
v_mfma_f32_16x16x32_bf16 a[224:227], v[168:171], v[224:227], a[224:227]
ds_read_b64_tr_b16 v[160:161], v188 offset:160
v_mfma_f32_16x16x32_bf16 a[224:227], v[172:175], v[228:231], a[224:227]
v_mfma_f32_16x16x32_bf16 a[228:231], v[168:171], v[232:235], a[228:231]
ds_read_b64_tr_b16 v[162:163], v188 offset:672
v_mfma_f32_16x16x32_bf16 a[228:231], v[172:175], v[236:239], a[228:231]
buffer_load_dwordx4 v[216:219], v4, s[20:23], s26 offen sc1
v_mfma_f32_16x16x32_bf16 a[232:235], v[168:171], v[240:243], a[232:235]
ds_read_b64_tr_b16 v[164:165], v188 offset:8608
v_mfma_f32_16x16x32_bf16 a[232:235], v[172:175], v[244:247], a[232:235]
v_mfma_f32_16x16x32_bf16 a[236:239], v[168:171], v[248:251], a[236:239]
ds_read_b64_tr_b16 v[166:167], v188 offset:9120
v_mfma_f32_16x16x32_bf16 a[236:239], v[172:175], v[252:255], a[236:239]
buffer_load_dwordx4 v[220:223], v4, s[20:23], s26 offen offset:1024 sc1
s_add_u32 s26, s26, 0x4000
v_mfma_f32_16x16x32_bf16 a[240:243], v[176:179], v[224:227], a[240:243]
ds_read_b64_tr_b16 v[168:169], v188 offset:192
v_mfma_f32_16x16x32_bf16 a[240:243], v[180:183], v[228:231], a[240:243]
v_mfma_f32_16x16x32_bf16 a[244:247], v[176:179], v[232:235], a[244:247]
ds_read_b64_tr_b16 v[170:171], v188 offset:704
v_mfma_f32_16x16x32_bf16 a[244:247], v[180:183], v[236:239], a[244:247]
v_mfma_f32_16x16x32_bf16 a[248:251], v[176:179], v[240:243], a[248:251]
ds_read_b64_tr_b16 v[172:173], v188 offset:8640
v_mfma_f32_16x16x32_bf16 a[248:251], v[180:183], v[244:247], a[248:251]
v_mfma_f32_16x16x32_bf16 a[252:255], v[176:179], v[248:251], a[252:255]
ds_read_b64_tr_b16 v[174:175], v188 offset:9152
v_mfma_f32_16x16x32_bf16 a[252:255], v[180:183], v[252:255], a[252:255]
s_waitcnt vmcnt(32)
s_barrier
v_mfma_f32_16x16x32_bf16 a[128:131], v[120:123], v[24:27], a[128:131]
ds_read_b64_tr_b16 v[176:177], v188 offset:224
s_add_u32 m0, s30, 0x1fbc0
v_mfma_f32_16x16x32_bf16 a[128:131], v[124:127], v[28:31], a[128:131]
buffer_load_dwordx4 v20, s[16:19], s27 offen lds
v_mfma_f32_16x16x32_bf16 a[132:135], v[120:123], v[32:35], a[132:135]
ds_read_b64_tr_b16 v[178:179], v188 offset:736
v_mfma_f32_16x16x32_bf16 a[132:135], v[124:127], v[36:39], a[132:135]
v_mfma_f32_16x16x32_bf16 a[136:139], v[120:123], v[40:43], a[136:139]
ds_read_b64_tr_b16 v[180:181], v188 offset:8672
v_mfma_f32_16x16x32_bf16 a[136:139], v[124:127], v[44:47], a[136:139]
buffer_load_dwordx4 v21, s[16:19], s27 offen offset:1056 lds
v_mfma_f32_16x16x32_bf16 a[140:143], v[120:123], v[48:51], a[140:143]
ds_read_b64_tr_b16 v[182:183], v188 offset:9184
v_mfma_f32_16x16x32_bf16 a[140:143], v[124:127], v[52:55], a[140:143]
v_mfma_f32_16x16x32_bf16 a[144:147], v[128:131], v[24:27], a[144:147]
ds_read_b64_tr_b16 v[120:121], v188 offset:16896
v_mfma_f32_16x16x32_bf16 a[144:147], v[132:135], v[28:31], a[144:147]
v_mfma_f32_16x16x32_bf16 a[148:151], v[128:131], v[32:35], a[148:151]
ds_read_b64_tr_b16 v[122:123], v188 offset:17408
v_mfma_f32_16x16x32_bf16 a[148:151], v[132:135], v[36:39], a[148:151]
buffer_load_dwordx4 v22, s[16:19], s27 offen offset:2112 lds
v_mfma_f32_16x16x32_bf16 a[152:155], v[128:131], v[40:43], a[152:155]
ds_read_b64_tr_b16 v[124:125], v188 offset:25344
v_mfma_f32_16x16x32_bf16 a[152:155], v[132:135], v[44:47], a[152:155]
v_mfma_f32_16x16x32_bf16 a[156:159], v[128:131], v[48:51], a[156:159]
ds_read_b64_tr_b16 v[126:127], v188 offset:25856
v_mfma_f32_16x16x32_bf16 a[156:159], v[132:135], v[52:55], a[156:159]
buffer_load_dwordx4 v23, s[16:19], s27 offen offset:3168 lds
s_add_u32 s27, s27, 0x20000
v_mfma_f32_16x16x32_bf16 a[160:163], v[136:139], v[24:27], a[160:163]
ds_read_b64_tr_b16 v[128:129], v188 offset:16928
v_mfma_f32_16x16x32_bf16 a[160:163], v[140:143], v[28:31], a[160:163]
v_mfma_f32_16x16x32_bf16 a[164:167], v[136:139], v[32:35], a[164:167]
ds_read_b64_tr_b16 v[130:131], v188 offset:17440
v_mfma_f32_16x16x32_bf16 a[164:167], v[140:143], v[36:39], a[164:167]
v_mfma_f32_16x16x32_bf16 a[168:171], v[136:139], v[40:43], a[168:171]
ds_read_b64_tr_b16 v[132:133], v188 offset:25376
v_mfma_f32_16x16x32_bf16 a[168:171], v[140:143], v[44:47], a[168:171]
buffer_load_dwordx4 v[224:227], v1, s[20:23], s26 offen sc1
v_mfma_f32_16x16x32_bf16 a[172:175], v[136:139], v[48:51], a[172:175]
ds_read_b64_tr_b16 v[134:135], v188 offset:25888
v_mfma_f32_16x16x32_bf16 a[172:175], v[140:143], v[52:55], a[172:175]
s_waitcnt lgkmcnt(3)
v_mfma_f32_16x16x32_bf16 a[176:179], v[144:147], v[24:27], a[176:179]
ds_read_b64_tr_b16 v[136:137], v188 offset:16960
v_mfma_f32_16x16x32_bf16 a[176:179], v[148:151], v[28:31], a[176:179]
buffer_load_dwordx4 v[228:231], v1, s[20:23], s26 offen offset:1024 sc1
v_mfma_f32_16x16x32_bf16 a[180:183], v[144:147], v[32:35], a[180:183]
ds_read_b64_tr_b16 v[138:139], v188 offset:17472
v_mfma_f32_16x16x32_bf16 a[180:183], v[148:151], v[36:39], a[180:183]
v_mfma_f32_16x16x32_bf16 a[184:187], v[144:147], v[40:43], a[184:187]
ds_read_b64_tr_b16 v[140:141], v188 offset:25408
v_mfma_f32_16x16x32_bf16 a[184:187], v[148:151], v[44:47], a[184:187]
v_mfma_f32_16x16x32_bf16 a[188:191], v[144:147], v[48:51], a[188:191]
ds_read_b64_tr_b16 v[142:143], v188 offset:25920
v_mfma_f32_16x16x32_bf16 a[188:191], v[148:151], v[52:55], a[188:191]
buffer_load_dwordx4 v[232:235], v2, s[20:23], s26 offen sc1
v_mfma_f32_16x16x32_bf16 a[192:195], v[152:155], v[24:27], a[192:195]
ds_read_b64_tr_b16 v[144:145], v188 offset:16992
v_mfma_f32_16x16x32_bf16 a[192:195], v[156:159], v[28:31], a[192:195]
v_mfma_f32_16x16x32_bf16 a[196:199], v[152:155], v[32:35], a[196:199]
ds_read_b64_tr_b16 v[146:147], v188 offset:17504
v_mfma_f32_16x16x32_bf16 a[196:199], v[156:159], v[36:39], a[196:199]
buffer_load_dwordx4 v[236:239], v2, s[20:23], s26 offen offset:1024 sc1
v_mfma_f32_16x16x32_bf16 a[200:203], v[152:155], v[40:43], a[200:203]
ds_read_b64_tr_b16 v[148:149], v188 offset:25440
v_mfma_f32_16x16x32_bf16 a[200:203], v[156:159], v[44:47], a[200:203]
v_mfma_f32_16x16x32_bf16 a[204:207], v[152:155], v[48:51], a[204:207]
ds_read_b64_tr_b16 v[150:151], v188 offset:25952
v_mfma_f32_16x16x32_bf16 a[204:207], v[156:159], v[52:55], a[204:207]
v_mfma_f32_16x16x32_bf16 a[208:211], v[160:163], v[24:27], a[208:211]
ds_read_b64_tr_b16 v[152:153], v188 offset:17024
v_mfma_f32_16x16x32_bf16 a[208:211], v[164:167], v[28:31], a[208:211]
buffer_load_dwordx4 v[240:243], v3, s[20:23], s26 offen sc1
v_mfma_f32_16x16x32_bf16 a[212:215], v[160:163], v[32:35], a[212:215]
ds_read_b64_tr_b16 v[154:155], v188 offset:17536
v_mfma_f32_16x16x32_bf16 a[212:215], v[164:167], v[36:39], a[212:215]
v_mfma_f32_16x16x32_bf16 a[216:219], v[160:163], v[40:43], a[216:219]
ds_read_b64_tr_b16 v[156:157], v188 offset:25472
v_mfma_f32_16x16x32_bf16 a[216:219], v[164:167], v[44:47], a[216:219]
buffer_load_dwordx4 v[244:247], v3, s[20:23], s26 offen offset:1024 sc1
v_mfma_f32_16x16x32_bf16 a[220:223], v[160:163], v[48:51], a[220:223]
ds_read_b64_tr_b16 v[158:159], v188 offset:25984
v_mfma_f32_16x16x32_bf16 a[220:223], v[164:167], v[52:55], a[220:223]
v_mfma_f32_16x16x32_bf16 a[224:227], v[168:171], v[24:27], a[224:227]
ds_read_b64_tr_b16 v[160:161], v188 offset:17056
v_mfma_f32_16x16x32_bf16 a[224:227], v[172:175], v[28:31], a[224:227]
v_mfma_f32_16x16x32_bf16 a[228:231], v[168:171], v[32:35], a[228:231]
ds_read_b64_tr_b16 v[162:163], v188 offset:17568
v_mfma_f32_16x16x32_bf16 a[228:231], v[172:175], v[36:39], a[228:231]
buffer_load_dwordx4 v[248:251], v4, s[20:23], s26 offen sc1
v_mfma_f32_16x16x32_bf16 a[232:235], v[168:171], v[40:43], a[232:235]
ds_read_b64_tr_b16 v[164:165], v188 offset:25504
v_mfma_f32_16x16x32_bf16 a[232:235], v[172:175], v[44:47], a[232:235]
v_mfma_f32_16x16x32_bf16 a[236:239], v[168:171], v[48:51], a[236:239]
ds_read_b64_tr_b16 v[166:167], v188 offset:26016
v_mfma_f32_16x16x32_bf16 a[236:239], v[172:175], v[52:55], a[236:239]
buffer_load_dwordx4 v[252:255], v4, s[20:23], s26 offen offset:1024 sc1
s_add_u32 s26, s26, 0x4000
v_mfma_f32_16x16x32_bf16 a[240:243], v[176:179], v[24:27], a[240:243]
ds_read_b64_tr_b16 v[168:169], v188 offset:17088
v_mfma_f32_16x16x32_bf16 a[240:243], v[180:183], v[28:31], a[240:243]
v_mfma_f32_16x16x32_bf16 a[244:247], v[176:179], v[32:35], a[244:247]
ds_read_b64_tr_b16 v[170:171], v188 offset:17600
v_mfma_f32_16x16x32_bf16 a[244:247], v[180:183], v[36:39], a[244:247]
v_mfma_f32_16x16x32_bf16 a[248:251], v[176:179], v[40:43], a[248:251]
ds_read_b64_tr_b16 v[172:173], v188 offset:25536
v_mfma_f32_16x16x32_bf16 a[248:251], v[180:183], v[44:47], a[248:251]
v_mfma_f32_16x16x32_bf16 a[252:255], v[176:179], v[48:51], a[252:255]
ds_read_b64_tr_b16 v[174:175], v188 offset:26048
v_mfma_f32_16x16x32_bf16 a[252:255], v[180:183], v[52:55], a[252:255]
s_waitcnt vmcnt(36)
v_mfma_f32_16x16x32_bf16 a[128:131], v[120:123], v[56:59], a[128:131]
ds_read_b64_tr_b16 v[176:177], v188 offset:17120
s_add_u32 m0, s30, 0x23dc0
v_mfma_f32_16x16x32_bf16 a[128:131], v[124:127], v[60:63], a[128:131]
buffer_load_dwordx4 v20, s[16:19], s27 offen lds
v_mfma_f32_16x16x32_bf16 a[132:135], v[120:123], v[64:67], a[132:135]
ds_read_b64_tr_b16 v[178:179], v188 offset:17632
v_mfma_f32_16x16x32_bf16 a[132:135], v[124:127], v[68:71], a[132:135]
v_mfma_f32_16x16x32_bf16 a[136:139], v[120:123], v[72:75], a[136:139]
ds_read_b64_tr_b16 v[180:181], v188 offset:25568
v_mfma_f32_16x16x32_bf16 a[136:139], v[124:127], v[76:79], a[136:139]
buffer_load_dwordx4 v21, s[16:19], s27 offen offset:1056 lds
v_mfma_f32_16x16x32_bf16 a[140:143], v[120:123], v[80:83], a[140:143]
ds_read_b64_tr_b16 v[182:183], v188 offset:26080
v_mfma_f32_16x16x32_bf16 a[140:143], v[124:127], v[84:87], a[140:143]
s_waitcnt lgkmcnt(3)
v_mfma_f32_16x16x32_bf16 a[144:147], v[128:131], v[56:59], a[144:147]
ds_read_b64_tr_b16 v[120:121], v188 offset:33792
v_mfma_f32_16x16x32_bf16 a[144:147], v[132:135], v[60:63], a[144:147]
v_mfma_f32_16x16x32_bf16 a[148:151], v[128:131], v[64:67], a[148:151]
ds_read_b64_tr_b16 v[122:123], v188 offset:34304
v_mfma_f32_16x16x32_bf16 a[148:151], v[132:135], v[68:71], a[148:151]
buffer_load_dwordx4 v22, s[16:19], s27 offen offset:2112 lds
v_mfma_f32_16x16x32_bf16 a[152:155], v[128:131], v[72:75], a[152:155]
ds_read_b64_tr_b16 v[124:125], v188 offset:42240
v_mfma_f32_16x16x32_bf16 a[152:155], v[132:135], v[76:79], a[152:155]
v_mfma_f32_16x16x32_bf16 a[156:159], v[128:131], v[80:83], a[156:159]
ds_read_b64_tr_b16 v[126:127], v188 offset:42752
v_mfma_f32_16x16x32_bf16 a[156:159], v[132:135], v[84:87], a[156:159]
buffer_load_dwordx4 v23, s[16:19], s27 offen offset:3168 lds
s_add_u32 s27, s27, 0x20000
v_mfma_f32_16x16x32_bf16 a[160:163], v[136:139], v[56:59], a[160:163]
ds_read_b64_tr_b16 v[128:129], v188 offset:33824
v_mfma_f32_16x16x32_bf16 a[160:163], v[140:143], v[60:63], a[160:163]
v_mfma_f32_16x16x32_bf16 a[164:167], v[136:139], v[64:67], a[164:167]
ds_read_b64_tr_b16 v[130:131], v188 offset:34336
v_mfma_f32_16x16x32_bf16 a[164:167], v[140:143], v[68:71], a[164:167]
v_mfma_f32_16x16x32_bf16 a[168:171], v[136:139], v[72:75], a[168:171]
ds_read_b64_tr_b16 v[132:133], v188 offset:42272
v_mfma_f32_16x16x32_bf16 a[168:171], v[140:143], v[76:79], a[168:171]
buffer_load_dwordx4 v[24:27], v1, s[20:23], s26 offen sc1
v_mfma_f32_16x16x32_bf16 a[172:175], v[136:139], v[80:83], a[172:175]
ds_read_b64_tr_b16 v[134:135], v188 offset:42784
v_mfma_f32_16x16x32_bf16 a[172:175], v[140:143], v[84:87], a[172:175]
v_mfma_f32_16x16x32_bf16 a[176:179], v[144:147], v[56:59], a[176:179]
ds_read_b64_tr_b16 v[136:137], v188 offset:33856
v_mfma_f32_16x16x32_bf16 a[176:179], v[148:151], v[60:63], a[176:179]
buffer_load_dwordx4 v[28:31], v1, s[20:23], s26 offen offset:1024 sc1
v_mfma_f32_16x16x32_bf16 a[180:183], v[144:147], v[64:67], a[180:183]
ds_read_b64_tr_b16 v[138:139], v188 offset:34368
v_mfma_f32_16x16x32_bf16 a[180:183], v[148:151], v[68:71], a[180:183]
v_mfma_f32_16x16x32_bf16 a[184:187], v[144:147], v[72:75], a[184:187]
ds_read_b64_tr_b16 v[140:141], v188 offset:42304
v_mfma_f32_16x16x32_bf16 a[184:187], v[148:151], v[76:79], a[184:187]
v_mfma_f32_16x16x32_bf16 a[188:191], v[144:147], v[80:83], a[188:191]
ds_read_b64_tr_b16 v[142:143], v188 offset:42816
v_mfma_f32_16x16x32_bf16 a[188:191], v[148:151], v[84:87], a[188:191]
buffer_load_dwordx4 v[32:35], v2, s[20:23], s26 offen sc1
v_mfma_f32_16x16x32_bf16 a[192:195], v[152:155], v[56:59], a[192:195]
ds_read_b64_tr_b16 v[144:145], v188 offset:33888
v_mfma_f32_16x16x32_bf16 a[192:195], v[156:159], v[60:63], a[192:195]
v_mfma_f32_16x16x32_bf16 a[196:199], v[152:155], v[64:67], a[196:199]
ds_read_b64_tr_b16 v[146:147], v188 offset:34400
v_mfma_f32_16x16x32_bf16 a[196:199], v[156:159], v[68:71], a[196:199]
buffer_load_dwordx4 v[36:39], v2, s[20:23], s26 offen offset:1024 sc1
v_mfma_f32_16x16x32_bf16 a[200:203], v[152:155], v[72:75], a[200:203]
ds_read_b64_tr_b16 v[148:149], v188 offset:42336
v_mfma_f32_16x16x32_bf16 a[200:203], v[156:159], v[76:79], a[200:203]
v_mfma_f32_16x16x32_bf16 a[204:207], v[152:155], v[80:83], a[204:207]
ds_read_b64_tr_b16 v[150:151], v188 offset:42848
v_mfma_f32_16x16x32_bf16 a[204:207], v[156:159], v[84:87], a[204:207]
v_mfma_f32_16x16x32_bf16 a[208:211], v[160:163], v[56:59], a[208:211]
ds_read_b64_tr_b16 v[152:153], v188 offset:33920
v_mfma_f32_16x16x32_bf16 a[208:211], v[164:167], v[60:63], a[208:211]
buffer_load_dwordx4 v[40:43], v3, s[20:23], s26 offen sc1
v_mfma_f32_16x16x32_bf16 a[212:215], v[160:163], v[64:67], a[212:215]
ds_read_b64_tr_b16 v[154:155], v188 offset:34432
v_mfma_f32_16x16x32_bf16 a[212:215], v[164:167], v[68:71], a[212:215]
v_mfma_f32_16x16x32_bf16 a[216:219], v[160:163], v[72:75], a[216:219]
)ASM"
R"ASM(ds_read_b64_tr_b16 v[156:157], v188 offset:42368
v_mfma_f32_16x16x32_bf16 a[216:219], v[164:167], v[76:79], a[216:219]
buffer_load_dwordx4 v[44:47], v3, s[20:23], s26 offen offset:1024 sc1
v_mfma_f32_16x16x32_bf16 a[220:223], v[160:163], v[80:83], a[220:223]
ds_read_b64_tr_b16 v[158:159], v188 offset:42880
v_mfma_f32_16x16x32_bf16 a[220:223], v[164:167], v[84:87], a[220:223]
v_mfma_f32_16x16x32_bf16 a[224:227], v[168:171], v[56:59], a[224:227]
ds_read_b64_tr_b16 v[160:161], v188 offset:33952
v_mfma_f32_16x16x32_bf16 a[224:227], v[172:175], v[60:63], a[224:227]
v_mfma_f32_16x16x32_bf16 a[228:231], v[168:171], v[64:67], a[228:231]
ds_read_b64_tr_b16 v[162:163], v188 offset:34464
v_mfma_f32_16x16x32_bf16 a[228:231], v[172:175], v[68:71], a[228:231]
buffer_load_dwordx4 v[48:51], v4, s[20:23], s26 offen sc1
v_mfma_f32_16x16x32_bf16 a[232:235], v[168:171], v[72:75], a[232:235]
ds_read_b64_tr_b16 v[164:165], v188 offset:42400
v_mfma_f32_16x16x32_bf16 a[232:235], v[172:175], v[76:79], a[232:235]
v_mfma_f32_16x16x32_bf16 a[236:239], v[168:171], v[80:83], a[236:239]
ds_read_b64_tr_b16 v[166:167], v188 offset:42912
v_mfma_f32_16x16x32_bf16 a[236:239], v[172:175], v[84:87], a[236:239]
buffer_load_dwordx4 v[52:55], v4, s[20:23], s26 offen offset:1024 sc1
s_add_u32 s26, s26, 0x4000
s_waitcnt lgkmcnt(3)
v_mfma_f32_16x16x32_bf16 a[240:243], v[176:179], v[56:59], a[240:243]
ds_read_b64_tr_b16 v[168:169], v188 offset:33984
v_mfma_f32_16x16x32_bf16 a[240:243], v[180:183], v[60:63], a[240:243]
v_mfma_f32_16x16x32_bf16 a[244:247], v[176:179], v[64:67], a[244:247]
ds_read_b64_tr_b16 v[170:171], v188 offset:34496
v_mfma_f32_16x16x32_bf16 a[244:247], v[180:183], v[68:71], a[244:247]
v_mfma_f32_16x16x32_bf16 a[248:251], v[176:179], v[72:75], a[248:251]
ds_read_b64_tr_b16 v[172:173], v188 offset:42432
v_mfma_f32_16x16x32_bf16 a[248:251], v[180:183], v[76:79], a[248:251]
v_mfma_f32_16x16x32_bf16 a[252:255], v[176:179], v[80:83], a[252:255]
ds_read_b64_tr_b16 v[174:175], v188 offset:42944
v_mfma_f32_16x16x32_bf16 a[252:255], v[180:183], v[84:87], a[252:255]
s_waitcnt vmcnt(32)
s_barrier
v_mfma_f32_16x16x32_bf16 a[0:3], v[120:123], v[88:91], 0
ds_read_b64_tr_b16 v[176:177], v188 offset:34016
s_add_u32 m0, s30, 0xb1c0
v_mfma_f32_16x16x32_bf16 a[0:3], v[124:127], v[92:95], a[0:3]
buffer_load_dwordx4 v20, s[16:19], s27 offen lds
v_mfma_f32_16x16x32_bf16 a[4:7], v[120:123], v[96:99], 0
ds_read_b64_tr_b16 v[178:179], v188 offset:34528
s_add_u32 s37, s74, 1
v_mfma_f32_16x16x32_bf16 a[4:7], v[124:127], v[100:103], a[4:7]
s_and_b32 s37, s37, 3
s_lshl_b32 s37, s37, 1
v_mfma_f32_16x16x32_bf16 a[8:11], v[120:123], v[104:107], 0
ds_read_b64_tr_b16 v[180:181], v188 offset:42464
s_and_b32 s35, s5, 0xf9
v_mfma_f32_16x16x32_bf16 a[8:11], v[124:127], v[108:111], a[8:11]
buffer_load_dwordx4 v21, s[16:19], s27 offen offset:1056 lds
v_mfma_f32_16x16x32_bf16 a[12:15], v[120:123], v[112:115], 0
ds_read_b64_tr_b16 v[182:183], v188 offset:42976
s_or_b32 s37, s37, s35
v_mfma_f32_16x16x32_bf16 a[12:15], v[124:127], v[116:119], a[12:15]
s_mul_i32 s37, s37, 0x18000
s_mul_i32 s35, s4, 0x6000
v_mfma_f32_16x16x32_bf16 a[16:19], v[128:131], v[88:91], 0
ds_read_b64_tr_b16 v[120:121], v189
s_add_u32 s37, s37, s35
v_mfma_f32_16x16x32_bf16 a[16:19], v[132:135], v[92:95], a[16:19]
s_add_u32 s37, s37, 0x2302000
v_accvgpr_read_b32 v6, a132
v_mfma_f32_16x16x32_bf16 a[20:23], v[128:131], v[96:99], 0
ds_read_b64_tr_b16 v[122:123], v189 offset:512
v_accvgpr_read_b32 v7, a133
v_mfma_f32_16x16x32_bf16 a[20:23], v[132:135], v[100:103], a[20:23]
buffer_load_dwordx4 v22, s[16:19], s27 offen offset:2112 lds
v_mfma_f32_16x16x32_bf16 a[24:27], v[128:131], v[104:107], 0
ds_read_b64_tr_b16 v[124:125], v189 offset:8448
v_accvgpr_read_b32 v8, a134
v_mfma_f32_16x16x32_bf16 a[24:27], v[132:135], v[108:111], a[24:27]
v_accvgpr_read_b32 v9, a135
v_accvgpr_read_b32 v10, a148
v_mfma_f32_16x16x32_bf16 a[28:31], v[128:131], v[112:115], 0
ds_read_b64_tr_b16 v[126:127], v189 offset:8960
v_accvgpr_read_b32 v11, a149
v_mfma_f32_16x16x32_bf16 a[28:31], v[132:135], v[116:119], a[28:31]
buffer_load_dwordx4 v23, s[16:19], s27 offen offset:3168 lds
s_add_u32 s27, s27, 0x20000
v_mfma_f32_16x16x32_bf16 a[32:35], v[136:139], v[88:91], 0
ds_read_b64_tr_b16 v[128:129], v189 offset:32
v_accvgpr_read_b32 v12, a150
v_mfma_f32_16x16x32_bf16 a[32:35], v[140:143], v[92:95], a[32:35]
v_accvgpr_read_b32 v13, a151
v_cvt_pk_bf16_f32 v6, v6, v7
v_mfma_f32_16x16x32_bf16 a[36:39], v[136:139], v[96:99], 0
ds_read_b64_tr_b16 v[130:131], v189 offset:544
v_cvt_pk_bf16_f32 v7, v8, v9
v_mfma_f32_16x16x32_bf16 a[36:39], v[140:143], v[100:103], a[36:39]
v_cvt_pk_bf16_f32 v8, v10, v11
v_cvt_pk_bf16_f32 v9, v12, v13
v_mfma_f32_16x16x32_bf16 a[40:43], v[136:139], v[104:107], 0
buffer_store_dwordx4 v[6:9], v0, s[76:79], s37 offen sc1
v_mfma_f32_16x16x32_bf16 a[40:43], v[140:143], v[108:111], a[40:43]
ds_read_b64_tr_b16 v[132:133], v189 offset:8480
v_accvgpr_read_b32 v6, a164
v_mfma_f32_16x16x32_bf16 a[44:47], v[136:139], v[112:115], 0
ds_read_b64_tr_b16 v[134:135], v189 offset:8992
v_accvgpr_read_b32 v7, a165
v_mfma_f32_16x16x32_bf16 a[44:47], v[140:143], v[116:119], a[44:47]
buffer_load_dwordx4 v[56:59], v1, s[20:23], s26 offen sc1
v_mfma_f32_16x16x32_bf16 a[48:51], v[144:147], v[88:91], 0
ds_read_b64_tr_b16 v[136:137], v189 offset:64
v_accvgpr_read_b32 v8, a166
v_mfma_f32_16x16x32_bf16 a[48:51], v[148:151], v[92:95], a[48:51]
buffer_load_dwordx4 v[60:63], v1, s[20:23], s26 offen offset:1024 sc1
v_mfma_f32_16x16x32_bf16 a[52:55], v[144:147], v[96:99], 0
ds_read_b64_tr_b16 v[138:139], v189 offset:576
v_accvgpr_read_b32 v9, a167
v_mfma_f32_16x16x32_bf16 a[52:55], v[148:151], v[100:103], a[52:55]
v_accvgpr_read_b32 v10, a180
v_accvgpr_read_b32 v11, a181
v_mfma_f32_16x16x32_bf16 a[56:59], v[144:147], v[104:107], 0
ds_read_b64_tr_b16 v[140:141], v189 offset:8512
v_accvgpr_read_b32 v12, a182
v_mfma_f32_16x16x32_bf16 a[56:59], v[148:151], v[108:111], a[56:59]
v_accvgpr_read_b32 v13, a183
v_cvt_pk_bf16_f32 v6, v6, v7
v_mfma_f32_16x16x32_bf16 a[60:63], v[144:147], v[112:115], 0
ds_read_b64_tr_b16 v[142:143], v189 offset:9024
v_cvt_pk_bf16_f32 v7, v8, v9
v_mfma_f32_16x16x32_bf16 a[60:63], v[148:151], v[116:119], a[60:63]
buffer_load_dwordx4 v[64:67], v2, s[20:23], s26 offen sc1
v_mfma_f32_16x16x32_bf16 a[64:67], v[152:155], v[88:91], 0
ds_read_b64_tr_b16 v[144:145], v189 offset:96
v_cvt_pk_bf16_f32 v8, v10, v11
v_mfma_f32_16x16x32_bf16 a[64:67], v[156:159], v[92:95], a[64:67]
v_cvt_pk_bf16_f32 v9, v12, v13
v_mfma_f32_16x16x32_bf16 a[68:71], v[152:155], v[96:99], 0
ds_read_b64_tr_b16 v[146:147], v189 offset:608
v_mfma_f32_16x16x32_bf16 a[68:71], v[156:159], v[100:103], a[68:71]
buffer_load_dwordx4 v[68:71], v2, s[20:23], s26 offen offset:1024 sc1
v_mfma_f32_16x16x32_bf16 a[72:75], v[152:155], v[104:107], 0
ds_read_b64_tr_b16 v[148:149], v189 offset:8544
v_mfma_f32_16x16x32_bf16 a[72:75], v[156:159], v[108:111], a[72:75]
buffer_store_dwordx4 v[6:9], v0, s[76:79], s37 offen offset:1024 sc1
v_mfma_f32_16x16x32_bf16 a[76:79], v[152:155], v[112:115], 0
ds_read_b64_tr_b16 v[150:151], v189 offset:9056
v_mfma_f32_16x16x32_bf16 a[76:79], v[156:159], v[116:119], a[76:79]
s_waitcnt lgkmcnt(3)
v_mfma_f32_16x16x32_bf16 a[80:83], v[160:163], v[88:91], 0
ds_read_b64_tr_b16 v[152:153], v189 offset:128
v_mfma_f32_16x16x32_bf16 a[80:83], v[164:167], v[92:95], a[80:83]
buffer_load_dwordx4 v[72:75], v3, s[20:23], s26 offen sc1
v_mfma_f32_16x16x32_bf16 a[84:87], v[160:163], v[96:99], 0
ds_read_b64_tr_b16 v[154:155], v189 offset:640
v_accvgpr_read_b32 v6, a196
v_mfma_f32_16x16x32_bf16 a[84:87], v[164:167], v[100:103], a[84:87]
v_accvgpr_read_b32 v7, a197
v_accvgpr_read_b32 v8, a198
v_mfma_f32_16x16x32_bf16 a[88:91], v[160:163], v[104:107], 0
ds_read_b64_tr_b16 v[156:157], v189 offset:8576
v_accvgpr_read_b32 v9, a199
v_mfma_f32_16x16x32_bf16 a[88:91], v[164:167], v[108:111], a[88:91]
buffer_load_dwordx4 v[76:79], v3, s[20:23], s26 offen offset:1024 sc1
v_mfma_f32_16x16x32_bf16 a[92:95], v[160:163], v[112:115], 0
ds_read_b64_tr_b16 v[158:159], v189 offset:9088
v_accvgpr_read_b32 v10, a212
v_mfma_f32_16x16x32_bf16 a[92:95], v[164:167], v[116:119], a[92:95]
v_accvgpr_read_b32 v11, a213
v_accvgpr_read_b32 v12, a214
v_mfma_f32_16x16x32_bf16 a[96:99], v[168:171], v[88:91], 0
ds_read_b64_tr_b16 v[160:161], v189 offset:160
v_accvgpr_read_b32 v13, a215
v_mfma_f32_16x16x32_bf16 a[96:99], v[172:175], v[92:95], a[96:99]
v_cvt_pk_bf16_f32 v6, v6, v7
v_cvt_pk_bf16_f32 v7, v8, v9
v_mfma_f32_16x16x32_bf16 a[100:103], v[168:171], v[96:99], 0
ds_read_b64_tr_b16 v[162:163], v189 offset:672
v_cvt_pk_bf16_f32 v8, v10, v11
v_mfma_f32_16x16x32_bf16 a[100:103], v[172:175], v[100:103], a[100:103]
buffer_load_dwordx4 v[80:83], v4, s[20:23], s26 offen sc1
v_mfma_f32_16x16x32_bf16 a[104:107], v[168:171], v[104:107], 0
ds_read_b64_tr_b16 v[164:165], v189 offset:8608
v_cvt_pk_bf16_f32 v9, v12, v13
v_mfma_f32_16x16x32_bf16 a[104:107], v[172:175], v[108:111], a[104:107]
buffer_store_dwordx4 v[6:9], v0, s[76:79], s37 offen offset:2048 sc1
v_mfma_f32_16x16x32_bf16 a[108:111], v[168:171], v[112:115], 0
ds_read_b64_tr_b16 v[166:167], v189 offset:9120
v_mfma_f32_16x16x32_bf16 a[108:111], v[172:175], v[116:119], a[108:111]
buffer_load_dwordx4 v[84:87], v4, s[20:23], s26 offen offset:1024 sc1
s_add_u32 s26, s26, 0x4000
v_mfma_f32_16x16x32_bf16 a[112:115], v[176:179], v[88:91], 0
ds_read_b64_tr_b16 v[168:169], v189 offset:192
v_mfma_f32_16x16x32_bf16 a[112:115], v[180:183], v[92:95], a[112:115]
v_mfma_f32_16x16x32_bf16 a[116:119], v[176:179], v[96:99], 0
ds_read_b64_tr_b16 v[170:171], v189 offset:704
v_mfma_f32_16x16x32_bf16 a[116:119], v[180:183], v[100:103], a[116:119]
v_mfma_f32_16x16x32_bf16 a[120:123], v[176:179], v[104:107], 0
ds_read_b64_tr_b16 v[172:173], v189 offset:8640
v_mfma_f32_16x16x32_bf16 a[120:123], v[180:183], v[108:111], a[120:123]
v_mfma_f32_16x16x32_bf16 a[124:127], v[176:179], v[112:115], 0
ds_read_b64_tr_b16 v[174:175], v189 offset:9152
v_mfma_f32_16x16x32_bf16 a[124:127], v[180:183], v[116:119], a[124:127]
s_waitcnt vmcnt(39)
v_mfma_f32_16x16x32_bf16 a[0:3], v[120:123], v[192:195], a[0:3]
ds_read_b64_tr_b16 v[176:177], v189 offset:224
s_add_u32 m0, s30, 0xf3c0
v_mfma_f32_16x16x32_bf16 a[0:3], v[124:127], v[196:199], a[0:3]
buffer_load_dwordx4 v20, s[16:19], s27 offen lds
v_mfma_f32_16x16x32_bf16 a[4:7], v[120:123], v[200:203], a[4:7]
ds_read_b64_tr_b16 v[178:179], v189 offset:736
v_accvgpr_read_b32 v6, a228
v_mfma_f32_16x16x32_bf16 a[4:7], v[124:127], v[204:207], a[4:7]
v_accvgpr_read_b32 v7, a229
v_accvgpr_read_b32 v8, a230
v_mfma_f32_16x16x32_bf16 a[8:11], v[120:123], v[208:211], a[8:11]
ds_read_b64_tr_b16 v[180:181], v189 offset:8672
v_accvgpr_read_b32 v9, a231
v_mfma_f32_16x16x32_bf16 a[8:11], v[124:127], v[212:215], a[8:11]
buffer_load_dwordx4 v21, s[16:19], s27 offen offset:1056 lds
v_mfma_f32_16x16x32_bf16 a[12:15], v[120:123], v[216:219], a[12:15]
ds_read_b64_tr_b16 v[182:183], v189 offset:9184
v_accvgpr_read_b32 v10, a244
v_mfma_f32_16x16x32_bf16 a[12:15], v[124:127], v[220:223], a[12:15]
v_accvgpr_read_b32 v11, a245
v_accvgpr_read_b32 v12, a246
v_mfma_f32_16x16x32_bf16 a[16:19], v[128:131], v[192:195], a[16:19]
ds_read_b64_tr_b16 v[120:121], v189 offset:16896
v_accvgpr_read_b32 v13, a247
v_mfma_f32_16x16x32_bf16 a[16:19], v[132:135], v[196:199], a[16:19]
v_cvt_pk_bf16_f32 v6, v6, v7
v_cvt_pk_bf16_f32 v7, v8, v9
v_mfma_f32_16x16x32_bf16 a[20:23], v[128:131], v[200:203], a[20:23]
ds_read_b64_tr_b16 v[122:123], v189 offset:17408
v_cvt_pk_bf16_f32 v8, v10, v11
v_mfma_f32_16x16x32_bf16 a[20:23], v[132:135], v[204:207], a[20:23]
buffer_load_dwordx4 v22, s[16:19], s27 offen offset:2112 lds
v_mfma_f32_16x16x32_bf16 a[24:27], v[128:131], v[208:211], a[24:27]
ds_read_b64_tr_b16 v[124:125], v189 offset:25344
v_cvt_pk_bf16_f32 v9, v12, v13
v_mfma_f32_16x16x32_bf16 a[24:27], v[132:135], v[212:215], a[24:27]
buffer_store_dwordx4 v[6:9], v0, s[76:79], s37 offen offset:3072 sc1
v_mfma_f32_16x16x32_bf16 a[28:31], v[128:131], v[216:219], a[28:31]
ds_read_b64_tr_b16 v[126:127], v189 offset:25856
v_mfma_f32_16x16x32_bf16 a[28:31], v[132:135], v[220:223], a[28:31]
buffer_load_dwordx4 v23, s[16:19], s27 offen offset:3168 lds
s_add_u32 s27, s27, 0x20000
v_mfma_f32_16x16x32_bf16 a[32:35], v[136:139], v[192:195], a[32:35]
ds_read_b64_tr_b16 v[128:129], v189 offset:16928
v_mfma_f32_16x16x32_bf16 a[32:35], v[140:143], v[196:199], a[32:35]
v_mfma_f32_16x16x32_bf16 a[36:39], v[136:139], v[200:203], a[36:39]
ds_read_b64_tr_b16 v[130:131], v189 offset:17440
v_mfma_f32_16x16x32_bf16 a[36:39], v[140:143], v[204:207], a[36:39]
v_mfma_f32_16x16x32_bf16 a[40:43], v[136:139], v[208:211], a[40:43]
ds_read_b64_tr_b16 v[132:133], v189 offset:25376
s_add_u32 s37, s74, 2
v_mfma_f32_16x16x32_bf16 a[40:43], v[140:143], v[212:215], a[40:43]
buffer_load_dwordx4 v[88:91], v1, s[20:23], s26 offen sc1
v_mfma_f32_16x16x32_bf16 a[44:47], v[136:139], v[216:219], a[44:47]
ds_read_b64_tr_b16 v[134:135], v189 offset:25888
s_and_b32 s37, s37, 3
v_mfma_f32_16x16x32_bf16 a[44:47], v[140:143], v[220:223], a[44:47]
s_lshl_b32 s37, s37, 1
s_and_b32 s35, s5, 0xf9
s_waitcnt lgkmcnt(3)
v_mfma_f32_16x16x32_bf16 a[48:51], v[144:147], v[192:195], a[48:51]
ds_read_b64_tr_b16 v[136:137], v189 offset:16960
s_or_b32 s37, s37, s35
v_mfma_f32_16x16x32_bf16 a[48:51], v[148:151], v[196:199], a[48:51]
buffer_load_dwordx4 v[92:95], v1, s[20:23], s26 offen offset:1024 sc1
v_mfma_f32_16x16x32_bf16 a[52:55], v[144:147], v[200:203], a[52:55]
ds_read_b64_tr_b16 v[138:139], v189 offset:17472
s_mul_i32 s37, s37, 0x18000
v_mfma_f32_16x16x32_bf16 a[52:55], v[148:151], v[204:207], a[52:55]
s_mul_i32 s35, s4, 0x6000
s_add_u32 s37, s37, s35
v_mfma_f32_16x16x32_bf16 a[56:59], v[144:147], v[208:211], a[56:59]
ds_read_b64_tr_b16 v[140:141], v189 offset:25408
s_add_u32 s37, s37, 0x2301000
v_mfma_f32_16x16x32_bf16 a[56:59], v[148:151], v[212:215], a[56:59]
v_accvgpr_read_b32 v6, a136
v_accvgpr_read_b32 v7, a137
v_mfma_f32_16x16x32_bf16 a[60:63], v[144:147], v[216:219], a[60:63]
ds_read_b64_tr_b16 v[142:143], v189 offset:25920
v_accvgpr_read_b32 v8, a138
v_mfma_f32_16x16x32_bf16 a[60:63], v[148:151], v[220:223], a[60:63]
buffer_load_dwordx4 v[96:99], v2, s[20:23], s26 offen sc1
v_mfma_f32_16x16x32_bf16 a[64:67], v[152:155], v[192:195], a[64:67]
ds_read_b64_tr_b16 v[144:145], v189 offset:16992
v_accvgpr_read_b32 v9, a139
v_mfma_f32_16x16x32_bf16 a[64:67], v[156:159], v[196:199], a[64:67]
v_accvgpr_read_b32 v10, a152
v_accvgpr_read_b32 v11, a153
v_mfma_f32_16x16x32_bf16 a[68:71], v[152:155], v[200:203], a[68:71]
ds_read_b64_tr_b16 v[146:147], v189 offset:17504
v_accvgpr_read_b32 v12, a154
v_mfma_f32_16x16x32_bf16 a[68:71], v[156:159], v[204:207], a[68:71]
buffer_load_dwordx4 v[100:103], v2, s[20:23], s26 offen offset:1024 sc1
v_mfma_f32_16x16x32_bf16 a[72:75], v[152:155], v[208:211], a[72:75]
ds_read_b64_tr_b16 v[148:149], v189 offset:25440
v_accvgpr_read_b32 v13, a155
v_mfma_f32_16x16x32_bf16 a[72:75], v[156:159], v[212:215], a[72:75]
v_cvt_pk_bf16_f32 v6, v6, v7
v_cvt_pk_bf16_f32 v7, v8, v9
v_mfma_f32_16x16x32_bf16 a[76:79], v[152:155], v[216:219], a[76:79]
ds_read_b64_tr_b16 v[150:151], v189 offset:25952
v_cvt_pk_bf16_f32 v8, v10, v11
v_mfma_f32_16x16x32_bf16 a[76:79], v[156:159], v[220:223], a[76:79]
v_cvt_pk_bf16_f32 v9, v12, v13
v_mfma_f32_16x16x32_bf16 a[80:83], v[160:163], v[192:195], a[80:83]
ds_read_b64_tr_b16 v[152:153], v189 offset:17024
v_mfma_f32_16x16x32_bf16 a[80:83], v[164:167], v[196:199], a[80:83]
buffer_store_dwordx4 v[6:9], v0, s[76:79], s37 offen sc1
v_mfma_f32_16x16x32_bf16 a[84:87], v[160:163], v[200:203], a[84:87]
ds_read_b64_tr_b16 v[154:155], v189 offset:17536
v_accvgpr_read_b32 v6, a168
v_mfma_f32_16x16x32_bf16 a[84:87], v[164:167], v[204:207], a[84:87]
buffer_load_dwordx4 v[104:107], v3, s[20:23], s26 offen sc1
v_mfma_f32_16x16x32_bf16 a[88:91], v[160:163], v[208:211], a[88:91]
ds_read_b64_tr_b16 v[156:157], v189 offset:25472
v_accvgpr_read_b32 v7, a169
v_mfma_f32_16x16x32_bf16 a[88:91], v[164:167], v[212:215], a[88:91]
buffer_load_dwordx4 v[108:111], v3, s[20:23], s26 offen offset:1024 sc1
v_mfma_f32_16x16x32_bf16 a[92:95], v[160:163], v[216:219], a[92:95]
ds_read_b64_tr_b16 v[158:159], v189 offset:25984
v_accvgpr_read_b32 v8, a170
v_mfma_f32_16x16x32_bf16 a[92:95], v[164:167], v[220:223], a[92:95]
v_accvgpr_read_b32 v9, a171
v_accvgpr_read_b32 v10, a184
v_mfma_f32_16x16x32_bf16 a[96:99], v[168:171], v[192:195], a[96:99]
ds_read_b64_tr_b16 v[160:161], v189 offset:17056
v_accvgpr_read_b32 v11, a185
v_mfma_f32_16x16x32_bf16 a[96:99], v[172:175], v[196:199], a[96:99]
v_accvgpr_read_b32 v12, a186
v_accvgpr_read_b32 v13, a187
v_mfma_f32_16x16x32_bf16 a[100:103], v[168:171], v[200:203], a[100:103]
ds_read_b64_tr_b16 v[162:163], v189 offset:17568
v_cvt_pk_bf16_f32 v6, v6, v7
v_mfma_f32_16x16x32_bf16 a[100:103], v[172:175], v[204:207], a[100:103]
buffer_load_dwordx4 v[112:115], v4, s[20:23], s26 offen sc1
v_mfma_f32_16x16x32_bf16 a[104:107], v[168:171], v[208:211], a[104:107]
ds_read_b64_tr_b16 v[164:165], v189 offset:25504
v_cvt_pk_bf16_f32 v7, v8, v9
v_mfma_f32_16x16x32_bf16 a[104:107], v[172:175], v[212:215], a[104:107]
v_cvt_pk_bf16_f32 v8, v10, v11
v_cvt_pk_bf16_f32 v9, v12, v13
v_mfma_f32_16x16x32_bf16 a[108:111], v[168:171], v[216:219], a[108:111]
ds_read_b64_tr_b16 v[166:167], v189 offset:26016
v_mfma_f32_16x16x32_bf16 a[108:111], v[172:175], v[220:223], a[108:111]
buffer_load_dwordx4 v[116:119], v4, s[20:23], s26 offen offset:1024 sc1
s_add_u32 s26, s26, 0x4000
v_mfma_f32_16x16x32_bf16 a[112:115], v[176:179], v[192:195], a[112:115]
ds_read_b64_tr_b16 v[168:169], v189 offset:17088
v_mfma_f32_16x16x32_bf16 a[112:115], v[180:183], v[196:199], a[112:115]
buffer_store_dwordx4 v[6:9], v0, s[76:79], s37 offen offset:1024 sc1
v_mfma_f32_16x16x32_bf16 a[116:119], v[176:179], v[200:203], a[116:119]
ds_read_b64_tr_b16 v[170:171], v189 offset:17600
v_mfma_f32_16x16x32_bf16 a[116:119], v[180:183], v[204:207], a[116:119]
v_mfma_f32_16x16x32_bf16 a[120:123], v[176:179], v[208:211], a[120:123]
ds_read_b64_tr_b16 v[172:173], v189 offset:25536
v_mfma_f32_16x16x32_bf16 a[120:123], v[180:183], v[212:215], a[120:123]
v_mfma_f32_16x16x32_bf16 a[124:127], v[176:179], v[216:219], a[124:127]
ds_read_b64_tr_b16 v[174:175], v189 offset:26048
v_mfma_f32_16x16x32_bf16 a[124:127], v[180:183], v[220:223], a[124:127]
s_waitcnt vmcnt(38)
s_barrier
v_mfma_f32_16x16x32_bf16 a[0:3], v[120:123], v[224:227], a[0:3]
ds_read_b64_tr_b16 v[176:177], v189 offset:17120
s_add_u32 m0, s30, 0x135c0
v_mfma_f32_16x16x32_bf16 a[0:3], v[124:127], v[228:231], a[0:3]
buffer_load_dwordx4 v20, s[16:19], s27 offen lds
v_mfma_f32_16x16x32_bf16 a[4:7], v[120:123], v[232:235], a[4:7]
ds_read_b64_tr_b16 v[178:179], v189 offset:17632
v_accvgpr_read_b32 v6, a200
v_mfma_f32_16x16x32_bf16 a[4:7], v[124:127], v[236:239], a[4:7]
v_accvgpr_read_b32 v7, a201
v_accvgpr_read_b32 v8, a202
v_mfma_f32_16x16x32_bf16 a[8:11], v[120:123], v[240:243], a[8:11]
ds_read_b64_tr_b16 v[180:181], v189 offset:25568
v_accvgpr_read_b32 v9, a203
v_mfma_f32_16x16x32_bf16 a[8:11], v[124:127], v[244:247], a[8:11]
buffer_load_dwordx4 v21, s[16:19], s27 offen offset:1056 lds
v_mfma_f32_16x16x32_bf16 a[12:15], v[120:123], v[248:251], a[12:15]
ds_read_b64_tr_b16 v[182:183], v189 offset:26080
v_accvgpr_read_b32 v10, a216
v_mfma_f32_16x16x32_bf16 a[12:15], v[124:127], v[252:255], a[12:15]
v_accvgpr_read_b32 v11, a217
v_accvgpr_read_b32 v12, a218
s_waitcnt lgkmcnt(3)
v_mfma_f32_16x16x32_bf16 a[16:19], v[128:131], v[224:227], a[16:19]
ds_read_b64_tr_b16 v[120:121], v189 offset:33792
v_accvgpr_read_b32 v13, a219
v_mfma_f32_16x16x32_bf16 a[16:19], v[132:135], v[228:231], a[16:19]
v_cvt_pk_bf16_f32 v6, v6, v7
v_cvt_pk_bf16_f32 v7, v8, v9
v_mfma_f32_16x16x32_bf16 a[20:23], v[128:131], v[232:235], a[20:23]
ds_read_b64_tr_b16 v[122:123], v189 offset:34304
v_cvt_pk_bf16_f32 v8, v10, v11
v_mfma_f32_16x16x32_bf16 a[20:23], v[132:135], v[236:239], a[20:23]
buffer_load_dwordx4 v22, s[16:19], s27 offen offset:2112 lds
v_mfma_f32_16x16x32_bf16 a[24:27], v[128:131], v[240:243], a[24:27]
ds_read_b64_tr_b16 v[124:125], v189 offset:42240
v_cvt_pk_bf16_f32 v9, v12, v13
v_mfma_f32_16x16x32_bf16 a[24:27], v[132:135], v[244:247], a[24:27]
buffer_store_dwordx4 v[6:9], v0, s[76:79], s37 offen offset:2048 sc1
v_mfma_f32_16x16x32_bf16 a[28:31], v[128:131], v[248:251], a[28:31]
ds_read_b64_tr_b16 v[126:127], v189 offset:42752
)ASM"
R"ASM(v_mfma_f32_16x16x32_bf16 a[28:31], v[132:135], v[252:255], a[28:31]
buffer_load_dwordx4 v23, s[16:19], s27 offen offset:3168 lds
s_add_u32 s27, s27, 0x20000
v_mfma_f32_16x16x32_bf16 a[32:35], v[136:139], v[224:227], a[32:35]
ds_read_b64_tr_b16 v[128:129], v189 offset:33824
v_mfma_f32_16x16x32_bf16 a[32:35], v[140:143], v[228:231], a[32:35]
v_mfma_f32_16x16x32_bf16 a[36:39], v[136:139], v[232:235], a[36:39]
ds_read_b64_tr_b16 v[130:131], v189 offset:34336
v_mfma_f32_16x16x32_bf16 a[36:39], v[140:143], v[236:239], a[36:39]
v_mfma_f32_16x16x32_bf16 a[40:43], v[136:139], v[240:243], a[40:43]
ds_read_b64_tr_b16 v[132:133], v189 offset:42272
v_accvgpr_read_b32 v6, a232
v_mfma_f32_16x16x32_bf16 a[40:43], v[140:143], v[244:247], a[40:43]
buffer_load_dwordx4 v[192:195], v1, s[20:23], s26 offen sc1
v_mfma_f32_16x16x32_bf16 a[44:47], v[136:139], v[248:251], a[44:47]
ds_read_b64_tr_b16 v[134:135], v189 offset:42784
v_accvgpr_read_b32 v7, a233
v_mfma_f32_16x16x32_bf16 a[44:47], v[140:143], v[252:255], a[44:47]
v_accvgpr_read_b32 v8, a234
v_accvgpr_read_b32 v9, a235
v_mfma_f32_16x16x32_bf16 a[48:51], v[144:147], v[224:227], a[48:51]
ds_read_b64_tr_b16 v[136:137], v189 offset:33856
v_accvgpr_read_b32 v10, a248
v_mfma_f32_16x16x32_bf16 a[48:51], v[148:151], v[228:231], a[48:51]
buffer_load_dwordx4 v[196:199], v1, s[20:23], s26 offen offset:1024 sc1
v_mfma_f32_16x16x32_bf16 a[52:55], v[144:147], v[232:235], a[52:55]
ds_read_b64_tr_b16 v[138:139], v189 offset:34368
v_accvgpr_read_b32 v11, a249
v_mfma_f32_16x16x32_bf16 a[52:55], v[148:151], v[236:239], a[52:55]
v_accvgpr_read_b32 v12, a250
v_accvgpr_read_b32 v13, a251
v_mfma_f32_16x16x32_bf16 a[56:59], v[144:147], v[240:243], a[56:59]
ds_read_b64_tr_b16 v[140:141], v189 offset:42304
v_cvt_pk_bf16_f32 v6, v6, v7
v_mfma_f32_16x16x32_bf16 a[56:59], v[148:151], v[244:247], a[56:59]
v_cvt_pk_bf16_f32 v7, v8, v9
v_cvt_pk_bf16_f32 v8, v10, v11
v_mfma_f32_16x16x32_bf16 a[60:63], v[144:147], v[248:251], a[60:63]
ds_read_b64_tr_b16 v[142:143], v189 offset:42816
v_cvt_pk_bf16_f32 v9, v12, v13
v_mfma_f32_16x16x32_bf16 a[60:63], v[148:151], v[252:255], a[60:63]
buffer_load_dwordx4 v[200:203], v2, s[20:23], s26 offen sc1
v_mfma_f32_16x16x32_bf16 a[64:67], v[152:155], v[224:227], a[64:67]
ds_read_b64_tr_b16 v[144:145], v189 offset:33888
v_mfma_f32_16x16x32_bf16 a[64:67], v[156:159], v[228:231], a[64:67]
buffer_store_dwordx4 v[6:9], v0, s[76:79], s37 offen offset:3072 sc1
v_mfma_f32_16x16x32_bf16 a[68:71], v[152:155], v[232:235], a[68:71]
ds_read_b64_tr_b16 v[146:147], v189 offset:34400
v_mfma_f32_16x16x32_bf16 a[68:71], v[156:159], v[236:239], a[68:71]
buffer_load_dwordx4 v[204:207], v2, s[20:23], s26 offen offset:1024 sc1
v_mfma_f32_16x16x32_bf16 a[72:75], v[152:155], v[240:243], a[72:75]
ds_read_b64_tr_b16 v[148:149], v189 offset:42336
v_mfma_f32_16x16x32_bf16 a[72:75], v[156:159], v[244:247], a[72:75]
v_mfma_f32_16x16x32_bf16 a[76:79], v[152:155], v[248:251], a[76:79]
ds_read_b64_tr_b16 v[150:151], v189 offset:42848
v_mfma_f32_16x16x32_bf16 a[76:79], v[156:159], v[252:255], a[76:79]
v_mfma_f32_16x16x32_bf16 a[80:83], v[160:163], v[224:227], a[80:83]
ds_read_b64_tr_b16 v[152:153], v189 offset:33920
v_mfma_f32_16x16x32_bf16 a[80:83], v[164:167], v[228:231], a[80:83]
buffer_load_dwordx4 v[208:211], v3, s[20:23], s26 offen sc1
v_mfma_f32_16x16x32_bf16 a[84:87], v[160:163], v[232:235], a[84:87]
ds_read_b64_tr_b16 v[154:155], v189 offset:34432
s_add_u32 s37, s74, 3
v_mfma_f32_16x16x32_bf16 a[84:87], v[164:167], v[236:239], a[84:87]
s_and_b32 s37, s37, 3
s_lshl_b32 s37, s37, 1
v_mfma_f32_16x16x32_bf16 a[88:91], v[160:163], v[240:243], a[88:91]
ds_read_b64_tr_b16 v[156:157], v189 offset:42368
s_and_b32 s35, s5, 0xf9
v_mfma_f32_16x16x32_bf16 a[88:91], v[164:167], v[244:247], a[88:91]
buffer_load_dwordx4 v[212:215], v3, s[20:23], s26 offen offset:1024 sc1
v_mfma_f32_16x16x32_bf16 a[92:95], v[160:163], v[248:251], a[92:95]
ds_read_b64_tr_b16 v[158:159], v189 offset:42880
s_or_b32 s37, s37, s35
v_mfma_f32_16x16x32_bf16 a[92:95], v[164:167], v[252:255], a[92:95]
s_mul_i32 s37, s37, 0x18000
s_mul_i32 s35, s4, 0x6000
v_mfma_f32_16x16x32_bf16 a[96:99], v[168:171], v[224:227], a[96:99]
ds_read_b64_tr_b16 v[160:161], v189 offset:33952
s_add_u32 s37, s37, s35
v_mfma_f32_16x16x32_bf16 a[96:99], v[172:175], v[228:231], a[96:99]
s_add_u32 s37, s37, 0x2300000
v_accvgpr_read_b32 v6, a140
v_mfma_f32_16x16x32_bf16 a[100:103], v[168:171], v[232:235], a[100:103]
ds_read_b64_tr_b16 v[162:163], v189 offset:34464
v_accvgpr_read_b32 v7, a141
v_mfma_f32_16x16x32_bf16 a[100:103], v[172:175], v[236:239], a[100:103]
buffer_load_dwordx4 v[216:219], v4, s[20:23], s26 offen sc1
v_mfma_f32_16x16x32_bf16 a[104:107], v[168:171], v[240:243], a[104:107]
ds_read_b64_tr_b16 v[164:165], v189 offset:42400
v_accvgpr_read_b32 v8, a142
v_mfma_f32_16x16x32_bf16 a[104:107], v[172:175], v[244:247], a[104:107]
v_accvgpr_read_b32 v9, a143
v_accvgpr_read_b32 v10, a156
v_mfma_f32_16x16x32_bf16 a[108:111], v[168:171], v[248:251], a[108:111]
ds_read_b64_tr_b16 v[166:167], v189 offset:42912
v_accvgpr_read_b32 v11, a157
v_mfma_f32_16x16x32_bf16 a[108:111], v[172:175], v[252:255], a[108:111]
buffer_load_dwordx4 v[220:223], v4, s[20:23], s26 offen offset:1024 sc1
s_add_u32 s26, s26, 0x4000
s_waitcnt lgkmcnt(3)
v_mfma_f32_16x16x32_bf16 a[112:115], v[176:179], v[224:227], a[112:115]
ds_read_b64_tr_b16 v[168:169], v189 offset:33984
v_accvgpr_read_b32 v12, a158
v_mfma_f32_16x16x32_bf16 a[112:115], v[180:183], v[228:231], a[112:115]
v_accvgpr_read_b32 v13, a159
v_cvt_pk_bf16_f32 v6, v6, v7
v_mfma_f32_16x16x32_bf16 a[116:119], v[176:179], v[232:235], a[116:119]
ds_read_b64_tr_b16 v[170:171], v189 offset:34496
v_cvt_pk_bf16_f32 v7, v8, v9
v_mfma_f32_16x16x32_bf16 a[116:119], v[180:183], v[236:239], a[116:119]
v_cvt_pk_bf16_f32 v8, v10, v11
v_cvt_pk_bf16_f32 v9, v12, v13
v_mfma_f32_16x16x32_bf16 a[120:123], v[176:179], v[240:243], a[120:123]
ds_read_b64_tr_b16 v[172:173], v189 offset:42432
v_mfma_f32_16x16x32_bf16 a[120:123], v[180:183], v[244:247], a[120:123]
buffer_store_dwordx4 v[6:9], v0, s[76:79], s37 offen sc1
v_mfma_f32_16x16x32_bf16 a[124:127], v[176:179], v[248:251], a[124:127]
ds_read_b64_tr_b16 v[174:175], v189 offset:42944
v_mfma_f32_16x16x32_bf16 a[124:127], v[180:183], v[252:255], a[124:127]
s_waitcnt vmcnt(45)
v_mfma_f32_16x16x32_bf16 a[0:3], v[120:123], v[24:27], a[0:3]
ds_read_b64_tr_b16 v[176:177], v189 offset:34016
s_add_u32 m0, s30, 0x177c0
v_mfma_f32_16x16x32_bf16 a[0:3], v[124:127], v[28:31], a[0:3]
buffer_load_dwordx4 v20, s[16:19], s27 offen lds
v_mfma_f32_16x16x32_bf16 a[4:7], v[120:123], v[32:35], a[4:7]
ds_read_b64_tr_b16 v[178:179], v189 offset:34528
v_accvgpr_read_b32 v6, a172
v_mfma_f32_16x16x32_bf16 a[4:7], v[124:127], v[36:39], a[4:7]
v_accvgpr_read_b32 v7, a173
v_accvgpr_read_b32 v8, a174
v_mfma_f32_16x16x32_bf16 a[8:11], v[120:123], v[40:43], a[8:11]
ds_read_b64_tr_b16 v[180:181], v189 offset:42464
v_accvgpr_read_b32 v9, a175
v_mfma_f32_16x16x32_bf16 a[8:11], v[124:127], v[44:47], a[8:11]
buffer_load_dwordx4 v21, s[16:19], s27 offen offset:1056 lds
v_mfma_f32_16x16x32_bf16 a[12:15], v[120:123], v[48:51], a[12:15]
ds_read_b64_tr_b16 v[182:183], v189 offset:42976
v_accvgpr_read_b32 v10, a188
v_mfma_f32_16x16x32_bf16 a[12:15], v[124:127], v[52:55], a[12:15]
v_accvgpr_read_b32 v11, a189
v_accvgpr_read_b32 v12, a190
v_mfma_f32_16x16x32_bf16 a[16:19], v[128:131], v[24:27], a[16:19]
ds_read_b64_tr_b16 v[120:121], v190
v_accvgpr_read_b32 v13, a191
v_mfma_f32_16x16x32_bf16 a[16:19], v[132:135], v[28:31], a[16:19]
v_cvt_pk_bf16_f32 v6, v6, v7
v_cvt_pk_bf16_f32 v7, v8, v9
v_mfma_f32_16x16x32_bf16 a[20:23], v[128:131], v[32:35], a[20:23]
ds_read_b64_tr_b16 v[122:123], v190 offset:512
v_cvt_pk_bf16_f32 v8, v10, v11
v_mfma_f32_16x16x32_bf16 a[20:23], v[132:135], v[36:39], a[20:23]
buffer_load_dwordx4 v22, s[16:19], s27 offen offset:2112 lds
v_mfma_f32_16x16x32_bf16 a[24:27], v[128:131], v[40:43], a[24:27]
ds_read_b64_tr_b16 v[124:125], v190 offset:8448
v_cvt_pk_bf16_f32 v9, v12, v13
v_mfma_f32_16x16x32_bf16 a[24:27], v[132:135], v[44:47], a[24:27]
buffer_store_dwordx4 v[6:9], v0, s[76:79], s37 offen offset:1024 sc1
v_mfma_f32_16x16x32_bf16 a[28:31], v[128:131], v[48:51], a[28:31]
ds_read_b64_tr_b16 v[126:127], v190 offset:8960
v_mfma_f32_16x16x32_bf16 a[28:31], v[132:135], v[52:55], a[28:31]
buffer_load_dwordx4 v23, s[16:19], s27 offen offset:3168 lds
s_add_u32 s27, s27, 0x20000
v_mfma_f32_16x16x32_bf16 a[32:35], v[136:139], v[24:27], a[32:35]
ds_read_b64_tr_b16 v[128:129], v190 offset:32
v_mfma_f32_16x16x32_bf16 a[32:35], v[140:143], v[28:31], a[32:35]
v_mfma_f32_16x16x32_bf16 a[36:39], v[136:139], v[32:35], a[36:39]
ds_read_b64_tr_b16 v[130:131], v190 offset:544
v_mfma_f32_16x16x32_bf16 a[36:39], v[140:143], v[36:39], a[36:39]
v_mfma_f32_16x16x32_bf16 a[40:43], v[136:139], v[40:43], a[40:43]
ds_read_b64_tr_b16 v[132:133], v190 offset:8480
v_accvgpr_read_b32 v6, a204
v_mfma_f32_16x16x32_bf16 a[40:43], v[140:143], v[44:47], a[40:43]
buffer_load_dwordx4 v[224:227], v1, s[20:23], s26 offen sc1
v_mfma_f32_16x16x32_bf16 a[44:47], v[136:139], v[48:51], a[44:47]
ds_read_b64_tr_b16 v[134:135], v190 offset:8992
v_accvgpr_read_b32 v7, a205
v_mfma_f32_16x16x32_bf16 a[44:47], v[140:143], v[52:55], a[44:47]
v_accvgpr_read_b32 v8, a206
v_accvgpr_read_b32 v9, a207
v_mfma_f32_16x16x32_bf16 a[48:51], v[144:147], v[24:27], a[48:51]
ds_read_b64_tr_b16 v[136:137], v190 offset:64
v_accvgpr_read_b32 v10, a220
v_mfma_f32_16x16x32_bf16 a[48:51], v[148:151], v[28:31], a[48:51]
buffer_load_dwordx4 v[228:231], v1, s[20:23], s26 offen offset:1024 sc1
v_mfma_f32_16x16x32_bf16 a[52:55], v[144:147], v[32:35], a[52:55]
ds_read_b64_tr_b16 v[138:139], v190 offset:576
v_accvgpr_read_b32 v11, a221
v_mfma_f32_16x16x32_bf16 a[52:55], v[148:151], v[36:39], a[52:55]
v_accvgpr_read_b32 v12, a222
v_accvgpr_read_b32 v13, a223
v_mfma_f32_16x16x32_bf16 a[56:59], v[144:147], v[40:43], a[56:59]
ds_read_b64_tr_b16 v[140:141], v190 offset:8512
v_cvt_pk_bf16_f32 v6, v6, v7
v_mfma_f32_16x16x32_bf16 a[56:59], v[148:151], v[44:47], a[56:59]
v_cvt_pk_bf16_f32 v7, v8, v9
v_cvt_pk_bf16_f32 v8, v10, v11
v_mfma_f32_16x16x32_bf16 a[60:63], v[144:147], v[48:51], a[60:63]
ds_read_b64_tr_b16 v[142:143], v190 offset:9024
v_cvt_pk_bf16_f32 v9, v12, v13
v_mfma_f32_16x16x32_bf16 a[60:63], v[148:151], v[52:55], a[60:63]
buffer_load_dwordx4 v[232:235], v2, s[20:23], s26 offen sc1
v_mfma_f32_16x16x32_bf16 a[64:67], v[152:155], v[24:27], a[64:67]
ds_read_b64_tr_b16 v[144:145], v190 offset:96
v_mfma_f32_16x16x32_bf16 a[64:67], v[156:159], v[28:31], a[64:67]
buffer_store_dwordx4 v[6:9], v0, s[76:79], s37 offen offset:2048 sc1
v_mfma_f32_16x16x32_bf16 a[68:71], v[152:155], v[32:35], a[68:71]
ds_read_b64_tr_b16 v[146:147], v190 offset:608
v_mfma_f32_16x16x32_bf16 a[68:71], v[156:159], v[36:39], a[68:71]
buffer_load_dwordx4 v[236:239], v2, s[20:23], s26 offen offset:1024 sc1
v_mfma_f32_16x16x32_bf16 a[72:75], v[152:155], v[40:43], a[72:75]
ds_read_b64_tr_b16 v[148:149], v190 offset:8544
v_mfma_f32_16x16x32_bf16 a[72:75], v[156:159], v[44:47], a[72:75]
v_mfma_f32_16x16x32_bf16 a[76:79], v[152:155], v[48:51], a[76:79]
ds_read_b64_tr_b16 v[150:151], v190 offset:9056
v_mfma_f32_16x16x32_bf16 a[76:79], v[156:159], v[52:55], a[76:79]
s_waitcnt lgkmcnt(3)
v_mfma_f32_16x16x32_bf16 a[80:83], v[160:163], v[24:27], a[80:83]
ds_read_b64_tr_b16 v[152:153], v190 offset:128
v_mfma_f32_16x16x32_bf16 a[80:83], v[164:167], v[28:31], a[80:83]
buffer_load_dwordx4 v[240:243], v3, s[20:23], s26 offen sc1
v_mfma_f32_16x16x32_bf16 a[84:87], v[160:163], v[32:35], a[84:87]
ds_read_b64_tr_b16 v[154:155], v190 offset:640
v_accvgpr_read_b32 v6, a236
v_mfma_f32_16x16x32_bf16 a[84:87], v[164:167], v[36:39], a[84:87]
v_accvgpr_read_b32 v7, a237
v_accvgpr_read_b32 v8, a238
v_mfma_f32_16x16x32_bf16 a[88:91], v[160:163], v[40:43], a[88:91]
ds_read_b64_tr_b16 v[156:157], v190 offset:8576
v_accvgpr_read_b32 v9, a239
v_mfma_f32_16x16x32_bf16 a[88:91], v[164:167], v[44:47], a[88:91]
buffer_load_dwordx4 v[244:247], v3, s[20:23], s26 offen offset:1024 sc1
v_mfma_f32_16x16x32_bf16 a[92:95], v[160:163], v[48:51], a[92:95]
ds_read_b64_tr_b16 v[158:159], v190 offset:9088
v_accvgpr_read_b32 v10, a252
v_mfma_f32_16x16x32_bf16 a[92:95], v[164:167], v[52:55], a[92:95]
v_accvgpr_read_b32 v11, a253
v_accvgpr_read_b32 v12, a254
v_mfma_f32_16x16x32_bf16 a[96:99], v[168:171], v[24:27], a[96:99]
ds_read_b64_tr_b16 v[160:161], v190 offset:160
v_accvgpr_read_b32 v13, a255
v_mfma_f32_16x16x32_bf16 a[96:99], v[172:175], v[28:31], a[96:99]
v_cvt_pk_bf16_f32 v6, v6, v7
v_cvt_pk_bf16_f32 v7, v8, v9
v_mfma_f32_16x16x32_bf16 a[100:103], v[168:171], v[32:35], a[100:103]
ds_read_b64_tr_b16 v[162:163], v190 offset:672
v_cvt_pk_bf16_f32 v8, v10, v11
v_mfma_f32_16x16x32_bf16 a[100:103], v[172:175], v[36:39], a[100:103]
buffer_load_dwordx4 v[248:251], v4, s[20:23], s26 offen sc1
v_mfma_f32_16x16x32_bf16 a[104:107], v[168:171], v[40:43], a[104:107]
ds_read_b64_tr_b16 v[164:165], v190 offset:8608
v_cvt_pk_bf16_f32 v9, v12, v13
v_mfma_f32_16x16x32_bf16 a[104:107], v[172:175], v[44:47], a[104:107]
buffer_store_dwordx4 v[6:9], v0, s[76:79], s37 offen offset:3072 sc1
v_mfma_f32_16x16x32_bf16 a[108:111], v[168:171], v[48:51], a[108:111]
ds_read_b64_tr_b16 v[166:167], v190 offset:9120
v_mfma_f32_16x16x32_bf16 a[108:111], v[172:175], v[52:55], a[108:111]
buffer_load_dwordx4 v[252:255], v4, s[20:23], s26 offen offset:1024 sc1
s_add_u32 s26, s26, 0x4000
v_mfma_f32_16x16x32_bf16 a[112:115], v[176:179], v[24:27], a[112:115]
ds_read_b64_tr_b16 v[168:169], v190 offset:192
v_mfma_f32_16x16x32_bf16 a[112:115], v[180:183], v[28:31], a[112:115]
v_mfma_f32_16x16x32_bf16 a[116:119], v[176:179], v[32:35], a[116:119]
ds_read_b64_tr_b16 v[170:171], v190 offset:704
v_mfma_f32_16x16x32_bf16 a[116:119], v[180:183], v[36:39], a[116:119]
v_mfma_f32_16x16x32_bf16 a[120:123], v[176:179], v[40:43], a[120:123]
ds_read_b64_tr_b16 v[172:173], v190 offset:8640
v_mfma_f32_16x16x32_bf16 a[120:123], v[180:183], v[44:47], a[120:123]
v_mfma_f32_16x16x32_bf16 a[124:127], v[176:179], v[48:51], a[124:127]
ds_read_b64_tr_b16 v[174:175], v190 offset:9152
v_mfma_f32_16x16x32_bf16 a[124:127], v[180:183], v[52:55], a[124:127]
s_waitcnt vmcnt(40)
s_barrier
v_mfma_f32_16x16x32_bf16 a[0:3], v[120:123], v[56:59], a[0:3]
ds_read_b64_tr_b16 v[176:177], v190 offset:224
s_add_u32 m0, s30, 0x1b9c0
v_mfma_f32_16x16x32_bf16 a[0:3], v[124:127], v[60:63], a[0:3]
buffer_load_dwordx4 v20, s[16:19], s27 offen lds
v_mfma_f32_16x16x32_bf16 a[4:7], v[120:123], v[64:67], a[4:7]
ds_read_b64_tr_b16 v[178:179], v190 offset:736
v_mfma_f32_16x16x32_bf16 a[4:7], v[124:127], v[68:71], a[4:7]
v_mfma_f32_16x16x32_bf16 a[8:11], v[120:123], v[72:75], a[8:11]
ds_read_b64_tr_b16 v[180:181], v190 offset:8672
v_mfma_f32_16x16x32_bf16 a[8:11], v[124:127], v[76:79], a[8:11]
buffer_load_dwordx4 v21, s[16:19], s27 offen offset:1056 lds
v_mfma_f32_16x16x32_bf16 a[12:15], v[120:123], v[80:83], a[12:15]
ds_read_b64_tr_b16 v[182:183], v190 offset:9184
v_mfma_f32_16x16x32_bf16 a[12:15], v[124:127], v[84:87], a[12:15]
v_mfma_f32_16x16x32_bf16 a[16:19], v[128:131], v[56:59], a[16:19]
ds_read_b64_tr_b16 v[120:121], v188
v_mfma_f32_16x16x32_bf16 a[16:19], v[132:135], v[60:63], a[16:19]
v_mfma_f32_16x16x32_bf16 a[20:23], v[128:131], v[64:67], a[20:23]
ds_read_b64_tr_b16 v[122:123], v188 offset:512
v_mfma_f32_16x16x32_bf16 a[20:23], v[132:135], v[68:71], a[20:23]
buffer_load_dwordx4 v22, s[16:19], s27 offen offset:2112 lds
v_mfma_f32_16x16x32_bf16 a[24:27], v[128:131], v[72:75], a[24:27]
ds_read_b64_tr_b16 v[124:125], v188 offset:8448
v_mfma_f32_16x16x32_bf16 a[24:27], v[132:135], v[76:79], a[24:27]
v_mfma_f32_16x16x32_bf16 a[28:31], v[128:131], v[80:83], a[28:31]
ds_read_b64_tr_b16 v[126:127], v188 offset:8960
v_mfma_f32_16x16x32_bf16 a[28:31], v[132:135], v[84:87], a[28:31]
buffer_load_dwordx4 v23, s[16:19], s27 offen offset:3168 lds
s_add_u32 s27, s27, 0x20000
v_mfma_f32_16x16x32_bf16 a[32:35], v[136:139], v[56:59], a[32:35]
ds_read_b64_tr_b16 v[128:129], v188 offset:32
v_mfma_f32_16x16x32_bf16 a[32:35], v[140:143], v[60:63], a[32:35]
v_mfma_f32_16x16x32_bf16 a[36:39], v[136:139], v[64:67], a[36:39]
ds_read_b64_tr_b16 v[130:131], v188 offset:544
v_mfma_f32_16x16x32_bf16 a[36:39], v[140:143], v[68:71], a[36:39]
v_mfma_f32_16x16x32_bf16 a[40:43], v[136:139], v[72:75], a[40:43]
ds_read_b64_tr_b16 v[132:133], v188 offset:8480
v_mfma_f32_16x16x32_bf16 a[40:43], v[140:143], v[76:79], a[40:43]
buffer_load_dwordx4 v[24:27], v1, s[20:23], s26 offen sc1
v_mfma_f32_16x16x32_bf16 a[44:47], v[136:139], v[80:83], a[44:47]
ds_read_b64_tr_b16 v[134:135], v188 offset:8992
v_mfma_f32_16x16x32_bf16 a[44:47], v[140:143], v[84:87], a[44:47]
s_waitcnt lgkmcnt(3)
v_mfma_f32_16x16x32_bf16 a[48:51], v[144:147], v[56:59], a[48:51]
ds_read_b64_tr_b16 v[136:137], v188 offset:64
v_mfma_f32_16x16x32_bf16 a[48:51], v[148:151], v[60:63], a[48:51]
buffer_load_dwordx4 v[28:31], v1, s[20:23], s26 offen offset:1024 sc1
v_mfma_f32_16x16x32_bf16 a[52:55], v[144:147], v[64:67], a[52:55]
ds_read_b64_tr_b16 v[138:139], v188 offset:576
v_mfma_f32_16x16x32_bf16 a[52:55], v[148:151], v[68:71], a[52:55]
v_mfma_f32_16x16x32_bf16 a[56:59], v[144:147], v[72:75], a[56:59]
ds_read_b64_tr_b16 v[140:141], v188 offset:8512
v_mfma_f32_16x16x32_bf16 a[56:59], v[148:151], v[76:79], a[56:59]
v_mfma_f32_16x16x32_bf16 a[60:63], v[144:147], v[80:83], a[60:63]
ds_read_b64_tr_b16 v[142:143], v188 offset:9024
v_mfma_f32_16x16x32_bf16 a[60:63], v[148:151], v[84:87], a[60:63]
buffer_load_dwordx4 v[32:35], v2, s[20:23], s26 offen sc1
v_mfma_f32_16x16x32_bf16 a[64:67], v[152:155], v[56:59], a[64:67]
ds_read_b64_tr_b16 v[144:145], v188 offset:96
v_mfma_f32_16x16x32_bf16 a[64:67], v[156:159], v[60:63], a[64:67]
v_mfma_f32_16x16x32_bf16 a[68:71], v[152:155], v[64:67], a[68:71]
ds_read_b64_tr_b16 v[146:147], v188 offset:608
v_mfma_f32_16x16x32_bf16 a[68:71], v[156:159], v[68:71], a[68:71]
buffer_load_dwordx4 v[36:39], v2, s[20:23], s26 offen offset:1024 sc1
v_mfma_f32_16x16x32_bf16 a[72:75], v[152:155], v[72:75], a[72:75]
ds_read_b64_tr_b16 v[148:149], v188 offset:8544
v_mfma_f32_16x16x32_bf16 a[72:75], v[156:159], v[76:79], a[72:75]
v_mfma_f32_16x16x32_bf16 a[76:79], v[152:155], v[80:83], a[76:79]
ds_read_b64_tr_b16 v[150:151], v188 offset:9056
v_mfma_f32_16x16x32_bf16 a[76:79], v[156:159], v[84:87], a[76:79]
v_mfma_f32_16x16x32_bf16 a[80:83], v[160:163], v[56:59], a[80:83]
ds_read_b64_tr_b16 v[152:153], v188 offset:128
v_mfma_f32_16x16x32_bf16 a[80:83], v[164:167], v[60:63], a[80:83]
buffer_load_dwordx4 v[40:43], v3, s[20:23], s26 offen sc1
v_mfma_f32_16x16x32_bf16 a[84:87], v[160:163], v[64:67], a[84:87]
ds_read_b64_tr_b16 v[154:155], v188 offset:640
v_mfma_f32_16x16x32_bf16 a[84:87], v[164:167], v[68:71], a[84:87]
v_mfma_f32_16x16x32_bf16 a[88:91], v[160:163], v[72:75], a[88:91]
ds_read_b64_tr_b16 v[156:157], v188 offset:8576
v_mfma_f32_16x16x32_bf16 a[88:91], v[164:167], v[76:79], a[88:91]
buffer_load_dwordx4 v[44:47], v3, s[20:23], s26 offen offset:1024 sc1
v_mfma_f32_16x16x32_bf16 a[92:95], v[160:163], v[80:83], a[92:95]
ds_read_b64_tr_b16 v[158:159], v188 offset:9088
v_mfma_f32_16x16x32_bf16 a[92:95], v[164:167], v[84:87], a[92:95]
v_mfma_f32_16x16x32_bf16 a[96:99], v[168:171], v[56:59], a[96:99]
ds_read_b64_tr_b16 v[160:161], v188 offset:160
v_mfma_f32_16x16x32_bf16 a[96:99], v[172:175], v[60:63], a[96:99]
v_mfma_f32_16x16x32_bf16 a[100:103], v[168:171], v[64:67], a[100:103]
ds_read_b64_tr_b16 v[162:163], v188 offset:672
v_mfma_f32_16x16x32_bf16 a[100:103], v[172:175], v[68:71], a[100:103]
buffer_load_dwordx4 v[48:51], v4, s[20:23], s26 offen sc1
v_mfma_f32_16x16x32_bf16 a[104:107], v[168:171], v[72:75], a[104:107]
ds_read_b64_tr_b16 v[164:165], v188 offset:8608
v_mfma_f32_16x16x32_bf16 a[104:107], v[172:175], v[76:79], a[104:107]
v_mfma_f32_16x16x32_bf16 a[108:111], v[168:171], v[80:83], a[108:111]
ds_read_b64_tr_b16 v[166:167], v188 offset:9120
v_mfma_f32_16x16x32_bf16 a[108:111], v[172:175], v[84:87], a[108:111]
buffer_load_dwordx4 v[52:55], v4, s[20:23], s26 offen offset:1024 sc1
s_add_u32 s26, s26, 0x4000
v_mfma_f32_16x16x32_bf16 a[112:115], v[176:179], v[56:59], a[112:115]
ds_read_b64_tr_b16 v[168:169], v188 offset:192
v_mfma_f32_16x16x32_bf16 a[112:115], v[180:183], v[60:63], a[112:115]
v_mfma_f32_16x16x32_bf16 a[116:119], v[176:179], v[64:67], a[116:119]
ds_read_b64_tr_b16 v[170:171], v188 offset:704
v_mfma_f32_16x16x32_bf16 a[116:119], v[180:183], v[68:71], a[116:119]
v_mfma_f32_16x16x32_bf16 a[120:123], v[176:179], v[72:75], a[120:123]
ds_read_b64_tr_b16 v[172:173], v188 offset:8640
v_mfma_f32_16x16x32_bf16 a[120:123], v[180:183], v[76:79], a[120:123]
v_mfma_f32_16x16x32_bf16 a[124:127], v[176:179], v[80:83], a[124:127]
ds_read_b64_tr_b16 v[174:175], v188 offset:9152
v_mfma_f32_16x16x32_bf16 a[124:127], v[180:183], v[84:87], a[124:127]
s_waitcnt vmcnt(43)
v_mfma_f32_16x16x32_bf16 a[0:3], v[120:123], v[88:91], a[0:3]
ds_read_b64_tr_b16 v[176:177], v188 offset:224
s_add_u32 m0, s30, 0x1fbc0
v_mfma_f32_16x16x32_bf16 a[0:3], v[124:127], v[92:95], a[0:3]
buffer_load_dwordx4 v20, s[16:19], s27 offen lds
v_mfma_f32_16x16x32_bf16 a[4:7], v[120:123], v[96:99], a[4:7]
ds_read_b64_tr_b16 v[178:179], v188 offset:736
v_mfma_f32_16x16x32_bf16 a[4:7], v[124:127], v[100:103], a[4:7]
v_mfma_f32_16x16x32_bf16 a[8:11], v[120:123], v[104:107], a[8:11]
ds_read_b64_tr_b16 v[180:181], v188 offset:8672
v_mfma_f32_16x16x32_bf16 a[8:11], v[124:127], v[108:111], a[8:11]
buffer_load_dwordx4 v21, s[16:19], s27 offen offset:1056 lds
v_mfma_f32_16x16x32_bf16 a[12:15], v[120:123], v[112:115], a[12:15]
)ASM"
R"ASM(ds_read_b64_tr_b16 v[182:183], v188 offset:9184
v_mfma_f32_16x16x32_bf16 a[12:15], v[124:127], v[116:119], a[12:15]
s_waitcnt lgkmcnt(3)
v_mfma_f32_16x16x32_bf16 a[16:19], v[128:131], v[88:91], a[16:19]
ds_read_b64_tr_b16 v[120:121], v188 offset:16896
v_mfma_f32_16x16x32_bf16 a[16:19], v[132:135], v[92:95], a[16:19]
v_mfma_f32_16x16x32_bf16 a[20:23], v[128:131], v[96:99], a[20:23]
ds_read_b64_tr_b16 v[122:123], v188 offset:17408
v_mfma_f32_16x16x32_bf16 a[20:23], v[132:135], v[100:103], a[20:23]
buffer_load_dwordx4 v22, s[16:19], s27 offen offset:2112 lds
v_mfma_f32_16x16x32_bf16 a[24:27], v[128:131], v[104:107], a[24:27]
ds_read_b64_tr_b16 v[124:125], v188 offset:25344
v_mfma_f32_16x16x32_bf16 a[24:27], v[132:135], v[108:111], a[24:27]
v_mfma_f32_16x16x32_bf16 a[28:31], v[128:131], v[112:115], a[28:31]
ds_read_b64_tr_b16 v[126:127], v188 offset:25856
v_mfma_f32_16x16x32_bf16 a[28:31], v[132:135], v[116:119], a[28:31]
buffer_load_dwordx4 v23, s[16:19], s27 offen offset:3168 lds
s_add_u32 s27, s27, 0x20000
v_mfma_f32_16x16x32_bf16 a[32:35], v[136:139], v[88:91], a[32:35]
ds_read_b64_tr_b16 v[128:129], v188 offset:16928
v_mfma_f32_16x16x32_bf16 a[32:35], v[140:143], v[92:95], a[32:35]
v_mfma_f32_16x16x32_bf16 a[36:39], v[136:139], v[96:99], a[36:39]
ds_read_b64_tr_b16 v[130:131], v188 offset:17440
v_mfma_f32_16x16x32_bf16 a[36:39], v[140:143], v[100:103], a[36:39]
v_mfma_f32_16x16x32_bf16 a[40:43], v[136:139], v[104:107], a[40:43]
ds_read_b64_tr_b16 v[132:133], v188 offset:25376
v_mfma_f32_16x16x32_bf16 a[40:43], v[140:143], v[108:111], a[40:43]
buffer_load_dwordx4 v[56:59], v1, s[20:23], s26 offen sc1
v_mfma_f32_16x16x32_bf16 a[44:47], v[136:139], v[112:115], a[44:47]
ds_read_b64_tr_b16 v[134:135], v188 offset:25888
v_mfma_f32_16x16x32_bf16 a[44:47], v[140:143], v[116:119], a[44:47]
v_mfma_f32_16x16x32_bf16 a[48:51], v[144:147], v[88:91], a[48:51]
ds_read_b64_tr_b16 v[136:137], v188 offset:16960
v_mfma_f32_16x16x32_bf16 a[48:51], v[148:151], v[92:95], a[48:51]
buffer_load_dwordx4 v[60:63], v1, s[20:23], s26 offen offset:1024 sc1
v_mfma_f32_16x16x32_bf16 a[52:55], v[144:147], v[96:99], a[52:55]
ds_read_b64_tr_b16 v[138:139], v188 offset:17472
v_mfma_f32_16x16x32_bf16 a[52:55], v[148:151], v[100:103], a[52:55]
v_mfma_f32_16x16x32_bf16 a[56:59], v[144:147], v[104:107], a[56:59]
ds_read_b64_tr_b16 v[140:141], v188 offset:25408
v_mfma_f32_16x16x32_bf16 a[56:59], v[148:151], v[108:111], a[56:59]
v_mfma_f32_16x16x32_bf16 a[60:63], v[144:147], v[112:115], a[60:63]
ds_read_b64_tr_b16 v[142:143], v188 offset:25920
v_mfma_f32_16x16x32_bf16 a[60:63], v[148:151], v[116:119], a[60:63]
buffer_load_dwordx4 v[64:67], v2, s[20:23], s26 offen sc1
v_mfma_f32_16x16x32_bf16 a[64:67], v[152:155], v[88:91], a[64:67]
ds_read_b64_tr_b16 v[144:145], v188 offset:16992
v_mfma_f32_16x16x32_bf16 a[64:67], v[156:159], v[92:95], a[64:67]
v_mfma_f32_16x16x32_bf16 a[68:71], v[152:155], v[96:99], a[68:71]
ds_read_b64_tr_b16 v[146:147], v188 offset:17504
v_mfma_f32_16x16x32_bf16 a[68:71], v[156:159], v[100:103], a[68:71]
buffer_load_dwordx4 v[68:71], v2, s[20:23], s26 offen offset:1024 sc1
v_mfma_f32_16x16x32_bf16 a[72:75], v[152:155], v[104:107], a[72:75]
ds_read_b64_tr_b16 v[148:149], v188 offset:25440
v_mfma_f32_16x16x32_bf16 a[72:75], v[156:159], v[108:111], a[72:75]
v_mfma_f32_16x16x32_bf16 a[76:79], v[152:155], v[112:115], a[76:79]
ds_read_b64_tr_b16 v[150:151], v188 offset:25952
v_mfma_f32_16x16x32_bf16 a[76:79], v[156:159], v[116:119], a[76:79]
v_mfma_f32_16x16x32_bf16 a[80:83], v[160:163], v[88:91], a[80:83]
ds_read_b64_tr_b16 v[152:153], v188 offset:17024
v_mfma_f32_16x16x32_bf16 a[80:83], v[164:167], v[92:95], a[80:83]
buffer_load_dwordx4 v[72:75], v3, s[20:23], s26 offen sc1
v_mfma_f32_16x16x32_bf16 a[84:87], v[160:163], v[96:99], a[84:87]
ds_read_b64_tr_b16 v[154:155], v188 offset:17536
v_mfma_f32_16x16x32_bf16 a[84:87], v[164:167], v[100:103], a[84:87]
v_mfma_f32_16x16x32_bf16 a[88:91], v[160:163], v[104:107], a[88:91]
ds_read_b64_tr_b16 v[156:157], v188 offset:25472
v_mfma_f32_16x16x32_bf16 a[88:91], v[164:167], v[108:111], a[88:91]
buffer_load_dwordx4 v[76:79], v3, s[20:23], s26 offen offset:1024 sc1
v_mfma_f32_16x16x32_bf16 a[92:95], v[160:163], v[112:115], a[92:95]
ds_read_b64_tr_b16 v[158:159], v188 offset:25984
v_mfma_f32_16x16x32_bf16 a[92:95], v[164:167], v[116:119], a[92:95]
v_mfma_f32_16x16x32_bf16 a[96:99], v[168:171], v[88:91], a[96:99]
ds_read_b64_tr_b16 v[160:161], v188 offset:17056
v_mfma_f32_16x16x32_bf16 a[96:99], v[172:175], v[92:95], a[96:99]
v_mfma_f32_16x16x32_bf16 a[100:103], v[168:171], v[96:99], a[100:103]
ds_read_b64_tr_b16 v[162:163], v188 offset:17568
v_mfma_f32_16x16x32_bf16 a[100:103], v[172:175], v[100:103], a[100:103]
buffer_load_dwordx4 v[80:83], v4, s[20:23], s26 offen sc1
v_mfma_f32_16x16x32_bf16 a[104:107], v[168:171], v[104:107], a[104:107]
ds_read_b64_tr_b16 v[164:165], v188 offset:25504
v_mfma_f32_16x16x32_bf16 a[104:107], v[172:175], v[108:111], a[104:107]
v_mfma_f32_16x16x32_bf16 a[108:111], v[168:171], v[112:115], a[108:111]
ds_read_b64_tr_b16 v[166:167], v188 offset:26016
v_mfma_f32_16x16x32_bf16 a[108:111], v[172:175], v[116:119], a[108:111]
buffer_load_dwordx4 v[84:87], v4, s[20:23], s26 offen offset:1024 sc1
s_add_u32 s26, s26, 0x4000
s_waitcnt lgkmcnt(3)
v_mfma_f32_16x16x32_bf16 a[112:115], v[176:179], v[88:91], a[112:115]
ds_read_b64_tr_b16 v[168:169], v188 offset:17088
v_mfma_f32_16x16x32_bf16 a[112:115], v[180:183], v[92:95], a[112:115]
v_mfma_f32_16x16x32_bf16 a[116:119], v[176:179], v[96:99], a[116:119]
ds_read_b64_tr_b16 v[170:171], v188 offset:17600
v_mfma_f32_16x16x32_bf16 a[116:119], v[180:183], v[100:103], a[116:119]
v_mfma_f32_16x16x32_bf16 a[120:123], v[176:179], v[104:107], a[120:123]
ds_read_b64_tr_b16 v[172:173], v188 offset:25536
v_mfma_f32_16x16x32_bf16 a[120:123], v[180:183], v[108:111], a[120:123]
v_mfma_f32_16x16x32_bf16 a[124:127], v[176:179], v[112:115], a[124:127]
ds_read_b64_tr_b16 v[174:175], v188 offset:26048
v_mfma_f32_16x16x32_bf16 a[124:127], v[180:183], v[116:119], a[124:127]
s_waitcnt vmcnt(25)
s_barrier
v_mfma_f32_16x16x32_bf16 a[0:3], v[120:123], v[192:195], a[0:3]
ds_read_b64_tr_b16 v[176:177], v188 offset:17120
s_add_u32 m0, s30, 0x23dc0
v_mfma_f32_16x16x32_bf16 a[0:3], v[124:127], v[196:199], a[0:3]
buffer_load_dwordx4 v20, s[16:19], s27 offen lds
v_mfma_f32_16x16x32_bf16 a[4:7], v[120:123], v[200:203], a[4:7]
ds_read_b64_tr_b16 v[178:179], v188 offset:17632
v_mfma_f32_16x16x32_bf16 a[4:7], v[124:127], v[204:207], a[4:7]
v_mfma_f32_16x16x32_bf16 a[8:11], v[120:123], v[208:211], a[8:11]
ds_read_b64_tr_b16 v[180:181], v188 offset:25568
v_mfma_f32_16x16x32_bf16 a[8:11], v[124:127], v[212:215], a[8:11]
buffer_load_dwordx4 v21, s[16:19], s27 offen offset:1056 lds
v_mfma_f32_16x16x32_bf16 a[12:15], v[120:123], v[216:219], a[12:15]
ds_read_b64_tr_b16 v[182:183], v188 offset:26080
v_mfma_f32_16x16x32_bf16 a[12:15], v[124:127], v[220:223], a[12:15]
v_mfma_f32_16x16x32_bf16 a[16:19], v[128:131], v[192:195], a[16:19]
ds_read_b64_tr_b16 v[120:121], v188 offset:33792
v_mfma_f32_16x16x32_bf16 a[16:19], v[132:135], v[196:199], a[16:19]
v_mfma_f32_16x16x32_bf16 a[20:23], v[128:131], v[200:203], a[20:23]
ds_read_b64_tr_b16 v[122:123], v188 offset:34304
v_mfma_f32_16x16x32_bf16 a[20:23], v[132:135], v[204:207], a[20:23]
buffer_load_dwordx4 v22, s[16:19], s27 offen offset:2112 lds
v_mfma_f32_16x16x32_bf16 a[24:27], v[128:131], v[208:211], a[24:27]
ds_read_b64_tr_b16 v[124:125], v188 offset:42240
v_mfma_f32_16x16x32_bf16 a[24:27], v[132:135], v[212:215], a[24:27]
v_mfma_f32_16x16x32_bf16 a[28:31], v[128:131], v[216:219], a[28:31]
ds_read_b64_tr_b16 v[126:127], v188 offset:42752
v_mfma_f32_16x16x32_bf16 a[28:31], v[132:135], v[220:223], a[28:31]
buffer_load_dwordx4 v23, s[16:19], s27 offen offset:3168 lds
s_add_u32 s27, s27, 0x20000
v_mfma_f32_16x16x32_bf16 a[32:35], v[136:139], v[192:195], a[32:35]
ds_read_b64_tr_b16 v[128:129], v188 offset:33824
v_mfma_f32_16x16x32_bf16 a[32:35], v[140:143], v[196:199], a[32:35]
v_mfma_f32_16x16x32_bf16 a[36:39], v[136:139], v[200:203], a[36:39]
ds_read_b64_tr_b16 v[130:131], v188 offset:34336
v_mfma_f32_16x16x32_bf16 a[36:39], v[140:143], v[204:207], a[36:39]
v_mfma_f32_16x16x32_bf16 a[40:43], v[136:139], v[208:211], a[40:43]
ds_read_b64_tr_b16 v[132:133], v188 offset:42272
v_mfma_f32_16x16x32_bf16 a[40:43], v[140:143], v[212:215], a[40:43]
buffer_load_dwordx4 v[88:91], v1, s[20:23], s26 offen sc1
v_mfma_f32_16x16x32_bf16 a[44:47], v[136:139], v[216:219], a[44:47]
ds_read_b64_tr_b16 v[134:135], v188 offset:42784
v_mfma_f32_16x16x32_bf16 a[44:47], v[140:143], v[220:223], a[44:47]
v_mfma_f32_16x16x32_bf16 a[48:51], v[144:147], v[192:195], a[48:51]
ds_read_b64_tr_b16 v[136:137], v188 offset:33856
v_mfma_f32_16x16x32_bf16 a[48:51], v[148:151], v[196:199], a[48:51]
buffer_load_dwordx4 v[92:95], v1, s[20:23], s26 offen offset:1024 sc1
v_mfma_f32_16x16x32_bf16 a[52:55], v[144:147], v[200:203], a[52:55]
ds_read_b64_tr_b16 v[138:139], v188 offset:34368
v_mfma_f32_16x16x32_bf16 a[52:55], v[148:151], v[204:207], a[52:55]
v_mfma_f32_16x16x32_bf16 a[56:59], v[144:147], v[208:211], a[56:59]
ds_read_b64_tr_b16 v[140:141], v188 offset:42304
v_mfma_f32_16x16x32_bf16 a[56:59], v[148:151], v[212:215], a[56:59]
v_mfma_f32_16x16x32_bf16 a[60:63], v[144:147], v[216:219], a[60:63]
s_cmp_eq_u32 s4, 0
s_cbranch_scc1 OOLwyp_%=
RETwyp_%=:
v_mfma_f32_16x16x32_bf16 a[60:63], v[148:151], v[220:223], a[60:63]
ds_read_b64_tr_b16 v[142:143], v188 offset:42816
v_mfma_f32_16x16x32_bf16 a[64:67], v[152:155], v[192:195], a[64:67]
ds_read_b64_tr_b16 v[144:145], v188 offset:33888
v_mfma_f32_16x16x32_bf16 a[64:67], v[156:159], v[196:199], a[64:67]
buffer_load_dwordx4 v[96:99], v2, s[20:23], s26 offen sc1
v_mfma_f32_16x16x32_bf16 a[68:71], v[152:155], v[200:203], a[68:71]
ds_read_b64_tr_b16 v[146:147], v188 offset:34400
v_mfma_f32_16x16x32_bf16 a[68:71], v[156:159], v[204:207], a[68:71]
buffer_load_dwordx4 v[100:103], v2, s[20:23], s26 offen offset:1024 sc1
v_mfma_f32_16x16x32_bf16 a[72:75], v[152:155], v[208:211], a[72:75]
ds_read_b64_tr_b16 v[148:149], v188 offset:42336
v_mfma_f32_16x16x32_bf16 a[72:75], v[156:159], v[212:215], a[72:75]
v_mfma_f32_16x16x32_bf16 a[76:79], v[152:155], v[216:219], a[76:79]
ds_read_b64_tr_b16 v[150:151], v188 offset:42848
v_mfma_f32_16x16x32_bf16 a[76:79], v[156:159], v[220:223], a[76:79]
s_waitcnt lgkmcnt(3)
v_mfma_f32_16x16x32_bf16 a[80:83], v[160:163], v[192:195], a[80:83]
ds_read_b64_tr_b16 v[152:153], v188 offset:33920
v_mfma_f32_16x16x32_bf16 a[80:83], v[164:167], v[196:199], a[80:83]
buffer_load_dwordx4 v[104:107], v3, s[20:23], s26 offen sc1
v_mfma_f32_16x16x32_bf16 a[84:87], v[160:163], v[200:203], a[84:87]
ds_read_b64_tr_b16 v[154:155], v188 offset:34432
v_mfma_f32_16x16x32_bf16 a[84:87], v[164:167], v[204:207], a[84:87]
v_mfma_f32_16x16x32_bf16 a[88:91], v[160:163], v[208:211], a[88:91]
ds_read_b64_tr_b16 v[156:157], v188 offset:42368
v_mfma_f32_16x16x32_bf16 a[88:91], v[164:167], v[212:215], a[88:91]
buffer_load_dwordx4 v[108:111], v3, s[20:23], s26 offen offset:1024 sc1
v_mfma_f32_16x16x32_bf16 a[92:95], v[160:163], v[216:219], a[92:95]
ds_read_b64_tr_b16 v[158:159], v188 offset:42880
v_mfma_f32_16x16x32_bf16 a[92:95], v[164:167], v[220:223], a[92:95]
v_mfma_f32_16x16x32_bf16 a[96:99], v[168:171], v[192:195], a[96:99]
ds_read_b64_tr_b16 v[160:161], v188 offset:33952
v_mfma_f32_16x16x32_bf16 a[96:99], v[172:175], v[196:199], a[96:99]
v_mfma_f32_16x16x32_bf16 a[100:103], v[168:171], v[200:203], a[100:103]
ds_read_b64_tr_b16 v[162:163], v188 offset:34464
v_mfma_f32_16x16x32_bf16 a[100:103], v[172:175], v[204:207], a[100:103]
buffer_load_dwordx4 v[112:115], v4, s[20:23], s26 offen sc1
v_mfma_f32_16x16x32_bf16 a[104:107], v[168:171], v[208:211], a[104:107]
ds_read_b64_tr_b16 v[164:165], v188 offset:42400
v_mfma_f32_16x16x32_bf16 a[104:107], v[172:175], v[212:215], a[104:107]
v_mfma_f32_16x16x32_bf16 a[108:111], v[168:171], v[216:219], a[108:111]
ds_read_b64_tr_b16 v[166:167], v188 offset:42912
v_mfma_f32_16x16x32_bf16 a[108:111], v[172:175], v[220:223], a[108:111]
buffer_load_dwordx4 v[116:119], v4, s[20:23], s26 offen offset:1024 sc1
s_add_u32 s26, s26, 0x4000
v_mfma_f32_16x16x32_bf16 a[112:115], v[176:179], v[192:195], a[112:115]
ds_read_b64_tr_b16 v[168:169], v188 offset:33984
v_mfma_f32_16x16x32_bf16 a[112:115], v[180:183], v[196:199], a[112:115]
v_mfma_f32_16x16x32_bf16 a[116:119], v[176:179], v[200:203], a[116:119]
ds_read_b64_tr_b16 v[170:171], v188 offset:34496
v_mfma_f32_16x16x32_bf16 a[116:119], v[180:183], v[204:207], a[116:119]
v_mfma_f32_16x16x32_bf16 a[120:123], v[176:179], v[208:211], a[120:123]
ds_read_b64_tr_b16 v[172:173], v188 offset:42432
v_mfma_f32_16x16x32_bf16 a[120:123], v[180:183], v[212:215], a[120:123]
v_mfma_f32_16x16x32_bf16 a[124:127], v[176:179], v[216:219], a[124:127]
ds_read_b64_tr_b16 v[174:175], v188 offset:42944
v_mfma_f32_16x16x32_bf16 a[124:127], v[180:183], v[220:223], a[124:127]
s_waitcnt vmcnt(36)
v_mfma_f32_16x16x32_bf16 a[0:3], v[120:123], v[224:227], a[0:3]
ds_read_b64_tr_b16 v[176:177], v188 offset:34016
s_add_u32 m0, s30, 0xb1c0
v_mfma_f32_16x16x32_bf16 a[0:3], v[124:127], v[228:231], a[0:3]
buffer_load_dwordx4 v20, s[16:19], s27 offen lds
v_mfma_f32_16x16x32_bf16 a[4:7], v[120:123], v[232:235], a[4:7]
ds_read_b64_tr_b16 v[178:179], v188 offset:34528
v_mfma_f32_16x16x32_bf16 a[4:7], v[124:127], v[236:239], a[4:7]
v_mfma_f32_16x16x32_bf16 a[8:11], v[120:123], v[240:243], a[8:11]
ds_read_b64_tr_b16 v[180:181], v188 offset:42464
v_mfma_f32_16x16x32_bf16 a[8:11], v[124:127], v[244:247], a[8:11]
buffer_load_dwordx4 v21, s[16:19], s27 offen offset:1056 lds
v_mfma_f32_16x16x32_bf16 a[12:15], v[120:123], v[248:251], a[12:15]
ds_read_b64_tr_b16 v[182:183], v188 offset:42976
v_mfma_f32_16x16x32_bf16 a[12:15], v[124:127], v[252:255], a[12:15]
v_mfma_f32_16x16x32_bf16 a[16:19], v[128:131], v[224:227], a[16:19]
ds_read_b64_tr_b16 v[120:121], v189
v_mfma_f32_16x16x32_bf16 a[16:19], v[132:135], v[228:231], a[16:19]
v_mfma_f32_16x16x32_bf16 a[20:23], v[128:131], v[232:235], a[20:23]
ds_read_b64_tr_b16 v[122:123], v189 offset:512
v_mfma_f32_16x16x32_bf16 a[20:23], v[132:135], v[236:239], a[20:23]
buffer_load_dwordx4 v22, s[16:19], s27 offen offset:2112 lds
v_mfma_f32_16x16x32_bf16 a[24:27], v[128:131], v[240:243], a[24:27]
ds_read_b64_tr_b16 v[124:125], v189 offset:8448
v_mfma_f32_16x16x32_bf16 a[24:27], v[132:135], v[244:247], a[24:27]
v_mfma_f32_16x16x32_bf16 a[28:31], v[128:131], v[248:251], a[28:31]
ds_read_b64_tr_b16 v[126:127], v189 offset:8960
v_mfma_f32_16x16x32_bf16 a[28:31], v[132:135], v[252:255], a[28:31]
buffer_load_dwordx4 v23, s[16:19], s27 offen offset:3168 lds
s_add_u32 s27, s27, 0x20000
v_mfma_f32_16x16x32_bf16 a[32:35], v[136:139], v[224:227], a[32:35]
ds_read_b64_tr_b16 v[128:129], v189 offset:32
v_mfma_f32_16x16x32_bf16 a[32:35], v[140:143], v[228:231], a[32:35]
v_mfma_f32_16x16x32_bf16 a[36:39], v[136:139], v[232:235], a[36:39]
ds_read_b64_tr_b16 v[130:131], v189 offset:544
v_mfma_f32_16x16x32_bf16 a[36:39], v[140:143], v[236:239], a[36:39]
v_mfma_f32_16x16x32_bf16 a[40:43], v[136:139], v[240:243], a[40:43]
ds_read_b64_tr_b16 v[132:133], v189 offset:8480
v_mfma_f32_16x16x32_bf16 a[40:43], v[140:143], v[244:247], a[40:43]
buffer_load_dwordx4 v[192:195], v1, s[20:23], s26 offen sc1
v_mfma_f32_16x16x32_bf16 a[44:47], v[136:139], v[248:251], a[44:47]
ds_read_b64_tr_b16 v[134:135], v189 offset:8992
v_mfma_f32_16x16x32_bf16 a[44:47], v[140:143], v[252:255], a[44:47]
s_waitcnt lgkmcnt(3)
v_mfma_f32_16x16x32_bf16 a[48:51], v[144:147], v[224:227], a[48:51]
ds_read_b64_tr_b16 v[136:137], v189 offset:64
v_mfma_f32_16x16x32_bf16 a[48:51], v[148:151], v[228:231], a[48:51]
buffer_load_dwordx4 v[196:199], v1, s[20:23], s26 offen offset:1024 sc1
v_mfma_f32_16x16x32_bf16 a[52:55], v[144:147], v[232:235], a[52:55]
ds_read_b64_tr_b16 v[138:139], v189 offset:576
v_mfma_f32_16x16x32_bf16 a[52:55], v[148:151], v[236:239], a[52:55]
v_mfma_f32_16x16x32_bf16 a[56:59], v[144:147], v[240:243], a[56:59]
ds_read_b64_tr_b16 v[140:141], v189 offset:8512
v_mfma_f32_16x16x32_bf16 a[56:59], v[148:151], v[244:247], a[56:59]
v_mfma_f32_16x16x32_bf16 a[60:63], v[144:147], v[248:251], a[60:63]
ds_read_b64_tr_b16 v[142:143], v189 offset:9024
v_mfma_f32_16x16x32_bf16 a[60:63], v[148:151], v[252:255], a[60:63]
buffer_load_dwordx4 v[200:203], v2, s[20:23], s26 offen sc1
v_mfma_f32_16x16x32_bf16 a[64:67], v[152:155], v[224:227], a[64:67]
ds_read_b64_tr_b16 v[144:145], v189 offset:96
v_mfma_f32_16x16x32_bf16 a[64:67], v[156:159], v[228:231], a[64:67]
v_mfma_f32_16x16x32_bf16 a[68:71], v[152:155], v[232:235], a[68:71]
ds_read_b64_tr_b16 v[146:147], v189 offset:608
v_mfma_f32_16x16x32_bf16 a[68:71], v[156:159], v[236:239], a[68:71]
buffer_load_dwordx4 v[204:207], v2, s[20:23], s26 offen offset:1024 sc1
v_mfma_f32_16x16x32_bf16 a[72:75], v[152:155], v[240:243], a[72:75]
ds_read_b64_tr_b16 v[148:149], v189 offset:8544
v_mfma_f32_16x16x32_bf16 a[72:75], v[156:159], v[244:247], a[72:75]
v_mfma_f32_16x16x32_bf16 a[76:79], v[152:155], v[248:251], a[76:79]
ds_read_b64_tr_b16 v[150:151], v189 offset:9056
v_mfma_f32_16x16x32_bf16 a[76:79], v[156:159], v[252:255], a[76:79]
v_mfma_f32_16x16x32_bf16 a[80:83], v[160:163], v[224:227], a[80:83]
ds_read_b64_tr_b16 v[152:153], v189 offset:128
v_mfma_f32_16x16x32_bf16 a[80:83], v[164:167], v[228:231], a[80:83]
buffer_load_dwordx4 v[208:211], v3, s[20:23], s26 offen sc1
v_mfma_f32_16x16x32_bf16 a[84:87], v[160:163], v[232:235], a[84:87]
ds_read_b64_tr_b16 v[154:155], v189 offset:640
v_mfma_f32_16x16x32_bf16 a[84:87], v[164:167], v[236:239], a[84:87]
v_mfma_f32_16x16x32_bf16 a[88:91], v[160:163], v[240:243], a[88:91]
ds_read_b64_tr_b16 v[156:157], v189 offset:8576
v_mfma_f32_16x16x32_bf16 a[88:91], v[164:167], v[244:247], a[88:91]
buffer_load_dwordx4 v[212:215], v3, s[20:23], s26 offen offset:1024 sc1
v_mfma_f32_16x16x32_bf16 a[92:95], v[160:163], v[248:251], a[92:95]
ds_read_b64_tr_b16 v[158:159], v189 offset:9088
v_mfma_f32_16x16x32_bf16 a[92:95], v[164:167], v[252:255], a[92:95]
v_mfma_f32_16x16x32_bf16 a[96:99], v[168:171], v[224:227], a[96:99]
ds_read_b64_tr_b16 v[160:161], v189 offset:160
v_mfma_f32_16x16x32_bf16 a[96:99], v[172:175], v[228:231], a[96:99]
v_mfma_f32_16x16x32_bf16 a[100:103], v[168:171], v[232:235], a[100:103]
ds_read_b64_tr_b16 v[162:163], v189 offset:672
v_mfma_f32_16x16x32_bf16 a[100:103], v[172:175], v[236:239], a[100:103]
buffer_load_dwordx4 v[216:219], v4, s[20:23], s26 offen sc1
v_mfma_f32_16x16x32_bf16 a[104:107], v[168:171], v[240:243], a[104:107]
ds_read_b64_tr_b16 v[164:165], v189 offset:8608
v_mfma_f32_16x16x32_bf16 a[104:107], v[172:175], v[244:247], a[104:107]
v_mfma_f32_16x16x32_bf16 a[108:111], v[168:171], v[248:251], a[108:111]
ds_read_b64_tr_b16 v[166:167], v189 offset:9120
v_mfma_f32_16x16x32_bf16 a[108:111], v[172:175], v[252:255], a[108:111]
buffer_load_dwordx4 v[220:223], v4, s[20:23], s26 offen offset:1024 sc1
s_add_u32 s26, s26, 0x4000
v_mfma_f32_16x16x32_bf16 a[112:115], v[176:179], v[224:227], a[112:115]
ds_read_b64_tr_b16 v[168:169], v189 offset:192
v_mfma_f32_16x16x32_bf16 a[112:115], v[180:183], v[228:231], a[112:115]
v_mfma_f32_16x16x32_bf16 a[116:119], v[176:179], v[232:235], a[116:119]
ds_read_b64_tr_b16 v[170:171], v189 offset:704
v_mfma_f32_16x16x32_bf16 a[116:119], v[180:183], v[236:239], a[116:119]
v_mfma_f32_16x16x32_bf16 a[120:123], v[176:179], v[240:243], a[120:123]
ds_read_b64_tr_b16 v[172:173], v189 offset:8640
v_mfma_f32_16x16x32_bf16 a[120:123], v[180:183], v[244:247], a[120:123]
v_mfma_f32_16x16x32_bf16 a[124:127], v[176:179], v[248:251], a[124:127]
ds_read_b64_tr_b16 v[174:175], v189 offset:9152
v_mfma_f32_16x16x32_bf16 a[124:127], v[180:183], v[252:255], a[124:127]
s_waitcnt vmcnt(32)
s_barrier
v_mfma_f32_16x16x32_bf16 a[0:3], v[120:123], v[24:27], a[0:3]
ds_read_b64_tr_b16 v[176:177], v189 offset:224
s_add_u32 m0, s30, 0xf3c0
v_mfma_f32_16x16x32_bf16 a[0:3], v[124:127], v[28:31], a[0:3]
buffer_load_dwordx4 v20, s[16:19], s27 offen lds
v_mfma_f32_16x16x32_bf16 a[4:7], v[120:123], v[32:35], a[4:7]
ds_read_b64_tr_b16 v[178:179], v189 offset:736
v_mfma_f32_16x16x32_bf16 a[4:7], v[124:127], v[36:39], a[4:7]
v_mfma_f32_16x16x32_bf16 a[8:11], v[120:123], v[40:43], a[8:11]
ds_read_b64_tr_b16 v[180:181], v189 offset:8672
v_mfma_f32_16x16x32_bf16 a[8:11], v[124:127], v[44:47], a[8:11]
buffer_load_dwordx4 v21, s[16:19], s27 offen offset:1056 lds
v_mfma_f32_16x16x32_bf16 a[12:15], v[120:123], v[48:51], a[12:15]
ds_read_b64_tr_b16 v[182:183], v189 offset:9184
v_mfma_f32_16x16x32_bf16 a[12:15], v[124:127], v[52:55], a[12:15]
s_waitcnt lgkmcnt(3)
v_mfma_f32_16x16x32_bf16 a[16:19], v[128:131], v[24:27], a[16:19]
ds_read_b64_tr_b16 v[120:121], v189 offset:16896
v_mfma_f32_16x16x32_bf16 a[16:19], v[132:135], v[28:31], a[16:19]
v_mfma_f32_16x16x32_bf16 a[20:23], v[128:131], v[32:35], a[20:23]
ds_read_b64_tr_b16 v[122:123], v189 offset:17408
v_mfma_f32_16x16x32_bf16 a[20:23], v[132:135], v[36:39], a[20:23]
buffer_load_dwordx4 v22, s[16:19], s27 offen offset:2112 lds
v_mfma_f32_16x16x32_bf16 a[24:27], v[128:131], v[40:43], a[24:27]
ds_read_b64_tr_b16 v[124:125], v189 offset:25344
v_mfma_f32_16x16x32_bf16 a[24:27], v[132:135], v[44:47], a[24:27]
v_mfma_f32_16x16x32_bf16 a[28:31], v[128:131], v[48:51], a[28:31]
ds_read_b64_tr_b16 v[126:127], v189 offset:25856
v_mfma_f32_16x16x32_bf16 a[28:31], v[132:135], v[52:55], a[28:31]
buffer_load_dwordx4 v23, s[16:19], s27 offen offset:3168 lds
s_add_u32 s27, s27, 0x20000
v_mfma_f32_16x16x32_bf16 a[32:35], v[136:139], v[24:27], a[32:35]
ds_read_b64_tr_b16 v[128:129], v189 offset:16928
v_mfma_f32_16x16x32_bf16 a[32:35], v[140:143], v[28:31], a[32:35]
v_mfma_f32_16x16x32_bf16 a[36:39], v[136:139], v[32:35], a[36:39]
ds_read_b64_tr_b16 v[130:131], v189 offset:17440
v_mfma_f32_16x16x32_bf16 a[36:39], v[140:143], v[36:39], a[36:39]
v_mfma_f32_16x16x32_bf16 a[40:43], v[136:139], v[40:43], a[40:43]
ds_read_b64_tr_b16 v[132:133], v189 offset:25376
v_mfma_f32_16x16x32_bf16 a[40:43], v[140:143], v[44:47], a[40:43]
buffer_load_dwordx4 v[224:227], v1, s[20:23], s26 offen sc1
v_mfma_f32_16x16x32_bf16 a[44:47], v[136:139], v[48:51], a[44:47]
ds_read_b64_tr_b16 v[134:135], v189 offset:25888
v_mfma_f32_16x16x32_bf16 a[44:47], v[140:143], v[52:55], a[44:47]
v_mfma_f32_16x16x32_bf16 a[48:51], v[144:147], v[24:27], a[48:51]
ds_read_b64_tr_b16 v[136:137], v189 offset:16960
v_mfma_f32_16x16x32_bf16 a[48:51], v[148:151], v[28:31], a[48:51]
buffer_load_dwordx4 v[228:231], v1, s[20:23], s26 offen offset:1024 sc1
v_mfma_f32_16x16x32_bf16 a[52:55], v[144:147], v[32:35], a[52:55]
ds_read_b64_tr_b16 v[138:139], v189 offset:17472
v_mfma_f32_16x16x32_bf16 a[52:55], v[148:151], v[36:39], a[52:55]
v_mfma_f32_16x16x32_bf16 a[56:59], v[144:147], v[40:43], a[56:59]
ds_read_b64_tr_b16 v[140:141], v189 offset:25408
v_mfma_f32_16x16x32_bf16 a[56:59], v[148:151], v[44:47], a[56:59]
v_mfma_f32_16x16x32_bf16 a[60:63], v[144:147], v[48:51], a[60:63]
ds_read_b64_tr_b16 v[142:143], v189 offset:25920
v_mfma_f32_16x16x32_bf16 a[60:63], v[148:151], v[52:55], a[60:63]
buffer_load_dwordx4 v[232:235], v2, s[20:23], s26 offen sc1
v_mfma_f32_16x16x32_bf16 a[64:67], v[152:155], v[24:27], a[64:67]
ds_read_b64_tr_b16 v[144:145], v189 offset:16992
v_mfma_f32_16x16x32_bf16 a[64:67], v[156:159], v[28:31], a[64:67]
v_mfma_f32_16x16x32_bf16 a[68:71], v[152:155], v[32:35], a[68:71]
ds_read_b64_tr_b16 v[146:147], v189 offset:17504
v_mfma_f32_16x16x32_bf16 a[68:71], v[156:159], v[36:39], a[68:71]
buffer_load_dwordx4 v[236:239], v2, s[20:23], s26 offen offset:1024 sc1
v_mfma_f32_16x16x32_bf16 a[72:75], v[152:155], v[40:43], a[72:75]
ds_read_b64_tr_b16 v[148:149], v189 offset:25440
v_mfma_f32_16x16x32_bf16 a[72:75], v[156:159], v[44:47], a[72:75]
)ASM"
R"ASM(v_mfma_f32_16x16x32_bf16 a[76:79], v[152:155], v[48:51], a[76:79]
ds_read_b64_tr_b16 v[150:151], v189 offset:25952
v_mfma_f32_16x16x32_bf16 a[76:79], v[156:159], v[52:55], a[76:79]
v_mfma_f32_16x16x32_bf16 a[80:83], v[160:163], v[24:27], a[80:83]
ds_read_b64_tr_b16 v[152:153], v189 offset:17024
v_mfma_f32_16x16x32_bf16 a[80:83], v[164:167], v[28:31], a[80:83]
buffer_load_dwordx4 v[240:243], v3, s[20:23], s26 offen sc1
v_mfma_f32_16x16x32_bf16 a[84:87], v[160:163], v[32:35], a[84:87]
ds_read_b64_tr_b16 v[154:155], v189 offset:17536
v_mfma_f32_16x16x32_bf16 a[84:87], v[164:167], v[36:39], a[84:87]
v_mfma_f32_16x16x32_bf16 a[88:91], v[160:163], v[40:43], a[88:91]
ds_read_b64_tr_b16 v[156:157], v189 offset:25472
v_mfma_f32_16x16x32_bf16 a[88:91], v[164:167], v[44:47], a[88:91]
buffer_load_dwordx4 v[244:247], v3, s[20:23], s26 offen offset:1024 sc1
v_mfma_f32_16x16x32_bf16 a[92:95], v[160:163], v[48:51], a[92:95]
ds_read_b64_tr_b16 v[158:159], v189 offset:25984
v_mfma_f32_16x16x32_bf16 a[92:95], v[164:167], v[52:55], a[92:95]
v_mfma_f32_16x16x32_bf16 a[96:99], v[168:171], v[24:27], a[96:99]
s_add_u32 s46, s74, 1
s_and_b32 s46, s46, 3
s_lshl_b32 s46, s46, 1
s_and_b32 s35, s5, 0xf9
s_or_b32 s46, s46, s35
s_lshl_b32 s46, s46, 6
s_add_u32 s46, s46, 0x10000
v_mov_b32 v17, 0
buffer_load_dword v184, v17, s[76:79], s46 offen sc1
v_mfma_f32_16x16x32_bf16 a[96:99], v[172:175], v[28:31], a[96:99]
ds_read_b64_tr_b16 v[160:161], v189 offset:17056
v_mfma_f32_16x16x32_bf16 a[100:103], v[168:171], v[32:35], a[100:103]
ds_read_b64_tr_b16 v[162:163], v189 offset:17568
v_mfma_f32_16x16x32_bf16 a[100:103], v[172:175], v[36:39], a[100:103]
buffer_load_dwordx4 v[248:251], v4, s[20:23], s26 offen sc1
v_mfma_f32_16x16x32_bf16 a[104:107], v[168:171], v[40:43], a[104:107]
s_add_u32 s46, s74, 2
s_and_b32 s46, s46, 3
s_lshl_b32 s46, s46, 1
s_and_b32 s35, s5, 0xf9
s_or_b32 s46, s46, s35
s_lshl_b32 s46, s46, 6
s_add_u32 s46, s46, 0x10000
v_mov_b32 v17, 0
buffer_load_dword v185, v17, s[76:79], s46 offen sc1
v_mfma_f32_16x16x32_bf16 a[104:107], v[172:175], v[44:47], a[104:107]
ds_read_b64_tr_b16 v[164:165], v189 offset:25504
v_mfma_f32_16x16x32_bf16 a[108:111], v[168:171], v[48:51], a[108:111]
ds_read_b64_tr_b16 v[166:167], v189 offset:26016
v_mfma_f32_16x16x32_bf16 a[108:111], v[172:175], v[52:55], a[108:111]
buffer_load_dwordx4 v[252:255], v4, s[20:23], s26 offen offset:1024 sc1
s_add_u32 s26, s26, 0x4000
s_waitcnt lgkmcnt(3)
v_mfma_f32_16x16x32_bf16 a[112:115], v[176:179], v[24:27], a[112:115]
s_add_u32 s46, s74, 3
s_and_b32 s46, s46, 3
s_lshl_b32 s46, s46, 1
s_and_b32 s35, s5, 0xf9
s_or_b32 s46, s46, s35
s_lshl_b32 s46, s46, 6
s_add_u32 s46, s46, 0x10000
v_mov_b32 v17, 0
buffer_load_dword v186, v17, s[76:79], s46 offen sc1
v_mfma_f32_16x16x32_bf16 a[112:115], v[180:183], v[28:31], a[112:115]
ds_read_b64_tr_b16 v[168:169], v189 offset:17088
v_mfma_f32_16x16x32_bf16 a[116:119], v[176:179], v[32:35], a[116:119]
ds_read_b64_tr_b16 v[170:171], v189 offset:17600
v_mfma_f32_16x16x32_bf16 a[116:119], v[180:183], v[36:39], a[116:119]
v_mfma_f32_16x16x32_bf16 a[120:123], v[176:179], v[40:43], a[120:123]
ds_read_b64_tr_b16 v[172:173], v189 offset:25536
v_mfma_f32_16x16x32_bf16 a[120:123], v[180:183], v[44:47], a[120:123]
v_mfma_f32_16x16x32_bf16 a[124:127], v[176:179], v[48:51], a[124:127]
ds_read_b64_tr_b16 v[174:175], v189 offset:26048
v_mfma_f32_16x16x32_bf16 a[124:127], v[180:183], v[52:55], a[124:127]
s_waitcnt vmcnt(39)
v_mfma_f32_16x16x32_bf16 a[0:3], v[120:123], v[56:59], a[0:3]
ds_read_b64_tr_b16 v[176:177], v189 offset:17120
s_add_u32 m0, s30, 0x135c0
v_mfma_f32_16x16x32_bf16 a[0:3], v[124:127], v[60:63], a[0:3]
buffer_load_dwordx4 v20, s[16:19], s27 offen lds
v_mfma_f32_16x16x32_bf16 a[4:7], v[120:123], v[64:67], a[4:7]
ds_read_b64_tr_b16 v[178:179], v189 offset:17632
v_mfma_f32_16x16x32_bf16 a[4:7], v[124:127], v[68:71], a[4:7]
v_mfma_f32_16x16x32_bf16 a[8:11], v[120:123], v[72:75], a[8:11]
ds_read_b64_tr_b16 v[180:181], v189 offset:25568
v_mfma_f32_16x16x32_bf16 a[8:11], v[124:127], v[76:79], a[8:11]
buffer_load_dwordx4 v21, s[16:19], s27 offen offset:1056 lds
v_mfma_f32_16x16x32_bf16 a[12:15], v[120:123], v[80:83], a[12:15]
ds_read_b64_tr_b16 v[182:183], v189 offset:26080
v_mfma_f32_16x16x32_bf16 a[12:15], v[124:127], v[84:87], a[12:15]
v_mfma_f32_16x16x32_bf16 a[16:19], v[128:131], v[56:59], a[16:19]
ds_read_b64_tr_b16 v[120:121], v189 offset:33792
v_mfma_f32_16x16x32_bf16 a[16:19], v[132:135], v[60:63], a[16:19]
v_mfma_f32_16x16x32_bf16 a[20:23], v[128:131], v[64:67], a[20:23]
ds_read_b64_tr_b16 v[122:123], v189 offset:34304
v_mfma_f32_16x16x32_bf16 a[20:23], v[132:135], v[68:71], a[20:23]
buffer_load_dwordx4 v22, s[16:19], s27 offen offset:2112 lds
v_mfma_f32_16x16x32_bf16 a[24:27], v[128:131], v[72:75], a[24:27]
ds_read_b64_tr_b16 v[124:125], v189 offset:42240
v_mfma_f32_16x16x32_bf16 a[24:27], v[132:135], v[76:79], a[24:27]
v_mfma_f32_16x16x32_bf16 a[28:31], v[128:131], v[80:83], a[28:31]
ds_read_b64_tr_b16 v[126:127], v189 offset:42752
v_mfma_f32_16x16x32_bf16 a[28:31], v[132:135], v[84:87], a[28:31]
buffer_load_dwordx4 v23, s[16:19], s27 offen offset:3168 lds
s_add_u32 s27, s27, 0x20000
v_mfma_f32_16x16x32_bf16 a[32:35], v[136:139], v[56:59], a[32:35]
ds_read_b64_tr_b16 v[128:129], v189 offset:33824
v_mfma_f32_16x16x32_bf16 a[32:35], v[140:143], v[60:63], a[32:35]
v_mfma_f32_16x16x32_bf16 a[36:39], v[136:139], v[64:67], a[36:39]
ds_read_b64_tr_b16 v[130:131], v189 offset:34336
v_mfma_f32_16x16x32_bf16 a[36:39], v[140:143], v[68:71], a[36:39]
v_mfma_f32_16x16x32_bf16 a[40:43], v[136:139], v[72:75], a[40:43]
ds_read_b64_tr_b16 v[132:133], v189 offset:42272
v_mfma_f32_16x16x32_bf16 a[40:43], v[140:143], v[76:79], a[40:43]
buffer_load_dwordx4 v[24:27], v1, s[20:23], s26 offen sc1
v_mfma_f32_16x16x32_bf16 a[44:47], v[136:139], v[80:83], a[44:47]
ds_read_b64_tr_b16 v[134:135], v189 offset:42784
v_mfma_f32_16x16x32_bf16 a[44:47], v[140:143], v[84:87], a[44:47]
v_mfma_f32_16x16x32_bf16 a[48:51], v[144:147], v[56:59], a[48:51]
ds_read_b64_tr_b16 v[136:137], v189 offset:33856
v_mfma_f32_16x16x32_bf16 a[48:51], v[148:151], v[60:63], a[48:51]
buffer_load_dwordx4 v[28:31], v1, s[20:23], s26 offen offset:1024 sc1
v_mfma_f32_16x16x32_bf16 a[52:55], v[144:147], v[64:67], a[52:55]
ds_read_b64_tr_b16 v[138:139], v189 offset:34368
v_mfma_f32_16x16x32_bf16 a[52:55], v[148:151], v[68:71], a[52:55]
v_mfma_f32_16x16x32_bf16 a[56:59], v[144:147], v[72:75], a[56:59]
ds_read_b64_tr_b16 v[140:141], v189 offset:42304
v_mfma_f32_16x16x32_bf16 a[56:59], v[148:151], v[76:79], a[56:59]
v_mfma_f32_16x16x32_bf16 a[60:63], v[144:147], v[80:83], a[60:63]
ds_read_b64_tr_b16 v[142:143], v189 offset:42816
v_mfma_f32_16x16x32_bf16 a[60:63], v[148:151], v[84:87], a[60:63]
buffer_load_dwordx4 v[32:35], v2, s[20:23], s26 offen sc1
v_mfma_f32_16x16x32_bf16 a[64:67], v[152:155], v[56:59], a[64:67]
ds_read_b64_tr_b16 v[144:145], v189 offset:33888
v_mfma_f32_16x16x32_bf16 a[64:67], v[156:159], v[60:63], a[64:67]
v_mfma_f32_16x16x32_bf16 a[68:71], v[152:155], v[64:67], a[68:71]
ds_read_b64_tr_b16 v[146:147], v189 offset:34400
v_mfma_f32_16x16x32_bf16 a[68:71], v[156:159], v[68:71], a[68:71]
buffer_load_dwordx4 v[36:39], v2, s[20:23], s26 offen offset:1024 sc1
v_mfma_f32_16x16x32_bf16 a[72:75], v[152:155], v[72:75], a[72:75]
ds_read_b64_tr_b16 v[148:149], v189 offset:42336
v_mfma_f32_16x16x32_bf16 a[72:75], v[156:159], v[76:79], a[72:75]
v_mfma_f32_16x16x32_bf16 a[76:79], v[152:155], v[80:83], a[76:79]
ds_read_b64_tr_b16 v[150:151], v189 offset:42848
v_mfma_f32_16x16x32_bf16 a[76:79], v[156:159], v[84:87], a[76:79]
s_waitcnt lgkmcnt(3)
v_mfma_f32_16x16x32_bf16 a[80:83], v[160:163], v[56:59], a[80:83]
ds_read_b64_tr_b16 v[152:153], v189 offset:33920
v_mfma_f32_16x16x32_bf16 a[80:83], v[164:167], v[60:63], a[80:83]
buffer_load_dwordx4 v[40:43], v3, s[20:23], s26 offen sc1
v_mfma_f32_16x16x32_bf16 a[84:87], v[160:163], v[64:67], a[84:87]
ds_read_b64_tr_b16 v[154:155], v189 offset:34432
v_mfma_f32_16x16x32_bf16 a[84:87], v[164:167], v[68:71], a[84:87]
v_mfma_f32_16x16x32_bf16 a[88:91], v[160:163], v[72:75], a[88:91]
ds_read_b64_tr_b16 v[156:157], v189 offset:42368
v_mfma_f32_16x16x32_bf16 a[88:91], v[164:167], v[76:79], a[88:91]
buffer_load_dwordx4 v[44:47], v3, s[20:23], s26 offen offset:1024 sc1
v_mfma_f32_16x16x32_bf16 a[92:95], v[160:163], v[80:83], a[92:95]
ds_read_b64_tr_b16 v[158:159], v189 offset:42880
v_mfma_f32_16x16x32_bf16 a[92:95], v[164:167], v[84:87], a[92:95]
v_mfma_f32_16x16x32_bf16 a[96:99], v[168:171], v[56:59], a[96:99]
ds_read_b64_tr_b16 v[160:161], v189 offset:33952
v_mfma_f32_16x16x32_bf16 a[96:99], v[172:175], v[60:63], a[96:99]
v_mfma_f32_16x16x32_bf16 a[100:103], v[168:171], v[64:67], a[100:103]
ds_read_b64_tr_b16 v[162:163], v189 offset:34464
v_mfma_f32_16x16x32_bf16 a[100:103], v[172:175], v[68:71], a[100:103]
buffer_load_dwordx4 v[48:51], v4, s[20:23], s26 offen sc1
v_mfma_f32_16x16x32_bf16 a[104:107], v[168:171], v[72:75], a[104:107]
ds_read_b64_tr_b16 v[164:165], v189 offset:42400
v_mfma_f32_16x16x32_bf16 a[104:107], v[172:175], v[76:79], a[104:107]
v_mfma_f32_16x16x32_bf16 a[108:111], v[168:171], v[80:83], a[108:111]
ds_read_b64_tr_b16 v[166:167], v189 offset:42912
v_mfma_f32_16x16x32_bf16 a[108:111], v[172:175], v[84:87], a[108:111]
buffer_load_dwordx4 v[52:55], v4, s[20:23], s26 offen offset:1024 sc1
s_add_u32 s26, s26, 0x4000
v_mfma_f32_16x16x32_bf16 a[112:115], v[176:179], v[56:59], a[112:115]
ds_read_b64_tr_b16 v[168:169], v189 offset:33984
v_mfma_f32_16x16x32_bf16 a[112:115], v[180:183], v[60:63], a[112:115]
v_mfma_f32_16x16x32_bf16 a[116:119], v[176:179], v[64:67], a[116:119]
ds_read_b64_tr_b16 v[170:171], v189 offset:34496
v_mfma_f32_16x16x32_bf16 a[116:119], v[180:183], v[68:71], a[116:119]
v_mfma_f32_16x16x32_bf16 a[120:123], v[176:179], v[72:75], a[120:123]
ds_read_b64_tr_b16 v[172:173], v189 offset:42432
v_mfma_f32_16x16x32_bf16 a[120:123], v[180:183], v[76:79], a[120:123]
v_mfma_f32_16x16x32_bf16 a[124:127], v[176:179], v[80:83], a[124:127]
ds_read_b64_tr_b16 v[174:175], v189 offset:42944
v_mfma_f32_16x16x32_bf16 a[124:127], v[180:183], v[84:87], a[124:127]
s_waitcnt vmcnt(12)
s_barrier
v_cmp_ne_u32 vcc, s33, v184
s_nop 1
s_cmp_lg_u64 vcc, 0
s_cbranch_scc1 OOLpyc1_%=
RETpyc1_%=:
v_cmp_ne_u32 vcc, s33, v185
s_nop 1
s_cmp_lg_u64 vcc, 0
s_cbranch_scc1 OOLpyc2_%=
RETpyc2_%=:
v_cmp_ne_u32 vcc, s33, v186
s_nop 1
s_cmp_lg_u64 vcc, 0
s_cbranch_scc1 OOLpyc3_%=
RETpyc3_%=:
v_mfma_f32_16x16x32_bf16 a[0:3], v[120:123], v[88:91], a[0:3]
ds_read_b64_tr_b16 v[176:177], v189 offset:34016
s_add_u32 m0, s30, 0x177c0
v_mfma_f32_16x16x32_bf16 a[0:3], v[124:127], v[92:95], a[0:3]
buffer_load_dwordx4 v20, s[16:19], s27 offen lds
v_mfma_f32_16x16x32_bf16 a[4:7], v[120:123], v[96:99], a[4:7]
ds_read_b64_tr_b16 v[178:179], v189 offset:34528
s_mul_i32 s34, s5, 0x18000
v_mfma_f32_16x16x32_bf16 a[4:7], v[124:127], v[100:103], a[4:7]
s_mul_i32 s35, s4, 0x6000
s_add_u32 s34, s34, s35
v_mfma_f32_16x16x32_bf16 a[8:11], v[120:123], v[104:107], a[8:11]
ds_read_b64_tr_b16 v[180:181], v189 offset:42464
s_add_u32 s34, s34, 0x2300000
v_mfma_f32_16x16x32_bf16 a[8:11], v[124:127], v[108:111], a[8:11]
buffer_load_dwordx4 v21, s[16:19], s27 offen offset:1056 lds
v_mfma_f32_16x16x32_bf16 a[12:15], v[120:123], v[112:115], a[12:15]
ds_read_b64_tr_b16 v[182:183], v189 offset:42976
s_add_u32 s38, s34, 0
v_mfma_f32_16x16x32_bf16 a[12:15], v[124:127], v[116:119], a[12:15]
buffer_load_dwordx4 a[132:135], v0, s[76:79], s38 offen sc1
v_mfma_f32_16x16x32_bf16 a[16:19], v[128:131], v[88:91], a[16:19]
ds_read_b64_tr_b16 v[120:121], v190
v_mfma_f32_16x16x32_bf16 a[16:19], v[132:135], v[92:95], a[16:19]
buffer_load_dwordx4 a[164:167], v0, s[76:79], s38 offen offset:1024 sc1
v_mfma_f32_16x16x32_bf16 a[20:23], v[128:131], v[96:99], a[20:23]
ds_read_b64_tr_b16 v[122:123], v190 offset:512
v_mfma_f32_16x16x32_bf16 a[20:23], v[132:135], v[100:103], a[20:23]
buffer_load_dwordx4 v22, s[16:19], s27 offen offset:2112 lds
v_mfma_f32_16x16x32_bf16 a[24:27], v[128:131], v[104:107], a[24:27]
ds_read_b64_tr_b16 v[124:125], v190 offset:8448
v_mfma_f32_16x16x32_bf16 a[24:27], v[132:135], v[108:111], a[24:27]
buffer_load_dwordx4 a[196:199], v0, s[76:79], s38 offen offset:2048 sc1
v_mfma_f32_16x16x32_bf16 a[28:31], v[128:131], v[112:115], a[28:31]
ds_read_b64_tr_b16 v[126:127], v190 offset:8960
v_mfma_f32_16x16x32_bf16 a[28:31], v[132:135], v[116:119], a[28:31]
buffer_load_dwordx4 v23, s[16:19], s27 offen offset:3168 lds
s_add_u32 s27, s27, 0x20000
v_mfma_f32_16x16x32_bf16 a[32:35], v[136:139], v[88:91], a[32:35]
ds_read_b64_tr_b16 v[128:129], v190 offset:32
v_mfma_f32_16x16x32_bf16 a[32:35], v[140:143], v[92:95], a[32:35]
buffer_load_dwordx4 a[228:231], v0, s[76:79], s38 offen offset:3072 sc1
v_mfma_f32_16x16x32_bf16 a[36:39], v[136:139], v[96:99], a[36:39]
ds_read_b64_tr_b16 v[130:131], v190 offset:544
s_add_u32 s38, s34, 0x1000
v_mfma_f32_16x16x32_bf16 a[36:39], v[140:143], v[100:103], a[36:39]
buffer_load_dwordx4 a[136:139], v0, s[76:79], s38 offen sc1
v_mfma_f32_16x16x32_bf16 a[40:43], v[136:139], v[104:107], a[40:43]
ds_read_b64_tr_b16 v[132:133], v190 offset:8480
v_mfma_f32_16x16x32_bf16 a[40:43], v[140:143], v[108:111], a[40:43]
buffer_load_dwordx4 v[56:59], v1, s[20:23], s26 offen sc1
v_mfma_f32_16x16x32_bf16 a[44:47], v[136:139], v[112:115], a[44:47]
ds_read_b64_tr_b16 v[134:135], v190 offset:8992
v_mfma_f32_16x16x32_bf16 a[44:47], v[140:143], v[116:119], a[44:47]
buffer_load_dwordx4 a[168:171], v0, s[76:79], s38 offen offset:1024 sc1
s_waitcnt lgkmcnt(3)
v_mfma_f32_16x16x32_bf16 a[48:51], v[144:147], v[88:91], a[48:51]
ds_read_b64_tr_b16 v[136:137], v190 offset:64
v_mfma_f32_16x16x32_bf16 a[48:51], v[148:151], v[92:95], a[48:51]
buffer_load_dwordx4 v[60:63], v1, s[20:23], s26 offen offset:1024 sc1
v_mfma_f32_16x16x32_bf16 a[52:55], v[144:147], v[96:99], a[52:55]
ds_read_b64_tr_b16 v[138:139], v190 offset:576
v_mfma_f32_16x16x32_bf16 a[52:55], v[148:151], v[100:103], a[52:55]
buffer_load_dwordx4 a[200:203], v0, s[76:79], s38 offen offset:2048 sc1
v_mfma_f32_16x16x32_bf16 a[56:59], v[144:147], v[104:107], a[56:59]
ds_read_b64_tr_b16 v[140:141], v190 offset:8512
v_mfma_f32_16x16x32_bf16 a[56:59], v[148:151], v[108:111], a[56:59]
buffer_load_dwordx4 a[232:235], v0, s[76:79], s38 offen offset:3072 sc1
v_mfma_f32_16x16x32_bf16 a[60:63], v[144:147], v[112:115], a[60:63]
ds_read_b64_tr_b16 v[142:143], v190 offset:9024
s_add_u32 s38, s34, 0x2000
v_mfma_f32_16x16x32_bf16 a[60:63], v[148:151], v[116:119], a[60:63]
buffer_load_dwordx4 v[64:67], v2, s[20:23], s26 offen sc1
v_mfma_f32_16x16x32_bf16 a[64:67], v[152:155], v[88:91], a[64:67]
ds_read_b64_tr_b16 v[144:145], v190 offset:96
v_mfma_f32_16x16x32_bf16 a[64:67], v[156:159], v[92:95], a[64:67]
buffer_load_dwordx4 a[140:143], v0, s[76:79], s38 offen sc1
v_mfma_f32_16x16x32_bf16 a[68:71], v[152:155], v[96:99], a[68:71]
ds_read_b64_tr_b16 v[146:147], v190 offset:608
v_mfma_f32_16x16x32_bf16 a[68:71], v[156:159], v[100:103], a[68:71]
buffer_load_dwordx4 v[68:71], v2, s[20:23], s26 offen offset:1024 sc1
v_mfma_f32_16x16x32_bf16 a[72:75], v[152:155], v[104:107], a[72:75]
ds_read_b64_tr_b16 v[148:149], v190 offset:8544
v_mfma_f32_16x16x32_bf16 a[72:75], v[156:159], v[108:111], a[72:75]
buffer_load_dwordx4 a[172:175], v0, s[76:79], s38 offen offset:1024 sc1
v_mfma_f32_16x16x32_bf16 a[76:79], v[152:155], v[112:115], a[76:79]
ds_read_b64_tr_b16 v[150:151], v190 offset:9056
v_mfma_f32_16x16x32_bf16 a[76:79], v[156:159], v[116:119], a[76:79]
buffer_load_dwordx4 a[204:207], v0, s[76:79], s38 offen offset:2048 sc1
v_mfma_f32_16x16x32_bf16 a[80:83], v[160:163], v[88:91], a[80:83]
ds_read_b64_tr_b16 v[152:153], v190 offset:128
v_mfma_f32_16x16x32_bf16 a[80:83], v[164:167], v[92:95], a[80:83]
buffer_load_dwordx4 v[72:75], v3, s[20:23], s26 offen sc1
v_mfma_f32_16x16x32_bf16 a[84:87], v[160:163], v[96:99], a[84:87]
ds_read_b64_tr_b16 v[154:155], v190 offset:640
v_mfma_f32_16x16x32_bf16 a[84:87], v[164:167], v[100:103], a[84:87]
buffer_load_dwordx4 a[236:239], v0, s[76:79], s38 offen offset:3072 sc1
v_mfma_f32_16x16x32_bf16 a[88:91], v[160:163], v[104:107], a[88:91]
ds_read_b64_tr_b16 v[156:157], v190 offset:8576
v_mfma_f32_16x16x32_bf16 a[88:91], v[164:167], v[108:111], a[88:91]
buffer_load_dwordx4 v[76:79], v3, s[20:23], s26 offen offset:1024 sc1
v_mfma_f32_16x16x32_bf16 a[92:95], v[160:163], v[112:115], a[92:95]
ds_read_b64_tr_b16 v[158:159], v190 offset:9088
v_mfma_f32_16x16x32_bf16 a[92:95], v[164:167], v[116:119], a[92:95]
v_mfma_f32_16x16x32_bf16 a[96:99], v[168:171], v[88:91], a[96:99]
ds_read_b64_tr_b16 v[160:161], v190 offset:160
v_mfma_f32_16x16x32_bf16 a[96:99], v[172:175], v[92:95], a[96:99]
v_mfma_f32_16x16x32_bf16 a[100:103], v[168:171], v[96:99], a[100:103]
ds_read_b64_tr_b16 v[162:163], v190 offset:672
v_mfma_f32_16x16x32_bf16 a[100:103], v[172:175], v[100:103], a[100:103]
buffer_load_dwordx4 v[80:83], v4, s[20:23], s26 offen sc1
v_mfma_f32_16x16x32_bf16 a[104:107], v[168:171], v[104:107], a[104:107]
ds_read_b64_tr_b16 v[164:165], v190 offset:8608
v_mfma_f32_16x16x32_bf16 a[104:107], v[172:175], v[108:111], a[104:107]
v_mfma_f32_16x16x32_bf16 a[108:111], v[168:171], v[112:115], a[108:111]
ds_read_b64_tr_b16 v[166:167], v190 offset:9120
v_mfma_f32_16x16x32_bf16 a[108:111], v[172:175], v[116:119], a[108:111]
buffer_load_dwordx4 v[84:87], v4, s[20:23], s26 offen offset:1024 sc1
s_add_u32 s26, s26, 0x4000
v_mfma_f32_16x16x32_bf16 a[112:115], v[176:179], v[88:91], a[112:115]
ds_read_b64_tr_b16 v[168:169], v190 offset:192
v_mfma_f32_16x16x32_bf16 a[112:115], v[180:183], v[92:95], a[112:115]
v_mfma_f32_16x16x32_bf16 a[116:119], v[176:179], v[96:99], a[116:119]
ds_read_b64_tr_b16 v[170:171], v190 offset:704
v_mfma_f32_16x16x32_bf16 a[116:119], v[180:183], v[100:103], a[116:119]
v_mfma_f32_16x16x32_bf16 a[120:123], v[176:179], v[104:107], a[120:123]
ds_read_b64_tr_b16 v[172:173], v190 offset:8640
v_mfma_f32_16x16x32_bf16 a[120:123], v[180:183], v[108:111], a[120:123]
v_mfma_f32_16x16x32_bf16 a[124:127], v[176:179], v[112:115], a[124:127]
ds_read_b64_tr_b16 v[174:175], v190 offset:9152
v_mfma_f32_16x16x32_bf16 a[124:127], v[180:183], v[116:119], a[124:127]
s_waitcnt vmcnt(51)
v_mfma_f32_16x16x32_bf16 a[0:3], v[120:123], v[192:195], a[0:3]
ds_read_b64_tr_b16 v[176:177], v190 offset:224
v_mfma_f32_16x16x32_bf16 a[0:3], v[124:127], v[196:199], a[0:3]
buffer_load_dwordx4 v[88:91], v1, s[20:23], s26 offen sc1
v_mfma_f32_16x16x32_bf16 a[4:7], v[120:123], v[200:203], a[4:7]
ds_read_b64_tr_b16 v[178:179], v190 offset:736
v_mfma_f32_16x16x32_bf16 a[4:7], v[124:127], v[204:207], a[4:7]
v_mfma_f32_16x16x32_bf16 a[8:11], v[120:123], v[208:211], a[8:11]
ds_read_b64_tr_b16 v[180:181], v190 offset:8672
v_mfma_f32_16x16x32_bf16 a[8:11], v[124:127], v[212:215], a[8:11]
buffer_load_dwordx4 v[92:95], v1, s[20:23], s26 offen offset:1024 sc1
v_mfma_f32_16x16x32_bf16 a[12:15], v[120:123], v[216:219], a[12:15]
ds_read_b64_tr_b16 v[182:183], v190 offset:9184
v_mfma_f32_16x16x32_bf16 a[12:15], v[124:127], v[220:223], a[12:15]
s_waitcnt lgkmcnt(3)
v_mfma_f32_16x16x32_bf16 a[16:19], v[128:131], v[192:195], a[16:19]
ds_read_b64_tr_b16 v[120:121], v188
v_mfma_f32_16x16x32_bf16 a[16:19], v[132:135], v[196:199], a[16:19]
v_mfma_f32_16x16x32_bf16 a[20:23], v[128:131], v[200:203], a[20:23]
ds_read_b64_tr_b16 v[122:123], v188 offset:512
v_mfma_f32_16x16x32_bf16 a[20:23], v[132:135], v[204:207], a[20:23]
buffer_load_dwordx4 v[96:99], v2, s[20:23], s26 offen sc1
v_mfma_f32_16x16x32_bf16 a[24:27], v[128:131], v[208:211], a[24:27]
ds_read_b64_tr_b16 v[124:125], v188 offset:8448
v_mfma_f32_16x16x32_bf16 a[24:27], v[132:135], v[212:215], a[24:27]
v_mfma_f32_16x16x32_bf16 a[28:31], v[128:131], v[216:219], a[28:31]
ds_read_b64_tr_b16 v[126:127], v188 offset:8960
v_mfma_f32_16x16x32_bf16 a[28:31], v[132:135], v[220:223], a[28:31]
buffer_load_dwordx4 v[100:103], v2, s[20:23], s26 offen offset:1024 sc1
v_mfma_f32_16x16x32_bf16 a[32:35], v[136:139], v[192:195], a[32:35]
ds_read_b64_tr_b16 v[128:129], v188 offset:32
v_mfma_f32_16x16x32_bf16 a[32:35], v[140:143], v[196:199], a[32:35]
v_mfma_f32_16x16x32_bf16 a[36:39], v[136:139], v[200:203], a[36:39]
ds_read_b64_tr_b16 v[130:131], v188 offset:544
v_mfma_f32_16x16x32_bf16 a[36:39], v[140:143], v[204:207], a[36:39]
v_mfma_f32_16x16x32_bf16 a[40:43], v[136:139], v[208:211], a[40:43]
ds_read_b64_tr_b16 v[132:133], v188 offset:8480
v_mfma_f32_16x16x32_bf16 a[40:43], v[140:143], v[212:215], a[40:43]
buffer_load_dwordx4 v[104:107], v3, s[20:23], s26 offen sc1
v_mfma_f32_16x16x32_bf16 a[44:47], v[136:139], v[216:219], a[44:47]
ds_read_b64_tr_b16 v[134:135], v188 offset:8992
v_mfma_f32_16x16x32_bf16 a[44:47], v[140:143], v[220:223], a[44:47]
v_mfma_f32_16x16x32_bf16 a[48:51], v[144:147], v[192:195], a[48:51]
ds_read_b64_tr_b16 v[136:137], v188 offset:64
v_mfma_f32_16x16x32_bf16 a[48:51], v[148:151], v[196:199], a[48:51]
buffer_load_dwordx4 v[108:111], v3, s[20:23], s26 offen offset:1024 sc1
v_mfma_f32_16x16x32_bf16 a[52:55], v[144:147], v[200:203], a[52:55]
ds_read_b64_tr_b16 v[138:139], v188 offset:576
v_mfma_f32_16x16x32_bf16 a[52:55], v[148:151], v[204:207], a[52:55]
v_mfma_f32_16x16x32_bf16 a[56:59], v[144:147], v[208:211], a[56:59]
ds_read_b64_tr_b16 v[140:141], v188 offset:8512
v_mfma_f32_16x16x32_bf16 a[56:59], v[148:151], v[212:215], a[56:59]
v_mfma_f32_16x16x32_bf16 a[60:63], v[144:147], v[216:219], a[60:63]
ds_read_b64_tr_b16 v[142:143], v188 offset:9024
v_mfma_f32_16x16x32_bf16 a[60:63], v[148:151], v[220:223], a[60:63]
buffer_load_dwordx4 v[112:115], v4, s[20:23], s26 offen sc1
v_mfma_f32_16x16x32_bf16 a[64:67], v[152:155], v[192:195], a[64:67]
ds_read_b64_tr_b16 v[144:145], v188 offset:96
v_mfma_f32_16x16x32_bf16 a[64:67], v[156:159], v[196:199], a[64:67]
v_mfma_f32_16x16x32_bf16 a[68:71], v[152:155], v[200:203], a[68:71]
ds_read_b64_tr_b16 v[146:147], v188 offset:608
v_mfma_f32_16x16x32_bf16 a[68:71], v[156:159], v[204:207], a[68:71]
buffer_load_dwordx4 v[116:119], v4, s[20:23], s26 offen offset:1024 sc1
s_add_u32 s26, s26, 0x4000
v_mfma_f32_16x16x32_bf16 a[72:75], v[152:155], v[208:211], a[72:75]
ds_read_b64_tr_b16 v[148:149], v188 offset:8544
)ASM"
R"ASM(v_mfma_f32_16x16x32_bf16 a[72:75], v[156:159], v[212:215], a[72:75]
v_mfma_f32_16x16x32_bf16 a[76:79], v[152:155], v[216:219], a[76:79]
ds_read_b64_tr_b16 v[150:151], v188 offset:9056
v_mfma_f32_16x16x32_bf16 a[76:79], v[156:159], v[220:223], a[76:79]
v_mfma_f32_16x16x32_bf16 a[80:83], v[160:163], v[192:195], a[80:83]
ds_read_b64_tr_b16 v[152:153], v188 offset:128
v_mfma_f32_16x16x32_bf16 a[80:83], v[164:167], v[196:199], a[80:83]
v_mfma_f32_16x16x32_bf16 a[84:87], v[160:163], v[200:203], a[84:87]
ds_read_b64_tr_b16 v[154:155], v188 offset:640
v_mfma_f32_16x16x32_bf16 a[84:87], v[164:167], v[204:207], a[84:87]
v_mfma_f32_16x16x32_bf16 a[88:91], v[160:163], v[208:211], a[88:91]
ds_read_b64_tr_b16 v[156:157], v188 offset:8576
v_mfma_f32_16x16x32_bf16 a[88:91], v[164:167], v[212:215], a[88:91]
v_mfma_f32_16x16x32_bf16 a[92:95], v[160:163], v[216:219], a[92:95]
ds_read_b64_tr_b16 v[158:159], v188 offset:9088
v_mfma_f32_16x16x32_bf16 a[92:95], v[164:167], v[220:223], a[92:95]
v_mfma_f32_16x16x32_bf16 a[96:99], v[168:171], v[192:195], a[96:99]
ds_read_b64_tr_b16 v[160:161], v188 offset:160
v_mfma_f32_16x16x32_bf16 a[96:99], v[172:175], v[196:199], a[96:99]
v_mfma_f32_16x16x32_bf16 a[100:103], v[168:171], v[200:203], a[100:103]
ds_read_b64_tr_b16 v[162:163], v188 offset:672
v_mfma_f32_16x16x32_bf16 a[100:103], v[172:175], v[204:207], a[100:103]
v_mfma_f32_16x16x32_bf16 a[104:107], v[168:171], v[208:211], a[104:107]
ds_read_b64_tr_b16 v[164:165], v188 offset:8608
v_mfma_f32_16x16x32_bf16 a[104:107], v[172:175], v[212:215], a[104:107]
v_mfma_f32_16x16x32_bf16 a[108:111], v[168:171], v[216:219], a[108:111]
ds_read_b64_tr_b16 v[166:167], v188 offset:9120
v_mfma_f32_16x16x32_bf16 a[108:111], v[172:175], v[220:223], a[108:111]
s_waitcnt lgkmcnt(3)
v_mfma_f32_16x16x32_bf16 a[112:115], v[176:179], v[192:195], a[112:115]
ds_read_b64_tr_b16 v[168:169], v188 offset:192
v_mfma_f32_16x16x32_bf16 a[112:115], v[180:183], v[196:199], a[112:115]
v_mfma_f32_16x16x32_bf16 a[116:119], v[176:179], v[200:203], a[116:119]
ds_read_b64_tr_b16 v[170:171], v188 offset:704
v_mfma_f32_16x16x32_bf16 a[116:119], v[180:183], v[204:207], a[116:119]
v_mfma_f32_16x16x32_bf16 a[120:123], v[176:179], v[208:211], a[120:123]
ds_read_b64_tr_b16 v[172:173], v188 offset:8640
v_mfma_f32_16x16x32_bf16 a[120:123], v[180:183], v[212:215], a[120:123]
v_mfma_f32_16x16x32_bf16 a[124:127], v[176:179], v[216:219], a[124:127]
ds_read_b64_tr_b16 v[174:175], v188 offset:9152
v_mfma_f32_16x16x32_bf16 a[124:127], v[180:183], v[220:223], a[124:127]
s_waitcnt vmcnt(40)
s_barrier
v_mfma_f32_16x16x32_bf16 a[0:3], v[120:123], v[224:227], a[0:3]
ds_read_b64_tr_b16 v[176:177], v188 offset:224
v_mfma_f32_16x16x32_bf16 a[0:3], v[124:127], v[228:231], a[0:3]
v_mfma_f32_16x16x32_bf16 a[4:7], v[120:123], v[232:235], a[4:7]
ds_read_b64_tr_b16 v[178:179], v188 offset:736
v_mfma_f32_16x16x32_bf16 a[4:7], v[124:127], v[236:239], a[4:7]
v_mfma_f32_16x16x32_bf16 a[8:11], v[120:123], v[240:243], a[8:11]
ds_read_b64_tr_b16 v[180:181], v188 offset:8672
v_mfma_f32_16x16x32_bf16 a[8:11], v[124:127], v[244:247], a[8:11]
v_mfma_f32_16x16x32_bf16 a[12:15], v[120:123], v[248:251], a[12:15]
ds_read_b64_tr_b16 v[182:183], v188 offset:9184
v_mfma_f32_16x16x32_bf16 a[12:15], v[124:127], v[252:255], a[12:15]
v_mfma_f32_16x16x32_bf16 a[16:19], v[128:131], v[224:227], a[16:19]
ds_read_b64_tr_b16 v[120:121], v188 offset:16896
v_mfma_f32_16x16x32_bf16 a[16:19], v[132:135], v[228:231], a[16:19]
v_mfma_f32_16x16x32_bf16 a[20:23], v[128:131], v[232:235], a[20:23]
ds_read_b64_tr_b16 v[122:123], v188 offset:17408
v_mfma_f32_16x16x32_bf16 a[20:23], v[132:135], v[236:239], a[20:23]
v_mfma_f32_16x16x32_bf16 a[24:27], v[128:131], v[240:243], a[24:27]
ds_read_b64_tr_b16 v[124:125], v188 offset:25344
v_mfma_f32_16x16x32_bf16 a[24:27], v[132:135], v[244:247], a[24:27]
v_mfma_f32_16x16x32_bf16 a[28:31], v[128:131], v[248:251], a[28:31]
ds_read_b64_tr_b16 v[126:127], v188 offset:25856
v_mfma_f32_16x16x32_bf16 a[28:31], v[132:135], v[252:255], a[28:31]
v_mfma_f32_16x16x32_bf16 a[32:35], v[136:139], v[224:227], a[32:35]
ds_read_b64_tr_b16 v[128:129], v188 offset:16928
v_mfma_f32_16x16x32_bf16 a[32:35], v[140:143], v[228:231], a[32:35]
v_mfma_f32_16x16x32_bf16 a[36:39], v[136:139], v[232:235], a[36:39]
ds_read_b64_tr_b16 v[130:131], v188 offset:17440
v_mfma_f32_16x16x32_bf16 a[36:39], v[140:143], v[236:239], a[36:39]
v_mfma_f32_16x16x32_bf16 a[40:43], v[136:139], v[240:243], a[40:43]
ds_read_b64_tr_b16 v[132:133], v188 offset:25376
v_mfma_f32_16x16x32_bf16 a[40:43], v[140:143], v[244:247], a[40:43]
v_mfma_f32_16x16x32_bf16 a[44:47], v[136:139], v[248:251], a[44:47]
ds_read_b64_tr_b16 v[134:135], v188 offset:25888
v_mfma_f32_16x16x32_bf16 a[44:47], v[140:143], v[252:255], a[44:47]
v_mfma_f32_16x16x32_bf16 a[48:51], v[144:147], v[224:227], a[48:51]
ds_read_b64_tr_b16 v[136:137], v188 offset:16960
v_mfma_f32_16x16x32_bf16 a[48:51], v[148:151], v[228:231], a[48:51]
v_mfma_f32_16x16x32_bf16 a[52:55], v[144:147], v[232:235], a[52:55]
ds_read_b64_tr_b16 v[138:139], v188 offset:17472
v_mfma_f32_16x16x32_bf16 a[52:55], v[148:151], v[236:239], a[52:55]
v_mfma_f32_16x16x32_bf16 a[56:59], v[144:147], v[240:243], a[56:59]
ds_read_b64_tr_b16 v[140:141], v188 offset:25408
v_mfma_f32_16x16x32_bf16 a[56:59], v[148:151], v[244:247], a[56:59]
v_mfma_f32_16x16x32_bf16 a[60:63], v[144:147], v[248:251], a[60:63]
ds_read_b64_tr_b16 v[142:143], v188 offset:25920
v_mfma_f32_16x16x32_bf16 a[60:63], v[148:151], v[252:255], a[60:63]
v_mfma_f32_16x16x32_bf16 a[64:67], v[152:155], v[224:227], a[64:67]
ds_read_b64_tr_b16 v[144:145], v188 offset:16992
v_mfma_f32_16x16x32_bf16 a[64:67], v[156:159], v[228:231], a[64:67]
v_mfma_f32_16x16x32_bf16 a[68:71], v[152:155], v[232:235], a[68:71]
ds_read_b64_tr_b16 v[146:147], v188 offset:17504
v_mfma_f32_16x16x32_bf16 a[68:71], v[156:159], v[236:239], a[68:71]
v_mfma_f32_16x16x32_bf16 a[72:75], v[152:155], v[240:243], a[72:75]
ds_read_b64_tr_b16 v[148:149], v188 offset:25440
v_mfma_f32_16x16x32_bf16 a[72:75], v[156:159], v[244:247], a[72:75]
v_mfma_f32_16x16x32_bf16 a[76:79], v[152:155], v[248:251], a[76:79]
ds_read_b64_tr_b16 v[150:151], v188 offset:25952
v_mfma_f32_16x16x32_bf16 a[76:79], v[156:159], v[252:255], a[76:79]
s_waitcnt lgkmcnt(3)
v_mfma_f32_16x16x32_bf16 a[80:83], v[160:163], v[224:227], a[80:83]
ds_read_b64_tr_b16 v[152:153], v188 offset:17024
v_mfma_f32_16x16x32_bf16 a[80:83], v[164:167], v[228:231], a[80:83]
v_mfma_f32_16x16x32_bf16 a[84:87], v[160:163], v[232:235], a[84:87]
ds_read_b64_tr_b16 v[154:155], v188 offset:17536
v_mfma_f32_16x16x32_bf16 a[84:87], v[164:167], v[236:239], a[84:87]
v_mfma_f32_16x16x32_bf16 a[88:91], v[160:163], v[240:243], a[88:91]
ds_read_b64_tr_b16 v[156:157], v188 offset:25472
v_mfma_f32_16x16x32_bf16 a[88:91], v[164:167], v[244:247], a[88:91]
v_mfma_f32_16x16x32_bf16 a[92:95], v[160:163], v[248:251], a[92:95]
ds_read_b64_tr_b16 v[158:159], v188 offset:25984
v_mfma_f32_16x16x32_bf16 a[92:95], v[164:167], v[252:255], a[92:95]
v_mfma_f32_16x16x32_bf16 a[96:99], v[168:171], v[224:227], a[96:99]
ds_read_b64_tr_b16 v[160:161], v188 offset:17056
v_mfma_f32_16x16x32_bf16 a[96:99], v[172:175], v[228:231], a[96:99]
v_mfma_f32_16x16x32_bf16 a[100:103], v[168:171], v[232:235], a[100:103]
ds_read_b64_tr_b16 v[162:163], v188 offset:17568
v_mfma_f32_16x16x32_bf16 a[100:103], v[172:175], v[236:239], a[100:103]
v_mfma_f32_16x16x32_bf16 a[104:107], v[168:171], v[240:243], a[104:107]
ds_read_b64_tr_b16 v[164:165], v188 offset:25504
v_mfma_f32_16x16x32_bf16 a[104:107], v[172:175], v[244:247], a[104:107]
v_mfma_f32_16x16x32_bf16 a[108:111], v[168:171], v[248:251], a[108:111]
ds_read_b64_tr_b16 v[166:167], v188 offset:26016
v_mfma_f32_16x16x32_bf16 a[108:111], v[172:175], v[252:255], a[108:111]
v_mfma_f32_16x16x32_bf16 a[112:115], v[176:179], v[224:227], a[112:115]
ds_read_b64_tr_b16 v[168:169], v188 offset:17088
v_mfma_f32_16x16x32_bf16 a[112:115], v[180:183], v[228:231], a[112:115]
v_mfma_f32_16x16x32_bf16 a[116:119], v[176:179], v[232:235], a[116:119]
ds_read_b64_tr_b16 v[170:171], v188 offset:17600
v_mfma_f32_16x16x32_bf16 a[116:119], v[180:183], v[236:239], a[116:119]
v_mfma_f32_16x16x32_bf16 a[120:123], v[176:179], v[240:243], a[120:123]
ds_read_b64_tr_b16 v[172:173], v188 offset:25536
v_mfma_f32_16x16x32_bf16 a[120:123], v[180:183], v[244:247], a[120:123]
v_mfma_f32_16x16x32_bf16 a[124:127], v[176:179], v[248:251], a[124:127]
ds_read_b64_tr_b16 v[174:175], v188 offset:26048
v_mfma_f32_16x16x32_bf16 a[124:127], v[180:183], v[252:255], a[124:127]
s_waitcnt vmcnt(32)
v_mfma_f32_16x16x32_bf16 a[0:3], v[120:123], v[24:27], a[0:3]
ds_read_b64_tr_b16 v[176:177], v188 offset:17120
v_mbcnt_lo_u32_b32 v14, -1, 0
v_mfma_f32_16x16x32_bf16 a[0:3], v[124:127], v[28:31], a[0:3]
v_mbcnt_hi_u32_b32 v14, -1, v14
v_and_b32 v15, 15, v14
v_mfma_f32_16x16x32_bf16 a[4:7], v[120:123], v[32:35], a[4:7]
ds_read_b64_tr_b16 v[178:179], v188 offset:17632
v_lshrrev_b32 v14, 4, v14
v_mfma_f32_16x16x32_bf16 a[4:7], v[124:127], v[36:39], a[4:7]
v_lshlrev_b32 v14, 3, v14
v_lshl_add_u32 v15, v15, 11, v14
v_mfma_f32_16x16x32_bf16 a[8:11], v[120:123], v[40:43], a[8:11]
ds_read_b64_tr_b16 v[180:181], v188 offset:25568
s_lshl_b32 s36, s72, 8
v_mfma_f32_16x16x32_bf16 a[8:11], v[124:127], v[44:47], a[8:11]
s_lshl_b32 s39, s4, 6
s_add_u32 s36, s36, s39
v_mfma_f32_16x16x32_bf16 a[12:15], v[120:123], v[48:51], a[12:15]
ds_read_b64_tr_b16 v[182:183], v188 offset:26080
s_lshl_b32 s39, s74, 4
v_mfma_f32_16x16x32_bf16 a[12:15], v[124:127], v[52:55], a[12:15]
s_add_u32 s36, s36, s39
s_lshl_b32 s36, s36, 11
v_mfma_f32_16x16x32_bf16 a[16:19], v[128:131], v[24:27], a[16:19]
ds_read_b64_tr_b16 v[120:121], v188 offset:33792
s_lshl_b32 s39, s73, 8
v_mfma_f32_16x16x32_bf16 a[16:19], v[132:135], v[28:31], a[16:19]
s_add_u32 s36, s36, s39
s_mov_b32 s8, s48
v_mfma_f32_16x16x32_bf16 a[20:23], v[128:131], v[32:35], a[20:23]
ds_read_b64_tr_b16 v[122:123], v188 offset:34304
s_and_b32 s9, s49, 0xffff
v_mfma_f32_16x16x32_bf16 a[20:23], v[132:135], v[36:39], a[20:23]
s_mov_b32 s10, 0x400000
s_mov_b32 s11, 0x20000
v_mfma_f32_16x16x32_bf16 a[24:27], v[128:131], v[40:43], a[24:27]
ds_read_b64_tr_b16 v[124:125], v188 offset:42240
s_mov_b32 s12, s64
v_mfma_f32_16x16x32_bf16 a[24:27], v[132:135], v[44:47], a[24:27]
s_and_b32 s13, s65, 0xffff
s_mov_b32 s14, 0x400000
v_mfma_f32_16x16x32_bf16 a[28:31], v[128:131], v[48:51], a[28:31]
ds_read_b64_tr_b16 v[126:127], v188 offset:42752
s_mov_b32 s15, 0x20000
v_mfma_f32_16x16x32_bf16 a[28:31], v[132:135], v[52:55], a[28:31]
buffer_load_dwordx2 v[224:225], v15, s[8:11], s36 offen
v_mfma_f32_16x16x32_bf16 a[32:35], v[136:139], v[24:27], a[32:35]
ds_read_b64_tr_b16 v[128:129], v188 offset:33824
v_mfma_f32_16x16x32_bf16 a[32:35], v[140:143], v[28:31], a[32:35]
buffer_load_dwordx2 v[226:227], v15, s[8:11], s36 offen offset:32
v_mfma_f32_16x16x32_bf16 a[36:39], v[136:139], v[32:35], a[36:39]
ds_read_b64_tr_b16 v[130:131], v188 offset:34336
v_mfma_f32_16x16x32_bf16 a[36:39], v[140:143], v[36:39], a[36:39]
buffer_load_dwordx2 v[228:229], v15, s[8:11], s36 offen offset:64
v_mfma_f32_16x16x32_bf16 a[40:43], v[136:139], v[40:43], a[40:43]
ds_read_b64_tr_b16 v[132:133], v188 offset:42272
v_mfma_f32_16x16x32_bf16 a[40:43], v[140:143], v[44:47], a[40:43]
buffer_load_dwordx2 v[230:231], v15, s[8:11], s36 offen offset:96
v_mfma_f32_16x16x32_bf16 a[44:47], v[136:139], v[48:51], a[44:47]
ds_read_b64_tr_b16 v[134:135], v188 offset:42784
v_mfma_f32_16x16x32_bf16 a[44:47], v[140:143], v[52:55], a[44:47]
buffer_load_dwordx2 v[232:233], v15, s[8:11], s36 offen offset:128
s_waitcnt lgkmcnt(3)
v_mfma_f32_16x16x32_bf16 a[48:51], v[144:147], v[24:27], a[48:51]
ds_read_b64_tr_b16 v[136:137], v188 offset:33856
v_mfma_f32_16x16x32_bf16 a[48:51], v[148:151], v[28:31], a[48:51]
buffer_load_dwordx2 v[234:235], v15, s[8:11], s36 offen offset:160
v_mfma_f32_16x16x32_bf16 a[52:55], v[144:147], v[32:35], a[52:55]
ds_read_b64_tr_b16 v[138:139], v188 offset:34368
v_mfma_f32_16x16x32_bf16 a[52:55], v[148:151], v[36:39], a[52:55]
buffer_load_dwordx2 v[236:237], v15, s[8:11], s36 offen offset:192
v_mfma_f32_16x16x32_bf16 a[56:59], v[144:147], v[40:43], a[56:59]
ds_read_b64_tr_b16 v[140:141], v188 offset:42304
v_mfma_f32_16x16x32_bf16 a[56:59], v[148:151], v[44:47], a[56:59]
buffer_load_dwordx2 v[238:239], v15, s[8:11], s36 offen offset:224
v_mfma_f32_16x16x32_bf16 a[60:63], v[144:147], v[48:51], a[60:63]
ds_read_b64_tr_b16 v[142:143], v188 offset:42816
v_mfma_f32_16x16x32_bf16 a[60:63], v[148:151], v[52:55], a[60:63]
v_mfma_f32_16x16x32_bf16 a[64:67], v[152:155], v[24:27], a[64:67]
ds_read_b64_tr_b16 v[144:145], v188 offset:33888
v_mfma_f32_16x16x32_bf16 a[64:67], v[156:159], v[28:31], a[64:67]
v_mfma_f32_16x16x32_bf16 a[68:71], v[152:155], v[32:35], a[68:71]
ds_read_b64_tr_b16 v[146:147], v188 offset:34400
v_mfma_f32_16x16x32_bf16 a[68:71], v[156:159], v[36:39], a[68:71]
v_mfma_f32_16x16x32_bf16 a[72:75], v[152:155], v[40:43], a[72:75]
ds_read_b64_tr_b16 v[148:149], v188 offset:42336
v_mfma_f32_16x16x32_bf16 a[72:75], v[156:159], v[44:47], a[72:75]
v_mfma_f32_16x16x32_bf16 a[76:79], v[152:155], v[48:51], a[76:79]
ds_read_b64_tr_b16 v[150:151], v188 offset:42848
v_mfma_f32_16x16x32_bf16 a[76:79], v[156:159], v[52:55], a[76:79]
v_mfma_f32_16x16x32_bf16 a[80:83], v[160:163], v[24:27], a[80:83]
ds_read_b64_tr_b16 v[152:153], v188 offset:33920
v_mfma_f32_16x16x32_bf16 a[80:83], v[164:167], v[28:31], a[80:83]
v_mfma_f32_16x16x32_bf16 a[84:87], v[160:163], v[32:35], a[84:87]
ds_read_b64_tr_b16 v[154:155], v188 offset:34432
v_mfma_f32_16x16x32_bf16 a[84:87], v[164:167], v[36:39], a[84:87]
v_mfma_f32_16x16x32_bf16 a[88:91], v[160:163], v[40:43], a[88:91]
ds_read_b64_tr_b16 v[156:157], v188 offset:42368
v_mfma_f32_16x16x32_bf16 a[88:91], v[164:167], v[44:47], a[88:91]
v_mfma_f32_16x16x32_bf16 a[92:95], v[160:163], v[48:51], a[92:95]
ds_read_b64_tr_b16 v[158:159], v188 offset:42880
v_mfma_f32_16x16x32_bf16 a[92:95], v[164:167], v[52:55], a[92:95]
v_mfma_f32_16x16x32_bf16 a[96:99], v[168:171], v[24:27], a[96:99]
ds_read_b64_tr_b16 v[160:161], v188 offset:33952
v_mfma_f32_16x16x32_bf16 a[96:99], v[172:175], v[28:31], a[96:99]
v_mfma_f32_16x16x32_bf16 a[100:103], v[168:171], v[32:35], a[100:103]
ds_read_b64_tr_b16 v[162:163], v188 offset:34464
v_mfma_f32_16x16x32_bf16 a[100:103], v[172:175], v[36:39], a[100:103]
v_mfma_f32_16x16x32_bf16 a[104:107], v[168:171], v[40:43], a[104:107]
ds_read_b64_tr_b16 v[164:165], v188 offset:42400
v_mfma_f32_16x16x32_bf16 a[104:107], v[172:175], v[44:47], a[104:107]
v_mfma_f32_16x16x32_bf16 a[108:111], v[168:171], v[48:51], a[108:111]
ds_read_b64_tr_b16 v[166:167], v188 offset:42912
v_mfma_f32_16x16x32_bf16 a[108:111], v[172:175], v[52:55], a[108:111]
v_mfma_f32_16x16x32_bf16 a[112:115], v[176:179], v[24:27], a[112:115]
ds_read_b64_tr_b16 v[168:169], v188 offset:33984
v_mfma_f32_16x16x32_bf16 a[112:115], v[180:183], v[28:31], a[112:115]
v_mfma_f32_16x16x32_bf16 a[116:119], v[176:179], v[32:35], a[116:119]
ds_read_b64_tr_b16 v[170:171], v188 offset:34496
v_mfma_f32_16x16x32_bf16 a[116:119], v[180:183], v[36:39], a[116:119]
v_mfma_f32_16x16x32_bf16 a[120:123], v[176:179], v[40:43], a[120:123]
ds_read_b64_tr_b16 v[172:173], v188 offset:42432
v_mfma_f32_16x16x32_bf16 a[120:123], v[180:183], v[44:47], a[120:123]
v_mfma_f32_16x16x32_bf16 a[124:127], v[176:179], v[48:51], a[124:127]
ds_read_b64_tr_b16 v[174:175], v188 offset:42944
v_mfma_f32_16x16x32_bf16 a[124:127], v[180:183], v[52:55], a[124:127]
s_waitcnt vmcnt(16)
s_barrier
v_mfma_f32_16x16x32_bf16 a[12:15], v[120:123], v[80:83], a[12:15]
ds_read_b64_tr_b16 v[176:177], v188 offset:34016
ds_read_b64_tr_b16 v[178:179], v188 offset:34528
v_mfma_f32_16x16x32_bf16 a[12:15], v[124:127], v[84:87], a[12:15]
ds_read_b64_tr_b16 v[180:181], v188 offset:42464
ds_read_b64_tr_b16 v[182:183], v188 offset:42976
s_waitcnt lgkmcnt(6)
v_mfma_f32_16x16x32_bf16 a[28:31], v[128:131], v[80:83], a[28:31]
ds_read_b64_tr_b16 v[120:121], v189
ds_read_b64_tr_b16 v[122:123], v189 offset:512
v_mfma_f32_16x16x32_bf16 a[28:31], v[132:135], v[84:87], a[28:31]
ds_read_b64_tr_b16 v[124:125], v189 offset:8448
ds_read_b64_tr_b16 v[126:127], v189 offset:8960
v_mfma_f32_16x16x32_bf16 a[44:47], v[136:139], v[80:83], a[44:47]
ds_read_b64_tr_b16 v[128:129], v189 offset:32
ds_read_b64_tr_b16 v[130:131], v189 offset:544
v_mfma_f32_16x16x32_bf16 a[44:47], v[140:143], v[84:87], a[44:47]
ds_read_b64_tr_b16 v[132:133], v189 offset:8480
ds_read_b64_tr_b16 v[134:135], v189 offset:8992
v_mfma_f32_16x16x32_bf16 a[60:63], v[144:147], v[80:83], a[60:63]
ds_read_b64_tr_b16 v[136:137], v189 offset:64
ds_read_b64_tr_b16 v[138:139], v189 offset:576
v_mfma_f32_16x16x32_bf16 a[60:63], v[148:151], v[84:87], a[60:63]
ds_read_b64_tr_b16 v[140:141], v189 offset:8512
ds_read_b64_tr_b16 v[142:143], v189 offset:9024
v_mfma_f32_16x16x32_bf16 a[76:79], v[152:155], v[80:83], a[76:79]
ds_read_b64_tr_b16 v[144:145], v189 offset:96
ds_read_b64_tr_b16 v[146:147], v189 offset:608
v_mfma_f32_16x16x32_bf16 a[76:79], v[156:159], v[84:87], a[76:79]
ds_read_b64_tr_b16 v[148:149], v189 offset:8544
ds_read_b64_tr_b16 v[150:151], v189 offset:9056
v_mfma_f32_16x16x32_bf16 a[92:95], v[160:163], v[80:83], a[92:95]
ds_read_b64_tr_b16 v[152:153], v189 offset:128
ds_read_b64_tr_b16 v[154:155], v189 offset:640
v_mfma_f32_16x16x32_bf16 a[92:95], v[164:167], v[84:87], a[92:95]
ds_read_b64_tr_b16 v[156:157], v189 offset:8576
ds_read_b64_tr_b16 v[158:159], v189 offset:9088
v_mfma_f32_16x16x32_bf16 a[108:111], v[168:171], v[80:83], a[108:111]
ds_read_b64_tr_b16 v[160:161], v189 offset:160
ds_read_b64_tr_b16 v[162:163], v189 offset:672
s_waitcnt lgkmcnt(12)
v_mfma_f32_16x16x32_bf16 a[108:111], v[172:175], v[84:87], a[108:111]
ds_read_b64_tr_b16 v[164:165], v189 offset:8608
ds_read_b64_tr_b16 v[166:167], v189 offset:9120
v_mfma_f32_16x16x32_bf16 a[124:127], v[176:179], v[80:83], a[124:127]
ds_read_b64_tr_b16 v[168:169], v189 offset:192
ds_read_b64_tr_b16 v[170:171], v189 offset:704
v_mfma_f32_16x16x32_bf16 a[124:127], v[180:183], v[84:87], a[124:127]
ds_read_b64_tr_b16 v[172:173], v189 offset:8640
ds_read_b64_tr_b16 v[174:175], v189 offset:9152
s_waitcnt vmcnt(8)
v_mfma_f32_16x16x32_bf16 a[12:15], v[120:123], v[112:115], a[12:15]
ds_read_b64_tr_b16 v[176:177], v189 offset:224
ds_read_b64_tr_b16 v[178:179], v189 offset:736
v_mfma_f32_16x16x32_bf16 a[12:15], v[124:127], v[116:119], a[12:15]
ds_read_b64_tr_b16 v[180:181], v189 offset:8672
ds_read_b64_tr_b16 v[182:183], v189 offset:9184
v_mfma_f32_16x16x32_bf16 a[28:31], v[128:131], v[112:115], a[28:31]
ds_read_b64_tr_b16 v[120:121], v188 offset:33792
ds_read_b64_tr_b16 v[122:123], v188 offset:34304
v_mfma_f32_16x16x32_bf16 a[28:31], v[132:135], v[116:119], a[28:31]
ds_read_b64_tr_b16 v[124:125], v188 offset:42240
ds_read_b64_tr_b16 v[126:127], v188 offset:42752
v_mfma_f32_16x16x32_bf16 a[44:47], v[136:139], v[112:115], a[44:47]
ds_read_b64_tr_b16 v[128:129], v188 offset:33824
ds_read_b64_tr_b16 v[130:131], v188 offset:34336
s_waitcnt lgkmcnt(12)
v_mfma_f32_16x16x32_bf16 a[44:47], v[140:143], v[116:119], a[44:47]
ds_read_b64_tr_b16 v[132:133], v188 offset:42272
ds_read_b64_tr_b16 v[134:135], v188 offset:42784
v_mfma_f32_16x16x32_bf16 a[60:63], v[144:147], v[112:115], a[60:63]
ds_read_b64_tr_b16 v[136:137], v188 offset:33856
ds_read_b64_tr_b16 v[138:139], v188 offset:34368
v_mfma_f32_16x16x32_bf16 a[60:63], v[148:151], v[116:119], a[60:63]
ds_read_b64_tr_b16 v[140:141], v188 offset:42304
ds_read_b64_tr_b16 v[142:143], v188 offset:42816
v_mfma_f32_16x16x32_bf16 a[76:79], v[152:155], v[112:115], a[76:79]
ds_read_b64_tr_b16 v[144:145], v188 offset:33888
ds_read_b64_tr_b16 v[146:147], v188 offset:34400
v_mfma_f32_16x16x32_bf16 a[76:79], v[156:159], v[116:119], a[76:79]
ds_read_b64_tr_b16 v[148:149], v188 offset:42336
ds_read_b64_tr_b16 v[150:151], v188 offset:42848
v_mfma_f32_16x16x32_bf16 a[92:95], v[160:163], v[112:115], a[92:95]
ds_read_b64_tr_b16 v[152:153], v188 offset:33920
ds_read_b64_tr_b16 v[154:155], v188 offset:34432
v_mfma_f32_16x16x32_bf16 a[92:95], v[164:167], v[116:119], a[92:95]
ds_read_b64_tr_b16 v[156:157], v188 offset:42368
ds_read_b64_tr_b16 v[158:159], v188 offset:42880
v_mfma_f32_16x16x32_bf16 a[108:111], v[168:171], v[112:115], a[108:111]
ds_read_b64_tr_b16 v[160:161], v188 offset:33952
ds_read_b64_tr_b16 v[162:163], v188 offset:34464
s_waitcnt lgkmcnt(12)
v_mfma_f32_16x16x32_bf16 a[108:111], v[172:175], v[116:119], a[108:111]
ds_read_b64_tr_b16 v[164:165], v188 offset:42400
ds_read_b64_tr_b16 v[166:167], v188 offset:42912
v_mfma_f32_16x16x32_bf16 a[124:127], v[176:179], v[112:115], a[124:127]
ds_read_b64_tr_b16 v[168:169], v188 offset:33984
ds_read_b64_tr_b16 v[170:171], v188 offset:34496
v_mfma_f32_16x16x32_bf16 a[124:127], v[180:183], v[116:119], a[124:127]
ds_read_b64_tr_b16 v[172:173], v188 offset:42432
ds_read_b64_tr_b16 v[174:175], v188 offset:42944
v_mfma_f32_16x16x32_bf16 a[8:11], v[120:123], v[72:75], a[8:11]
ds_read_b64_tr_b16 v[176:177], v188 offset:34016
ds_read_b64_tr_b16 v[178:179], v188 offset:34528
v_mfma_f32_16x16x32_bf16 a[8:11], v[124:127], v[76:79], a[8:11]
ds_read_b64_tr_b16 v[180:181], v188 offset:42464
ds_read_b64_tr_b16 v[182:183], v188 offset:42976
v_mfma_f32_16x16x32_bf16 a[24:27], v[128:131], v[72:75], a[24:27]
ds_read_b64_tr_b16 v[120:121], v189
ds_read_b64_tr_b16 v[122:123], v189 offset:512
s_add_u32 s37, s74, 3
s_and_b32 s37, s37, 3
v_mfma_f32_16x16x32_bf16 a[24:27], v[132:135], v[76:79], a[24:27]
ds_read_b64_tr_b16 v[124:125], v189 offset:8448
ds_read_b64_tr_b16 v[126:127], v189 offset:8960
s_lshl_b32 s37, s37, 1
s_and_b32 s35, s5, 0xf9
v_mfma_f32_16x16x32_bf16 a[40:43], v[136:139], v[72:75], a[40:43]
ds_read_b64_tr_b16 v[128:129], v189 offset:32
ds_read_b64_tr_b16 v[130:131], v189 offset:544
s_or_b32 s37, s37, s35
s_mul_i32 s37, s37, 0x18000
s_waitcnt lgkmcnt(12)
v_mfma_f32_16x16x32_bf16 a[40:43], v[140:143], v[76:79], a[40:43]
ds_read_b64_tr_b16 v[132:133], v189 offset:8480
ds_read_b64_tr_b16 v[134:135], v189 offset:8992
s_mul_i32 s35, s4, 0x6000
s_add_u32 s37, s37, s35
v_mfma_f32_16x16x32_bf16 a[56:59], v[144:147], v[72:75], a[56:59]
ds_read_b64_tr_b16 v[136:137], v189 offset:64
)ASM"
R"ASM(ds_read_b64_tr_b16 v[138:139], v189 offset:576
s_add_u32 s37, s37, 0x2303000
v_accvgpr_read_b32 v6, a12
v_mfma_f32_16x16x32_bf16 a[56:59], v[148:151], v[76:79], a[56:59]
ds_read_b64_tr_b16 v[140:141], v189 offset:8512
ds_read_b64_tr_b16 v[142:143], v189 offset:9024
v_accvgpr_read_b32 v7, a13
v_accvgpr_read_b32 v8, a14
v_mfma_f32_16x16x32_bf16 a[72:75], v[152:155], v[72:75], a[72:75]
ds_read_b64_tr_b16 v[144:145], v189 offset:96
ds_read_b64_tr_b16 v[146:147], v189 offset:608
v_accvgpr_read_b32 v9, a15
v_accvgpr_read_b32 v10, a28
v_mfma_f32_16x16x32_bf16 a[72:75], v[156:159], v[76:79], a[72:75]
ds_read_b64_tr_b16 v[148:149], v189 offset:8544
ds_read_b64_tr_b16 v[150:151], v189 offset:9056
v_accvgpr_read_b32 v11, a29
v_accvgpr_read_b32 v12, a30
v_mfma_f32_16x16x32_bf16 a[88:91], v[160:163], v[72:75], a[88:91]
ds_read_b64_tr_b16 v[152:153], v189 offset:128
ds_read_b64_tr_b16 v[154:155], v189 offset:640
v_accvgpr_read_b32 v13, a31
v_cvt_pk_bf16_f32 v6, v6, v7
v_mfma_f32_16x16x32_bf16 a[88:91], v[164:167], v[76:79], a[88:91]
ds_read_b64_tr_b16 v[156:157], v189 offset:8576
ds_read_b64_tr_b16 v[158:159], v189 offset:9088
v_cvt_pk_bf16_f32 v7, v8, v9
v_cvt_pk_bf16_f32 v8, v10, v11
v_mfma_f32_16x16x32_bf16 a[104:107], v[168:171], v[72:75], a[104:107]
ds_read_b64_tr_b16 v[160:161], v189 offset:160
ds_read_b64_tr_b16 v[162:163], v189 offset:672
v_cvt_pk_bf16_f32 v9, v12, v13
buffer_store_dwordx4 v[6:9], v0, s[76:79], s37 offen sc1
s_waitcnt lgkmcnt(12)
v_mfma_f32_16x16x32_bf16 a[104:107], v[172:175], v[76:79], a[104:107]
ds_read_b64_tr_b16 v[164:165], v189 offset:8608
ds_read_b64_tr_b16 v[166:167], v189 offset:9120
v_accvgpr_read_b32 v6, a44
v_accvgpr_read_b32 v7, a45
v_mfma_f32_16x16x32_bf16 a[120:123], v[176:179], v[72:75], a[120:123]
ds_read_b64_tr_b16 v[168:169], v189 offset:192
ds_read_b64_tr_b16 v[170:171], v189 offset:704
v_accvgpr_read_b32 v8, a46
v_accvgpr_read_b32 v9, a47
v_mfma_f32_16x16x32_bf16 a[120:123], v[180:183], v[76:79], a[120:123]
ds_read_b64_tr_b16 v[172:173], v189 offset:8640
ds_read_b64_tr_b16 v[174:175], v189 offset:9152
v_accvgpr_read_b32 v10, a60
v_accvgpr_read_b32 v11, a61
v_mfma_f32_16x16x32_bf16 a[8:11], v[120:123], v[104:107], a[8:11]
ds_read_b64_tr_b16 v[176:177], v189 offset:224
ds_read_b64_tr_b16 v[178:179], v189 offset:736
v_mfma_f32_16x16x32_bf16 a[8:11], v[124:127], v[108:111], a[8:11]
ds_read_b64_tr_b16 v[180:181], v189 offset:8672
ds_read_b64_tr_b16 v[182:183], v189 offset:9184
v_mfma_f32_16x16x32_bf16 a[24:27], v[128:131], v[104:107], a[24:27]
ds_read_b64_tr_b16 v[120:121], v188 offset:33792
ds_read_b64_tr_b16 v[122:123], v188 offset:34304
v_accvgpr_read_b32 v12, a62
v_accvgpr_read_b32 v13, a63
v_mfma_f32_16x16x32_bf16 a[24:27], v[132:135], v[108:111], a[24:27]
ds_read_b64_tr_b16 v[124:125], v188 offset:42240
ds_read_b64_tr_b16 v[126:127], v188 offset:42752
v_cvt_pk_bf16_f32 v6, v6, v7
v_cvt_pk_bf16_f32 v7, v8, v9
v_mfma_f32_16x16x32_bf16 a[40:43], v[136:139], v[104:107], a[40:43]
ds_read_b64_tr_b16 v[128:129], v188 offset:33824
ds_read_b64_tr_b16 v[130:131], v188 offset:34336
v_cvt_pk_bf16_f32 v8, v10, v11
v_cvt_pk_bf16_f32 v9, v12, v13
s_waitcnt lgkmcnt(12)
v_mfma_f32_16x16x32_bf16 a[40:43], v[140:143], v[108:111], a[40:43]
ds_read_b64_tr_b16 v[132:133], v188 offset:42272
ds_read_b64_tr_b16 v[134:135], v188 offset:42784
buffer_store_dwordx4 v[6:9], v0, s[76:79], s37 offen offset:1024 sc1
s_nop 1
v_accvgpr_read_b32 v6, a76
v_mfma_f32_16x16x32_bf16 a[56:59], v[144:147], v[104:107], a[56:59]
ds_read_b64_tr_b16 v[136:137], v188 offset:33856
ds_read_b64_tr_b16 v[138:139], v188 offset:34368
v_accvgpr_read_b32 v7, a77
v_accvgpr_read_b32 v8, a78
v_mfma_f32_16x16x32_bf16 a[56:59], v[148:151], v[108:111], a[56:59]
ds_read_b64_tr_b16 v[140:141], v188 offset:42304
ds_read_b64_tr_b16 v[142:143], v188 offset:42816
v_accvgpr_read_b32 v9, a79
v_accvgpr_read_b32 v10, a92
v_mfma_f32_16x16x32_bf16 a[72:75], v[152:155], v[104:107], a[72:75]
ds_read_b64_tr_b16 v[144:145], v188 offset:33888
ds_read_b64_tr_b16 v[146:147], v188 offset:34400
v_accvgpr_read_b32 v11, a93
v_accvgpr_read_b32 v12, a94
v_mfma_f32_16x16x32_bf16 a[72:75], v[156:159], v[108:111], a[72:75]
ds_read_b64_tr_b16 v[148:149], v188 offset:42336
ds_read_b64_tr_b16 v[150:151], v188 offset:42848
v_accvgpr_read_b32 v13, a95
v_cvt_pk_bf16_f32 v6, v6, v7
v_mfma_f32_16x16x32_bf16 a[88:91], v[160:163], v[104:107], a[88:91]
ds_read_b64_tr_b16 v[152:153], v188 offset:33920
ds_read_b64_tr_b16 v[154:155], v188 offset:34432
v_cvt_pk_bf16_f32 v7, v8, v9
v_cvt_pk_bf16_f32 v8, v10, v11
v_mfma_f32_16x16x32_bf16 a[88:91], v[164:167], v[108:111], a[88:91]
ds_read_b64_tr_b16 v[156:157], v188 offset:42368
ds_read_b64_tr_b16 v[158:159], v188 offset:42880
v_cvt_pk_bf16_f32 v9, v12, v13
buffer_store_dwordx4 v[6:9], v0, s[76:79], s37 offen offset:2048 sc1
v_mfma_f32_16x16x32_bf16 a[104:107], v[168:171], v[104:107], a[104:107]
ds_read_b64_tr_b16 v[160:161], v188 offset:33952
ds_read_b64_tr_b16 v[162:163], v188 offset:34464
v_accvgpr_read_b32 v6, a108
v_accvgpr_read_b32 v7, a109
s_waitcnt lgkmcnt(12)
v_mfma_f32_16x16x32_bf16 a[104:107], v[172:175], v[108:111], a[104:107]
ds_read_b64_tr_b16 v[164:165], v188 offset:42400
ds_read_b64_tr_b16 v[166:167], v188 offset:42912
v_accvgpr_read_b32 v8, a110
v_accvgpr_read_b32 v9, a111
v_mfma_f32_16x16x32_bf16 a[120:123], v[176:179], v[104:107], a[120:123]
ds_read_b64_tr_b16 v[168:169], v188 offset:33984
ds_read_b64_tr_b16 v[170:171], v188 offset:34496
v_accvgpr_read_b32 v10, a124
v_accvgpr_read_b32 v11, a125
v_mfma_f32_16x16x32_bf16 a[120:123], v[180:183], v[108:111], a[120:123]
ds_read_b64_tr_b16 v[172:173], v188 offset:42432
ds_read_b64_tr_b16 v[174:175], v188 offset:42944
v_accvgpr_read_b32 v12, a126
v_accvgpr_read_b32 v13, a127
v_mfma_f32_16x16x32_bf16 a[4:7], v[120:123], v[64:67], a[4:7]
ds_read_b64_tr_b16 v[176:177], v188 offset:34016
ds_read_b64_tr_b16 v[178:179], v188 offset:34528
v_mfma_f32_16x16x32_bf16 a[4:7], v[124:127], v[68:71], a[4:7]
ds_read_b64_tr_b16 v[180:181], v188 offset:42464
ds_read_b64_tr_b16 v[182:183], v188 offset:42976
v_mfma_f32_16x16x32_bf16 a[20:23], v[128:131], v[64:67], a[20:23]
ds_read_b64_tr_b16 v[120:121], v189
ds_read_b64_tr_b16 v[122:123], v189 offset:512
v_cvt_pk_bf16_f32 v6, v6, v7
v_cvt_pk_bf16_f32 v7, v8, v9
v_mfma_f32_16x16x32_bf16 a[20:23], v[132:135], v[68:71], a[20:23]
ds_read_b64_tr_b16 v[124:125], v189 offset:8448
ds_read_b64_tr_b16 v[126:127], v189 offset:8960
v_cvt_pk_bf16_f32 v8, v10, v11
v_cvt_pk_bf16_f32 v9, v12, v13
v_mfma_f32_16x16x32_bf16 a[36:39], v[136:139], v[64:67], a[36:39]
ds_read_b64_tr_b16 v[128:129], v189 offset:32
ds_read_b64_tr_b16 v[130:131], v189 offset:544
buffer_store_dwordx4 v[6:9], v0, s[76:79], s37 offen offset:3072 sc1
s_add_u32 s37, s74, 2
s_waitcnt lgkmcnt(12)
v_mfma_f32_16x16x32_bf16 a[36:39], v[140:143], v[68:71], a[36:39]
ds_read_b64_tr_b16 v[132:133], v189 offset:8480
ds_read_b64_tr_b16 v[134:135], v189 offset:8992
s_and_b32 s37, s37, 3
s_lshl_b32 s37, s37, 1
v_mfma_f32_16x16x32_bf16 a[52:55], v[144:147], v[64:67], a[52:55]
ds_read_b64_tr_b16 v[136:137], v189 offset:64
ds_read_b64_tr_b16 v[138:139], v189 offset:576
s_and_b32 s35, s5, 0xf9
s_or_b32 s37, s37, s35
v_mfma_f32_16x16x32_bf16 a[52:55], v[148:151], v[68:71], a[52:55]
ds_read_b64_tr_b16 v[140:141], v189 offset:8512
ds_read_b64_tr_b16 v[142:143], v189 offset:9024
s_mul_i32 s37, s37, 0x18000
s_mul_i32 s35, s4, 0x6000
v_mfma_f32_16x16x32_bf16 a[68:71], v[152:155], v[64:67], a[68:71]
ds_read_b64_tr_b16 v[144:145], v189 offset:96
ds_read_b64_tr_b16 v[146:147], v189 offset:608
s_add_u32 s37, s37, s35
s_add_u32 s37, s37, 0x2304000
v_mfma_f32_16x16x32_bf16 a[68:71], v[156:159], v[68:71], a[68:71]
ds_read_b64_tr_b16 v[148:149], v189 offset:8544
ds_read_b64_tr_b16 v[150:151], v189 offset:9056
v_accvgpr_read_b32 v6, a8
v_accvgpr_read_b32 v7, a9
v_mfma_f32_16x16x32_bf16 a[84:87], v[160:163], v[64:67], a[84:87]
ds_read_b64_tr_b16 v[152:153], v189 offset:128
ds_read_b64_tr_b16 v[154:155], v189 offset:640
v_accvgpr_read_b32 v8, a10
v_accvgpr_read_b32 v9, a11
v_mfma_f32_16x16x32_bf16 a[84:87], v[164:167], v[68:71], a[84:87]
ds_read_b64_tr_b16 v[156:157], v189 offset:8576
ds_read_b64_tr_b16 v[158:159], v189 offset:9088
v_accvgpr_read_b32 v10, a24
v_accvgpr_read_b32 v11, a25
v_mfma_f32_16x16x32_bf16 a[100:103], v[168:171], v[64:67], a[100:103]
ds_read_b64_tr_b16 v[160:161], v189 offset:160
ds_read_b64_tr_b16 v[162:163], v189 offset:672
v_accvgpr_read_b32 v12, a26
v_accvgpr_read_b32 v13, a27
s_waitcnt lgkmcnt(12)
v_mfma_f32_16x16x32_bf16 a[100:103], v[172:175], v[68:71], a[100:103]
ds_read_b64_tr_b16 v[164:165], v189 offset:8608
ds_read_b64_tr_b16 v[166:167], v189 offset:9120
v_cvt_pk_bf16_f32 v6, v6, v7
v_cvt_pk_bf16_f32 v7, v8, v9
v_mfma_f32_16x16x32_bf16 a[116:119], v[176:179], v[64:67], a[116:119]
ds_read_b64_tr_b16 v[168:169], v189 offset:192
ds_read_b64_tr_b16 v[170:171], v189 offset:704
v_cvt_pk_bf16_f32 v8, v10, v11
v_cvt_pk_bf16_f32 v9, v12, v13
v_mfma_f32_16x16x32_bf16 a[116:119], v[180:183], v[68:71], a[116:119]
ds_read_b64_tr_b16 v[172:173], v189 offset:8640
ds_read_b64_tr_b16 v[174:175], v189 offset:9152
buffer_store_dwordx4 v[6:9], v0, s[76:79], s37 offen sc1
s_nop 1
v_accvgpr_read_b32 v6, a40
v_mfma_f32_16x16x32_bf16 a[4:7], v[120:123], v[96:99], a[4:7]
ds_read_b64_tr_b16 v[176:177], v189 offset:224
ds_read_b64_tr_b16 v[178:179], v189 offset:736
v_mfma_f32_16x16x32_bf16 a[4:7], v[124:127], v[100:103], a[4:7]
ds_read_b64_tr_b16 v[180:181], v189 offset:8672
ds_read_b64_tr_b16 v[182:183], v189 offset:9184
v_mfma_f32_16x16x32_bf16 a[20:23], v[128:131], v[96:99], a[20:23]
ds_read_b64_tr_b16 v[120:121], v188 offset:33792
ds_read_b64_tr_b16 v[122:123], v188 offset:34304
v_accvgpr_read_b32 v7, a41
v_accvgpr_read_b32 v8, a42
v_mfma_f32_16x16x32_bf16 a[20:23], v[132:135], v[100:103], a[20:23]
ds_read_b64_tr_b16 v[124:125], v188 offset:42240
ds_read_b64_tr_b16 v[126:127], v188 offset:42752
v_accvgpr_read_b32 v9, a43
v_accvgpr_read_b32 v10, a56
v_mfma_f32_16x16x32_bf16 a[36:39], v[136:139], v[96:99], a[36:39]
ds_read_b64_tr_b16 v[128:129], v188 offset:33824
ds_read_b64_tr_b16 v[130:131], v188 offset:34336
v_accvgpr_read_b32 v11, a57
v_accvgpr_read_b32 v12, a58
s_waitcnt lgkmcnt(12)
v_mfma_f32_16x16x32_bf16 a[36:39], v[140:143], v[100:103], a[36:39]
ds_read_b64_tr_b16 v[132:133], v188 offset:42272
ds_read_b64_tr_b16 v[134:135], v188 offset:42784
v_accvgpr_read_b32 v13, a59
v_cvt_pk_bf16_f32 v6, v6, v7
v_mfma_f32_16x16x32_bf16 a[52:55], v[144:147], v[96:99], a[52:55]
ds_read_b64_tr_b16 v[136:137], v188 offset:33856
ds_read_b64_tr_b16 v[138:139], v188 offset:34368
v_cvt_pk_bf16_f32 v7, v8, v9
v_cvt_pk_bf16_f32 v8, v10, v11
v_mfma_f32_16x16x32_bf16 a[52:55], v[148:151], v[100:103], a[52:55]
ds_read_b64_tr_b16 v[140:141], v188 offset:42304
ds_read_b64_tr_b16 v[142:143], v188 offset:42816
v_cvt_pk_bf16_f32 v9, v12, v13
buffer_store_dwordx4 v[6:9], v0, s[76:79], s37 offen offset:1024 sc1
v_mfma_f32_16x16x32_bf16 a[68:71], v[152:155], v[96:99], a[68:71]
ds_read_b64_tr_b16 v[144:145], v188 offset:33888
ds_read_b64_tr_b16 v[146:147], v188 offset:34400
v_accvgpr_read_b32 v6, a72
v_accvgpr_read_b32 v7, a73
v_mfma_f32_16x16x32_bf16 a[68:71], v[156:159], v[100:103], a[68:71]
ds_read_b64_tr_b16 v[148:149], v188 offset:42336
ds_read_b64_tr_b16 v[150:151], v188 offset:42848
v_accvgpr_read_b32 v8, a74
v_accvgpr_read_b32 v9, a75
v_mfma_f32_16x16x32_bf16 a[84:87], v[160:163], v[96:99], a[84:87]
ds_read_b64_tr_b16 v[152:153], v188 offset:33920
ds_read_b64_tr_b16 v[154:155], v188 offset:34432
v_accvgpr_read_b32 v10, a88
v_accvgpr_read_b32 v11, a89
v_mfma_f32_16x16x32_bf16 a[84:87], v[164:167], v[100:103], a[84:87]
ds_read_b64_tr_b16 v[156:157], v188 offset:42368
ds_read_b64_tr_b16 v[158:159], v188 offset:42880
v_accvgpr_read_b32 v12, a90
v_accvgpr_read_b32 v13, a91
v_mfma_f32_16x16x32_bf16 a[100:103], v[168:171], v[96:99], a[100:103]
ds_read_b64_tr_b16 v[160:161], v188 offset:33952
ds_read_b64_tr_b16 v[162:163], v188 offset:34464
v_cvt_pk_bf16_f32 v6, v6, v7
v_cvt_pk_bf16_f32 v7, v8, v9
s_waitcnt lgkmcnt(12)
v_mfma_f32_16x16x32_bf16 a[100:103], v[172:175], v[100:103], a[100:103]
ds_read_b64_tr_b16 v[164:165], v188 offset:42400
ds_read_b64_tr_b16 v[166:167], v188 offset:42912
v_cvt_pk_bf16_f32 v8, v10, v11
v_cvt_pk_bf16_f32 v9, v12, v13
v_mfma_f32_16x16x32_bf16 a[116:119], v[176:179], v[96:99], a[116:119]
ds_read_b64_tr_b16 v[168:169], v188 offset:33984
ds_read_b64_tr_b16 v[170:171], v188 offset:34496
buffer_store_dwordx4 v[6:9], v0, s[76:79], s37 offen offset:2048 sc1
s_nop 1
v_accvgpr_read_b32 v6, a104
v_mfma_f32_16x16x32_bf16 a[116:119], v[180:183], v[100:103], a[116:119]
ds_read_b64_tr_b16 v[172:173], v188 offset:42432
ds_read_b64_tr_b16 v[174:175], v188 offset:42944
v_accvgpr_read_b32 v7, a105
v_accvgpr_read_b32 v8, a106
v_mfma_f32_16x16x32_bf16 a[0:3], v[120:123], v[56:59], a[0:3]
ds_read_b64_tr_b16 v[176:177], v188 offset:34016
ds_read_b64_tr_b16 v[178:179], v188 offset:34528
v_mfma_f32_16x16x32_bf16 a[0:3], v[124:127], v[60:63], a[0:3]
ds_read_b64_tr_b16 v[180:181], v188 offset:42464
ds_read_b64_tr_b16 v[182:183], v188 offset:42976
v_mfma_f32_16x16x32_bf16 a[16:19], v[128:131], v[56:59], a[16:19]
ds_read_b64_tr_b16 v[120:121], v189
ds_read_b64_tr_b16 v[122:123], v189 offset:512
v_accvgpr_read_b32 v9, a107
v_accvgpr_read_b32 v10, a120
v_mfma_f32_16x16x32_bf16 a[16:19], v[132:135], v[60:63], a[16:19]
ds_read_b64_tr_b16 v[124:125], v189 offset:8448
ds_read_b64_tr_b16 v[126:127], v189 offset:8960
v_accvgpr_read_b32 v11, a121
v_accvgpr_read_b32 v12, a122
v_mfma_f32_16x16x32_bf16 a[32:35], v[136:139], v[56:59], a[32:35]
ds_read_b64_tr_b16 v[128:129], v189 offset:32
ds_read_b64_tr_b16 v[130:131], v189 offset:544
v_accvgpr_read_b32 v13, a123
v_cvt_pk_bf16_f32 v6, v6, v7
s_waitcnt lgkmcnt(12)
v_mfma_f32_16x16x32_bf16 a[32:35], v[140:143], v[60:63], a[32:35]
ds_read_b64_tr_b16 v[132:133], v189 offset:8480
ds_read_b64_tr_b16 v[134:135], v189 offset:8992
v_cvt_pk_bf16_f32 v7, v8, v9
v_cvt_pk_bf16_f32 v8, v10, v11
v_mfma_f32_16x16x32_bf16 a[48:51], v[144:147], v[56:59], a[48:51]
ds_read_b64_tr_b16 v[136:137], v189 offset:64
ds_read_b64_tr_b16 v[138:139], v189 offset:576
v_cvt_pk_bf16_f32 v9, v12, v13
buffer_store_dwordx4 v[6:9], v0, s[76:79], s37 offen offset:3072 sc1
v_mfma_f32_16x16x32_bf16 a[48:51], v[148:151], v[60:63], a[48:51]
ds_read_b64_tr_b16 v[140:141], v189 offset:8512
ds_read_b64_tr_b16 v[142:143], v189 offset:9024
s_add_u32 s37, s74, 1
s_and_b32 s37, s37, 3
v_mfma_f32_16x16x32_bf16 a[64:67], v[152:155], v[56:59], a[64:67]
ds_read_b64_tr_b16 v[144:145], v189 offset:96
ds_read_b64_tr_b16 v[146:147], v189 offset:608
s_lshl_b32 s37, s37, 1
s_and_b32 s35, s5, 0xf9
v_mfma_f32_16x16x32_bf16 a[64:67], v[156:159], v[60:63], a[64:67]
ds_read_b64_tr_b16 v[148:149], v189 offset:8544
ds_read_b64_tr_b16 v[150:151], v189 offset:9056
s_or_b32 s37, s37, s35
s_mul_i32 s37, s37, 0x18000
v_mfma_f32_16x16x32_bf16 a[80:83], v[160:163], v[56:59], a[80:83]
ds_read_b64_tr_b16 v[152:153], v189 offset:128
ds_read_b64_tr_b16 v[154:155], v189 offset:640
s_mul_i32 s35, s4, 0x6000
s_add_u32 s37, s37, s35
v_mfma_f32_16x16x32_bf16 a[80:83], v[164:167], v[60:63], a[80:83]
ds_read_b64_tr_b16 v[156:157], v189 offset:8576
ds_read_b64_tr_b16 v[158:159], v189 offset:9088
s_add_u32 s37, s37, 0x2305000
v_accvgpr_read_b32 v6, a4
v_mfma_f32_16x16x32_bf16 a[96:99], v[168:171], v[56:59], a[96:99]
ds_read_b64_tr_b16 v[160:161], v189 offset:160
ds_read_b64_tr_b16 v[162:163], v189 offset:672
v_accvgpr_read_b32 v7, a5
v_accvgpr_read_b32 v8, a6
s_waitcnt lgkmcnt(12)
v_mfma_f32_16x16x32_bf16 a[96:99], v[172:175], v[60:63], a[96:99]
ds_read_b64_tr_b16 v[164:165], v189 offset:8608
ds_read_b64_tr_b16 v[166:167], v189 offset:9120
v_accvgpr_read_b32 v9, a7
v_accvgpr_read_b32 v10, a20
v_mfma_f32_16x16x32_bf16 a[112:115], v[176:179], v[56:59], a[112:115]
ds_read_b64_tr_b16 v[168:169], v189 offset:192
ds_read_b64_tr_b16 v[170:171], v189 offset:704
v_accvgpr_read_b32 v11, a21
v_accvgpr_read_b32 v12, a22
v_mfma_f32_16x16x32_bf16 a[112:115], v[180:183], v[60:63], a[112:115]
ds_read_b64_tr_b16 v[172:173], v189 offset:8640
ds_read_b64_tr_b16 v[174:175], v189 offset:9152
v_accvgpr_read_b32 v13, a23
v_cvt_pk_bf16_f32 v6, v6, v7
v_mfma_f32_16x16x32_bf16 a[0:3], v[120:123], v[88:91], a[0:3]
ds_read_b64_tr_b16 v[176:177], v189 offset:224
ds_read_b64_tr_b16 v[178:179], v189 offset:736
v_mfma_f32_16x16x32_bf16 a[0:3], v[124:127], v[92:95], a[0:3]
ds_read_b64_tr_b16 v[180:181], v189 offset:8672
ds_read_b64_tr_b16 v[182:183], v189 offset:9184
v_mfma_f32_16x16x32_bf16 a[16:19], v[128:131], v[88:91], a[16:19]
v_cvt_pk_bf16_f32 v7, v8, v9
v_cvt_pk_bf16_f32 v8, v10, v11
v_mfma_f32_16x16x32_bf16 a[16:19], v[132:135], v[92:95], a[16:19]
v_cvt_pk_bf16_f32 v9, v12, v13
buffer_store_dwordx4 v[6:9], v0, s[76:79], s37 offen sc1
v_mfma_f32_16x16x32_bf16 a[32:35], v[136:139], v[88:91], a[32:35]
s_nop 0
v_accvgpr_read_b32 v6, a36
v_accvgpr_read_b32 v7, a37
s_waitcnt lgkmcnt(6)
v_mfma_f32_16x16x32_bf16 a[32:35], v[140:143], v[92:95], a[32:35]
v_accvgpr_read_b32 v8, a38
v_accvgpr_read_b32 v9, a39
v_mfma_f32_16x16x32_bf16 a[48:51], v[144:147], v[88:91], a[48:51]
v_accvgpr_read_b32 v10, a52
v_accvgpr_read_b32 v11, a53
v_mfma_f32_16x16x32_bf16 a[48:51], v[148:151], v[92:95], a[48:51]
v_accvgpr_read_b32 v12, a54
v_accvgpr_read_b32 v13, a55
v_mfma_f32_16x16x32_bf16 a[64:67], v[152:155], v[88:91], a[64:67]
v_cvt_pk_bf16_f32 v6, v6, v7
v_cvt_pk_bf16_f32 v7, v8, v9
v_mfma_f32_16x16x32_bf16 a[64:67], v[156:159], v[92:95], a[64:67]
v_cvt_pk_bf16_f32 v8, v10, v11
v_cvt_pk_bf16_f32 v9, v12, v13
v_mfma_f32_16x16x32_bf16 a[80:83], v[160:163], v[88:91], a[80:83]
buffer_store_dwordx4 v[6:9], v0, s[76:79], s37 offen offset:1024 sc1
s_nop 1
v_accvgpr_read_b32 v6, a68
)ASM"
R"ASM(v_mfma_f32_16x16x32_bf16 a[80:83], v[164:167], v[92:95], a[80:83]
v_accvgpr_read_b32 v7, a69
v_accvgpr_read_b32 v8, a70
v_mfma_f32_16x16x32_bf16 a[96:99], v[168:171], v[88:91], a[96:99]
v_accvgpr_read_b32 v9, a71
v_accvgpr_read_b32 v10, a84
s_waitcnt lgkmcnt(0)
v_mfma_f32_16x16x32_bf16 a[96:99], v[172:175], v[92:95], a[96:99]
v_accvgpr_read_b32 v11, a85
v_accvgpr_read_b32 v12, a86
v_mfma_f32_16x16x32_bf16 a[112:115], v[176:179], v[88:91], a[112:115]
v_accvgpr_read_b32 v13, a87
v_cvt_pk_bf16_f32 v6, v6, v7
v_mfma_f32_16x16x32_bf16 a[112:115], v[180:183], v[92:95], a[112:115]
v_cvt_pk_bf16_f32 v7, v8, v9
v_cvt_pk_bf16_f32 v8, v10, v11
s_nop 7
v_cvt_pk_bf16_f32 v9, v12, v13
buffer_store_dwordx4 v[6:9], v0, s[76:79], s37 offen offset:2048 sc1
s_nop 1
v_accvgpr_read_b32 v6, a100
v_accvgpr_read_b32 v7, a101
v_accvgpr_read_b32 v8, a102
v_accvgpr_read_b32 v9, a103
v_accvgpr_read_b32 v10, a116
v_accvgpr_read_b32 v11, a117
v_accvgpr_read_b32 v12, a118
v_accvgpr_read_b32 v13, a119
v_cvt_pk_bf16_f32 v6, v6, v7
v_cvt_pk_bf16_f32 v7, v8, v9
v_cvt_pk_bf16_f32 v8, v10, v11
v_cvt_pk_bf16_f32 v9, v12, v13
buffer_store_dwordx4 v[6:9], v0, s[76:79], s37 offen offset:3072 sc1
s_waitcnt vmcnt(12)
v_accvgpr_read_b32 v192, a128
v_accvgpr_read_b32 v193, a129
v_accvgpr_read_b32 v194, a130
v_accvgpr_read_b32 v195, a131
v_accvgpr_read_b32 v8, a132
v_accvgpr_read_b32 v9, a133
v_lshlrev_b32 v6, 16, v8
v_and_b32 v7, 0xffff0000, v8
v_lshlrev_b32 v8, 16, v9
v_and_b32 v9, 0xffff0000, v9
v_add_f32 v192, v192, v6
v_add_f32 v193, v193, v7
v_add_f32 v194, v194, v8
v_add_f32 v195, v195, v9
v_accvgpr_read_b32 v8, a136
v_accvgpr_read_b32 v9, a137
v_lshlrev_b32 v6, 16, v8
v_and_b32 v7, 0xffff0000, v8
v_lshlrev_b32 v8, 16, v9
v_and_b32 v9, 0xffff0000, v9
v_add_f32 v192, v192, v6
v_add_f32 v193, v193, v7
v_add_f32 v194, v194, v8
v_add_f32 v195, v195, v9
v_accvgpr_read_b32 v8, a140
v_accvgpr_read_b32 v9, a141
v_lshlrev_b32 v6, 16, v8
v_and_b32 v7, 0xffff0000, v8
v_lshlrev_b32 v8, 16, v9
v_and_b32 v9, 0xffff0000, v9
v_add_f32 v192, v192, v6
v_add_f32 v193, v193, v7
v_add_f32 v194, v194, v8
v_add_f32 v195, v195, v9
v_mul_f32 v6, 0xbfb8aa3b, v192
v_mul_f32 v7, 0xbfb8aa3b, v193
v_mul_f32 v8, 0xbfb8aa3b, v194
v_mul_f32 v9, 0xbfb8aa3b, v195
v_exp_f32 v6, v6
v_exp_f32 v7, v7
v_exp_f32 v8, v8
v_exp_f32 v9, v9
v_add_f32 v6, 1.0, v6
v_add_f32 v7, 1.0, v7
v_add_f32 v8, 1.0, v8
v_add_f32 v9, 1.0, v9
v_rcp_f32 v6, v6
v_rcp_f32 v7, v7
v_rcp_f32 v8, v8
v_rcp_f32 v9, v9
v_mul_f32 v192, v192, v6
v_mul_f32 v193, v193, v7
v_mul_f32 v194, v194, v8
v_mul_f32 v195, v195, v9
v_accvgpr_read_b32 v196, a144
v_accvgpr_read_b32 v197, a145
v_accvgpr_read_b32 v198, a146
v_accvgpr_read_b32 v199, a147
v_accvgpr_read_b32 v12, a134
v_accvgpr_read_b32 v13, a135
v_lshlrev_b32 v10, 16, v12
v_and_b32 v11, 0xffff0000, v12
v_lshlrev_b32 v12, 16, v13
v_and_b32 v13, 0xffff0000, v13
v_add_f32 v196, v196, v10
v_add_f32 v197, v197, v11
v_add_f32 v198, v198, v12
v_add_f32 v199, v199, v13
v_accvgpr_read_b32 v12, a138
v_accvgpr_read_b32 v13, a139
v_lshlrev_b32 v10, 16, v12
v_and_b32 v11, 0xffff0000, v12
v_lshlrev_b32 v12, 16, v13
v_and_b32 v13, 0xffff0000, v13
v_add_f32 v196, v196, v10
v_add_f32 v197, v197, v11
v_add_f32 v198, v198, v12
v_add_f32 v199, v199, v13
v_accvgpr_read_b32 v12, a142
v_accvgpr_read_b32 v13, a143
v_lshlrev_b32 v10, 16, v12
v_and_b32 v11, 0xffff0000, v12
v_lshlrev_b32 v12, 16, v13
v_and_b32 v13, 0xffff0000, v13
v_add_f32 v196, v196, v10
v_add_f32 v197, v197, v11
v_add_f32 v198, v198, v12
v_add_f32 v199, v199, v13
v_mul_f32 v10, 0xbfb8aa3b, v196
v_mul_f32 v11, 0xbfb8aa3b, v197
v_mul_f32 v12, 0xbfb8aa3b, v198
v_mul_f32 v13, 0xbfb8aa3b, v199
v_exp_f32 v10, v10
v_exp_f32 v11, v11
v_exp_f32 v12, v12
v_exp_f32 v13, v13
v_add_f32 v10, 1.0, v10
v_add_f32 v11, 1.0, v11
v_add_f32 v12, 1.0, v12
v_add_f32 v13, 1.0, v13
v_rcp_f32 v10, v10
v_rcp_f32 v11, v11
v_rcp_f32 v12, v12
v_rcp_f32 v13, v13
v_mul_f32 v196, v196, v10
v_mul_f32 v197, v197, v11
v_mul_f32 v198, v198, v12
v_mul_f32 v199, v199, v13
v_accvgpr_read_b32 v200, a160
v_accvgpr_read_b32 v201, a161
v_accvgpr_read_b32 v202, a162
v_accvgpr_read_b32 v203, a163
v_accvgpr_read_b32 v8, a164
v_accvgpr_read_b32 v9, a165
v_lshlrev_b32 v6, 16, v8
v_and_b32 v7, 0xffff0000, v8
v_lshlrev_b32 v8, 16, v9
v_and_b32 v9, 0xffff0000, v9
v_add_f32 v200, v200, v6
v_add_f32 v201, v201, v7
v_add_f32 v202, v202, v8
v_add_f32 v203, v203, v9
v_accvgpr_read_b32 v8, a168
v_accvgpr_read_b32 v9, a169
v_lshlrev_b32 v6, 16, v8
v_and_b32 v7, 0xffff0000, v8
v_lshlrev_b32 v8, 16, v9
v_and_b32 v9, 0xffff0000, v9
v_add_f32 v200, v200, v6
v_add_f32 v201, v201, v7
v_add_f32 v202, v202, v8
v_add_f32 v203, v203, v9
v_accvgpr_read_b32 v8, a172
v_accvgpr_read_b32 v9, a173
v_lshlrev_b32 v6, 16, v8
v_and_b32 v7, 0xffff0000, v8
v_lshlrev_b32 v8, 16, v9
v_and_b32 v9, 0xffff0000, v9
v_add_f32 v200, v200, v6
v_add_f32 v201, v201, v7
v_add_f32 v202, v202, v8
v_add_f32 v203, v203, v9
v_mul_f32 v6, 0xbfb8aa3b, v200
v_mul_f32 v7, 0xbfb8aa3b, v201
v_mul_f32 v8, 0xbfb8aa3b, v202
v_mul_f32 v9, 0xbfb8aa3b, v203
v_exp_f32 v6, v6
v_exp_f32 v7, v7
v_exp_f32 v8, v8
v_exp_f32 v9, v9
v_add_f32 v6, 1.0, v6
v_add_f32 v7, 1.0, v7
v_add_f32 v8, 1.0, v8
v_add_f32 v9, 1.0, v9
v_rcp_f32 v6, v6
v_rcp_f32 v7, v7
v_rcp_f32 v8, v8
v_rcp_f32 v9, v9
v_mul_f32 v200, v200, v6
v_mul_f32 v201, v201, v7
v_mul_f32 v202, v202, v8
v_mul_f32 v203, v203, v9
v_accvgpr_read_b32 v204, a176
v_accvgpr_read_b32 v205, a177
v_accvgpr_read_b32 v206, a178
v_accvgpr_read_b32 v207, a179
v_accvgpr_read_b32 v12, a166
v_accvgpr_read_b32 v13, a167
v_lshlrev_b32 v10, 16, v12
v_and_b32 v11, 0xffff0000, v12
v_lshlrev_b32 v12, 16, v13
v_and_b32 v13, 0xffff0000, v13
v_add_f32 v204, v204, v10
v_add_f32 v205, v205, v11
v_add_f32 v206, v206, v12
v_add_f32 v207, v207, v13
v_accvgpr_read_b32 v12, a170
v_accvgpr_read_b32 v13, a171
v_lshlrev_b32 v10, 16, v12
v_and_b32 v11, 0xffff0000, v12
v_lshlrev_b32 v12, 16, v13
v_and_b32 v13, 0xffff0000, v13
v_add_f32 v204, v204, v10
v_add_f32 v205, v205, v11
v_add_f32 v206, v206, v12
v_add_f32 v207, v207, v13
v_accvgpr_read_b32 v12, a174
v_accvgpr_read_b32 v13, a175
v_lshlrev_b32 v10, 16, v12
v_and_b32 v11, 0xffff0000, v12
v_lshlrev_b32 v12, 16, v13
v_and_b32 v13, 0xffff0000, v13
v_add_f32 v204, v204, v10
v_add_f32 v205, v205, v11
v_add_f32 v206, v206, v12
v_add_f32 v207, v207, v13
v_mul_f32 v10, 0xbfb8aa3b, v204
v_mul_f32 v11, 0xbfb8aa3b, v205
v_mul_f32 v12, 0xbfb8aa3b, v206
v_mul_f32 v13, 0xbfb8aa3b, v207
v_exp_f32 v10, v10
v_exp_f32 v11, v11
v_exp_f32 v12, v12
v_exp_f32 v13, v13
v_add_f32 v10, 1.0, v10
v_add_f32 v11, 1.0, v11
v_add_f32 v12, 1.0, v12
v_add_f32 v13, 1.0, v13
v_rcp_f32 v10, v10
v_rcp_f32 v11, v11
v_rcp_f32 v12, v12
v_rcp_f32 v13, v13
v_mul_f32 v204, v204, v10
v_mul_f32 v205, v205, v11
v_mul_f32 v206, v206, v12
v_mul_f32 v207, v207, v13
s_mul_i32 s34, s5, 0x18000
s_mul_i32 s35, s4, 0x6000
s_add_u32 s34, s34, s35
s_add_u32 s34, s34, 0x2303000
s_add_u32 s38, s34, 0
buffer_load_dwordx4 v[24:27], v0, s[76:79], s38 offen sc1
buffer_load_dwordx4 v[28:31], v0, s[76:79], s38 offen offset:1024 sc1
buffer_load_dwordx4 v[32:35], v0, s[76:79], s38 offen offset:2048 sc1
buffer_load_dwordx4 v[36:39], v0, s[76:79], s38 offen offset:3072 sc1
s_add_u32 s38, s34, 0x1000
buffer_load_dwordx4 v[40:43], v0, s[76:79], s38 offen sc1
buffer_load_dwordx4 v[44:47], v0, s[76:79], s38 offen offset:1024 sc1
buffer_load_dwordx4 v[48:51], v0, s[76:79], s38 offen offset:2048 sc1
buffer_load_dwordx4 v[52:55], v0, s[76:79], s38 offen offset:3072 sc1
s_add_u32 s38, s34, 0x2000
buffer_load_dwordx4 v[56:59], v0, s[76:79], s38 offen sc1
buffer_load_dwordx4 v[60:63], v0, s[76:79], s38 offen offset:1024 sc1
buffer_load_dwordx4 v[64:67], v0, s[76:79], s38 offen offset:2048 sc1
buffer_load_dwordx4 v[68:71], v0, s[76:79], s38 offen offset:3072 sc1
v_accvgpr_read_b32 v208, a192
v_accvgpr_read_b32 v209, a193
v_accvgpr_read_b32 v210, a194
v_accvgpr_read_b32 v211, a195
v_accvgpr_read_b32 v8, a196
v_accvgpr_read_b32 v9, a197
v_lshlrev_b32 v6, 16, v8
v_and_b32 v7, 0xffff0000, v8
v_lshlrev_b32 v8, 16, v9
v_and_b32 v9, 0xffff0000, v9
v_add_f32 v208, v208, v6
v_add_f32 v209, v209, v7
v_add_f32 v210, v210, v8
v_add_f32 v211, v211, v9
v_accvgpr_read_b32 v8, a200
v_accvgpr_read_b32 v9, a201
v_lshlrev_b32 v6, 16, v8
v_and_b32 v7, 0xffff0000, v8
v_lshlrev_b32 v8, 16, v9
v_and_b32 v9, 0xffff0000, v9
v_add_f32 v208, v208, v6
v_add_f32 v209, v209, v7
v_add_f32 v210, v210, v8
v_add_f32 v211, v211, v9
v_accvgpr_read_b32 v8, a204
v_accvgpr_read_b32 v9, a205
v_lshlrev_b32 v6, 16, v8
v_and_b32 v7, 0xffff0000, v8
v_lshlrev_b32 v8, 16, v9
v_and_b32 v9, 0xffff0000, v9
v_add_f32 v208, v208, v6
v_add_f32 v209, v209, v7
v_add_f32 v210, v210, v8
v_add_f32 v211, v211, v9
v_mul_f32 v6, 0xbfb8aa3b, v208
v_mul_f32 v7, 0xbfb8aa3b, v209
v_mul_f32 v8, 0xbfb8aa3b, v210
v_mul_f32 v9, 0xbfb8aa3b, v211
v_exp_f32 v6, v6
v_exp_f32 v7, v7
v_exp_f32 v8, v8
v_exp_f32 v9, v9
v_add_f32 v6, 1.0, v6
v_add_f32 v7, 1.0, v7
v_add_f32 v8, 1.0, v8
v_add_f32 v9, 1.0, v9
v_rcp_f32 v6, v6
v_rcp_f32 v7, v7
v_rcp_f32 v8, v8
v_rcp_f32 v9, v9
v_mul_f32 v208, v208, v6
v_mul_f32 v209, v209, v7
v_mul_f32 v210, v210, v8
v_mul_f32 v211, v211, v9
v_accvgpr_read_b32 v212, a208
v_accvgpr_read_b32 v213, a209
v_accvgpr_read_b32 v214, a210
v_accvgpr_read_b32 v215, a211
v_accvgpr_read_b32 v12, a198
v_accvgpr_read_b32 v13, a199
v_lshlrev_b32 v10, 16, v12
v_and_b32 v11, 0xffff0000, v12
v_lshlrev_b32 v12, 16, v13
v_and_b32 v13, 0xffff0000, v13
v_add_f32 v212, v212, v10
v_add_f32 v213, v213, v11
v_add_f32 v214, v214, v12
v_add_f32 v215, v215, v13
v_accvgpr_read_b32 v12, a202
v_accvgpr_read_b32 v13, a203
v_lshlrev_b32 v10, 16, v12
v_and_b32 v11, 0xffff0000, v12
v_lshlrev_b32 v12, 16, v13
v_and_b32 v13, 0xffff0000, v13
v_add_f32 v212, v212, v10
v_add_f32 v213, v213, v11
v_add_f32 v214, v214, v12
v_add_f32 v215, v215, v13
v_accvgpr_read_b32 v12, a206
v_accvgpr_read_b32 v13, a207
v_lshlrev_b32 v10, 16, v12
v_and_b32 v11, 0xffff0000, v12
v_lshlrev_b32 v12, 16, v13
v_and_b32 v13, 0xffff0000, v13
v_add_f32 v212, v212, v10
v_add_f32 v213, v213, v11
v_add_f32 v214, v214, v12
v_add_f32 v215, v215, v13
v_mul_f32 v10, 0xbfb8aa3b, v212
v_mul_f32 v11, 0xbfb8aa3b, v213
v_mul_f32 v12, 0xbfb8aa3b, v214
v_mul_f32 v13, 0xbfb8aa3b, v215
v_exp_f32 v10, v10
v_exp_f32 v11, v11
v_exp_f32 v12, v12
v_exp_f32 v13, v13
v_add_f32 v10, 1.0, v10
v_add_f32 v11, 1.0, v11
v_add_f32 v12, 1.0, v12
v_add_f32 v13, 1.0, v13
v_rcp_f32 v10, v10
v_rcp_f32 v11, v11
v_rcp_f32 v12, v12
v_rcp_f32 v13, v13
v_mul_f32 v212, v212, v10
v_mul_f32 v213, v213, v11
v_mul_f32 v214, v214, v12
v_mul_f32 v215, v215, v13
v_accvgpr_read_b32 v216, a224
v_accvgpr_read_b32 v217, a225
v_accvgpr_read_b32 v218, a226
v_accvgpr_read_b32 v219, a227
v_accvgpr_read_b32 v8, a228
v_accvgpr_read_b32 v9, a229
v_lshlrev_b32 v6, 16, v8
v_and_b32 v7, 0xffff0000, v8
v_lshlrev_b32 v8, 16, v9
v_and_b32 v9, 0xffff0000, v9
v_add_f32 v216, v216, v6
v_add_f32 v217, v217, v7
v_add_f32 v218, v218, v8
v_add_f32 v219, v219, v9
v_accvgpr_read_b32 v8, a232
v_accvgpr_read_b32 v9, a233
v_lshlrev_b32 v6, 16, v8
v_and_b32 v7, 0xffff0000, v8
v_lshlrev_b32 v8, 16, v9
v_and_b32 v9, 0xffff0000, v9
v_add_f32 v216, v216, v6
v_add_f32 v217, v217, v7
v_add_f32 v218, v218, v8
)ASM"
R"ASM(v_add_f32 v219, v219, v9
v_accvgpr_read_b32 v8, a236
v_accvgpr_read_b32 v9, a237
v_lshlrev_b32 v6, 16, v8
v_and_b32 v7, 0xffff0000, v8
v_lshlrev_b32 v8, 16, v9
v_and_b32 v9, 0xffff0000, v9
v_add_f32 v216, v216, v6
v_add_f32 v217, v217, v7
v_add_f32 v218, v218, v8
v_add_f32 v219, v219, v9
v_mul_f32 v6, 0xbfb8aa3b, v216
v_mul_f32 v7, 0xbfb8aa3b, v217
v_mul_f32 v8, 0xbfb8aa3b, v218
v_mul_f32 v9, 0xbfb8aa3b, v219
v_exp_f32 v6, v6
v_exp_f32 v7, v7
v_exp_f32 v8, v8
v_exp_f32 v9, v9
v_add_f32 v6, 1.0, v6
v_add_f32 v7, 1.0, v7
v_add_f32 v8, 1.0, v8
v_add_f32 v9, 1.0, v9
v_rcp_f32 v6, v6
v_rcp_f32 v7, v7
v_rcp_f32 v8, v8
v_rcp_f32 v9, v9
v_mul_f32 v216, v216, v6
v_mul_f32 v217, v217, v7
v_mul_f32 v218, v218, v8
v_mul_f32 v219, v219, v9
v_accvgpr_read_b32 v220, a240
v_accvgpr_read_b32 v221, a241
v_accvgpr_read_b32 v222, a242
v_accvgpr_read_b32 v223, a243
v_accvgpr_read_b32 v12, a230
v_accvgpr_read_b32 v13, a231
v_lshlrev_b32 v10, 16, v12
v_and_b32 v11, 0xffff0000, v12
v_lshlrev_b32 v12, 16, v13
v_and_b32 v13, 0xffff0000, v13
v_add_f32 v220, v220, v10
v_add_f32 v221, v221, v11
v_add_f32 v222, v222, v12
v_add_f32 v223, v223, v13
v_accvgpr_read_b32 v12, a234
v_accvgpr_read_b32 v13, a235
v_lshlrev_b32 v10, 16, v12
v_and_b32 v11, 0xffff0000, v12
v_lshlrev_b32 v12, 16, v13
v_and_b32 v13, 0xffff0000, v13
v_add_f32 v220, v220, v10
v_add_f32 v221, v221, v11
v_add_f32 v222, v222, v12
v_add_f32 v223, v223, v13
v_accvgpr_read_b32 v12, a238
v_accvgpr_read_b32 v13, a239
v_lshlrev_b32 v10, 16, v12
v_and_b32 v11, 0xffff0000, v12
v_lshlrev_b32 v12, 16, v13
v_and_b32 v13, 0xffff0000, v13
v_add_f32 v220, v220, v10
v_add_f32 v221, v221, v11
v_add_f32 v222, v222, v12
v_add_f32 v223, v223, v13
v_mul_f32 v10, 0xbfb8aa3b, v220
v_mul_f32 v11, 0xbfb8aa3b, v221
v_mul_f32 v12, 0xbfb8aa3b, v222
v_mul_f32 v13, 0xbfb8aa3b, v223
v_exp_f32 v10, v10
v_exp_f32 v11, v11
v_exp_f32 v12, v12
v_exp_f32 v13, v13
v_add_f32 v10, 1.0, v10
v_add_f32 v11, 1.0, v11
v_add_f32 v12, 1.0, v12
v_add_f32 v13, 1.0, v13
v_rcp_f32 v10, v10
v_rcp_f32 v11, v11
v_rcp_f32 v12, v12
v_rcp_f32 v13, v13
v_mul_f32 v220, v220, v10
v_mul_f32 v221, v221, v11
v_mul_f32 v222, v222, v12
v_mul_f32 v223, v223, v13
s_nop 7
v_accvgpr_read_b32 v88, a0
v_accvgpr_read_b32 v89, a1
v_accvgpr_read_b32 v90, a2
v_accvgpr_read_b32 v91, a3
v_lshlrev_b32 v72, 16, v224
v_and_b32 v73, 0xffff0000, v224
v_lshlrev_b32 v74, 16, v225
v_and_b32 v75, 0xffff0000, v225
v_fma_f32 v88, v88, v192, v72
v_fma_f32 v89, v89, v193, v73
v_fma_f32 v90, v90, v194, v74
v_fma_f32 v91, v91, v195, v75
v_accvgpr_read_b32 v92, a16
v_accvgpr_read_b32 v93, a17
v_accvgpr_read_b32 v94, a18
v_accvgpr_read_b32 v95, a19
v_lshlrev_b32 v76, 16, v226
v_and_b32 v77, 0xffff0000, v226
v_lshlrev_b32 v78, 16, v227
v_and_b32 v79, 0xffff0000, v227
v_fma_f32 v92, v92, v196, v76
v_fma_f32 v93, v93, v197, v77
v_fma_f32 v94, v94, v198, v78
v_fma_f32 v95, v95, v199, v79
v_accvgpr_read_b32 v96, a32
v_accvgpr_read_b32 v97, a33
v_accvgpr_read_b32 v98, a34
v_accvgpr_read_b32 v99, a35
v_lshlrev_b32 v72, 16, v228
v_and_b32 v73, 0xffff0000, v228
v_lshlrev_b32 v74, 16, v229
v_and_b32 v75, 0xffff0000, v229
v_fma_f32 v96, v96, v200, v72
v_fma_f32 v97, v97, v201, v73
v_fma_f32 v98, v98, v202, v74
v_fma_f32 v99, v99, v203, v75
v_accvgpr_read_b32 v100, a48
v_accvgpr_read_b32 v101, a49
v_accvgpr_read_b32 v102, a50
v_accvgpr_read_b32 v103, a51
v_lshlrev_b32 v76, 16, v230
v_and_b32 v77, 0xffff0000, v230
v_lshlrev_b32 v78, 16, v231
v_and_b32 v79, 0xffff0000, v231
v_fma_f32 v100, v100, v204, v76
v_fma_f32 v101, v101, v205, v77
v_fma_f32 v102, v102, v206, v78
v_fma_f32 v103, v103, v207, v79
v_accvgpr_read_b32 v104, a64
v_accvgpr_read_b32 v105, a65
v_accvgpr_read_b32 v106, a66
v_accvgpr_read_b32 v107, a67
v_lshlrev_b32 v72, 16, v232
v_and_b32 v73, 0xffff0000, v232
v_lshlrev_b32 v74, 16, v233
v_and_b32 v75, 0xffff0000, v233
v_fma_f32 v104, v104, v208, v72
v_fma_f32 v105, v105, v209, v73
v_fma_f32 v106, v106, v210, v74
v_fma_f32 v107, v107, v211, v75
v_accvgpr_read_b32 v108, a80
v_accvgpr_read_b32 v109, a81
v_accvgpr_read_b32 v110, a82
v_accvgpr_read_b32 v111, a83
v_lshlrev_b32 v76, 16, v234
v_and_b32 v77, 0xffff0000, v234
v_lshlrev_b32 v78, 16, v235
v_and_b32 v79, 0xffff0000, v235
v_fma_f32 v108, v108, v212, v76
v_fma_f32 v109, v109, v213, v77
v_fma_f32 v110, v110, v214, v78
v_fma_f32 v111, v111, v215, v79
v_accvgpr_read_b32 v112, a96
v_accvgpr_read_b32 v113, a97
v_accvgpr_read_b32 v114, a98
v_accvgpr_read_b32 v115, a99
v_lshlrev_b32 v72, 16, v236
v_and_b32 v73, 0xffff0000, v236
v_lshlrev_b32 v74, 16, v237
v_and_b32 v75, 0xffff0000, v237
v_fma_f32 v112, v112, v216, v72
v_fma_f32 v113, v113, v217, v73
v_fma_f32 v114, v114, v218, v74
v_fma_f32 v115, v115, v219, v75
v_accvgpr_read_b32 v116, a112
v_accvgpr_read_b32 v117, a113
v_accvgpr_read_b32 v118, a114
v_accvgpr_read_b32 v119, a115
v_lshlrev_b32 v76, 16, v238
v_and_b32 v77, 0xffff0000, v238
v_lshlrev_b32 v78, 16, v239
v_and_b32 v79, 0xffff0000, v239
v_fma_f32 v116, v116, v220, v76
v_fma_f32 v117, v117, v221, v77
v_fma_f32 v118, v118, v222, v78
v_fma_f32 v119, v119, v223, v79
s_waitcnt vmcnt(0)
v_max3_u32 v17, v24, v25, v26
v_max3_u32 v17, v17, v27, v28
v_max3_u32 v17, v17, v29, v30
v_max3_u32 v17, v17, v31, v32
v_max3_u32 v17, v17, v33, v34
v_max3_u32 v17, v17, v35, v36
v_max3_u32 v17, v17, v37, v38
v_max3_u32 v17, v17, v39, v40
v_max3_u32 v17, v17, v41, v42
v_max3_u32 v17, v17, v43, v44
v_max3_u32 v17, v17, v45, v46
v_max3_u32 v17, v17, v47, v48
v_max3_u32 v17, v17, v49, v50
v_max3_u32 v17, v17, v51, v52
v_max3_u32 v17, v17, v53, v54
v_max3_u32 v17, v17, v55, v56
v_max3_u32 v17, v17, v57, v58
v_max3_u32 v17, v17, v59, v60
v_max3_u32 v17, v17, v61, v62
v_max3_u32 v17, v17, v63, v64
v_max3_u32 v17, v17, v65, v66
v_max3_u32 v17, v17, v67, v68
v_max3_u32 v17, v17, v69, v70
v_max_u32 v17, v17, v71
v_cmp_eq_u32 vcc, -1, v17
s_nop 1
s_cmp_lg_u64 vcc, 0
s_cbranch_scc1 OOLrxs_%=
RETrxs_%=:
v_lshlrev_b32 v72, 16, v24
v_and_b32 v73, 0xffff0000, v24
v_lshlrev_b32 v74, 16, v25
v_and_b32 v75, 0xffff0000, v25
v_lshlrev_b32 v76, 16, v40
v_and_b32 v77, 0xffff0000, v40
v_lshlrev_b32 v78, 16, v41
v_and_b32 v79, 0xffff0000, v41
v_add_f32 v72, v72, v76
v_add_f32 v73, v73, v77
v_add_f32 v74, v74, v78
v_add_f32 v75, v75, v79
v_lshlrev_b32 v76, 16, v56
v_and_b32 v77, 0xffff0000, v56
v_lshlrev_b32 v78, 16, v57
v_and_b32 v79, 0xffff0000, v57
v_add_f32 v72, v72, v76
v_add_f32 v73, v73, v77
v_add_f32 v74, v74, v78
v_add_f32 v75, v75, v79
v_fma_f32 v88, v72, v192, v88
v_fma_f32 v89, v73, v193, v89
v_fma_f32 v90, v74, v194, v90
v_fma_f32 v91, v75, v195, v91
v_cvt_pk_bf16_f32 v240, v88, v89
v_cvt_pk_bf16_f32 v241, v90, v91
buffer_store_dwordx2 v[240:241], v15, s[12:15], s36 offen
v_lshlrev_b32 v80, 16, v26
v_and_b32 v81, 0xffff0000, v26
v_lshlrev_b32 v82, 16, v27
v_and_b32 v83, 0xffff0000, v27
v_lshlrev_b32 v84, 16, v42
v_and_b32 v85, 0xffff0000, v42
v_lshlrev_b32 v86, 16, v43
v_and_b32 v87, 0xffff0000, v43
v_add_f32 v80, v80, v84
v_add_f32 v81, v81, v85
v_add_f32 v82, v82, v86
v_add_f32 v83, v83, v87
v_lshlrev_b32 v84, 16, v58
v_and_b32 v85, 0xffff0000, v58
v_lshlrev_b32 v86, 16, v59
v_and_b32 v87, 0xffff0000, v59
v_add_f32 v80, v80, v84
v_add_f32 v81, v81, v85
v_add_f32 v82, v82, v86
v_add_f32 v83, v83, v87
v_fma_f32 v92, v80, v196, v92
v_fma_f32 v93, v81, v197, v93
v_fma_f32 v94, v82, v198, v94
v_fma_f32 v95, v83, v199, v95
v_cvt_pk_bf16_f32 v242, v92, v93
v_cvt_pk_bf16_f32 v243, v94, v95
buffer_store_dwordx2 v[242:243], v15, s[12:15], s36 offen offset:32
v_lshlrev_b32 v72, 16, v28
v_and_b32 v73, 0xffff0000, v28
v_lshlrev_b32 v74, 16, v29
v_and_b32 v75, 0xffff0000, v29
v_lshlrev_b32 v76, 16, v44
v_and_b32 v77, 0xffff0000, v44
v_lshlrev_b32 v78, 16, v45
v_and_b32 v79, 0xffff0000, v45
v_add_f32 v72, v72, v76
v_add_f32 v73, v73, v77
v_add_f32 v74, v74, v78
v_add_f32 v75, v75, v79
v_lshlrev_b32 v76, 16, v60
v_and_b32 v77, 0xffff0000, v60
v_lshlrev_b32 v78, 16, v61
v_and_b32 v79, 0xffff0000, v61
v_add_f32 v72, v72, v76
v_add_f32 v73, v73, v77
v_add_f32 v74, v74, v78
v_add_f32 v75, v75, v79
v_fma_f32 v96, v72, v200, v96
v_fma_f32 v97, v73, v201, v97
v_fma_f32 v98, v74, v202, v98
v_fma_f32 v99, v75, v203, v99
v_cvt_pk_bf16_f32 v244, v96, v97
v_cvt_pk_bf16_f32 v245, v98, v99
buffer_store_dwordx2 v[244:245], v15, s[12:15], s36 offen offset:64
v_lshlrev_b32 v80, 16, v30
v_and_b32 v81, 0xffff0000, v30
v_lshlrev_b32 v82, 16, v31
v_and_b32 v83, 0xffff0000, v31
v_lshlrev_b32 v84, 16, v46
v_and_b32 v85, 0xffff0000, v46
v_lshlrev_b32 v86, 16, v47
v_and_b32 v87, 0xffff0000, v47
v_add_f32 v80, v80, v84
v_add_f32 v81, v81, v85
v_add_f32 v82, v82, v86
v_add_f32 v83, v83, v87
v_lshlrev_b32 v84, 16, v62
v_and_b32 v85, 0xffff0000, v62
v_lshlrev_b32 v86, 16, v63
v_and_b32 v87, 0xffff0000, v63
v_add_f32 v80, v80, v84
v_add_f32 v81, v81, v85
v_add_f32 v82, v82, v86
v_add_f32 v83, v83, v87
v_fma_f32 v100, v80, v204, v100
v_fma_f32 v101, v81, v205, v101
v_fma_f32 v102, v82, v206, v102
v_fma_f32 v103, v83, v207, v103
v_cvt_pk_bf16_f32 v246, v100, v101
v_cvt_pk_bf16_f32 v247, v102, v103
buffer_store_dwordx2 v[246:247], v15, s[12:15], s36 offen offset:96
v_lshlrev_b32 v72, 16, v32
v_and_b32 v73, 0xffff0000, v32
v_lshlrev_b32 v74, 16, v33
v_and_b32 v75, 0xffff0000, v33
v_lshlrev_b32 v76, 16, v48
v_and_b32 v77, 0xffff0000, v48
v_lshlrev_b32 v78, 16, v49
v_and_b32 v79, 0xffff0000, v49
v_add_f32 v72, v72, v76
v_add_f32 v73, v73, v77
v_add_f32 v74, v74, v78
v_add_f32 v75, v75, v79
v_lshlrev_b32 v76, 16, v64
v_and_b32 v77, 0xffff0000, v64
v_lshlrev_b32 v78, 16, v65
v_and_b32 v79, 0xffff0000, v65
v_add_f32 v72, v72, v76
v_add_f32 v73, v73, v77
v_add_f32 v74, v74, v78
v_add_f32 v75, v75, v79
v_fma_f32 v104, v72, v208, v104
v_fma_f32 v105, v73, v209, v105
v_fma_f32 v106, v74, v210, v106
v_fma_f32 v107, v75, v211, v107
v_cvt_pk_bf16_f32 v240, v104, v105
v_cvt_pk_bf16_f32 v241, v106, v107
buffer_store_dwordx2 v[240:241], v15, s[12:15], s36 offen offset:128
v_lshlrev_b32 v80, 16, v34
v_and_b32 v81, 0xffff0000, v34
v_lshlrev_b32 v82, 16, v35
v_and_b32 v83, 0xffff0000, v35
v_lshlrev_b32 v84, 16, v50
v_and_b32 v85, 0xffff0000, v50
v_lshlrev_b32 v86, 16, v51
v_and_b32 v87, 0xffff0000, v51
v_add_f32 v80, v80, v84
v_add_f32 v81, v81, v85
v_add_f32 v82, v82, v86
v_add_f32 v83, v83, v87
v_lshlrev_b32 v84, 16, v66
v_and_b32 v85, 0xffff0000, v66
v_lshlrev_b32 v86, 16, v67
v_and_b32 v87, 0xffff0000, v67
v_add_f32 v80, v80, v84
v_add_f32 v81, v81, v85
v_add_f32 v82, v82, v86
v_add_f32 v83, v83, v87
v_fma_f32 v108, v80, v212, v108
v_fma_f32 v109, v81, v213, v109
v_fma_f32 v110, v82, v214, v110
v_fma_f32 v111, v83, v215, v111
v_cvt_pk_bf16_f32 v242, v108, v109
v_cvt_pk_bf16_f32 v243, v110, v111
buffer_store_dwordx2 v[242:243], v15, s[12:15], s36 offen offset:160
v_lshlrev_b32 v72, 16, v36
v_and_b32 v73, 0xffff0000, v36
v_lshlrev_b32 v74, 16, v37
v_and_b32 v75, 0xffff0000, v37
v_lshlrev_b32 v76, 16, v52
v_and_b32 v77, 0xffff0000, v52
v_lshlrev_b32 v78, 16, v53
v_and_b32 v79, 0xffff0000, v53
v_add_f32 v72, v72, v76
v_add_f32 v73, v73, v77
v_add_f32 v74, v74, v78
v_add_f32 v75, v75, v79
v_lshlrev_b32 v76, 16, v68
v_and_b32 v77, 0xffff0000, v68
v_lshlrev_b32 v78, 16, v69
v_and_b32 v79, 0xffff0000, v69
v_add_f32 v72, v72, v76
v_add_f32 v73, v73, v77
v_add_f32 v74, v74, v78
v_add_f32 v75, v75, v79
v_fma_f32 v112, v72, v216, v112
v_fma_f32 v113, v73, v217, v113
v_fma_f32 v114, v74, v218, v114
v_fma_f32 v115, v75, v219, v115
v_cvt_pk_bf16_f32 v244, v112, v113
v_cvt_pk_bf16_f32 v245, v114, v115
)ASM"
R"ASM(buffer_store_dwordx2 v[244:245], v15, s[12:15], s36 offen offset:192
v_lshlrev_b32 v80, 16, v38
v_and_b32 v81, 0xffff0000, v38
v_lshlrev_b32 v82, 16, v39
v_and_b32 v83, 0xffff0000, v39
v_lshlrev_b32 v84, 16, v54
v_and_b32 v85, 0xffff0000, v54
v_lshlrev_b32 v86, 16, v55
v_and_b32 v87, 0xffff0000, v55
v_add_f32 v80, v80, v84
v_add_f32 v81, v81, v85
v_add_f32 v82, v82, v86
v_add_f32 v83, v83, v87
v_lshlrev_b32 v84, 16, v70
v_and_b32 v85, 0xffff0000, v70
v_lshlrev_b32 v86, 16, v71
v_and_b32 v87, 0xffff0000, v71
v_add_f32 v80, v80, v84
v_add_f32 v81, v81, v85
v_add_f32 v82, v82, v86
v_add_f32 v83, v83, v87
v_fma_f32 v116, v80, v220, v116
v_fma_f32 v117, v81, v221, v117
v_fma_f32 v118, v82, v222, v118
v_fma_f32 v119, v83, v223, v119
v_cvt_pk_bf16_f32 v246, v116, v117
v_cvt_pk_bf16_f32 v247, v118, v119
buffer_store_dwordx2 v[246:247], v15, s[12:15], s36 offen offset:224
s_or_b32 s34, s5, s4
s_cmp_eq_u32 s34, 0
s_cbranch_scc1 OOLep_%=
RETep_%=:
s_waitcnt vmcnt(0) lgkmcnt(0)
s_branch END_%=
FALLBACK_%=:
s_waitcnt vmcnt(0) lgkmcnt(0)
s_barrier
v_mbcnt_lo_u32_b32 v20, -1, 0
v_mbcnt_hi_u32_b32 v20, -1, v20
v_lshrrev_b32 v21, 4, v20
v_and_b32 v22, 15, v20
v_add_u32 v23, 0, v21
v_xor_b32 v23, v22, v23
v_lshlrev_b32 v23, 4, v23
v_lshl_add_u32 v1, v21, 8, v23
v_add_u32 v23, 4, v21
v_xor_b32 v23, v22, v23
v_lshlrev_b32 v23, 4, v23
v_lshl_add_u32 v2, v21, 8, v23
v_add_u32 v23, 8, v21
v_xor_b32 v23, v22, v23
v_lshlrev_b32 v23, 4, v23
v_lshl_add_u32 v3, v21, 8, v23
v_add_u32 v23, 12, v21
v_xor_b32 v23, v22, v23
v_lshlrev_b32 v23, 4, v23
v_lshl_add_u32 v4, v21, 8, v23
v_lshlrev_b32 v23, 4, v22
v_lshl_add_u32 v5, v21, 11, v23
v_add_u32 v5, 0x960, v5
v_subrev_u32 v6, 0x320, v5
v_subrev_u32 v7, 0x640, v5
v_subrev_u32 v8, 0x960, v5
s_mov_b32 s8, s50
s_and_b32 s9, s51, 0xffff
s_mov_b32 s10, 0x200000
s_mov_b32 s11, 0x20000
s_mov_b32 s12, s52
s_and_b32 s13, s53, 0xffff
s_mov_b32 s14, 0x200000
s_mov_b32 s15, 0x20000
s_lshr_b32 s35, s7, 3
s_lshl_b32 s35, s35, 19
s_add_u32 s35, s35, 0x60000
s_lshl_b32 s36, s4, 12
s_add_u32 s24, s35, s36
s_lshl_b32 s36, s68, 13
s_add_u32 s25, s35, s36
s_lshl_b32 s36, s69, 10
s_add_u32 s25, s25, s36
s_sub_u32 s25, s25, 0x960
s_lshl_b32 s28, s4, 12
s_add_u32 s28, s28, 0
s_mul_i32 s29, s68, 0x2100
s_mul_i32 s36, s69, 0x1080
s_add_u32 s29, s29, s36
s_add_u32 s29, s29, 0x8000
v_mov_b32 v168, 0xff800000
v_mov_b32 v172, 0xff800000
v_mov_b32 v176, 0xff800000
v_mov_b32 v180, 0xff800000
s_mov_b32 s70, 8
FB_LOOP_%=:
s_add_u32 m0, s28, 0
s_nop 0
buffer_load_dwordx4 v1, s[8:11], s24 offen lds
buffer_load_dwordx4 v2, s[8:11], s24 offen offset:1024 lds
buffer_load_dwordx4 v3, s[8:11], s24 offen offset:2048 lds
buffer_load_dwordx4 v4, s[8:11], s24 offen offset:3072 lds
s_add_u32 s24, s24, 0x4000
s_waitcnt vmcnt(0)
s_barrier
ds_read_b128 a[192:195], v12
ds_read_b128 a[196:199], v13
ds_read_b128 a[200:203], v14
ds_read_b128 a[204:207], v15
s_waitcnt lgkmcnt(3)
v_mfma_f32_16x16x32_bf16 v[24:27], a[192:195], a[128:131], 0
s_waitcnt lgkmcnt(2)
v_mfma_f32_16x16x32_bf16 v[24:27], a[196:199], a[132:135], v[24:27]
s_waitcnt lgkmcnt(1)
v_mfma_f32_16x16x32_bf16 v[24:27], a[200:203], a[136:139], v[24:27]
s_waitcnt lgkmcnt(0)
v_mfma_f32_16x16x32_bf16 v[24:27], a[204:207], a[140:143], v[24:27]
v_mfma_f32_16x16x32_bf16 v[28:31], a[192:195], a[144:147], 0
v_mfma_f32_16x16x32_bf16 v[28:31], a[196:199], a[148:151], v[28:31]
v_mfma_f32_16x16x32_bf16 v[28:31], a[200:203], a[152:155], v[28:31]
v_mfma_f32_16x16x32_bf16 v[28:31], a[204:207], a[156:159], v[28:31]
v_mfma_f32_16x16x32_bf16 v[32:35], a[192:195], a[160:163], 0
v_mfma_f32_16x16x32_bf16 v[32:35], a[196:199], a[164:167], v[32:35]
v_mfma_f32_16x16x32_bf16 v[32:35], a[200:203], a[168:171], v[32:35]
v_mfma_f32_16x16x32_bf16 v[32:35], a[204:207], a[172:175], v[32:35]
v_mfma_f32_16x16x32_bf16 v[36:39], a[192:195], a[176:179], 0
v_mfma_f32_16x16x32_bf16 v[36:39], a[196:199], a[180:183], v[36:39]
v_mfma_f32_16x16x32_bf16 v[36:39], a[200:203], a[184:187], v[36:39]
v_mfma_f32_16x16x32_bf16 v[36:39], a[204:207], a[188:191], v[36:39]
s_nop 7
v_max3_f32 v168, v168, v24, v25
v_max3_f32 v168, v168, v26, v27
v_max3_f32 v172, v172, v28, v29
v_max3_f32 v172, v172, v30, v31
v_max3_f32 v176, v176, v32, v33
v_max3_f32 v176, v176, v34, v35
v_max3_f32 v180, v180, v36, v37
v_max3_f32 v180, v180, v38, v39
s_nop 3
ds_read_b128 a[192:195], v12 offset:4096
ds_read_b128 a[196:199], v13 offset:4096
ds_read_b128 a[200:203], v14 offset:4096
ds_read_b128 a[204:207], v15 offset:4096
s_waitcnt lgkmcnt(3)
v_mfma_f32_16x16x32_bf16 v[24:27], a[192:195], a[128:131], 0
s_waitcnt lgkmcnt(2)
v_mfma_f32_16x16x32_bf16 v[24:27], a[196:199], a[132:135], v[24:27]
s_waitcnt lgkmcnt(1)
v_mfma_f32_16x16x32_bf16 v[24:27], a[200:203], a[136:139], v[24:27]
s_waitcnt lgkmcnt(0)
v_mfma_f32_16x16x32_bf16 v[24:27], a[204:207], a[140:143], v[24:27]
v_mfma_f32_16x16x32_bf16 v[28:31], a[192:195], a[144:147], 0
v_mfma_f32_16x16x32_bf16 v[28:31], a[196:199], a[148:151], v[28:31]
v_mfma_f32_16x16x32_bf16 v[28:31], a[200:203], a[152:155], v[28:31]
v_mfma_f32_16x16x32_bf16 v[28:31], a[204:207], a[156:159], v[28:31]
v_mfma_f32_16x16x32_bf16 v[32:35], a[192:195], a[160:163], 0
v_mfma_f32_16x16x32_bf16 v[32:35], a[196:199], a[164:167], v[32:35]
v_mfma_f32_16x16x32_bf16 v[32:35], a[200:203], a[168:171], v[32:35]
v_mfma_f32_16x16x32_bf16 v[32:35], a[204:207], a[172:175], v[32:35]
v_mfma_f32_16x16x32_bf16 v[36:39], a[192:195], a[176:179], 0
v_mfma_f32_16x16x32_bf16 v[36:39], a[196:199], a[180:183], v[36:39]
v_mfma_f32_16x16x32_bf16 v[36:39], a[200:203], a[184:187], v[36:39]
v_mfma_f32_16x16x32_bf16 v[36:39], a[204:207], a[188:191], v[36:39]
s_nop 7
v_max3_f32 v168, v168, v24, v25
v_max3_f32 v168, v168, v26, v27
v_max3_f32 v172, v172, v28, v29
v_max3_f32 v172, v172, v30, v31
v_max3_f32 v176, v176, v32, v33
v_max3_f32 v176, v176, v34, v35
v_max3_f32 v180, v180, v36, v37
v_max3_f32 v180, v180, v38, v39
s_nop 3
ds_read_b128 a[192:195], v12 offset:8192
ds_read_b128 a[196:199], v13 offset:8192
ds_read_b128 a[200:203], v14 offset:8192
ds_read_b128 a[204:207], v15 offset:8192
s_waitcnt lgkmcnt(3)
v_mfma_f32_16x16x32_bf16 v[24:27], a[192:195], a[128:131], 0
s_waitcnt lgkmcnt(2)
v_mfma_f32_16x16x32_bf16 v[24:27], a[196:199], a[132:135], v[24:27]
s_waitcnt lgkmcnt(1)
v_mfma_f32_16x16x32_bf16 v[24:27], a[200:203], a[136:139], v[24:27]
s_waitcnt lgkmcnt(0)
v_mfma_f32_16x16x32_bf16 v[24:27], a[204:207], a[140:143], v[24:27]
v_mfma_f32_16x16x32_bf16 v[28:31], a[192:195], a[144:147], 0
v_mfma_f32_16x16x32_bf16 v[28:31], a[196:199], a[148:151], v[28:31]
v_mfma_f32_16x16x32_bf16 v[28:31], a[200:203], a[152:155], v[28:31]
v_mfma_f32_16x16x32_bf16 v[28:31], a[204:207], a[156:159], v[28:31]
v_mfma_f32_16x16x32_bf16 v[32:35], a[192:195], a[160:163], 0
v_mfma_f32_16x16x32_bf16 v[32:35], a[196:199], a[164:167], v[32:35]
v_mfma_f32_16x16x32_bf16 v[32:35], a[200:203], a[168:171], v[32:35]
v_mfma_f32_16x16x32_bf16 v[32:35], a[204:207], a[172:175], v[32:35]
v_mfma_f32_16x16x32_bf16 v[36:39], a[192:195], a[176:179], 0
v_mfma_f32_16x16x32_bf16 v[36:39], a[196:199], a[180:183], v[36:39]
v_mfma_f32_16x16x32_bf16 v[36:39], a[200:203], a[184:187], v[36:39]
v_mfma_f32_16x16x32_bf16 v[36:39], a[204:207], a[188:191], v[36:39]
s_nop 7
v_max3_f32 v168, v168, v24, v25
v_max3_f32 v168, v168, v26, v27
v_max3_f32 v172, v172, v28, v29
v_max3_f32 v172, v172, v30, v31
v_max3_f32 v176, v176, v32, v33
v_max3_f32 v176, v176, v34, v35
v_max3_f32 v180, v180, v36, v37
v_max3_f32 v180, v180, v38, v39
s_nop 3
ds_read_b128 a[192:195], v12 offset:12288
ds_read_b128 a[196:199], v13 offset:12288
ds_read_b128 a[200:203], v14 offset:12288
ds_read_b128 a[204:207], v15 offset:12288
s_waitcnt lgkmcnt(3)
v_mfma_f32_16x16x32_bf16 v[24:27], a[192:195], a[128:131], 0
s_waitcnt lgkmcnt(2)
v_mfma_f32_16x16x32_bf16 v[24:27], a[196:199], a[132:135], v[24:27]
s_waitcnt lgkmcnt(1)
v_mfma_f32_16x16x32_bf16 v[24:27], a[200:203], a[136:139], v[24:27]
s_waitcnt lgkmcnt(0)
v_mfma_f32_16x16x32_bf16 v[24:27], a[204:207], a[140:143], v[24:27]
v_mfma_f32_16x16x32_bf16 v[28:31], a[192:195], a[144:147], 0
v_mfma_f32_16x16x32_bf16 v[28:31], a[196:199], a[148:151], v[28:31]
v_mfma_f32_16x16x32_bf16 v[28:31], a[200:203], a[152:155], v[28:31]
v_mfma_f32_16x16x32_bf16 v[28:31], a[204:207], a[156:159], v[28:31]
v_mfma_f32_16x16x32_bf16 v[32:35], a[192:195], a[160:163], 0
v_mfma_f32_16x16x32_bf16 v[32:35], a[196:199], a[164:167], v[32:35]
v_mfma_f32_16x16x32_bf16 v[32:35], a[200:203], a[168:171], v[32:35]
v_mfma_f32_16x16x32_bf16 v[32:35], a[204:207], a[172:175], v[32:35]
v_mfma_f32_16x16x32_bf16 v[36:39], a[192:195], a[176:179], 0
v_mfma_f32_16x16x32_bf16 v[36:39], a[196:199], a[180:183], v[36:39]
v_mfma_f32_16x16x32_bf16 v[36:39], a[200:203], a[184:187], v[36:39]
v_mfma_f32_16x16x32_bf16 v[36:39], a[204:207], a[188:191], v[36:39]
s_nop 7
v_max3_f32 v168, v168, v24, v25
v_max3_f32 v168, v168, v26, v27
v_max3_f32 v172, v172, v28, v29
v_max3_f32 v172, v172, v30, v31
v_max3_f32 v176, v176, v32, v33
v_max3_f32 v176, v176, v34, v35
v_max3_f32 v180, v180, v36, v37
v_max3_f32 v180, v180, v38, v39
s_nop 3
s_waitcnt lgkmcnt(0)
s_barrier
s_sub_u32 s70, s70, 1
s_cmp_lg_u32 s70, 0
s_cbranch_scc1 FB_LOOP_%=
v_mov_b32 v169, v168
v_mov_b32 v173, v172
v_mov_b32 v177, v176
v_mov_b32 v181, v180
s_nop 1
v_permlane16_swap_b32 v168, v169
v_permlane16_swap_b32 v172, v173
v_permlane16_swap_b32 v176, v177
v_permlane16_swap_b32 v180, v181
v_max_f32 v168, v168, v169
v_mov_b32 v169, v168
v_max_f32 v172, v172, v173
v_mov_b32 v173, v172
v_max_f32 v176, v176, v177
v_mov_b32 v177, v176
v_max_f32 v180, v180, v181
v_mov_b32 v181, v180
s_nop 1
v_permlane32_swap_b32 v168, v169
v_permlane32_swap_b32 v172, v173
v_permlane32_swap_b32 v176, v177
v_permlane32_swap_b32 v180, v181
v_max_f32 v168, v168, v169
v_max_f32 v172, v172, v173
v_max_f32 v176, v176, v177
v_max_f32 v180, v180, v181
v_sub_f32 v168, 0, v168
v_sub_f32 v172, 0, v172
v_sub_f32 v176, 0, v176
v_sub_f32 v180, 0, v180
v_mov_b32 v169, v168
v_mov_b32 v170, v168
v_mov_b32 v171, v168
v_mov_b32 v173, v172
v_mov_b32 v174, v172
v_mov_b32 v175, v172
v_mov_b32 v177, v176
v_mov_b32 v178, v176
v_mov_b32 v179, v176
v_mov_b32 v181, v180
v_mov_b32 v182, v180
v_mov_b32 v183, v180
s_sub_u32 s24, s24, 0x20000
s_add_u32 m0, s28, 0
s_nop 0
buffer_load_dwordx4 v1, s[8:11], s24 offen lds
buffer_load_dwordx4 v2, s[8:11], s24 offen offset:1024 lds
buffer_load_dwordx4 v3, s[8:11], s24 offen offset:2048 lds
buffer_load_dwordx4 v4, s[8:11], s24 offen offset:3072 lds
s_add_u32 s24, s24, 0x4000
s_add_u32 m0, s29, 0
s_nop 0
buffer_load_dwordx4 v5, s[12:15], s25 offen lds
buffer_load_dwordx4 v6, s[12:15], s25 offen offset:1056 lds
buffer_load_dwordx4 v7, s[12:15], s25 offen offset:2112 lds
buffer_load_dwordx4 v8, s[12:15], s25 offen offset:3168 lds
s_add_u32 s25, s25, 0x4000
s_add_u32 m0, s28, 0x4000
s_nop 0
buffer_load_dwordx4 v1, s[8:11], s24 offen lds
buffer_load_dwordx4 v2, s[8:11], s24 offen offset:1024 lds
buffer_load_dwordx4 v3, s[8:11], s24 offen offset:2048 lds
buffer_load_dwordx4 v4, s[8:11], s24 offen offset:3072 lds
s_add_u32 s24, s24, 0x4000
s_mov_b32 s71, 1
s_waitcnt vmcnt(0)
s_barrier
s_branch ATT_START_%=
OOLwxf_%=:
s_nop 1
s_lshl_b32 s46, s5, 6
s_add_u32 s46, s46, 0xc000
v_mov_b32 v117, 0
v_mov_b32 v118, s33
s_mov_b64 exec, 1
buffer_store_dword v118, v117, s[76:79], s46 offen sc1
s_mov_b64 exec, -1
s_branch RETwxf_%=
OOLxp_%=:
s_nop 1
s_sleep 24
s_lshl_b32 s46, s6, 11
s_add_u32 s46, s46, 0xc000
v_mbcnt_lo_u32_b32 v20, -1, 0
v_mbcnt_hi_u32_b32 v20, -1, v20
v_min_u32 v20, 31, v20
v_lshlrev_b32 v20, 6, v20
s_mov_b32 s47, 0
POLLx_%=:
buffer_load_dword v21, v20, s[76:79], s46 offen sc1
s_waitcnt vmcnt(0)
v_cmp_ne_u32 vcc, s33, v21
s_nop 1
s_cmp_eq_u64 vcc, 0
s_cbranch_scc1 POLLDx_%=
s_sleep 2
s_add_u32 s47, s47, 1
s_cmp_lt_u32 s47, 0x20000
s_cbranch_scc1 POLLx_%=
s_mov_b32 s46, 0x18000
v_mov_b32 v20, 0
v_mov_b32 v21, 1
buffer_store_dword v21, v20, s[76:79], s46 offen sc1
POLLDx_%=:
s_branch RETxp_%=
OOLall_%=:
s_nop 1
v_mbcnt_lo_u32_b32 v22, -1, 0
v_mbcnt_hi_u32_b32 v22, -1, v22
v_min_u32 v22, 63, v22
v_lshlrev_b32 v22, 6, v22
s_mov_b32 s47, 0
POLLALL_%=:
s_mov_b32 s46, 0xc000
buffer_load_dword v24, v22, s[76:79], s46 offen sc1
s_mov_b32 s46, 0xd000
buffer_load_dword v25, v22, s[76:79], s46 offen sc1
s_mov_b32 s46, 0xe000
buffer_load_dword v26, v22, s[76:79], s46 offen sc1
s_mov_b32 s46, 0xf000
buffer_load_dword v27, v22, s[76:79], s46 offen sc1
s_waitcnt vmcnt(0)
v_cmp_ne_u32 vcc, s33, v24
s_nop 1
s_mov_b64 s[42:43], vcc
v_cmp_ne_u32 vcc, s33, v25
s_nop 1
s_or_b64 s[42:43], s[42:43], vcc
v_cmp_ne_u32 vcc, s33, v26
s_nop 1
s_or_b64 s[42:43], s[42:43], vcc
v_cmp_ne_u32 vcc, s33, v27
s_nop 1
s_or_b64 s[42:43], s[42:43], vcc
s_cmp_eq_u64 s[42:43], 0
s_cbranch_scc1 POLLALLD_%=
s_sleep 1
s_add_u32 s47, s47, 1
s_cmp_lt_u32 s47, 0x20000
s_cbranch_scc1 POLLALL_%=
POLLALLD_%=:
v_mov_b32 v24, 0
v_mov_b32 v25, 0
v_mov_b32 v26, 0
v_mov_b32 v27, 0
s_branch RETall_%=
OOLwpf_%=:
s_nop 1
s_lshl_b32 s46, s5, 6
s_add_u32 s46, s46, 0x14000
v_mov_b32 v17, 0
v_mov_b32 v18, s33
s_mov_b64 exec, 1
buffer_store_dword v18, v17, s[76:79], s46 offen sc1
s_mov_b64 exec, -1
s_branch RETwpf_%=
OOLwph_%=:
)ASM"
R"ASM(s_nop 1
s_lshl_b32 s46, s6, 5
s_add_u32 s46, s46, s7
s_lshl_b32 s46, s46, 6
s_add_u32 s46, s46, 0x4000
v_mov_b32 v17, 0
v_mov_b32 v18, s33
s_mov_b64 exec, 1
buffer_store_dword v18, v17, s[76:79], s46 offen sc1
s_mov_b64 exec, -1
s_branch RETwph_%=
OOLcct3_%=:
s_nop 1
s_getpc_b64 s[40:41]
CTc_%=:
s_sub_u32 s40, s40, CTc_%=-KSTART_%=
s_subb_u32 s41, s41, 0
s_lshr_b32 s45, s5, 3
s_lshl_b32 s45, s45, 2
s_add_u32 s45, s45, s4
s_lshl_b32 s45, s45, 13
s_cmp_lt_u32 s45, END_%=-KSTART_%=
s_cselect_b32 s44, 0x80, 0
s_min_u32 s45, s45, END_%=-KSTART_%=-8192
v_mbcnt_lo_u32_b32 v226, -1, 0
v_mbcnt_hi_u32_b32 v226, -1, v226
v_mul_u32_u24 v226, s44, v226
v_add_u32 v226, s45, v226
global_load_dword v227, v226, s[40:41]
s_branch RETcct3_%=
OOLphc_%=:
s_nop 1
s_lshl_b32 s46, s72, 2
s_add_u32 s46, s46, s74
s_lshl_b32 s46, s46, 9
s_add_u32 s46, s46, 0x4000
v_mbcnt_lo_u32_b32 v17, -1, 0
v_mbcnt_hi_u32_b32 v17, -1, v17
v_min_u32 v17, 7, v17
v_lshlrev_b32 v17, 6, v17
s_mov_b32 s47, 0
POLLhc_%=:
buffer_load_dword v18, v17, s[76:79], s46 offen sc1
s_waitcnt vmcnt(0)
v_cmp_ne_u32 vcc, s33, v18
s_nop 1
s_cmp_eq_u64 vcc, 0
s_cbranch_scc1 POLLDhc_%=
s_sleep 1
s_add_u32 s47, s47, 1
s_cmp_lt_u32 s47, 0x20000
s_cbranch_scc1 POLLhc_%=
s_mov_b32 s46, 0x18000
v_mov_b32 v17, 0
v_mov_b32 v18, 1
buffer_store_dword v18, v17, s[76:79], s46 offen sc1
POLLDhc_%=:
s_branch RETphc_%=
OOLfw6_%=:
s_nop 1
s_waitcnt vmcnt(0)
s_branch RETfw6_%=
OOLwop_%=:
s_nop 1
s_lshl_b32 s46, s6, 5
s_add_u32 s46, s46, s7
s_lshl_b32 s46, s46, 6
s_add_u32 s46, s46, 0x8000
v_mov_b32 v17, 0
v_mov_b32 v18, s33
s_mov_b64 exec, 1
buffer_store_dword v18, v17, s[76:79], s46 offen sc1
s_mov_b64 exec, -1
s_branch RETwop_%=
OOLpoc_%=:
s_nop 1
s_lshl_b32 s46, s72, 2
s_add_u32 s46, s46, s74
s_lshl_b32 s46, s46, 9
s_add_u32 s46, s46, 0x8000
v_mbcnt_lo_u32_b32 v17, -1, 0
v_mbcnt_hi_u32_b32 v17, -1, v17
v_min_u32 v17, 7, v17
v_lshlrev_b32 v17, 6, v17
s_mov_b32 s47, 0
POLLoc_%=:
buffer_load_dword v18, v17, s[76:79], s46 offen sc1
s_waitcnt vmcnt(0)
v_cmp_ne_u32 vcc, s33, v18
s_nop 1
s_cmp_eq_u64 vcc, 0
s_cbranch_scc1 POLLDoc_%=
s_sleep 1
s_add_u32 s47, s47, 1
s_cmp_lt_u32 s47, 0x20000
s_cbranch_scc1 POLLoc_%=
s_mov_b32 s46, 0x18000
v_mov_b32 v17, 0
v_mov_b32 v18, 1
buffer_store_dword v18, v17, s[76:79], s46 offen sc1
POLLDoc_%=:
s_branch RETpoc_%=
OOLppc1_%=:
s_nop 1
s_add_u32 s46, s74, 1
s_and_b32 s46, s46, 3
s_lshl_b32 s46, s46, 1
s_and_b32 s35, s5, 0xf9
s_or_b32 s46, s46, s35
s_lshl_b32 s46, s46, 6
s_add_u32 s46, s46, 0x14000
v_mov_b32 v17, 0
s_mov_b32 s47, 0
POLLpc1_%=:
buffer_load_dword v18, v17, s[76:79], s46 offen sc1
s_waitcnt vmcnt(0)
v_cmp_ne_u32 vcc, s33, v18
s_nop 1
s_cmp_eq_u64 vcc, 0
s_cbranch_scc1 POLLDpc1_%=
s_sleep 1
s_add_u32 s47, s47, 1
s_cmp_lt_u32 s47, 0x20000
s_cbranch_scc1 POLLpc1_%=
s_mov_b32 s46, 0x18000
v_mov_b32 v17, 0
v_mov_b32 v18, 1
buffer_store_dword v18, v17, s[76:79], s46 offen sc1
POLLDpc1_%=:
s_branch RETppc1_%=
OOLppc2_%=:
s_nop 1
s_add_u32 s46, s74, 2
s_and_b32 s46, s46, 3
s_lshl_b32 s46, s46, 1
s_and_b32 s35, s5, 0xf9
s_or_b32 s46, s46, s35
s_lshl_b32 s46, s46, 6
s_add_u32 s46, s46, 0x14000
v_mov_b32 v17, 0
s_mov_b32 s47, 0
POLLpc2_%=:
buffer_load_dword v18, v17, s[76:79], s46 offen sc1
s_waitcnt vmcnt(0)
v_cmp_ne_u32 vcc, s33, v18
s_nop 1
s_cmp_eq_u64 vcc, 0
s_cbranch_scc1 POLLDpc2_%=
s_sleep 1
s_add_u32 s47, s47, 1
s_cmp_lt_u32 s47, 0x20000
s_cbranch_scc1 POLLpc2_%=
s_mov_b32 s46, 0x18000
v_mov_b32 v17, 0
v_mov_b32 v18, 1
buffer_store_dword v18, v17, s[76:79], s46 offen sc1
POLLDpc2_%=:
s_branch RETppc2_%=
OOLppc3_%=:
s_nop 1
s_add_u32 s46, s74, 3
s_and_b32 s46, s46, 3
s_lshl_b32 s46, s46, 1
s_and_b32 s35, s5, 0xf9
s_or_b32 s46, s46, s35
s_lshl_b32 s46, s46, 6
s_add_u32 s46, s46, 0x14000
v_mov_b32 v17, 0
s_mov_b32 s47, 0
POLLpc3_%=:
buffer_load_dword v18, v17, s[76:79], s46 offen sc1
s_waitcnt vmcnt(0)
v_cmp_ne_u32 vcc, s33, v18
s_nop 1
s_cmp_eq_u64 vcc, 0
s_cbranch_scc1 POLLDpc3_%=
s_sleep 1
s_add_u32 s47, s47, 1
s_cmp_lt_u32 s47, 0x20000
s_cbranch_scc1 POLLpc3_%=
s_mov_b32 s46, 0x18000
v_mov_b32 v17, 0
v_mov_b32 v18, 1
buffer_store_dword v18, v17, s[76:79], s46 offen sc1
POLLDpc3_%=:
s_branch RETppc3_%=
OOLwyp_%=:
s_nop 1
s_lshl_b32 s46, s5, 6
s_add_u32 s46, s46, 0x10000
v_mov_b32 v17, 0
v_mov_b32 v18, s33
s_mov_b64 exec, 1
buffer_store_dword v18, v17, s[76:79], s46 offen sc1
s_mov_b64 exec, -1
s_branch RETwyp_%=
OOLpyc1_%=:
s_nop 1
s_add_u32 s46, s74, 1
s_and_b32 s46, s46, 3
s_lshl_b32 s46, s46, 1
s_and_b32 s35, s5, 0xf9
s_or_b32 s46, s46, s35
s_lshl_b32 s46, s46, 6
s_add_u32 s46, s46, 0x10000
v_mov_b32 v17, 0
s_mov_b32 s47, 0
POLLyc1_%=:
buffer_load_dword v18, v17, s[76:79], s46 offen sc1
s_waitcnt vmcnt(0)
v_cmp_ne_u32 vcc, s33, v18
s_nop 1
s_cmp_eq_u64 vcc, 0
s_cbranch_scc1 POLLDyc1_%=
s_sleep 1
s_add_u32 s47, s47, 1
s_cmp_lt_u32 s47, 0x20000
s_cbranch_scc1 POLLyc1_%=
s_mov_b32 s46, 0x18000
v_mov_b32 v17, 0
v_mov_b32 v18, 1
buffer_store_dword v18, v17, s[76:79], s46 offen sc1
POLLDyc1_%=:
s_branch RETpyc1_%=
OOLpyc2_%=:
s_nop 1
s_add_u32 s46, s74, 2
s_and_b32 s46, s46, 3
s_lshl_b32 s46, s46, 1
s_and_b32 s35, s5, 0xf9
s_or_b32 s46, s46, s35
s_lshl_b32 s46, s46, 6
s_add_u32 s46, s46, 0x10000
v_mov_b32 v17, 0
s_mov_b32 s47, 0
POLLyc2_%=:
buffer_load_dword v18, v17, s[76:79], s46 offen sc1
s_waitcnt vmcnt(0)
v_cmp_ne_u32 vcc, s33, v18
s_nop 1
s_cmp_eq_u64 vcc, 0
s_cbranch_scc1 POLLDyc2_%=
s_sleep 1
s_add_u32 s47, s47, 1
s_cmp_lt_u32 s47, 0x20000
s_cbranch_scc1 POLLyc2_%=
s_mov_b32 s46, 0x18000
v_mov_b32 v17, 0
v_mov_b32 v18, 1
buffer_store_dword v18, v17, s[76:79], s46 offen sc1
POLLDyc2_%=:
s_branch RETpyc2_%=
OOLpyc3_%=:
s_nop 1
s_add_u32 s46, s74, 3
s_and_b32 s46, s46, 3
s_lshl_b32 s46, s46, 1
s_and_b32 s35, s5, 0xf9
s_or_b32 s46, s46, s35
s_lshl_b32 s46, s46, 6
s_add_u32 s46, s46, 0x10000
v_mov_b32 v17, 0
s_mov_b32 s47, 0
POLLyc3_%=:
buffer_load_dword v18, v17, s[76:79], s46 offen sc1
s_waitcnt vmcnt(0)
v_cmp_ne_u32 vcc, s33, v18
s_nop 1
s_cmp_eq_u64 vcc, 0
s_cbranch_scc1 POLLDyc3_%=
s_sleep 1
s_add_u32 s47, s47, 1
s_cmp_lt_u32 s47, 0x20000
s_cbranch_scc1 POLLyc3_%=
s_mov_b32 s46, 0x18000
v_mov_b32 v17, 0
v_mov_b32 v18, 1
buffer_store_dword v18, v17, s[76:79], s46 offen sc1
POLLDyc3_%=:
s_branch RETpyc3_%=
OOLrxs_%=:
s_nop 1
s_mul_i32 s34, s5, 0x18000
s_mul_i32 s35, s4, 0x6000
s_add_u32 s34, s34, s35
s_add_u32 s34, s34, 0x2303000
s_mov_b32 s47, 0
RXS_%=:
s_add_u32 s38, s34, 0
buffer_load_dwordx4 v[24:27], v0, s[76:79], s38 offen sc1
buffer_load_dwordx4 v[28:31], v0, s[76:79], s38 offen offset:1024 sc1
buffer_load_dwordx4 v[32:35], v0, s[76:79], s38 offen offset:2048 sc1
buffer_load_dwordx4 v[36:39], v0, s[76:79], s38 offen offset:3072 sc1
s_add_u32 s38, s34, 0x1000
buffer_load_dwordx4 v[40:43], v0, s[76:79], s38 offen sc1
buffer_load_dwordx4 v[44:47], v0, s[76:79], s38 offen offset:1024 sc1
buffer_load_dwordx4 v[48:51], v0, s[76:79], s38 offen offset:2048 sc1
buffer_load_dwordx4 v[52:55], v0, s[76:79], s38 offen offset:3072 sc1
s_add_u32 s38, s34, 0x2000
buffer_load_dwordx4 v[56:59], v0, s[76:79], s38 offen sc1
buffer_load_dwordx4 v[60:63], v0, s[76:79], s38 offen offset:1024 sc1
buffer_load_dwordx4 v[64:67], v0, s[76:79], s38 offen offset:2048 sc1
buffer_load_dwordx4 v[68:71], v0, s[76:79], s38 offen offset:3072 sc1
s_waitcnt vmcnt(0)
v_max3_u32 v17, v24, v25, v26
v_max3_u32 v17, v17, v27, v28
v_max3_u32 v17, v17, v29, v30
v_max3_u32 v17, v17, v31, v32
v_max3_u32 v17, v17, v33, v34
v_max3_u32 v17, v17, v35, v36
v_max3_u32 v17, v17, v37, v38
v_max3_u32 v17, v17, v39, v40
v_max3_u32 v17, v17, v41, v42
v_max3_u32 v17, v17, v43, v44
v_max3_u32 v17, v17, v45, v46
v_max3_u32 v17, v17, v47, v48
v_max3_u32 v17, v17, v49, v50
v_max3_u32 v17, v17, v51, v52
v_max3_u32 v17, v17, v53, v54
v_max3_u32 v17, v17, v55, v56
v_max3_u32 v17, v17, v57, v58
v_max3_u32 v17, v17, v59, v60
v_max3_u32 v17, v17, v61, v62
v_max3_u32 v17, v17, v63, v64
v_max3_u32 v17, v17, v65, v66
v_max3_u32 v17, v17, v67, v68
v_max3_u32 v17, v17, v69, v70
v_max_u32 v17, v17, v71
v_cmp_eq_u32 vcc, -1, v17
s_nop 1
s_cmp_eq_u64 vcc, 0
s_cbranch_scc1 RXDS_%=
s_sleep 4
s_add_u32 s47, s47, 1
s_cmp_lt_u32 s47, 0x20000
s_cbranch_scc1 RXS_%=
s_mov_b32 s46, 0x18000
v_mov_b32 v17, 0
v_mov_b32 v18, 1
buffer_store_dword v18, v17, s[76:79], s46 offen sc1
RXDS_%=:
s_branch RETrxs_%=
OOLep_%=:
s_nop 1
s_mov_b32 s46, 0
v_mov_b32 v20, 0
v_mov_b32 v21, s33
s_mov_b64 exec, 1
buffer_store_dword v21, v20, s[76:79], s46 offen sc1
s_mov_b64 exec, -1
s_branch RETep_%=
END_%=:

)ASM"
    :: "s"(__builtin_amdgcn_kernarg_segment_ptr()), "s"(blockIdx.x), "s"(__builtin_amdgcn_readfirstlane(threadIdx.x >> 6))
    : "v0","v1","v2","v3","v4","v5","v6","v7","v8","v9","v10","v11","v12","v13","v14","v15","v16","v17","v18","v19","v20","v21","v22","v23","v24","v25","v26","v27","v28","v29","v30","v31","v32","v33","v34","v35","v36","v37","v38","v39","v40","v41","v42","v43","v44","v45","v46","v47","v48","v49","v50","v51","v52","v53","v54","v55","v56","v57","v58","v59","v60","v61","v62","v63","v64","v65","v66","v67","v68","v69","v70","v71","v72","v73","v74","v75","v76","v77","v78","v79","v80","v81","v82","v83","v84","v85","v86","v87","v88","v89","v90","v91","v92","v93","v94","v95","v96","v97","v98","v99","v100","v101","v102","v103","v104","v105","v106","v107","v108","v109","v110","v111","v112","v113","v114","v115","v116","v117","v118","v119","v120","v121","v122","v123","v124","v125","v126","v127","v128","v129","v130","v131","v132","v133","v134","v135","v136","v137","v138","v139","v140","v141","v142","v143","v144","v145","v146","v147","v148","v149","v150","v151","v152","v153","v154","v155","v156","v157","v158","v159","v160","v161","v162","v163","v164","v165","v166","v167","v168","v169","v170","v171","v172","v173","v174","v175","v176","v177","v178","v179","v180","v181","v182","v183","v184","v185","v186","v187","v188","v189","v190","v191","v192","v193","v194","v195","v196","v197","v198","v199","v200","v201","v202","v203","v204","v205","v206","v207","v208","v209","v210","v211","v212","v213","v214","v215","v216","v217","v218","v219","v220","v221","v222","v223","v224","v225","v226","v227","v228","v229","v230","v231","v232","v233","v234","v235","v236","v237","v238","v239","v240","v241","v242","v243","v244","v245","v246","v247","v248","v249","v250","v251","v252","v253","v254","v255","a0","a1","a2","a3","a4","a5","a6","a7","a8","a9","a10","a11","a12","a13","a14","a15","a16","a17","a18","a19","a20","a21","a22","a23","a24","a25","a26","a27","a28","a29","a30","a31","a32","a33","a34","a35","a36","a37","a38","a39","a40","a41","a42","a43","a44","a45","a46","a47","a48","a49","a50","a51","a52","a53","a54","a55","a56","a57","a58","a59","a60","a61","a62","a63","a64","a65","a66","a67","a68","a69","a70","a71","a72","a73","a74","a75","a76","a77","a78","a79","a80","a81","a82","a83","a84","a85","a86","a87","a88","a89","a90","a91","a92","a93","a94","a95","a96","a97","a98","a99","a100","a101","a102","a103","a104","a105","a106","a107","a108","a109","a110","a111","a112","a113","a114","a115","a116","a117","a118","a119","a120","a121","a122","a123","a124","a125","a126","a127","a128","a129","a130","a131","a132","a133","a134","a135","a136","a137","a138","a139","a140","a141","a142","a143","a144","a145","a146","a147","a148","a149","a150","a151","a152","a153","a154","a155","a156","a157","a158","a159","a160","a161","a162","a163","a164","a165","a166","a167","a168","a169","a170","a171","a172","a173","a174","a175","a176","a177","a178","a179","a180","a181","a182","a183","a184","a185","a186","a187","a188","a189","a190","a191","a192","a193","a194","a195","a196","a197","a198","a199","a200","a201","a202","a203","a204","a205","a206","a207","a208","a209","a210","a211","a212","a213","a214","a215","a216","a217","a218","a219","a220","a221","a222","a223","a224","a225","a226","a227","a228","a229","a230","a231","a232","a233","a234","a235","a236","a237","a238","a239","a240","a241","a242","a243","a244","a245","a246","a247","a248","a249","a250","a251","a252","a253","a254","a255","s4","s5","s6","s7","s8","s9","s10","s11","s12","s13","s14","s15","s16","s17","s18","s19","s20","s21","s22","s23","s24","s25","s26","s27","s28","s29","s30","s31","s33","s34","s35","s36","s37","s38","s39","s40","s41","s42","s43","s44","s45","s46","s47","s48","s49","s50","s51","s52","s53","s54","s55","s56","s57","s58","s59","s60","s61","s62","s63","s64","s65","s66","s67","s68","s69","s70","s71","s72","s73","s74","s75","s76","s77","s78","s79","s80","s81","s82","s83","s84","s85","s86","s87","s88","s89","s90","s91","s92","s93","s94","s95","s96","s97","s98","s99","vcc","memory");
}


extern "C" void kernel_launch(void* const* d_in, const int* in_sizes, int n_in, void* d_out, int out_size, void* d_ws, size_t ws_size, hipStream_t stream) {
  (void)in_sizes; (void)n_in; (void)out_size; (void)ws_size;
  (void)hipFuncSetAttribute((const void*)fused_main, hipFuncAttributeMaxDynamicSharedMemorySize, 163840);
  Params p;
  p.x = d_in[0]; p.kc = d_in[1]; p.vc = d_in[2]; p.wq = d_in[3]; p.wo = d_in[4];
  p.wg = d_in[6]; p.wu = d_in[7]; p.wd = d_in[8]; p.out = d_out; p.ws = d_ws;
  fused_main<<<256, 256, 163840, stream>>>(p);
}
```
